```python
import torch

def _softmax(x, axis=-1):
    return torch.softmax(x, dim=axis)
def _gelu(x):
    return torch.nn.functional.gelu(x, approximate="tanh")
def _silu(x):
    return torch.nn.functional.silu(x)
def _sigmoid(x):
    return torch.sigmoid(x)
def _layernorm(x, axis=-1, eps=1e-5):
    m = x.mean(dim=axis, keepdim=True)
    v = x.var(dim=axis, keepdim=True, unbiased=False)
    return (x - m) / torch.sqrt(v + eps)
def _scatter_add(tgt, idx, src, axis=0):
    return tgt.clone().index_add_(axis, idx, src)
def _segment_sum(src, seg, n):
    out = torch.zeros((n,) + src.shape[1:], dtype=src.dtype)
    return out.index_add_(0, seg, src)
def _pad(x, pw):
    flat = tuple(v for pair in reversed(pw) for v in pair)
    return torch.nn.functional.pad(x, flat)

kernel_name = 'sequential_preLN__compressed_kv_cla__dense_activation_squared_relu__prompt'

def setup_inputs():

    _w = (lambda a: (lambda t: t.to(torch.bfloat16) if t.is_floating_point() else t)(torch.as_tensor(a)))
    def _A_setup():
        B, H, Sq, Sk, D, M = 1, 32, 2048, 2048, 128, 576
        Dc = 4 * D
        return {
            "x": _w((lambda *s: torch.randn(*s, dtype=torch.bfloat16) * 0.05)(B, Sq, M)),
            "c_kv": _w((lambda *s: torch.randn(*s, dtype=torch.bfloat16) * 0.4)(B, Sk, Dc)),
            "w_q": _w((lambda *s: torch.randn(*s, dtype=torch.bfloat16) * 3.2)(M, H, D) * (M ** -0.5)),
            "w_uk": _w((lambda *s: torch.randn(*s, dtype=torch.bfloat16) * 3.2)(Dc, H, D) * (Dc ** -0.5)),
            "w_uv": _w((lambda *s: torch.randn(*s, dtype=torch.bfloat16) * 0.4)(Dc, H, D) * (Dc ** -0.5)),
            "w_o": _w((lambda *s: torch.randn(*s, dtype=torch.bfloat16) * 0.4)(H, D, M) * ((H * D) ** -0.5)),
        }
    _A_in = _A_setup()

    def _B_setup():
        B, S, D, F = 1, 2048, 576, 4608
        return {
            "x": _w((lambda *s: torch.randn(*s, dtype=torch.bfloat16) * 0.05)(B, S, D)),
            "w1": _w((lambda *s: torch.randn(*s, dtype=torch.bfloat16) * 0.05)(D, F)),
            "b1": _w((lambda *s: torch.randn(*s, dtype=torch.bfloat16) * 0.4)(F)),
            "w2": _w((lambda *s: torch.randn(*s, dtype=torch.bfloat16) * 0.05)(F, D)),
            "b2": _w((lambda *s: torch.randn(*s, dtype=torch.bfloat16) * 3.2)(D)),
        }
    _B_in = _B_setup()

    return {
        "A_x": _A_in["x"],
        "A_c_kv": _A_in["c_kv"],
        "A_w_q": _A_in["w_q"],
        "A_w_uk": _A_in["w_uk"],
        "A_w_uv": _A_in["w_uv"],
        "A_w_o": _A_in["w_o"],
        "B_x": _B_in["x"],
        "B_w1": _B_in["w1"],
        "B_b1": _B_in["b1"],
        "B_w2": _B_in["w2"],
        "B_b2": _B_in["b2"],
    }

def reference(A_x, A_c_kv, A_w_q, A_w_uk, A_w_uv, A_w_o, B_x, B_w1, B_b1, B_w2, B_b2):
    def _A_ref(x, c_kv, w_q, w_uk, w_uv, w_o):
        B, Sq, M = x.shape[0], x.shape[1], x.shape[2]
        Sk = c_kv.shape[1]
        H, D = w_q.shape[1], w_q.shape[2]
        q = torch.einsum("bqm,mhd->bhqd", x, w_q)
        k = torch.einsum("bkc,chd->bhkd", c_kv, w_uk)
        v = torch.einsum("bkc,chd->bhkd", c_kv, w_uv)
        scores = torch.einsum("bhqd,bhkd->bhqk", q, k) * (D ** -0.5)
        off = Sk - Sq
        qi = torch.arange(Sq)[:, None]
        kj = torch.arange(Sk)[None, :]
        cmask = kj <= (qi + off)
        scores = torch.where(cmask[None, None], scores, -1e9)
        p = _softmax(scores, axis=-1)
        ctx = torch.einsum("bhqk,bhkd->bhqd", p, v)
        return torch.einsum("bhqd,hdm->bqm", ctx, w_o)

    def _B_ref(x, w1, b1, w2, b2):
        h = torch.einsum("bsd,df->bsf", x, w1) + b1
        zero = torch.zeros_like(h)
        r = torch.where(h > zero, h, zero)
        h = r * r
        out = torch.einsum("bsf,fd->bsd", h, w2) + b2
        return out

    x = A_x
    h = x + _A_ref(_layernorm(x, axis=-1), A_c_kv, A_w_q, A_w_uk, A_w_uv, A_w_o)
    h2 = h + _B_ref(_layernorm(h, axis=-1), B_w1, B_b1, B_w2, B_b2)
    return h2
```

```cpp
#include <hip/hip_runtime.h>
#include <stdint.h>
#include <utility>

typedef __bf16 bf16x8 __attribute__((ext_vector_type(8)));
typedef __bf16 bf16x4 __attribute__((ext_vector_type(4)));
typedef __bf16 bf16x2 __attribute__((ext_vector_type(2)));
typedef float f32x16 __attribute__((ext_vector_type(16)));
typedef float f32x4 __attribute__((ext_vector_type(4)));
typedef float f32x2 __attribute__((ext_vector_type(2)));
typedef unsigned u32x4 __attribute__((ext_vector_type(4)));
typedef unsigned u32x2 __attribute__((ext_vector_type(2)));
typedef unsigned short u16;
#define LAS __attribute__((address_space(3)))
#define DI __device__ __forceinline__
typedef __amdgpu_buffer_rsrc_t rsrc_t;

constexpr int SEQ = 2048, DM = 576, NH = 32, HD = 128, DC = 512, FF = 4608, NQ = 4096;

constexpr size_t WS_XN = 0;
constexpr size_t WS_Q = WS_XN + (size_t)SEQ * DM * 2;
constexpr size_t WS_K = WS_Q + (size_t)SEQ * NQ * 2;
constexpr size_t WS_V = WS_K + (size_t)SEQ * NQ * 2;
constexpr size_t WS_CTX = WS_V + (size_t)SEQ * NQ * 2;
constexpr size_t WS_P1 = WS_CTX + (size_t)SEQ * NQ * 2;
constexpr size_t WS_H = WS_P1 + (size_t)4 * SEQ * DM * 4;
constexpr size_t WS_HN = WS_H + (size_t)SEQ * DM * 2;
constexpr size_t WS_H1 = WS_HN + (size_t)SEQ * DM * 2;
constexpr size_t WS_P2 = WS_H1 + (size_t)SEQ * FF * 2;
constexpr size_t WS_END = WS_P2 + (size_t)4 * SEQ * DM * 4;

template <int... Is> using iseq = std::integer_sequence<int, Is...>;
template <int N> using make_iseq = std::make_integer_sequence<int, N>;
template <int I> struct IC { static constexpr int v = I; };
template <class F, int... Is> DI void static_for_(F&& f, iseq<Is...>) { (f(IC<Is>{}), ...); }
template <int N, class F> DI void static_for(F&& f) { static_for_(f, make_iseq<N>{}); }

DI unsigned rfl(unsigned x) { return __builtin_amdgcn_readfirstlane(x); }
DI rsrc_t mk_rsrc(const void* p, unsigned bytes) { return __builtin_amdgcn_make_buffer_rsrc((void*)p, (short)0, (int)bytes, 0x00020000); }
DI void dma16(unsigned lds_addr, unsigned voff, rsrc_t r, unsigned soff) {
  asm volatile("s_mov_b32 m0, %0\n\ts_nop 0\n\tbuffer_load_dwordx4 %1, %2, %3 offen lds" ::"s"(lds_addr), "v"(voff), "s"(r), "s"(soff) : "memory", "m0");
}
template <int N> DI void wait_vm() { asm volatile("s_waitcnt vmcnt(%0)" ::"i"(N) : "memory"); }
DI void bar() { asm volatile("s_waitcnt lgkmcnt(0)" ::: "memory"); __builtin_amdgcn_s_barrier(); asm volatile("" ::: "memory"); }
DI unsigned cvtpk(float lo, float hi) { f32x2 v = {lo, hi}; bf16x2 b = __builtin_convertvector(v, bf16x2); return __builtin_bit_cast(unsigned, b); }
DI float bf2f(u16 x) { return __uint_as_float((unsigned)x << 16); }
DI float bflo(unsigned x) { return __uint_as_float(x << 16); }
DI float bfhi(unsigned x) { return __uint_as_float(x & 0xffff0000u); }
DI bf16x8 lds_b128(unsigned a) { return *(LAS bf16x8*)(size_t)a; }
DI bf16x4 lds_tr(unsigned a) { return __builtin_amdgcn_ds_read_tr16_b64_v4bf16((LAS bf16x4*)(size_t)a); }
DI bf16x8 cat(bf16x4 a, bf16x4 b) { return __builtin_shufflevector(a, b, 0, 1, 2, 3, 4, 5, 6, 7); }
#define MFMA32(a, b, c) __builtin_amdgcn_mfma_f32_32x32x16_bf16((a), (b), (c), 0, 0, 0)
#define MFMA16(a, b, c) __builtin_amdgcn_mfma_f32_16x16x32_bf16((a), (b), (c), 0, 0, 0)
template <int S> DI u32x4 pack8(const f32x16& x, float sc) {
  u32x4 w;
  w[0] = cvtpk(x[8 * S + 0] * sc, x[8 * S + 1] * sc); w[1] = cvtpk(x[8 * S + 2] * sc, x[8 * S + 3] * sc);
  w[2] = cvtpk(x[8 * S + 4] * sc, x[8 * S + 5] * sc); w[3] = cvtpk(x[8 * S + 6] * sc, x[8 * S + 7] * sc);
  return w;
}
template <int S> DI u32x4 pack8(const f32x16& x) {
  u32x4 w;
  w[0] = cvtpk(x[8 * S + 0], x[8 * S + 1]); w[1] = cvtpk(x[8 * S + 2], x[8 * S + 3]);
  w[2] = cvtpk(x[8 * S + 4], x[8 * S + 5]); w[3] = cvtpk(x[8 * S + 6], x[8 * S + 7]);
  return w;
}

__global__ __launch_bounds__(64) void ln1_kernel(const u16* __restrict__ x, char* __restrict__ xn) {
  const int lane = threadIdx.x, j = lane & 7, row = blockIdx.x * 8 + (lane >> 3);
  u32x4 v[9];
#pragma unroll
  for (int i = 0; i < 9; ++i) v[i] = *(const u32x4*)(x + (size_t)row * DM + (j + 8 * i) * 8);
  float s = 0.f;
#pragma unroll
  for (int i = 0; i < 9; ++i)
#pragma unroll
    for (int e = 0; e < 4; ++e) s += bflo(v[i][e]) + bfhi(v[i][e]);
  s += __shfl_xor(s, 1); s += __shfl_xor(s, 2); s += __shfl_xor(s, 4);
  const float mean = s * (1.f / DM);
  float q = 0.f;
#pragma unroll
  for (int i = 0; i < 9; ++i)
#pragma unroll
    for (int e = 0; e < 4; ++e) { float a = bflo(v[i][e]) - mean, b = bfhi(v[i][e]) - mean; q += a * a + b * b; }
  q += __shfl_xor(q, 1); q += __shfl_xor(q, 2); q += __shfl_xor(q, 4);
  const float rs = rsqrtf(q * (1.f / DM) + 1e-5f);
  const int mb = row >> 5, r = row & 31;
#pragma unroll
  for (int i = 0; i < 9; ++i) {
    const int c = j + 8 * i;
    u32x4 w;
#pragma unroll
    for (int e = 0; e < 4; ++e) w[e] = cvtpk((bflo(v[i][e]) - mean) * rs, (bfhi(v[i][e]) - mean) * rs);
    *(u32x4*)(xn + ((size_t)(mb * 36 + (c >> 1)) * 64 + (c & 1) * 32 + r) * 16) = w;
  }
}

constexpr float QSCALE = 0.08838834764831845f * 1.4426950408889634f;
template <bool ISV> DI void kv_compute(f32x16 (&acc)[4][2], unsigned slot, unsigned xb0, unsigned xb1, unsigned wb0, unsigned wb1) {
#pragma unroll
  for (int s = 0; s < 2; ++s) {
    bf16x8 xf[4], wf[2];
    const unsigned xb = s ? xb1 : xb0;
#pragma unroll
    for (int i = 0; i < 4; ++i) xf[i] = lds_b128(xb + slot + i * 2048);
    wf[0] = cat(lds_tr(wb0 + slot + (16 * s) * 128), lds_tr(wb0 + slot + (16 * s + 4) * 128));
    wf[1] = cat(lds_tr(wb1 + slot + (16 * s) * 128), lds_tr(wb1 + slot + (16 * s + 4) * 128));
#pragma unroll
    for (int i = 0; i < 4; ++i)
#pragma unroll
      for (int j = 0; j < 2; ++j) acc[i][j] = ISV ? MFMA32(xf[i], wf[j], acc[i][j]) : MFMA32(wf[j], xf[i], acc[i][j]);
  }
}

__global__ __launch_bounds__(512, 2) void qkv_kernel(const u16* __restrict__ ckv, const char* __restrict__ xn, const u16* __restrict__ wq,
                                                     const u16* __restrict__ wuk, const u16* __restrict__ wuv, char* __restrict__ Qo,
                                                     char* __restrict__ Ko, char* __restrict__ Vo) {
  extern __shared__ __attribute__((aligned(16))) char lds[];
  const unsigned ldsb = (unsigned)(size_t)lds;
  const unsigned tid = threadIdx.x, lane = tid & 63, w = rfl(tid >> 6);
  const unsigned b = blockIdx.x, xcd = b & 7, jj = b >> 3, head = xcd * 4 + (jj >> 3), c = jj & 7;
  const unsigned l31 = lane & 31, h = lane >> 5, g2 = (lane >> 4) & 1, q = (lane & 15) >> 2, pp = lane & 3;
  {
    constexpr int NS = 4, NSTEPS = 16, P = 4;
    constexpr unsigned SLOT = 32768;
    const rsrc_t rx = mk_rsrc(ckv, SEQ * DC * 2);
    const rsrc_t rw = mk_rsrc(w < 4 ? wuk : wuv, DC * NQ * 2);
    const unsigned voffX = (lane >> 2) * 1024 + (((lane & 3) ^ ((lane >> 4) & 3)) << 4);
    const unsigned voffW = (lane >> 3) * 8192 + (((lane & 7) ^ (((lane >> 4) & 1) << 2)) << 4);
    const unsigned sX = (c * 256 + 32 * w) * 1024;
    const unsigned sW = ((2 * w & 3) * 8) * 8192 + (head * 128 + ((w >> 1) & 1) * 64) * 2;
    const unsigned dX = ldsb + 2 * w * 1024, dW = ldsb + 16384 + 2 * w * 1024;
    auto issue = [&](auto T) {
      constexpr int t = decltype(T)::v; constexpr unsigned so = (t % NS) * SLOT;
      dma16(dX + so, voffX, rx, sX + 64 * t);
      dma16(dX + so + 1024, voffX, rx, sX + 64 * t + 16 * 1024);
      dma16(dW + so, voffW, rw, sW + 32 * t * 8192);
      dma16(dW + so + 1024, voffW, rw, sW + 32 * t * 8192 + 8 * 8192);
    };
    const unsigned wm = w >> 2, wn = w & 3;
    const unsigned xsw = (l31 >> 2) & 3;
    const unsigned xb0 = ldsb + (wm * 128 + l31) * 64 + ((h ^ xsw) << 4), xb1 = ldsb + (wm * 128 + l31) * 64 + (((2 + h) ^ xsw) << 4);
    const unsigned wrow = 8 * h + q, wch = (g2 * 2 + (pp >> 1)) ^ ((q >> 1) << 2);
    const unsigned wb0 = ldsb + 16384 + wn * 4096 + wrow * 128 + (wch << 4) + 8 * (pp & 1);
    const unsigned wb1 = ldsb + 16384 + wn * 4096 + wrow * 128 + ((wch ^ 4) << 4) + 8 * (pp & 1);
    static_for<NS - 1>([&](auto T) { issue(T); });
    auto body = [&](auto ISV_) {
      constexpr bool ISV = decltype(ISV_)::v != 0;
      f32x16 acc[4][2];
#pragma unroll
      for (int i = 0; i < 4; ++i)
#pragma unroll
        for (int j = 0; j < 2; ++j) acc[i][j] = f32x16{};
      static_for<NSTEPS>([&](auto T) {
        constexpr int t = decltype(T)::v;
        constexpr int ahead = (NS - 2) < (NSTEPS - 1 - t) ? (NS - 2) : (NSTEPS - 1 - t);
        wait_vm<P * ahead>();
        bar();
        if constexpr (t + NS - 1 < NSTEPS) issue(IC<t + NS - 1>{});
        kv_compute<ISV>(acc, (t % NS) * SLOT, xb0, xb1, wb0, wb1);
      });
#pragma unroll
      for (int i = 0; i < 4; ++i)
#pragma unroll
        for (int j = 0; j < 2; ++j) {
          const unsigned kb = c * 8 + wm * 4 + i, db = 2 * (wn & 1) + j;
          char* p = ISV ? Vo + ((size_t)(((head * 32 + (kb >> 1)) * 4 + db) * 4 + 2 * (kb & 1)) * 64 + lane) * 16
                        : Ko + ((size_t)((head * 64 + kb) * 8 + 2 * db) * 64 + lane) * 16;
          *(u32x4*)p = pack8<0>(acc[i][j]);
          *(u32x4*)(p + 1024) = pack8<1>(acc[i][j]);
        }
    };
    if (wn >= 2) body(IC<1>{}); else body(IC<0>{});
  }
  bar();
  {
    constexpr int NS = 4, NSTEPS = 18, P = 3;
    constexpr unsigned SLOT = 24576;
    const rsrc_t rx = mk_rsrc(xn, SEQ * DM * 2);
    const rsrc_t rw = mk_rsrc(wq, DM * NQ * 2);
    const unsigned voffX = lane * 16;
    const unsigned voffW = (lane >> 3) * 8192 + (((lane & 7) ^ (((lane >> 4) & 1) << 2)) << 4);
    const unsigned sX = (c * 8 + w) * 36 * 1024;
    const unsigned sW = ((w & 3) * 8) * 8192 + (head * 128 + (w >> 2) * 64) * 2;
    const unsigned dX = ldsb + 2 * w * 1024, dW = ldsb + 16384 + w * 1024;
    auto issue = [&](auto T) {
      constexpr int t = decltype(T)::v; constexpr unsigned so = (t % NS) * SLOT;
      dma16(dX + so, voffX, rx, sX + 2048 * t);
      dma16(dX + so + 1024, voffX, rx, sX + 2048 * t + 1024);
      dma16(dW + so, voffW, rw, sW + 32 * t * 8192);
    };
    const unsigned wm = w >> 1, wn = w & 1;
    const unsigned xb = ldsb + wm * 4096 + lane * 16;
    const unsigned wrow = 8 * h + q, wch = (g2 * 2 + (pp >> 1)) ^ ((q >> 1) << 2);
    const unsigned wb0 = ldsb + 16384 + wn * 4096 + wrow * 128 + (wch << 4) + 8 * (pp & 1);
    const unsigned wb1 = ldsb + 16384 + wn * 4096 + wrow * 128 + ((wch ^ 4) << 4) + 8 * (pp & 1);
    f32x16 acc[2][2];
#pragma unroll
    for (int i = 0; i < 2; ++i)
#pragma unroll
      for (int j = 0; j < 2; ++j) acc[i][j] = f32x16{};
    static_for<NS - 1>([&](auto T) { issue(T); });
    static_for<NSTEPS>([&](auto T) {
      constexpr int t = decltype(T)::v;
      constexpr int ahead = (NS - 2) < (NSTEPS - 1 - t) ? (NS - 2) : (NSTEPS - 1 - t);
      wait_vm<P * ahead>();
      bar();
      if constexpr (t + NS - 1 < NSTEPS) issue(IC<t + NS - 1>{});
      constexpr unsigned slot = (t % NS) * SLOT;
#pragma unroll
      for (int s = 0; s < 2; ++s) {
        bf16x8 xf[2], wf[2];
#pragma unroll
        for (int i = 0; i < 2; ++i) xf[i] = lds_b128(xb + slot + i * 2048 + s * 1024);
        wf[0] = cat(lds_tr(wb0 + slot + (16 * s) * 128), lds_tr(wb0 + slot + (16 * s + 4) * 128));
        wf[1] = cat(lds_tr(wb1 + slot + (16 * s) * 128), lds_tr(wb1 + slot + (16 * s + 4) * 128));
#pragma unroll
        for (int i = 0; i < 2; ++i)
#pragma unroll
          for (int j = 0; j < 2; ++j) acc[i][j] = MFMA32(wf[j], xf[i], acc[i][j]);
      }
    });
#pragma unroll
    for (int i = 0; i < 2; ++i)
#pragma unroll
      for (int j = 0; j < 2; ++j) {
        const unsigned qb = c * 8 + wm * 2 + i, db = 2 * wn + j;
        char* p = Qo + ((size_t)((head * 64 + qb) * 8 + 2 * db) * 64 + lane) * 16;
        *(u32x4*)p = pack8<0>(acc[i][j], QSCALE);
        *(u32x4*)(p + 1024) = pack8<1>(acc[i][j], QSCALE);
      }
  }
}

DI float xhalf_max(float x) {
  auto r = __builtin_amdgcn_permlane32_swap(__float_as_uint(x), __float_as_uint(x), false, false);
  return fmaxf(__uint_as_float(r[0]), __uint_as_float(r[1]));
}
DI float xhalf_sum(float x) {
  auto r = __builtin_amdgcn_permlane32_swap(__float_as_uint(x), __float_as_uint(x), false, false);
  return __uint_as_float(r[0]) + __uint_as_float(r[1]);
}

__global__ __launch_bounds__(512, 2) void attn_kernel(const char* __restrict__ Q, const char* __restrict__ K, const char* __restrict__ V,
                                                      char* __restrict__ ctx) {
  extern __shared__ __attribute__((aligned(16))) char lds[];
  const unsigned ldsb = (unsigned)(size_t)lds;
  const unsigned tid = threadIdx.x, lane = tid & 63, w = rfl(tid >> 6);
  const unsigned b = blockIdx.x, xcd = b & 7, jj = b >> 3, head = xcd * 4 + (jj >> 3), pi = jj & 7;
  const unsigned l31 = lane & 31, h = lane >> 5;
  const unsigned kh = w >> 2, rg = kh ? 7 - w : w, partner = kh ? rg : 7 - rg;
  const rsrc_t rk = mk_rsrc(K + (size_t)head * SEQ * HD * 2, SEQ * HD * 2);
  const rsrc_t rv = mk_rsrc(V + (size_t)head * SEQ * HD * 2, SEQ * HD * 2);
  const unsigned voff = lane * 16;
  const unsigned nhi = 16 - pi;
  auto issue = [&](unsigned s) {
    const unsigned kblk = s < nhi ? s : s - nhi;
    const unsigned so = kblk * 32768 + w * 4096, d = ldsb + (s & 1) * 65536 + w * 4096;
#pragma unroll
    for (int p = 0; p < 4; ++p) dma16(d + p * 1024, voff, rk, so + p * 1024);
#pragma unroll
    for (int p = 0; p < 4; ++p) dma16(d + 32768 + p * 1024, voff, rv, so + p * 1024);
  };
  issue(0);
  bf16x8 qf[8];
  f32x16 o[4];
  float m = -1e30f, l = 0.f;
  for (unsigned s = 0; s < 17; ++s) {
    const bool first = (s == 0) || (s == nhi), last = (s == nhi - 1) || (s == 16);
    const unsigned qblk = s < nhi ? 15 - pi : pi;
    if (first) {
      const char* qp = Q + ((size_t)((head * 64 + qblk * 4 + rg) * 8) * 64 + lane) * 16;
#pragma unroll
      for (int d = 0; d < 8; ++d) qf[d] = *(const bf16x8*)(qp + d * 1024);
#pragma unroll
      for (int d = 0; d < 4; ++d) o[d] = f32x16{};
      m = -1e30f; l = 0.f;
    }
    wait_vm<0>();
    bar();
    if (s + 1 < 17) issue(s + 1);
    const unsigned kbase = ldsb + (s & 1) * 65536 + kh * 16384 + lane * 16, vbase = kbase + 32768;
    const bool active = !last || (kh * 64 <= rg * 32 + 31);
    if (active) {
      f32x16 s0 = f32x16{}, s1 = f32x16{};
#pragma unroll
      for (int d = 0; d < 8; ++d) {
        s0 = MFMA32(lds_b128(kbase + d * 1024), qf[d], s0);
        s1 = MFMA32(lds_b128(kbase + 8192 + d * 1024), qf[d], s1);
      }
      if (last) {
        const int dq = (int)(rg * 32 + l31) - (int)(kh * 64) - 4 * (int)h;
#pragma unroll
        for (int r = 0; r < 16; ++r) {
          const int cr = (r & 3) + 8 * (r >> 2);
          if (cr > dq) s0[r] = -__builtin_inff();
          if (cr + 32 > dq) s1[r] = -__builtin_inff();
        }
      }
      float mx = s0[0];
#pragma unroll
      for (int r = 1; r < 16; ++r) mx = fmaxf(mx, s0[r]);
#pragma unroll
      for (int r = 0; r < 16; ++r) mx = fmaxf(mx, s1[r]);
      mx = xhalf_max(mx);
      const float mn = fmaxf(m, mx);
      const float alpha = __builtin_amdgcn_exp2f(m - mn);
      m = mn;
      float ps = 0.f;
#pragma unroll
      for (int r = 0; r < 16; ++r) { s0[r] = __builtin_amdgcn_exp2f(s0[r] - mn); ps += s0[r]; }
#pragma unroll
      for (int r = 0; r < 16; ++r) { s1[r] = __builtin_amdgcn_exp2f(s1[r] - mn); ps += s1[r]; }
      l = l * alpha + ps;
#pragma unroll
      for (int d = 0; d < 4; ++d)
#pragma unroll
        for (int r = 0; r < 16; ++r) o[d][r] *= alpha;
      bf16x8 pf[4];
      pf[0] = __builtin_bit_cast(bf16x8, pack8<0>(s0)); pf[1] = __builtin_bit_cast(bf16x8, pack8<1>(s0));
      pf[2] = __builtin_bit_cast(bf16x8, pack8<0>(s1)); pf[3] = __builtin_bit_cast(bf16x8, pack8<1>(s1));
#pragma unroll
      for (int d = 0; d < 4; ++d)
#pragma unroll
        for (int ks = 0; ks < 4; ++ks) o[d] = MFMA32(lds_b128(vbase + (d * 4 + ks) * 1024), pf[ks], o[d]);
    }
    if (last) {
      bar();
      const unsigned sc = ldsb + (s & 1) * 65536;
      const float lt = xhalf_sum(l);
      *(LAS f32x2*)(size_t)(sc + 65536 - 4096 + w * 512 + lane * 8) = f32x2{m, lt};
      bar();
      const f32x2 ps = *(LAS f32x2*)(size_t)(sc + 65536 - 4096 + partner * 512 + lane * 8);
      const float mm = fmaxf(m, ps[0]);
      const float fo = __builtin_amdgcn_exp2f(m - mm), fp = __builtin_amdgcn_exp2f(ps[0] - mm);
      const float f = fo / (lt * fo + ps[1] * fp);
      bar();
#pragma unroll
      for (int r = 0; r < 2; ++r) {
        const f32x16 x = kh ? o[r] : o[2 + r];
#pragma unroll
        for (int e = 0; e < 4; ++e)
          *(LAS f32x4*)(size_t)(sc + w * 8192 + (r * 4 + e) * 1024 + lane * 16) = f32x4{x[4 * e] * f, x[4 * e + 1] * f, x[4 * e + 2] * f, x[4 * e + 3] * f};
      }
      bar();
      const unsigned mb = qblk * 4 + rg;
#pragma unroll
      for (int r = 0; r < 2; ++r) {
        f32x16 x = kh ? o[2 + r] : o[r];
#pragma unroll
        for (int e = 0; e < 4; ++e) {
          const f32x4 y = *(LAS f32x4*)(size_t)(sc + partner * 8192 + (r * 4 + e) * 1024 + lane * 16);
#pragma unroll
          for (int k = 0; k < 4; ++k) x[4 * e + k] = x[4 * e + k] * f + y[k];
        }
        const unsigned db = kh * 2 + r;
        char* p = ctx + ((size_t)(mb * 256 + head * 8 + 2 * db) * 64 + lane) * 16;
        *(u32x4*)p = pack8<0>(x);
        *(u32x4*)(p + 1024) = pack8<1>(x);
      }
    }
  }
}

template <int MODE, int KORD, int NSTEPS, int NS>
DI void gemm16_core(f32x4 (&acc)[2][9], unsigned ldsb, unsigned w, unsigned lane, rsrc_t rx, unsigned xoff0  ,
                    unsigned xmb_stride, rsrc_t rw, unsigned woff0  , unsigned wpitch) {
  constexpr unsigned XB = MODE == 0 ? 32768 : 16384, SLOT = XB + 18432;
  constexpr int PX = MODE == 0 ? 4 : 2, P = PX + 3;
  const unsigned voffX = lane * 16;
  unsigned voffW[3];
#pragma unroll
  for (int i = 0; i < 3; ++i) { const unsigned ci = lane + 64 * i, r = ci / 18, cc = ci - r * 18; voffW[i] = r * wpitch + cc * 16; }
  const unsigned sX = MODE == 0 ? xoff0 + w * xmb_stride : xoff0 + (w >> 1) * xmb_stride + (w & 1) * 2048;
  const unsigned sW = woff0 + 8 * w * wpitch;
  const unsigned dX = ldsb + w * (PX * 1024), dW = ldsb + XB + w * 2304;
  auto issue = [&](auto T) {
    constexpr int t = decltype(T)::v; constexpr unsigned so = (t % NS) * SLOT;
#pragma unroll
    for (int p = 0; p < PX; ++p) dma16(dX + so + p * 1024, voffX, rx, sX + 4096 * t + p * 1024);
    const unsigned sw = sW + 64 * t * wpitch;
    dma16(dW + so, voffW[0], rw, sw);
    dma16(dW + so + 1024, voffW[1], rw, sw);
    if (lane < 16) dma16(dW + so + 2048, voffW[2], rw, sw);
  };
  const unsigned g = lane >> 4, q = (lane & 15) >> 2, pp = lane & 3, l15 = lane & 15;
  const unsigned mg = MODE == 0 ? w : (w & 3), kg = MODE == 0 ? 0 : (w >> 2);
  const unsigned xb = ldsb + mg * 4096 + kg * 2048 + (g >> 1) * 1024 + ((g & 1) * 32 + l15) * 16;
  const unsigned wrow = (KORD == 0 ? 4 * g : 16 * (g >> 1) + 4 * (g & 1)) + q + kg * 32;
  constexpr unsigned RT = KORD == 0 ? 16 : 8;
  const unsigned wb = ldsb + XB + wrow * 288 + pp * 8;
#pragma unroll
  for (int u = 0; u < 2; ++u)
#pragma unroll
    for (int a = 0; a < 9; ++a) acc[u][a] = f32x4{};
  static_for<NS - 1>([&](auto T) { issue(T); });
  static_for<NSTEPS>([&](auto T) {
    constexpr int t = decltype(T)::v;
    constexpr int ahead = (NS - 2) < (NSTEPS - 1 - t) ? (NS - 2) : (NSTEPS - 1 - t);
    wait_vm<P * ahead>();
    bar();
    if constexpr (t + NS - 1 < NSTEPS) issue(IC<t + NS - 1>{});
    constexpr unsigned slot = (t % NS) * SLOT;
#pragma unroll
    for (int v = 0; v < (MODE == 0 ? 2 : 1); ++v) {
      bf16x8 xf[2];
#pragma unroll
      for (int u = 0; u < 2; ++u) xf[u] = lds_b128(xb + slot + v * 2048 + u * 256);
#pragma unroll
      for (int a = 0; a < 9; ++a) {
        const bf16x8 wf = cat(lds_tr(wb + slot + v * 32 * 288 + a * 32), lds_tr(wb + slot + (v * 32 + RT) * 288 + a * 32));
#pragma unroll
        for (int u = 0; u < 2; ++u) acc[u][a] = MFMA16(wf, xf[u], acc[u][a]);
      }
    }
  });
}

template <int KORD, int KTOT>
__global__ __launch_bounds__(512, 2) void splitk_kernel(const char* __restrict__ X, const u16* __restrict__ W, float* __restrict__ part) {
  extern __shared__ __attribute__((aligned(16))) char lds[];
  const unsigned ldsb = (unsigned)(size_t)lds;
  const unsigned tid = threadIdx.x, lane = tid & 63, w = rfl(tid >> 6);
  constexpr int KC = KTOT / 4, NSTEPS = KC / 64, KT16 = KTOT / 16;
  const unsigned b = blockIdx.x, xcd = b & 7, jj = b >> 3;
  const unsigned sp = xcd >> 1, mt = (xcd & 1) * 8 + (jj >> 2), nt = jj & 3;
  const rsrc_t rx = mk_rsrc(X, SEQ * KTOT * 2), rw = mk_rsrc(W, KTOT * DM * 2);
  f32x4 acc[2][9];
  gemm16_core<1, KORD, NSTEPS, 4>(acc, ldsb, w, lane, rx, (mt * 4 * KT16 + sp * (KC / 16)) * 1024, KT16 * 1024, rw, (sp * KC * DM + nt * 144) * 2, DM * 2);
  bar();
  const unsigned mg = w & 3, kg = w >> 2;
  {
#pragma unroll
    for (int a = 0; a < 9; ++a) *(LAS f32x4*)(size_t)(ldsb + (w * 9 + a) * 1024 + lane * 16) = kg ? acc[0][a] : acc[1][a];
  }
  bar();
  const unsigned pw = w ^ 4, g = lane >> 4, l15 = lane & 15;
  const unsigned tok = mt * 128 + mg * 32 + kg * 16 + l15;
  float* dst = part + ((size_t)sp * SEQ + tok) * DM + nt * 144 + 4 * g;
#pragma unroll
  for (int a = 0; a < 9; ++a) {
    const f32x4 y = *(LAS f32x4*)(size_t)(ldsb + (pw * 9 + a) * 1024 + lane * 16);
    *(f32x4*)(dst + 16 * a) = (kg ? acc[1][a] : acc[0][a]) + y;
  }
}

__global__ __launch_bounds__(512, 2) void mlp1_kernel(const char* __restrict__ hn, const u16* __restrict__ W1, const u16* __restrict__ b1, char* __restrict__ h1) {
  extern __shared__ __attribute__((aligned(16))) char lds[];
  const unsigned ldsb = (unsigned)(size_t)lds;
  const unsigned tid = threadIdx.x, lane = tid & 63, w = rfl(tid >> 6);
  const unsigned b = blockIdx.x, xcd = b & 7, jj = b >> 3;
  const unsigned mt = (xcd & 1) * 4 + (jj >> 3), nt = (xcd >> 1) * 8 + (jj & 7);
  const rsrc_t rx = mk_rsrc(hn, SEQ * DM * 2), rw = mk_rsrc(W1, DM * FF * 2);
  f32x4 acc[2][9];
  gemm16_core<0, 0, 9, 3>(acc, ldsb, w, lane, rx, (mt * 8 * 36) * 1024, 36 * 1024, rw, nt * 144 * 2, FF * 2);
  const unsigned g = lane >> 4, l15 = lane & 15, mb = mt * 8 + w;
#pragma unroll
  for (int a = 0; a < 9; ++a) {
    const unsigned ag = nt * 9 + a;
    const u32x2 bb = *(const u32x2*)(b1 + ag * 16 + 4 * g);
    const float bv[4] = {bflo(bb[0]), bfhi(bb[0]), bflo(bb[1]), bfhi(bb[1])};
#pragma unroll
    for (int u = 0; u < 2; ++u) {
      float r[4];
#pragma unroll
      for (int k = 0; k < 4; ++k) { const float z = fmaxf(acc[u][a][k] + bv[k], 0.f); r[k] = z * z; }
      char* p = h1 + ((size_t)(mb * 288 + (ag >> 1) * 2 + (g >> 1)) * 64 + (g & 1) * 32 + u * 16 + l15) * 16 + (ag & 1) * 8;
      *(u32x2*)p = u32x2{cvtpk(r[0], r[1]), cvtpk(r[2], r[3])};
    }
  }
}

__global__ __launch_bounds__(64) void red_ln2_kernel(const float* __restrict__ part, const u16* __restrict__ x, u16* __restrict__ hout, char* __restrict__ hn) {
  const int lane = threadIdx.x, j = lane & 7, row = blockIdx.x * 8 + (lane >> 3);
  float v[18][4];
#pragma unroll
  for (int i = 0; i < 18; ++i) {
    const int col = (j + 8 * i) * 4;
    f32x4 a = *(const f32x4*)(part + (size_t)row * DM + col);
#pragma unroll
    for (int s = 1; s < 4; ++s) a += *(const f32x4*)(part + ((size_t)s * SEQ + row) * DM + col);
    const u32x2 xx = *(const u32x2*)(x + (size_t)row * DM + col);
    const unsigned w0 = cvtpk(a[0] + bflo(xx[0]), a[1] + bfhi(xx[0])), w1 = cvtpk(a[2] + bflo(xx[1]), a[3] + bfhi(xx[1]));
    *(u32x2*)(hout + (size_t)row * DM + col) = u32x2{w0, w1};
    v[i][0] = bflo(w0); v[i][1] = bfhi(w0); v[i][2] = bflo(w1); v[i][3] = bfhi(w1);
  }
  float s = 0.f;
#pragma unroll
  for (int i = 0; i < 18; ++i) s += (v[i][0] + v[i][1]) + (v[i][2] + v[i][3]);
  s += __shfl_xor(s, 1); s += __shfl_xor(s, 2); s += __shfl_xor(s, 4);
  const float mean = s * (1.f / DM);
  float qq = 0.f;
#pragma unroll
  for (int i = 0; i < 18; ++i)
#pragma unroll
    for (int k = 0; k < 4; ++k) { const float d = v[i][k] - mean; qq += d * d; }
  qq += __shfl_xor(qq, 1); qq += __shfl_xor(qq, 2); qq += __shfl_xor(qq, 4);
  const float rs = rsqrtf(qq * (1.f / DM) + 1e-5f);
  const int mb = row >> 5, r = row & 31;
#pragma unroll
  for (int i = 0; i < 18; ++i) {
    const int g = j & 3, half = j >> 2;
    char* p = hn + ((size_t)(mb * 36 + 2 * i + (g >> 1)) * 64 + (g & 1) * 32 + r) * 16 + half * 8;
    *(u32x2*)p = u32x2{cvtpk((v[i][0] - mean) * rs, (v[i][1] - mean) * rs), cvtpk((v[i][2] - mean) * rs, (v[i][3] - mean) * rs)};
  }
}

__global__ __launch_bounds__(256) void red_out_kernel(const float* __restrict__ part, const u16* __restrict__ hin, const u16* __restrict__ b2, u16* __restrict__ out) {
  const int idx = blockIdx.x * 256 + threadIdx.x;
  const int e = idx * 4, col = e % DM;
  f32x4 a = *(const f32x4*)(part + e);
#pragma unroll
  for (int s = 1; s < 4; ++s) a += *(const f32x4*)(part + (size_t)s * SEQ * DM + e);
  const u32x2 hh = *(const u32x2*)(hin + e), bb = *(const u32x2*)(b2 + col);
  *(u32x2*)(out + e) = u32x2{cvtpk(a[0] + bflo(bb[0]) + bflo(hh[0]), a[1] + bfhi(bb[0]) + bfhi(hh[0])),
                             cvtpk(a[2] + bflo(bb[1]) + bflo(hh[1]), a[3] + bfhi(bb[1]) + bfhi(hh[1]))};
}

extern "C" void kernel_launch(void* const* d_in, const int* in_sizes, int n_in, void* d_out, int out_size, void* d_ws, size_t ws_size,
                              hipStream_t stream) {
  const u16* x = (const u16*)d_in[0];
  const u16* ckv = (const u16*)d_in[1];
  const u16* wq = (const u16*)d_in[2];
  const u16* wuk = (const u16*)d_in[3];
  const u16* wuv = (const u16*)d_in[4];
  const u16* wo = (const u16*)d_in[5];
  const u16* w1 = (const u16*)d_in[7];
  const u16* b1 = (const u16*)d_in[8];
  const u16* w2 = (const u16*)d_in[9];
  const u16* b2 = (const u16*)d_in[10];
  char* ws = (char*)d_ws;
  hipFuncSetAttribute((const void*)qkv_kernel, hipFuncAttributeMaxDynamicSharedMemorySize, 131072);
  hipFuncSetAttribute((const void*)attn_kernel, hipFuncAttributeMaxDynamicSharedMemorySize, 131072);
  hipFuncSetAttribute((const void*)splitk_kernel<1, NQ>, hipFuncAttributeMaxDynamicSharedMemorySize, 4 * 34816);
  hipFuncSetAttribute((const void*)splitk_kernel<0, FF>, hipFuncAttributeMaxDynamicSharedMemorySize, 4 * 34816);
  hipFuncSetAttribute((const void*)mlp1_kernel, hipFuncAttributeMaxDynamicSharedMemorySize, 3 * 51200);
  ln1_kernel<<<256, 64, 0, stream>>>(x, ws + WS_XN);
  qkv_kernel<<<256, 512, 131072, stream>>>(ckv, ws + WS_XN, wq, wuk, wuv, ws + WS_Q, ws + WS_K, ws + WS_V);
  attn_kernel<<<256, 512, 131072, stream>>>(ws + WS_Q, ws + WS_K, ws + WS_V, ws + WS_CTX);
  splitk_kernel<1, NQ><<<256, 512, 4 * 34816, stream>>>(ws + WS_CTX, wo, (float*)(ws + WS_P1));
  red_ln2_kernel<<<256, 64, 0, stream>>>((const float*)(ws + WS_P1), x, (u16*)(ws + WS_H), ws + WS_HN);
  mlp1_kernel<<<256, 512, 3 * 51200, stream>>>(ws + WS_HN, w1, b1, ws + WS_H1);
  splitk_kernel<0, FF><<<256, 512, 4 * 34816, stream>>>(ws + WS_H1, w2, (float*)(ws + WS_P2));
  red_out_kernel<<<SEQ * DM / 4 / 256, 256, 0, stream>>>((const float*)(ws + WS_P2), (const u16*)(ws + WS_H), b2, (u16*)d_out);
}
```

```cpp
#include <hip/hip_runtime.h>
#include <stdint.h>
#include <utility>

typedef __bf16 bf16x8 __attribute__((ext_vector_type(8)));
typedef __bf16 bf16x4 __attribute__((ext_vector_type(4)));
typedef __bf16 bf16x2 __attribute__((ext_vector_type(2)));
typedef float f32x16 __attribute__((ext_vector_type(16)));
typedef float f32x4 __attribute__((ext_vector_type(4)));
typedef float f32x2 __attribute__((ext_vector_type(2)));
typedef unsigned u32x4 __attribute__((ext_vector_type(4)));
typedef unsigned u32x2 __attribute__((ext_vector_type(2)));
typedef unsigned short u16;
typedef unsigned long long u64;
#define LAS __attribute__((address_space(3)))
#define DI __device__ __forceinline__
#define AI __attribute__((always_inline))
typedef __amdgpu_buffer_rsrc_t rsrc_t;

constexpr int SEQ = 2048, DM = 576, NH = 32, HD = 128, DC = 512, FF = 4608, NQ = 4096;

constexpr unsigned WS_FLAGS = 0;
constexpr unsigned WS_XN = 65536;
constexpr unsigned WS_K = WS_XN + SEQ * DM * 2;
constexpr unsigned WS_V = WS_K + SEQ * NQ * 2;
constexpr unsigned WS_CTX = WS_V + SEQ * NQ * 2;
constexpr unsigned WS_P1 = WS_CTX + SEQ * NQ * 2;
constexpr unsigned WS_H = WS_P1 + 8 * SEQ * DM * 2;
constexpr unsigned WS_HN = WS_H + SEQ * DM * 2;
constexpr unsigned WS_H1 = WS_HN + SEQ * DM * 2;
constexpr unsigned WS_P2 = WS_H1 + SEQ * FF * 2;
constexpr unsigned WS_END = WS_P2 + 8 * SEQ * DM * 2;
constexpr unsigned F_EPOCH = 0, F_XN = 4096, F_KV = 8192, F_CTX = 12288, F_P1 = 16384, F_HN = 20480, F_H1 = 24576, F_P2 = 28672;

template <int... Is> using iseq = std::integer_sequence<int, Is...>;
template <int N> using make_iseq = std::make_integer_sequence<int, N>;
template <int I> struct IC { static constexpr int v = I; };
template <class F, int... Is> DI void static_for_(F&& f, iseq<Is...>) { (f(IC<Is>{}), ...); }
template <int N, class F> DI void static_for(F&& f) { static_for_(f, make_iseq<N>{}); }

#define SB() __builtin_amdgcn_sched_barrier(0)
#define PIN(x) asm volatile("" : "+v"(x))
DI unsigned rfl(unsigned x) { return __builtin_amdgcn_readfirstlane(x); }
DI rsrc_t mk_rsrc(const void* p, unsigned bytes) { return __builtin_amdgcn_make_buffer_rsrc((void*)p, (short)0, (int)bytes, 0x00020000); }
constexpr unsigned OOB = 0xFFFFFF00u;
DI void dma16(unsigned lds_addr, unsigned voff, rsrc_t r, unsigned soff) {
  asm volatile("s_mov_b32 m0, %0\n\ts_nop 3\n\tbuffer_load_dwordx4 %1, %2, %3 offen lds" ::"s"(lds_addr), "v"(voff), "s"(r), "s"(soff) : "memory", "m0");
}
DI void dma16c(unsigned lds_addr, unsigned voff, rsrc_t r, unsigned soff) {
  asm volatile("s_mov_b32 m0, %0\n\ts_nop 3\n\tbuffer_load_dwordx4 %1, %2, %3 offen sc1 lds" ::"s"(lds_addr), "v"(voff), "s"(r), "s"(soff) : "memory", "m0");
}
template <int IMM> DI void gld16c(u32x4& d, unsigned voff, rsrc_t r, unsigned soff) {
  asm volatile("s_nop 4\n\tbuffer_load_dwordx4 %0, %1, %2, %3 offen offset:%4 sc1" : "=v"(d) : "v"(voff), "s"(r), "s"(soff), "i"(IMM) : "memory");
}
template <int IMM> DI void gld16(u32x4& d, unsigned voff, rsrc_t r, unsigned soff) {
  asm volatile("s_nop 4\n\tbuffer_load_dwordx4 %0, %1, %2, %3 offen offset:%4" : "=v"(d) : "v"(voff), "s"(r), "s"(soff), "i"(IMM) : "memory");
}
DI void gld8c(u32x2& d, unsigned voff, rsrc_t r, unsigned soff) {
  asm volatile("s_nop 4\n\tbuffer_load_dwordx2 %0, %1, %2, %3 offen sc1" : "=v"(d) : "v"(voff), "s"(r), "s"(soff) : "memory");
}
DI void gst16c(u32x4 v, rsrc_t r, unsigned voff, unsigned soff) {
  asm volatile("s_nop 4\n\tbuffer_store_dwordx4 %0, %1, %2, %3 offen sc1\n\ts_nop 1" ::"v"(v), "v"(voff), "s"(r), "s"(soff) : "memory");
}
DI void gst8c(u32x2 v, rsrc_t r, unsigned voff, unsigned soff) { __builtin_amdgcn_raw_buffer_store_b64(v, r, voff, soff, 16); }
template <int N> DI void wait_vm() { asm volatile("s_waitcnt vmcnt(%0)" ::"i"(N) : "memory"); }
DI void bar() { asm volatile("s_waitcnt lgkmcnt(0)" ::: "memory"); __builtin_amdgcn_s_barrier(); asm volatile("" ::: "memory"); }
DI unsigned cvtpk(float lo, float hi) { f32x2 v = {lo, hi}; bf16x2 b = __builtin_convertvector(v, bf16x2); return __builtin_bit_cast(unsigned, b); }
DI float bflo(unsigned x) { return __uint_as_float(x << 16); }
DI float bfhi(unsigned x) { return __uint_as_float(x & 0xffff0000u); }
DI bf16x8 lds_b128(unsigned a) { return *(LAS bf16x8*)(size_t)a; }
DI bf16x4 lds_tr(unsigned a) { return __builtin_amdgcn_ds_read_tr16_b64_v4bf16((LAS bf16x4*)(size_t)a); }
DI bf16x8 cat(bf16x4 a, bf16x4 b) { return __builtin_shufflevector(a, b, 0, 1, 2, 3, 4, 5, 6, 7); }
DI bf16x8 asbf(u32x4 x) { return __builtin_bit_cast(bf16x8, x); }
#define MFMA32(a, b, c) __builtin_amdgcn_mfma_f32_32x32x16_bf16((a), (b), (c), 0, 0, 0)
#define MFMA16(a, b, c) __builtin_amdgcn_mfma_f32_16x16x32_bf16((a), (b), (c), 0, 0, 0)
template <int S> DI u32x4 pack8(const f32x16& x, float sc) {
  u32x4 w;
  w[0] = cvtpk(x[8 * S + 0] * sc, x[8 * S + 1] * sc); w[1] = cvtpk(x[8 * S + 2] * sc, x[8 * S + 3] * sc);
  w[2] = cvtpk(x[8 * S + 4] * sc, x[8 * S + 5] * sc); w[3] = cvtpk(x[8 * S + 6] * sc, x[8 * S + 7] * sc);
  return w;
}
template <int S> DI u32x4 pack8(const f32x16& x) {
  u32x4 w;
  w[0] = cvtpk(x[8 * S + 0], x[8 * S + 1]); w[1] = cvtpk(x[8 * S + 2], x[8 * S + 3]);
  w[2] = cvtpk(x[8 * S + 4], x[8 * S + 5]); w[3] = cvtpk(x[8 * S + 6], x[8 * S + 7]);
  return w;
}
DI float max3f(float a, float b, float c) { float r; asm("v_max3_f32 %0, %1, %2, %3" : "=v"(r) : "v"(a), "v"(b), "v"(c)); return r; }
DI float max2f(float a, float b) { float r; asm("v_max_f32_e32 %0, %1, %2" : "=v"(r) : "v"(a), "v"(b)); return r; }
DI float fadd_s(float a, float b) { float r; asm("v_add_f32_e32 %0, %1, %2" : "=v"(r) : "v"(a), "v"(b)); return r; }
DI float xhalf_sum(float x) {
  auto r = __builtin_amdgcn_permlane32_swap(__float_as_uint(x), __float_as_uint(x), false, false);
  return __uint_as_float(r[0]) + __uint_as_float(r[1]);
}
DI float wave_sum(float x) {
#pragma unroll
  for (int o = 1; o < 64; o <<= 1) x += __shfl_xor(x, o);
  return x;
}

#define STAMP(i)

struct Ctx {
  unsigned ldsb, w, lane, b;
  unsigned ep0, ep1;
  rsrc_t rws;
};

DI void fresh(Ctx& c) {
  asm volatile("v_mbcnt_lo_u32_b32 %0, -1, 0\n\tv_mbcnt_hi_u32_b32 %0, -1, %0" : "=v"(c.lane));
  asm volatile("" : "+s"(c.w), "+s"(c.b), "+s"(c.ldsb));
}
DI void publish(Ctx& c, unsigned flag_off) {
  wait_vm<0>();
  bar();
  gst8c(u32x2{c.ep0, c.ep1}, c.rws, (c.w == 0 && c.lane == 0) ? flag_off : OOB, WS_FLAGS);
}
DI void poll_flags(Ctx& c, unsigned flag_off, unsigned n, unsigned stride) {
  const unsigned li = c.lane < n ? c.lane : 0;
  const unsigned voff = flag_off + li * 8 * stride;
  const u64 t0 = __builtin_amdgcn_s_memrealtime();
  for (;;) {
    u32x2 f;
    gld8c(f, voff, c.rws, WS_FLAGS);
    asm volatile("s_waitcnt vmcnt(0)" : "+v"(f)::"memory");
    if (__all(f[0] == c.ep0 && f[1] == c.ep1)) break;
    if (__builtin_amdgcn_s_memrealtime() - t0 > 5000000ull) break;
    __builtin_amdgcn_s_sleep(2);
  }
}
DI void wait_flags(Ctx& c, unsigned flag_off, unsigned n, unsigned stride) {
  if (c.w == 0) poll_flags(c, flag_off, n, stride);
  bar();
}

DI void flag_store(Ctx& c, unsigned flag_off) {
  gst8c(u32x2{c.ep0, c.ep1}, c.rws, (c.w == 0 && c.lane == 0) ? flag_off : OOB, WS_FLAGS);
}
DI void flag_load(Ctx& c, u32x2& f, unsigned flag_off, unsigned n) { gld8c(f, flag_off + (c.lane < n ? c.lane : 0) * 8, c.rws, WS_FLAGS); }
DI void flag_check(Ctx& c, u32x2& f, unsigned flag_off, unsigned n) {
  PIN(f);
  if (__builtin_expect(!__all(f[0] == c.ep0 && f[1] == c.ep1), 0)) poll_flags(c, flag_off, n, 1);
}

constexpr float QSCALE = 0.08838834764831845f * 1.4426950408889634f;
constexpr unsigned LE = 131072;
constexpr int XR = 12;

constexpr int kv_wait(int t) {
  int seq[256] = {}; int n = 0;
  auto push = [&](int tag, int cnt) { for (int i = 0; i < cnt; ++i) seq[n++] = tag; };
  push(-1, 3);
  for (int s = 0; s < 4; ++s) push(s, 4);
  push(-2, 2);
  for (int s = 0;; ++s) {
    if (s >= 1) push(s + 3, 2);
    if (s == 6) push(-4, 2);
    if (s == t) break;
    if (s == 3) push(-3, 1);
    if (s == 10) push(-5, 1);
    push(s + 4, 2);
    if (s == 5) push(-4, 2);
  }
  int last = -1;
  for (int i = 0; i < n; ++i) if (seq[i] == t + 1) last = i;
  return n - 1 - last;
}
constexpr int q_wait(int ks) {
  int seq[256] = {}; int n = 0;
  for (int s = 0; s < ks; ++s) {
    if (s % 2 == 0 && s < 32) seq[n++] = -1;
    if (s >= 1 && s - 1 + XR < 36) seq[n++] = s - 1 + XR;
  }
  int last = -1;
  for (int i = 0; i < n; ++i) if (seq[i] == ks + 2) last = i;
  return n - 1 - last;
}

struct QOut { u32x4 q[8]; };

template <bool ISV>
DI void qkv_body(Ctx& c, QOut& qo, const rsrc_t rx, const rsrc_t rw, const rsrc_t rwq, const unsigned voffX, const unsigned voffW, const unsigned sX,
                 const unsigned sW, const unsigned sWq, const unsigned sWq4, const unsigned rot, const unsigned qmb, const unsigned head, const unsigned cb) {
  const unsigned w = c.w, lane = c.lane, ldsb = c.ldsb;
  const unsigned l31 = lane & 31, h = lane >> 5, g2 = (lane >> 4) & 1, q = (lane & 15) >> 2, pp = lane & 3;
  const unsigned wm = w >> 2, wn = w & 3, xsw = (l31 >> 2) & 3;
  const unsigned wrow = 8 * h + q, wch = (g2 * 2 + (pp >> 1)) ^ ((q >> 1) << 2);
  const unsigned wl0 = ldsb + wrow * 128 + (wch << 4) + 8 * (pp & 1), wl1 = ldsb + wrow * 128 + ((wch ^ 4) << 4) + 8 * (pp & 1);
  u32x4 pw[16], xq[36]; u32x2 xnf;
  const unsigned xso = WS_XN + qmb * 36 * 1024;
  auto ldx = [&](auto K_) AI { constexpr int K = decltype(K_)::v; gld16c<(K & 3) * 1024>(xq[K], lane * 16, c.rws, xso + (K & ~3) * 1024); };
  {
    unsigned xb[2][2], wb[2][2];
    xb[0][0] = ldsb + (wm * 128 + l31) * 64 + ((h ^ xsw) << 4); xb[0][1] = ldsb + (wm * 128 + l31) * 64 + (((2 + h) ^ xsw) << 4);
    wb[0][0] = wl0 + 16384 + wn * 4096; wb[0][1] = wl1 + 16384 + wn * 4096;
    xb[1][0] = xb[0][0] + 65536; xb[1][1] = xb[0][1] + 65536; wb[1][0] = wb[0][0] + 65536; wb[1][1] = wb[0][1] + 65536;
    PIN(xb[0][0]); PIN(xb[0][1]); PIN(xb[1][0]); PIN(xb[1][1]); PIN(wb[0][0]); PIN(wb[0][1]); PIN(wb[1][0]); PIN(wb[1][1]);
    const unsigned dX = ldsb + 2 * w * 1024, dW = ldsb + 16384 + 2 * w * 1024, dQ = ldsb + w * 4096;
    f32x16 acc[4][2];
#pragma unroll
    for (int i = 0; i < 4; ++i)
#pragma unroll
      for (int j = 0; j < 2; ++j) acc[i][j] = f32x16{};
    bf16x8 xf[2][4]; bf16x4 wl[2][4];
    auto rd = [&](auto T_, auto S_, auto R_) AI {
      constexpr int T = decltype(T_)::v, S = decltype(S_)::v, R = decltype(R_)::v;
      constexpr unsigned so = (T & 1) * 32768; constexpr int sp = (T >> 1) & 1;
      if constexpr (R < 2) wl[S][R] = lds_tr(wb[sp][0] + so + (16 * S + 4 * R) * 128);
      else if constexpr (R == 2) xf[S][0] = lds_b128(xb[sp][S] + so);
      else if constexpr (R < 5) wl[S][R - 1] = lds_tr(wb[sp][1] + so + (16 * S + 4 * (R - 3)) * 128);
      else xf[S][R - 4] = lds_b128(xb[sp][S] + so + (R - 4) * 2048);
    };
    auto issue = [&](auto U_, auto H_, auto P_) AI {
      constexpr int U = decltype(U_)::v, H = decltype(H_)::v, P = decltype(P_)::v;
      constexpr unsigned so = (U & 3) * 32768;
      if constexpr (U < 16) {
        const unsigned tr = (U + rot) & 15;
        if constexpr (H == 0) dma16(dX + so + P * 1024, voffX, rx, sX + 64 * tr + P * 16 * 1024);
        else dma16(dW + so + P * 1024, voffW, rw, sW + 32 * tr * 8192 + P * 8 * 8192);
      } else if constexpr (U < 19) {
        dma16(dQ + so + (2 * H + P) * 1024, voffW, rwq, sWq + (U - 15) * 128 * 8192 + (2 * H + P) * 8 * 8192);
      } else if constexpr (U == 19 && H == 0) {
        dma16(ldsb + so + (w >> 2) * 16384 + ((w & 3) * 2 + P) * 1024, voffW, rwq, sWq4 + P * 8 * 8192);
      }
    };
    static_for<8>([&](auto R) AI { rd(IC<0>{}, IC<0>{}, R); });
    SB();
    static_for<16>([&](auto T_) AI {
      constexpr int T = decltype(T_)::v;
      static_for<2>([&](auto S_) AI {
        constexpr int S = decltype(S_)::v;
        if constexpr (S == 1) {
          if constexpr (T < 15) wait_vm<kv_wait(T)>();
          bar();
          if constexpr (T == 3) flag_store(c, F_XN + c.b * 8);
          if constexpr (T == 10) flag_load(c, xnf, F_XN + qmb * 32, 4);
          if constexpr (T == 14) flag_check(c, xnf, F_XN + qmb * 32, 4);
          SB();
        }
        static_for<8>([&](auto M_) AI {
          constexpr int M = decltype(M_)::v, i = M >> 1, j = M & 1;
          const bf16x8 wf = cat(wl[S][2 * j], wl[S][2 * j + 1]);
          acc[i][j] = ISV ? MFMA32(xf[S][i], wf, acc[i][j]) : MFMA32(wf, xf[S][i], acc[i][j]);
          if constexpr (M < 4 && (S == 0 || T < 15)) {
            rd(IC<(S == 0 ? T : T + 1)>{}, IC<(S ^ 1)>{}, IC<2 * M>{});
            rd(IC<(S == 0 ? T : T + 1)>{}, IC<(S ^ 1)>{}, IC<2 * M + 1>{});
          }
          if constexpr (M == 4 || M == 6) {
            constexpr int P = (M - 4) >> 1;
            if constexpr (S == 1) issue(IC<T + 4>{}, IC<0>{}, IC<P>{});
            else if constexpr (T >= 1) issue(IC<T + 3>{}, IC<1>{}, IC<P>{});
          }
          if constexpr (M == 7 && ((S == 1 && T == 5) || (S == 0 && T == 6))) {
            constexpr int H = S == 1 ? 0 : 1;
            dma16(ldsb + LE + w * 4096 + (2 * H) * 1024, voffW, rwq, sWq + (2 * H) * 8 * 8192);
            dma16(ldsb + LE + w * 4096 + (2 * H + 1) * 1024, voffW, rwq, sWq + (2 * H + 1) * 8 * 8192);
          }
          if constexpr ((M & 1) && ((T == 14 && S == 1) || T == 15)) {
            constexpr int K = (T == 14 ? 0 : S == 0 ? 4 : 8) + (M >> 1);
            ldx(IC<K>{});
          }
          SB();
        });
      });
    });
#pragma unroll
    for (int i = 0; i < 4; ++i)
#pragma unroll
      for (int j = 0; j < 2; ++j) { pw[(i * 2 + j) * 2] = pack8<0>(acc[i][j]); pw[(i * 2 + j) * 2 + 1] = pack8<1>(acc[i][j]); }
  }
  STAMP(3);
  unsigned wbase[3][2];
  wbase[0][0] = wl0; wbase[0][1] = wl1; wbase[1][0] = wl0 + 65536; wbase[1][1] = wl1 + 65536; wbase[2][0] = wl0 + LE; wbase[2][1] = wl1 + LE;
  PIN(wbase[0][0]); PIN(wbase[0][1]); PIN(wbase[1][0]); PIN(wbase[1][1]); PIN(wbase[2][0]); PIN(wbase[2][1]);
  const unsigned sto = ISV ? WS_V + (((head * 32 + cb * 4 + wm * 2) * 4 + 2 * (wn & 1)) * 4) * 1024 : WS_K + ((head * 64 + cb * 8 + wm * 4) * 8 + 4 * (wn & 1)) * 1024;
  asm volatile("s_waitcnt vmcnt(12)" : "+v"(xq[0]), "+v"(xq[1]), "+v"(xq[2]), "+v"(xq[3])::"memory");
  STAMP(5);
  f32x16 acc[4];
#pragma unroll
  for (int j = 0; j < 4; ++j) acc[j] = f32x16{};
  bf16x4 wl[8][2];
  auto rdq = [&](auto N_) AI {
    constexpr int N = decltype(N_)::v, ks = N >> 2, j = N & 3, ci = ks >> 3;
    constexpr int reg = ci == 0 ? 2 : (ci - 1) >> 1;
    constexpr unsigned off = (ci == 0 ? 0 : ((ci - 1) & 1) * 32768) + (j >> 1) * 16384 + (16 * (ks & 7)) * 128;
    wl[N & 7][0] = lds_tr(wbase[reg][j & 1] + off);
    wl[N & 7][1] = lds_tr(wbase[reg][j & 1] + off + 512);
  };
  static_for<5>([&](auto N) AI { rdq(N); });
  SB();
  static_for<144>([&](auto N_) AI {
    constexpr int N = decltype(N_)::v, ks = N >> 2, j = N & 3;
    if constexpr (j == 0 && ks == 4) asm volatile("s_waitcnt vmcnt(11)" : "+v"(xq[4]), "+v"(xq[5]), "+v"(xq[6]), "+v"(xq[7])::"memory");
    if constexpr (j == 0 && ks == 6) {
      asm volatile("s_waitcnt vmcnt(8)" : "+v"(xq[8]), "+v"(xq[9]), "+v"(xq[10]), "+v"(xq[11])::"memory");
      bar(); SB();
    }
    if constexpr (j == 0 && ks >= XR && ks % 3 == 0)
      asm volatile("s_waitcnt vmcnt(%3)" : "+v"(xq[ks]), "+v"(xq[ks + 1]), "+v"(xq[ks + 2]) : "i"(q_wait(ks)) : "memory");
    acc[j] = MFMA32(cat(wl[N & 7][0], wl[N & 7][1]), asbf(xq[ks]), acc[j]);
    if constexpr (N + 5 < 144) rdq(IC<N + 5>{});
    if constexpr (j == 0 && ks % 2 == 0 && ks < 32) {
      constexpr int e = ks >> 1, i = e >> 2, jj = (e >> 1) & 1, s = e & 1;
      constexpr unsigned fo = ISV ? (((i >> 1) * 4 + jj) * 4 + 2 * (i & 1) + s) : (i * 8 + 2 * jj + s);
      gst16c(pw[e], c.rws, lane * 16, sto + fo * 1024);
    }
    if constexpr (j == 2 && ks >= 1 && ks - 1 + XR < 36) ldx(IC<ks - 1 + XR>{});
    SB();
  });
#pragma unroll
  for (int j = 0; j < 4; ++j) { qo.q[2 * j] = pack8<0>(acc[j], QSCALE); qo.q[2 * j + 1] = pack8<1>(acc[j], QSCALE); }
  STAMP(6);
}

DI void phase_qkv(Ctx& c, QOut& qo, const u16* x, const u16* ckv, const u16* wq, const u16* wuk, const u16* wuv) {
  const unsigned w = c.w, lane = c.lane, ldsb = c.ldsb, b = c.b;
  const unsigned xcd = b & 7, jj = b >> 3, head = xcd * 4 + (jj >> 3), cb = jj & 7;
  const rsrc_t rxx = mk_rsrc(x, SEQ * DM * 2);
  const rsrc_t rx = mk_rsrc(ckv, SEQ * DC * 2);
  const rsrc_t rw = mk_rsrc(w < 4 ? wuk : wuv, DC * NQ * 2);
  const rsrc_t rwq = mk_rsrc(wq, DM * NQ * 2);
  u32x2 ep; u32x4 xr0, xr1;
  gld8c(ep, F_EPOCH, c.rws, WS_FLAGS);
  {
    const unsigned vo = lane < 36 ? lane * 32 : OOB;
    gld16<0>(xr0, vo, rxx, (b * 8 + w) * (DM * 2));
    gld16<16>(xr1, vo, rxx, (b * 8 + w) * (DM * 2));
  }
  const unsigned voffX = (lane >> 2) * 1024 + (((lane & 3) ^ ((lane >> 4) & 3)) << 4);
  const unsigned voffW = (lane >> 3) * 8192 + (((lane & 7) ^ (((lane >> 4) & 1) << 2)) << 4);
  const unsigned sX = (cb * 256 + 32 * w) * 1024;
  const unsigned sW = ((2 * w & 3) * 8) * 8192 + (head * 128 + ((w >> 1) & 1) * 64) * 2;
  const unsigned sWq = ((w & 3) * 32) * 8192 + (head * 128 + (w >> 2) * 64) * 2;
  const unsigned sWq4 = (512 + (w & 3) * 16) * 8192 + (head * 128 + (w >> 2) * 64) * 2;
  const unsigned rot = (2 * cb + 4 * (head & 3)) & 15;
  {
    const unsigned dX = ldsb + 2 * w * 1024, dW = ldsb + 16384 + 2 * w * 1024;
#pragma unroll
    for (int u = 0; u < 4; ++u) {
      const unsigned tr = (u + rot) & 15;
      dma16(dX + u * 32768, voffX, rx, sX + 64 * tr);
      dma16(dX + u * 32768 + 1024, voffX, rx, sX + 64 * tr + 16 * 1024);
      dma16(dW + u * 32768, voffW, rw, sW + 32 * tr * 8192);
      dma16(dW + u * 32768 + 1024, voffW, rw, sW + 32 * tr * 8192 + 8 * 8192);
    }
  }
  asm volatile("s_waitcnt vmcnt(16)" : "+v"(ep), "+v"(xr0), "+v"(xr1)::"memory");
  STAMP(1);
  {
    u64 z = (((u64)rfl(ep[1]) << 32) | rfl(ep[0])) + 0x9E3779B97F4A7C15ull;
    z = (z ^ (z >> 30)) * 0xBF58476D1CE4E5B9ull; z = (z ^ (z >> 27)) * 0x94D049BB133111EBull; z ^= z >> 31;
    c.ep0 = (unsigned)z; c.ep1 = (unsigned)(z >> 32);
  }
  {
    float v[16];
#pragma unroll
    for (int e = 0; e < 4; ++e) { v[2 * e] = bflo(xr0[e]); v[2 * e + 1] = bfhi(xr0[e]); v[8 + 2 * e] = bflo(xr1[e]); v[9 + 2 * e] = bfhi(xr1[e]); }
    float s = 0.f;
#pragma unroll
    for (int e = 0; e < 16; ++e) s += v[e];
    const float mean = wave_sum(s) * (1.f / DM);
    float qq = 0.f;
#pragma unroll
    for (int e = 0; e < 16; ++e) { v[e] -= mean; qq += v[e] * v[e]; }
    qq = lane < 36 ? qq : 0.f;
    const float rs = rsqrtf(wave_sum(qq) * (1.f / DM) + 1e-5f);
    u32x4 o0, o1;
#pragma unroll
    for (int e = 0; e < 4; ++e) { o0[e] = cvtpk(v[2 * e] * rs, v[2 * e + 1] * rs); o1[e] = cvtpk(v[8 + 2 * e] * rs, v[9 + 2 * e] * rs); }
    if (lane < 36) {
      *(LAS u32x4*)(size_t)(ldsb + LE + ((2 * lane) * 8 + w) * 16) = o0;
      *(LAS u32x4*)(size_t)(ldsb + LE + ((2 * lane + 1) * 8 + w) * 16) = o1;
    }
  }
  wait_vm<12>();
  bar();
  STAMP(2);
  {
    const unsigned mb = b >> 2, r0 = (b & 3) * 8;
    const unsigned c0 = 9 * w + (lane >> 3), c1 = 9 * w + 8;
    const u32x4 a0 = *(LAS u32x4*)(size_t)(ldsb + LE + (9 * w * 8 + lane) * 16);
    const u32x4 a1 = *(LAS u32x4*)(size_t)(ldsb + LE + (c1 * 8 + (lane & 7)) * 16);
    gst16c(a0, c.rws, ((mb * 36 + (c0 >> 1)) * 64 + (c0 & 1) * 32 + r0 + (lane & 7)) * 16, WS_XN);
    gst16c(a1, c.rws, lane < 8 ? ((mb * 36 + (c1 >> 1)) * 64 + (c1 & 1) * 32 + r0 + lane) * 16 : OOB, WS_XN);
  }
  const unsigned kh = w >> 2, rg = kh ? 7 - w : w;
  const unsigned qmb = (kh ? cb : 15 - cb) * 4 + rg;
  if ((w & 3) >= 2) qkv_body<true>(c, qo, rx, rw, rwq, voffX, voffW, sX, sW, sWq, sWq4, rot, qmb, head, cb);
  else qkv_body<false>(c, qo, rx, rw, rwq, voffX, voffW, sX, sW, sWq, sWq4, rot, qmb, head, cb);
}

constexpr float THR = 32.f;

struct AttnState {
  bf16x8 qf[8];
  f32x16 o[4];
  f32x16 S[2][2];
  f32x16 negm;
  u32x4 pk[4];
  bf16x8 fr[8];
  float mhat, l0, l1, fct, mx0, mx1, t0, t1;
  bool pend;
  unsigned kb[2], vb[2];
  unsigned voff, ldsb, w, kbase, vbase;
};

constexpr int RD0[16] = {4, 5, 6, 7, 9, 10, 12, 13, 15, -1, -1, -1, -1, -1, -1, -1};
constexpr int RD1[16] = {-1, -1, -1, 8, -1, 11, -1, 14, -1, -1, -1, -1, -1, -1, -1, -1};

template <int X, bool QK, bool PV>
DI void attn_iter(Ctx& c, AttnState& a, const unsigned s, const unsigned nhi, const bool first, const bool diag, const int dq) {
  constexpr int Y = X ^ 1;
  auto kaddr = [&](int slot, int n) AI { return a.kb[slot] + ((n & 1) * 8 + (n >> 1)) * 1024; };
  auto vaddr = [&](int slot, int n) AI { return a.vb[slot] + ((n & 3) * 4 + (n >> 2)) * 1024; };
  unsigned sk = s + 2; sk = sk > 16 ? 16 : sk; sk = sk < nhi ? sk : sk - nhi;
  unsigned sv = s > 16 ? 16 : s; sv = sv < nhi ? sv : sv - nhi;
  const unsigned sok = a.kbase + sk * 32768, sov = a.vbase + sv * 32768;
  const unsigned dk = a.ldsb + X * 32768 + a.w * 4096, dv = dk + 65536;
  static_for<16>([&](auto M_) AI {
    constexpr int M = decltype(M_)::v, d = M >> 1, kb = M & 1;
    if constexpr (QK) {
      if constexpr (d == 0) a.S[X][kb] = MFMA32(a.fr[M & 7], a.qf[d], a.negm);
      else a.S[X][kb] = MFMA32(a.fr[M & 7], a.qf[d], a.S[X][kb]);
      if constexpr (RD0[M] >= 0) a.fr[RD0[M] & 7] = lds_b128(kaddr(X, RD0[M]));
      if constexpr (RD1[M] >= 0) a.fr[RD1[M] & 7] = lds_b128(kaddr(X, RD1[M]));
      if constexpr (M == 0 || M == 2 || M == 6 || M == 9) { constexpr int P = M == 0 ? 0 : M == 2 ? 1 : M == 6 ? 2 : 3; dma16c(dv + P * 1024, a.voff, c.rws, sov + P * 1024); }
    }
    if constexpr (PV) {
      if constexpr (M < 8) {
        a.S[Y][1][2 * M] = __builtin_amdgcn_exp2f(a.S[Y][1][2 * M]);
        a.S[Y][1][2 * M + 1] = __builtin_amdgcn_exp2f(a.S[Y][1][2 * M + 1]);
        a.pk[M >> 2][M & 3] = cvtpk(a.S[Y][0][2 * M], a.S[Y][0][2 * M + 1]);
        PIN(a.pk[M >> 2][M & 3]);
        if constexpr (M == 0) { a.l0 = fadd_s(a.l0, a.S[Y][0][14]); a.l1 = fadd_s(a.l1, a.S[Y][0][15]); }
      } else {
        constexpr int e = M - 8;
        a.pk[2 + (e >> 2)][e & 3] = cvtpk(a.S[Y][1][2 * e], a.S[Y][1][2 * e + 1]);
        PIN(a.pk[2 + (e >> 2)][e & 3]);
        a.l0 = fadd_s(a.l0, a.S[Y][1][2 * e]); a.l1 = fadd_s(a.l1, a.S[Y][1][2 * e + 1]);
      }
    }
    if constexpr (M == 11) { SB(); if constexpr (QK) wait_vm<8>(); else wait_vm<0>(); bar(); }
    if constexpr (PV && M >= 12) a.fr[(M - 12) & 7] = lds_b128(vaddr(Y, M - 12));
    SB();
  });
  static_for<16>([&](auto M_) AI {
    constexpr int M = decltype(M_)::v, ks = M >> 2, db = M & 3;
    if constexpr (PV) {
      a.o[db] = MFMA32(a.fr[M & 7], asbf(a.pk[ks]), a.o[db]);
      if constexpr (RD0[M] >= 0) a.fr[RD0[M] & 7] = lds_b128(vaddr(Y, RD0[M]));
      if constexpr (RD1[M] >= 0) a.fr[RD1[M] & 7] = lds_b128(vaddr(Y, RD1[M]));
    }
    if constexpr (QK) {
      if constexpr (M == 0 || M == 1 || M == 6 || M == 9) { constexpr int P = M == 0 ? 0 : M == 1 ? 1 : M == 6 ? 2 : 3; dma16c(dk + P * 1024, a.voff, c.rws, sok + P * 1024); }
      if constexpr (M == 2) {
        asm volatile("s_nop 7\n\ts_nop 3" : "+v"(a.S[X][0]), "+v"(a.S[X][1]));
        if (__builtin_expect(diag, 0)) {
          int dq2 = dq; PIN(dq2);
#pragma unroll
          for (int r = 0; r < 16; ++r) {
            const int cr = (r & 3) + 8 * (r >> 2);
            if (cr > dq2) a.S[X][0][r] = -__builtin_inff();
            if (cr + 32 > dq2) a.S[X][1][r] = -__builtin_inff();
          }
        }
      }
      if constexpr (M >= 2 && M < 6) {
        constexpr int r = 4 * (M - 2);
        if constexpr (M == 2) {
          a.mx0 = max3f(a.S[X][0][0], a.S[X][0][1], a.S[X][1][0]); a.mx1 = max3f(a.S[X][0][2], a.S[X][0][3], a.S[X][1][1]);
          a.mx0 = max3f(a.mx0, a.S[X][1][2], a.S[X][1][3]);
        } else {
          a.mx0 = max3f(a.mx0, a.S[X][0][r], a.S[X][0][r + 1]); a.mx1 = max3f(a.mx1, a.S[X][0][r + 2], a.S[X][0][r + 3]);
          a.mx0 = max3f(a.mx0, a.S[X][1][r], a.S[X][1][r + 1]); a.mx1 = max3f(a.mx1, a.S[X][1][r + 2], a.S[X][1][r + 3]);
        }
      }
      if constexpr (M == 6) {
        const float mm = max2f(a.mx0, a.mx1);
        auto pr = __builtin_amdgcn_permlane32_swap(__float_as_uint(mm), __float_as_uint(mm), false, false);
        a.mx0 = max2f(__uint_as_float(pr[0]), __uint_as_float(pr[1]));
      }
      if constexpr (M == 7) {
        if (__builtin_expect(first || __any(a.mx0 > THR), 0)) {
          const float dl = first ? fmaxf(a.mx0, -1e30f) : fmaxf(a.mx0, 0.f);
          a.mhat = first ? dl : a.mhat + dl;
          a.fct = __builtin_amdgcn_exp2f(-dl);
#pragma unroll
          for (int r = 0; r < 16; ++r) { a.S[X][0][r] -= dl; a.S[X][1][r] -= dl; a.negm[r] = -a.mhat; }
          a.pend = !first;
        }
      }
      if constexpr (M >= 8) {
        constexpr int e = M - 8;
        a.S[X][0][2 * e] = __builtin_amdgcn_exp2f(a.S[X][0][2 * e]);
        a.S[X][0][2 * e + 1] = __builtin_amdgcn_exp2f(a.S[X][0][2 * e + 1]);
        PIN(a.S[X][0][2 * e]); PIN(a.S[X][0][2 * e + 1]);
        if constexpr (e > 0) {
          a.t0 = e == 1 ? a.S[X][0][0] : fadd_s(a.t0, a.S[X][0][2 * e - 2]); a.t1 = e == 1 ? a.S[X][0][1] : fadd_s(a.t1, a.S[X][0][2 * e - 1]);
          PIN(a.t0); PIN(a.t1);
        }
      }
      if constexpr (M == 11) { SB(); wait_vm<8>(); bar(); }
      if constexpr (M >= 12) a.fr[(M - 12) & 7] = lds_b128(kaddr(Y, M - 12));
    }
    SB();
  });
}

constexpr unsigned WP = 288;
constexpr unsigned LSTAT = 161280;
struct WSrc { unsigned voff[5]; rsrc_t r; unsigned main, tail, pitch; };
DI void wsrc_setup(Ctx& c, WSrc& s, rsrc_t r, unsigned pitch, unsigned colA, unsigned colB, unsigned rowMain, unsigned rowTail) {
#pragma unroll
  for (int p = 0; p < 5; ++p) {
    const unsigned ci = c.lane + 64 * p, rr = ci / 18, cc = ci - rr * 18;
    s.voff[p] = rr * pitch + (cc < 16 ? colA + cc * 16 : colB + (cc - 16) * 16);
  }
  s.r = r; s.pitch = pitch; s.main = rowMain * pitch; s.tail = rowTail * pitch;
}
DI void w_group(Ctx& c, const WSrc& s, int q) {
  const unsigned so = s.main + (128 * q + 16 * c.w) * s.pitch, d = c.ldsb + (128 * q + 16 * c.w) * WP;
#pragma unroll
  for (int p = 0; p < 4; ++p) dma16(d + p * 1024, s.voff[p], s.r, so);
  if (c.lane < 32) dma16(d + 4096, s.voff[4], s.r, so);
}
DI void w_tail1(Ctx& c, const WSrc& s) {
  const unsigned so = s.tail + 4 * c.w * s.pitch, d = c.ldsb + (512 + 4 * c.w) * WP;
  dma16(d, s.voff[0], s.r, so);
  if (c.lane < 8) dma16(d + 1024, s.voff[1], s.r, so);
  if (c.lane < 36) dma16(c.ldsb + (544 + 2 * c.w) * WP, s.voff[0], s.r, s.tail + (48 + 2 * c.w) * s.pitch);
}
DI void w_tail2(Ctx& c, const WSrc& s) {
  if (c.lane < 36) dma16(c.ldsb + (544 + 2 * c.w) * WP, s.voff[0], s.r, s.tail + (32 + 2 * c.w) * s.pitch);
}
struct WRd { unsigned b[3]; };
template <int KORD> DI void wrd_setup(Ctx& c, WRd& r) {
  const unsigned g = c.lane >> 4, q = (c.lane & 15) >> 2, pp = c.lane & 3;
  const unsigned wrow = (KORD == 0 ? 4 * g : 16 * (g >> 1) + 4 * (g & 1)) + q;
#pragma unroll
  for (int i = 0; i < 3; ++i) { r.b[i] = c.ldsb + wrow * WP + pp * 8 + i * 7 * 32 * WP; PIN(r.b[i]); }
}
DI void tail_read(const WRd& r, bf16x4 (&tl)[9]) {
#pragma unroll
  for (int a = 0; a < 9; ++a) tl[a] = lds_tr(r.b[2] + 3 * 32 * WP + a * 32);
#pragma unroll
  for (int a = 0; a < 9; ++a) PIN(tl[a]);
}
template <int NK, int KORD, class Hook>
DI void gemm_r(f32x4 (&acc)[9], u32x4 (&x)[18], const WRd& r, bf16x4 (&tl)[9], Hook&& hook) {
  constexpr unsigned RT = KORD == 0 ? 16 : 8;
  constexpr int LA = 5;
#pragma unroll
  for (int a = 0; a < 9; ++a) acc[a] = f32x4{};
  bf16x4 wl[8][2];
  auto rdw = [&](auto N_) AI {
    constexpr int N = decltype(N_)::v, v = N / 9, a = N % 9;
    wl[N & 7][0] = lds_tr(r.b[v / 7] + (v % 7) * 32 * WP + a * 32);
    if constexpr (v < 17) wl[N & 7][1] = lds_tr(r.b[v / 7] + ((v % 7) * 32 + RT) * WP + a * 32);
  };
  static_for<LA>([&](auto N) AI { rdw(N); });
  SB();
  static_for<NK * 9>([&](auto N_) AI {
    constexpr int N = decltype(N_)::v, v = N / 9, a = N % 9;
    if constexpr (a == 0) { hook(IC<v>{}); SB(); }
    acc[a] = MFMA16(cat(wl[N & 7][0], v < 17 ? wl[N & 7][1] : tl[a]), asbf(x[v]), acc[a]);
    if constexpr (N + LA < NK * 9) rdw(IC<N + LA>{});
    SB();
  });
}
#define XWAIT4(x, i, n) asm volatile("s_waitcnt vmcnt(%4)" : "+v"(x[i]), "+v"(x[i + 1]), "+v"(x[i + 2]), "+v"(x[i + 3]) : "i"(n) : "memory")
#define XWAIT6(x, i, n) asm volatile("s_waitcnt vmcnt(%6)" : "+v"(x[i]), "+v"(x[i + 1]), "+v"(x[i + 2]), "+v"(x[i + 3]), "+v"(x[i + 4]), "+v"(x[i + 5]) : "i"(n) : "memory")
template <int NK, int NM> DI void x_load(Ctx& c, u32x4 (&x)[18], unsigned so, unsigned so2) {
  const unsigned g = c.lane >> 4, l15 = c.lane & 15;
  const unsigned voff = (g >> 1) * 1024 + ((g & 1) * 32 + (c.w & 1) * 16 + l15) * 16;
  static_for<NK>([&](auto V_) AI {
    constexpr int v = decltype(V_)::v;
    if constexpr (v < NM) gld16c<(v & 1) * 2048>(x[v], voff, c.rws, so + (v >> 1) * 4096);
    else gld16c<((v - NM) & 1) * 2048>(x[v], voff, c.rws, so2 + ((v - NM) >> 1) * 4096);
  });
}
struct Packed { u32x4 p[4]; u32x2 s; };
DI void pack_plain(Packed& o, const f32x4 (&r)[9]) {
#pragma unroll
  for (int p = 0; p < 4; ++p)
    o.p[p] = u32x4{cvtpk(r[2 * p][0], r[2 * p][1]), cvtpk(r[2 * p][2], r[2 * p][3]), cvtpk(r[2 * p + 1][0], r[2 * p + 1][1]), cvtpk(r[2 * p + 1][2], r[2 * p + 1][3])};
  o.s = u32x2{cvtpk(r[8][0], r[8][1]), cvtpk(r[8][2], r[8][3])};
}
template <int I> DI void part_store(Ctx& c, const Packed& o, unsigned so) {
  const unsigned g = c.lane >> 4, l15 = c.lane & 15, base = (l15 >> 2) * 1152;
  if constexpr (I < 4) gst16c(o.p[I], c.rws, base + I * 256 + g * 64 + (l15 & 3) * 16, so);
  else gst8c(o.s, c.rws, base + 1024 + g * 32 + (l15 & 3) * 8, so);
}
template <int I> DI void h1_store(Ctx& c, const Packed& o, unsigned mb, unsigned nt) {
  const unsigned g = c.lane >> 4, l15 = c.lane & 15;
  const unsigned vo = ((g >> 1) * 64 + (g & 1) * 32 + (c.w & 1) * 16 + l15) * 16;
  if constexpr (I < 4) gst16c(o.p[I], c.rws, vo, WS_H1 + (mb * 288 + 2 * (nt * 4 + I)) * 1024);
  else gst8c(o.s, c.rws, vo + (nt & 1) * 8, WS_H1 + (mb * 288 + 256 + 2 * (nt >> 1)) * 1024);
}
struct Red { u32x4 p[8]; u32x2 s[8]; u32x2 r[3]; };
DI void red_load(Ctx& c, Red& d, unsigned so) {
  const bool act = c.w < 4;
  const unsigned v0 = act ? c.lane * 16 : OOB, v1 = (act && c.lane < 16) ? 1024 + c.lane * 8 : OOB;
#pragma unroll
  for (int s = 0; s < 8; ++s) { gld16c<0>(d.p[s], v0, c.rws, so + s * 36864); gld8c(d.s[s], v1, c.rws, so + s * 36864); }
}
#define RED_PIN(d, n) asm volatile("s_waitcnt vmcnt(%19)" : "+v"(d.p[0]), "+v"(d.p[1]), "+v"(d.p[2]), "+v"(d.p[3]), "+v"(d.p[4]), "+v"(d.p[5]), "+v"(d.p[6]), "+v"(d.p[7]), \
    "+v"(d.s[0]), "+v"(d.s[1]), "+v"(d.s[2]), "+v"(d.s[3]), "+v"(d.s[4]), "+v"(d.s[5]), "+v"(d.s[6]), "+v"(d.s[7]), "+v"(d.r[0]), "+v"(d.r[1]), "+v"(d.r[2]) : "i"(n) : "memory")
DI void gld8(u32x2& d, unsigned voff, rsrc_t r) { asm volatile("s_nop 4\n\tbuffer_load_dwordx2 %0, %1, %2, 0 offen" : "=v"(d) : "v"(voff), "s"(r) : "memory"); }
template <bool COH> DI void res_load(Ctx& c, Red& d, rsrc_t r, unsigned base, unsigned row, unsigned nt) {
  const bool act = c.w < 4;
  const unsigned pr = c.lane >> 4, g = (c.lane >> 2) & 3;
  const unsigned o0 = act ? base + row * (DM * 2) + (nt * 128 + pr * 32 + 4 * g) * 2 : OOB;
  const unsigned o2 = (act && c.lane < 16) ? base + row * (DM * 2) + (512 + nt * 16 + 4 * g) * 2 : OOB;
  if constexpr (COH) { gld8c(d.r[0], o0, r, 0); gld8c(d.r[1], o0, r, 32); gld8c(d.r[2], o2, r, 0); }
  else { gld8(d.r[0], o0, r); gld8(d.r[1], act ? o0 + 32 : OOB, r); gld8(d.r[2], o2, r); }
}
DI void red_sum(const Red& d, float (&a)[12]) {
#pragma unroll
  for (int e = 0; e < 12; ++e) a[e] = 0.f;
#pragma unroll
  for (int s = 0; s < 8; ++s) {
#pragma unroll
    for (int e = 0; e < 4; ++e) { a[2 * e] += bflo(d.p[s][e]); a[2 * e + 1] += bfhi(d.p[s][e]); }
#pragma unroll
    for (int e = 0; e < 2; ++e) { a[8 + 2 * e] += bflo(d.s[s][e]); a[9 + 2 * e] += bfhi(d.s[s][e]); }
  }
}
DI float sum_tok4(float x) {
  x += __uint_as_float(__builtin_amdgcn_update_dpp(0, __float_as_uint(x), 0x124, 0xf, 0xf, false));
  x += __uint_as_float(__builtin_amdgcn_update_dpp(0, __float_as_uint(x), 0x128, 0xf, 0xf, false));
  { auto r = __builtin_amdgcn_permlane16_swap(__float_as_uint(x), __float_as_uint(x), false, false); x = __uint_as_float(r[0]) + __uint_as_float(r[1]); }
  return xhalf_sum(x);
}
DI void ln2_job(Ctx& c, const Red& d, unsigned row0, unsigned stat) {
  const unsigned lane = c.lane, w = c.w, tok = lane & 3, g = (lane >> 2) & 3, pr = lane >> 4, nt = w & 3;
  const bool act = w < 4, sing = lane < 16;
  float a[12], v[12]; unsigned hw[6];
  red_sum(d, a);
#pragma unroll
  for (int e = 0; e < 6; ++e) {
    const unsigned xx = d.r[e >> 1][e & 1];
    hw[e] = cvtpk(bflo(xx) + a[2 * e], bfhi(xx) + a[2 * e + 1]); v[2 * e] = bflo(hw[e]); v[2 * e + 1] = bfhi(hw[e]);
  }
  float s1 = 0.f;
#pragma unroll
  for (int e = 0; e < 12; ++e) s1 += v[e];
  const float mw = sum_tok4(s1) * (1.f / 144);
  float s2 = 0.f;
#pragma unroll
  for (int e = 0; e < 12; ++e) { const float t = v[e] - mw; s2 += (e < 8 || sing) ? t * t : 0.f; }
  s2 = sum_tok4(s2);
  if (act && lane < 4) *(LAS f32x2*)(size_t)(stat + (w * 4 + lane) * 8) = f32x2{mw, s2};
  bar();
  f32x2 st[4];
#pragma unroll
  for (int i = 0; i < 4; ++i) st[i] = *(LAS f32x2*)(size_t)(stat + (i * 4 + tok) * 8);
  const float mean = (st[0][0] + st[1][0] + st[2][0] + st[3][0]) * 0.25f;
  float m2 = st[0][1] + st[1][1] + st[2][1] + st[3][1];
#pragma unroll
  for (int i = 0; i < 4; ++i) { const float t = st[i][0] - mean; m2 += 144.f * t * t; }
  const float rs = rsqrtf(m2 * (1.f / DM) + 1e-5f);
#pragma unroll
  for (int e = 0; e < 12; ++e) v[e] = (v[e] - mean) * rs;
  const unsigned row = row0 + tok, mb = row >> 5, r32 = row & 31;
  const unsigned o0 = act ? ((mb * 36 + 2 * (nt * 4 + pr) + (g >> 1)) * 64 + (g & 1) * 32 + r32) * 16 : OOB;
  const unsigned o1 = (act && sing) ? ((mb * 36 + 32 + 2 * (nt >> 1) + (g >> 1)) * 64 + (g & 1) * 32 + r32) * 16 + (nt & 1) * 8 : OOB;
  const unsigned h0 = act ? row * (DM * 2) + (nt * 128 + pr * 32 + 4 * g) * 2 : OOB;
  const unsigned h2 = (act && sing) ? row * (DM * 2) + (512 + nt * 16 + 4 * g) * 2 : OOB;
  gst16c(u32x4{cvtpk(v[0], v[1]), cvtpk(v[2], v[3]), cvtpk(v[4], v[5]), cvtpk(v[6], v[7])}, c.rws, o0, WS_HN);
  gst8c(u32x2{cvtpk(v[8], v[9]), cvtpk(v[10], v[11])}, c.rws, o1, WS_HN);
  gst8c(u32x2{hw[0], hw[1]}, c.rws, h0, WS_H);
  gst8c(u32x2{hw[2], hw[3]}, c.rws, h0, WS_H + 32);
  gst8c(u32x2{hw[4], hw[5]}, c.rws, h2, WS_H);
}
DI void fin_job(Ctx& c, const Red& d, const u32x2 (&bb)[3], rsrc_t ro, unsigned row, unsigned nt) {
  const unsigned lane = c.lane, g = (lane >> 2) & 3, pr = lane >> 4;
  const bool act = c.w < 4;
  float a[12]; unsigned ow[6];
  red_sum(d, a);
#pragma unroll
  for (int e = 0; e < 6; ++e) {
    const unsigned hh = d.r[e >> 1][e & 1], b2 = bb[e >> 1][e & 1];
    ow[e] = cvtpk(a[2 * e] + bflo(b2) + bflo(hh), a[2 * e + 1] + bfhi(b2) + bfhi(hh));
  }
  const unsigned h0 = act ? row * (DM * 2) + (nt * 128 + pr * 32 + 4 * g) * 2 : OOB;
  const unsigned h2 = (act && lane < 16) ? row * (DM * 2) + (512 + nt * 16 + 4 * g) * 2 : OOB;
  __builtin_amdgcn_raw_buffer_store_b64(u32x2{ow[0], ow[1]}, ro, h0, 0, 0);
  __builtin_amdgcn_raw_buffer_store_b64(u32x2{ow[2], ow[3]}, ro, h0, 32, 0);
  __builtin_amdgcn_raw_buffer_store_b64(u32x2{ow[4], ow[5]}, ro, h2, 0, 0);
}

DI void phase_attn(Ctx& c, QOut& qo, u32x4 (&xa)[18], const u16* wo, const u16* w1, const u16* w2) {
  AttnState a;
  const unsigned ldsb = c.ldsb, lane = c.lane, w = c.w, b = c.b;
  const unsigned xcd = b & 7, jj = b >> 3, head = xcd * 4 + (jj >> 3), pi = jj & 7;
  const unsigned l31 = lane & 31, h = lane >> 5;
  const unsigned kh = w >> 2, rg = kh ? 7 - w : w, partner = 7 - w;
  a.voff = lane * 16; a.ldsb = ldsb; a.w = w;
  a.kbase = WS_K + head * (SEQ * HD * 2) + w * 4096; a.vbase = WS_V + head * (SEQ * HD * 2) + w * 4096;
  a.kb[0] = ldsb + kh * 16384 + lane * 16; a.kb[1] = a.kb[0] + 32768; a.vb[0] = a.kb[0] + 65536; a.vb[1] = a.kb[0] + 98304;
  PIN(a.kb[0]); PIN(a.kb[1]); PIN(a.vb[0]); PIN(a.vb[1]);
  const unsigned nhi = 16 - pi;
  publish(c, F_KV + (head * 8 + pi) * 8);
  STAMP(7);
#pragma unroll
  for (int d = 0; d < 8; ++d) *(LAS u32x4*)(size_t)(ldsb + (kh ? LE : 98304) + rg * 8192 + d * 1024 + lane * 16) = qo.q[d];
  wait_flags(c, F_KV + head * 64, 8, 1);
  STAMP(8);
#pragma unroll
  for (int p = 0; p < 4; ++p) dma16c(ldsb + w * 4096 + p * 1024, a.voff, c.rws, a.kbase + p * 1024);
#pragma unroll
  for (int p = 0; p < 4; ++p) dma16c(ldsb + 32768 + w * 4096 + p * 1024, a.voff, c.rws, a.kbase + 32768 + p * 1024);
#pragma unroll
  for (int d = 0; d < 8; ++d) a.qf[d] = lds_b128(ldsb + 98304 + rg * 8192 + d * 1024 + lane * 16);
#pragma unroll
  for (int d = 0; d < 4; ++d) a.o[d] = f32x16{};
  a.negm = f32x16{}; a.mhat = 0.f; a.l0 = a.l1 = 0.f; a.fct = 1.f; a.pend = false;
  wait_vm<4>();
  bar();
  STAMP(9);
#pragma unroll
  for (int n = 0; n < 4; ++n) a.fr[n] = lds_b128(a.kb[0] + ((n & 1) * 8 + (n >> 1)) * 1024);
  const int dq = (int)(rg * 32 + l31) - (int)(kh * 64) - 4 * (int)h;
  float m_old = 0.f;
  unsigned dummy = 0;

  auto merge = [&](unsigned sc0, unsigned sc1, unsigned qblk, float mo, float lo) AI {
    bar();
    const float lt = xhalf_sum(lo);
    *(LAS f32x2*)(size_t)(sc0 + w * 512 + lane * 8) = f32x2{mo, lt};
    bar();
    const f32x2 ps = *(LAS f32x2*)(size_t)(sc0 + partner * 512 + lane * 8);
    const float mm = fmaxf(mo, ps[0]);
    const float fo = __builtin_amdgcn_exp2f(mo - mm), fp = __builtin_amdgcn_exp2f(ps[0] - mm);
    const float f = fo / (lt * fo + ps[1] * fp);
    bar();
    const unsigned mine = (kh ? sc1 : sc0) + (w & 3) * 8192 + lane * 16, theirs = (kh ? sc0 : sc1) + (partner & 3) * 8192 + lane * 16;
#pragma unroll
    for (int r = 0; r < 2; ++r) {
      const f32x16 x = kh ? a.o[r] : a.o[2 + r];
#pragma unroll
      for (int e = 0; e < 4; ++e)
        *(LAS f32x4*)(size_t)(mine + (r * 4 + e) * 1024) = f32x4{x[4 * e] * f, x[4 * e + 1] * f, x[4 * e + 2] * f, x[4 * e + 3] * f};
    }
    bar();
    const unsigned mb = qblk * 4 + rg;
#pragma unroll
    for (int r = 0; r < 2; ++r) {
      f32x16 x = kh ? a.o[2 + r] : a.o[r];
#pragma unroll
      for (int e = 0; e < 4; ++e) {
        const f32x4 y = *(LAS f32x4*)(size_t)(theirs + (r * 4 + e) * 1024);
#pragma unroll
        for (int k = 0; k < 4; ++k) x[4 * e + k] = x[4 * e + k] * f + y[k];
      }
      const unsigned so = WS_CTX + (mb * 256 + head * 8 + 2 * (kh * 2 + r)) * 1024;
      gst16c(pack8<0>(x), c.rws, lane * 16, so);
      gst16c(pack8<1>(x), c.rws, lane * 16, so + 1024);
    }
#pragma unroll
    for (int d = 0; d < 4; ++d) a.o[d] = f32x16{};
  };
  auto post = [&](auto X_, unsigned s) AI {
    constexpr int X = decltype(X_)::v;
    if (__builtin_expect(s == nhi, 0)) {
      merge(ldsb + 65536 + (X ^ 1) * 32768, ldsb + LE, 15 - pi, m_old, a.l0 + a.l1);
      a.l0 = a.l1 = 0.f;
      publish(c, F_CTX + ((15 - pi) * 32 + head) * 8);
    }
    if (__builtin_expect(a.pend, 0)) {
#pragma unroll
      for (int d = 0; d < 4; ++d)
#pragma unroll
        for (int r = 0; r < 16; ++r) a.o[d][r] *= a.fct;
      a.l0 *= a.fct; a.l1 *= a.fct; a.pend = false;
    }
    a.l0 = fadd_s(a.l0, a.t0); a.l1 = fadd_s(a.l1, a.t1);
    if (__builtin_expect(s + 1 == nhi, 0)) {
#pragma unroll
      for (int d = 0; d < 8; ++d) a.qf[d] = lds_b128(ldsb + LE + rg * 8192 + d * 1024 + lane * 16);
      a.negm = f32x16{};
      m_old = a.mhat;
    }
    if constexpr (X == 1) {
      if (__builtin_expect(s <= 5, 0)) {
        const u16* wp = s == 1 ? wo : s == 3 ? w1 : w2;
        const unsigned per = s == 1 ? 18 : 21, tot = s == 1 ? 144 : 162;
        const unsigned li = w * per + lane;
        const rsrc_t rp = mk_rsrc(wp, s == 1 ? NQ * DM * 2 : FF * DM * 2);
        asm volatile("s_nop 4\n\tbuffer_load_dword %0, %1, %2, 0 offen" : "+v"(dummy) : "v"((lane < per && li < tot) ? (b * tot + li) * 128 : OOB), "s"(rp) : "memory");
      }
    }
    SB();
  };
  attn_iter<0, true, false>(c, a, 0, nhi, true, false, dq); post(IC<0>{}, 0);
  for (unsigned s = 1; s < 17; s += 2) {
    attn_iter<1, true, true>(c, a, s, nhi, s == nhi, s + 1 == nhi, dq); post(IC<1>{}, s);
    attn_iter<0, true, true>(c, a, s + 1, nhi, s + 1 == nhi, s + 2 == nhi || s + 1 == 16, dq); post(IC<0>{}, s + 1);
  }
  const unsigned mtH = 8 + (jj >> 2);
  u32x2 fl;
  flag_load(c, fl, F_CTX + (mtH * 32 + xcd * 4) * 8, 4);
  attn_iter<1, false, true>(c, a, 17, nhi, false, false, dq);
  asm volatile("" ::"v"(dummy));
  STAMP(10);
  flag_check(c, fl, F_CTX + (mtH * 32 + xcd * 4) * 8, 4);
  x_load<16, 16>(c, xa, WS_CTX + ((mtH * 4 + (w >> 1)) * 256 + xcd * 32) * 1024, 0);
  merge(ldsb, ldsb + 32768, pi, a.mhat, a.l0 + a.l1);
  bar();
  STAMP(11);
}

DI void phase_tail(Ctx& c, u32x4 (&xa)[18], const u16* x, const u16* wo, const u16* w1, const u16* b1, const u16* w2, const u16* b2, u16* out) {
  const unsigned b = c.b, xcd = b & 7, jj = b >> 3, w = c.w, lane = c.lane;
  const unsigned sp = xcd, m8 = jj >> 2, nt = jj & 3, mtH = 8 + m8, mtL = m8, nt32 = xcd * 4 + nt;
  const unsigned head = xcd * 4 + (jj >> 3), pi = jj & 7;
  const unsigned own = nt * 8 + sp;
  u32x4 xb[18]; f32x4 acc[9]; bf16x4 tl[9]; Packed pk; u32x2 fl; Red rd; WSrc ws; WRd wr;
  auto p_off = [&](unsigned base, unsigned mt) AI { return base + (((mt * 4 + nt) * 8 + sp) * 8 + w) * 4608; };
  wsrc_setup(c, ws, mk_rsrc(wo, NQ * DM * 2), DM * 2, nt * 256, 1024 + nt * 32, 512 * sp, 0);
  wrd_setup<1>(c, wr);
#pragma unroll
  for (int q = 0; q < 4; ++q) w_group(c, ws, q);
  XWAIT6(xa, 0, 15); XWAIT6(xa, 6, 15); XWAIT4(xa, 12, 15);
  bar();
  flag_store(c, F_CTX + (pi * 32 + head) * 8);
  STAMP(12);
  gemm_r<16, 1>(acc, xa, wr, tl, [&](auto V_) AI {
    constexpr int v = decltype(V_)::v;
    if constexpr (v == 3) { wait_vm<11>(); bar(); }
    if constexpr (v == 7) { wait_vm<6>(); bar(); }
    if constexpr (v == 10) flag_load(c, fl, F_CTX + (mtL * 32 + xcd * 4) * 8, 4);
    if constexpr (v == 11) { wait_vm<2>(); bar(); }
    if constexpr (v == 13) {
      wait_vm<0>();
      flag_check(c, fl, F_CTX + (mtL * 32 + xcd * 4) * 8, 4);
      x_load<16, 16>(c, xb, WS_CTX + ((mtL * 4 + (w >> 1)) * 256 + xcd * 32) * 1024, 0);
    }
  });
  pack_plain(pk, acc);
  STAMP(13);
  const rsrc_t rxx = mk_rsrc(x, SEQ * DM * 2);
  const unsigned rdo = ((w & 3) * 64 + (own >> 2)) * 4608 + (own & 3) * 1152;
  gemm_r<16, 1>(acc, xb, wr, tl, [&](auto V_) AI {
    constexpr int v = decltype(V_)::v;
    if constexpr (v == 0) XWAIT4(xb, 0, 12);
    if constexpr (v == 4) XWAIT4(xb, 4, 12);
    if constexpr (v < 5) part_store<v>(c, pk, p_off(WS_P1, mtH));
    if constexpr (v == 8) XWAIT4(xb, 8, 9);
    if constexpr (v == 9) { XWAIT4(xb, 12, 0); bar(); flag_store(c, F_P1 + (mtH * 32 + own) * 8); res_load<false>(c, rd, rxx, 0, mtH * 128 + own * 4 + (lane & 3), w & 3); }
    if constexpr (v == 13) flag_load(c, fl, F_P1 + mtH * 256, 32);
    if constexpr (v == 15) {
      wait_vm<0>();
      flag_check(c, fl, F_P1 + mtH * 256, 32);
      red_load(c, rd, WS_P1 + mtH * 256 * 4608 + rdo);
    }
  });
  pack_plain(pk, acc);
  static_for<5>([&](auto I) AI { part_store<decltype(I)::v>(c, pk, p_off(WS_P1, mtL)); });
  STAMP(14);
  RED_PIN(rd, 5);
  ln2_job(c, rd, mtH * 128 + own * 4, c.ldsb + LSTAT);
  wsrc_setup(c, ws, mk_rsrc(w1, DM * FF * 2), FF * 2, nt32 * 256, 8192 + nt32 * 32, 0, 512);
  wrd_setup<0>(c, wr);
  w_tail1(c, ws);
#pragma unroll
  for (int q = 0; q < 4; ++q) w_group(c, ws, q);
  wait_vm<23>();
  bar();
  flag_store(c, F_P1 + (mtL * 32 + own) * 8);
  flag_store(c, F_HN + (mtH * 32 + own) * 8);
  STAMP(15);
  u32x2 bias[9];
  {
    const rsrc_t rb = mk_rsrc(b1, FF * 2);
#pragma unroll
    for (int a = 0; a < 9; ++a) gld8(bias[a], ((a < 8 ? nt32 * 128 + a * 16 : 4096 + nt32 * 16) + 4 * (lane >> 4)) * 2, rb);
  }
  res_load<false>(c, rd, rxx, 0, mtL * 128 + own * 4 + (lane & 3), w & 3);
  poll_flags(c, F_P1 + mtL * 256, 32, 1);
  red_load(c, rd, WS_P1 + mtL * 256 * 4608 + rdo);
  poll_flags(c, F_HN + mtH * 256, 32, 1);
  x_load<18, 18>(c, xa, WS_HN + (mtH * 4 + (w >> 1)) * 36 * 1024, 0);
  STAMP(16);
  RED_PIN(rd, 18);
#pragma unroll
  for (int a = 0; a < 9; ++a) PIN(bias[a]);
  ln2_job(c, rd, mtL * 128 + own * 4, c.ldsb + LSTAT + 128);
  tail_read(wr, tl);
  STAMP(17);
  auto pack_act = [&]() AI {
#pragma unroll
    for (int a = 0; a < 9; ++a) {
      const float r0 = fmaxf(acc[a][0] + bflo(bias[a][0]), 0.f), r1 = fmaxf(acc[a][1] + bfhi(bias[a][0]), 0.f);
      const float r2 = fmaxf(acc[a][2] + bflo(bias[a][1]), 0.f), r3 = fmaxf(acc[a][3] + bfhi(bias[a][1]), 0.f);
      acc[a] = f32x4{r0 * r0, r1 * r1, r2 * r2, r3 * r3};
    }
    pack_plain(pk, acc);
  };
  gemm_r<18, 0>(acc, xa, wr, tl, [&](auto V_) AI {
    constexpr int v = decltype(V_)::v;
    if constexpr (v == 0) XWAIT6(xa, 0, 17);
    if constexpr (v == 5) { XWAIT6(xa, 6, 0); XWAIT6(xa, 12, 0); bar(); flag_store(c, F_HN + (mtL * 32 + own) * 8); w_tail2(c, ws); }
    if constexpr (v == 11) flag_load(c, fl, F_HN + mtL * 256, 32);
    if constexpr (v == 14) {
      wait_vm<0>(); bar();
      flag_check(c, fl, F_HN + mtL * 256, 32);
      x_load<18, 18>(c, xb, WS_HN + (mtL * 4 + (w >> 1)) * 36 * 1024, 0);
    }
  });
  pack_act();
  STAMP(18);
  WSrc ws2;
  wsrc_setup(c, ws2, mk_rsrc(w2, FF * DM * 2), DM * 2, nt * 256, 1024 + nt * 32, 512 * sp, 4096 + 64 * sp);
  gemm_r<18, 0>(acc, xb, wr, tl, [&](auto V_) AI {
    constexpr int v = decltype(V_)::v;
    if constexpr (v == 0) XWAIT6(xb, 0, 12);
    if constexpr (v < 5) h1_store<v>(c, pk, mtH * 4 + (w >> 1), nt32);
    if constexpr (v == 5) { bar(); w_group(c, ws2, 0); }
    if constexpr (v == 6) XWAIT6(xb, 6, 16);
    if constexpr (v == 9) { XWAIT6(xb, 12, 5); bar(); flag_store(c, F_H1 + (mtH * 32 + nt32) * 8); w_group(c, ws2, 1); }
    if constexpr (v == 13) { bar(); flag_load(c, fl, F_H1 + (mtH * 32 + sp * 4) * 8, 4); w_group(c, ws2, 2); }
    if constexpr (v == 16) {
      wait_vm<5>();
      flag_check(c, fl, F_H1 + (mtH * 32 + sp * 4) * 8, 4);
      x_load<18, 16>(c, xa, WS_H1 + ((mtH * 4 + (w >> 1)) * 288 + sp * 32) * 1024, WS_H1 + ((mtH * 4 + (w >> 1)) * 288 + 256 + sp * 4) * 1024);
    }
    if constexpr (v == 17) { bar(); w_group(c, ws2, 3); }
  });
  pack_act();
  bar();
  w_tail1(c, ws2);
  STAMP(19);
  gemm_r<18, 0>(acc, xa, wr, tl, [&](auto V_) AI {
    constexpr int v = decltype(V_)::v;
    if constexpr (v == 0) XWAIT6(xa, 0, 20);
    if constexpr (v < 5) h1_store<v>(c, pk, mtL * 4 + (w >> 1), nt32);
    if constexpr (v == 6) { XWAIT6(xa, 6, 5); XWAIT6(xa, 12, 5); bar(); tail_read(wr, tl); }
    if constexpr (v == 9) { wait_vm<0>(); bar(); flag_store(c, F_H1 + (mtL * 32 + nt32) * 8); w_tail2(c, ws2); }
    if constexpr (v == 13) flag_load(c, fl, F_H1 + (mtL * 32 + sp * 4) * 8, 4);
    if constexpr (v == 15) {
      wait_vm<0>(); bar();
      flag_check(c, fl, F_H1 + (mtL * 32 + sp * 4) * 8, 4);
      x_load<18, 16>(c, xb, WS_H1 + ((mtL * 4 + (w >> 1)) * 288 + sp * 32) * 1024, WS_H1 + ((mtL * 4 + (w >> 1)) * 288 + 256 + sp * 4) * 1024);
    }
  });
  pack_plain(pk, acc);
  STAMP(20);
  const rsrc_t ro = mk_rsrc(out, SEQ * DM * 2);
  u32x2 bb[3];
  const unsigned frow = sp * 16 + (w & 3) * 4 + (lane & 3);
  const unsigned fdo = (nt * 64 + sp) * 4608 + (w & 3) * 1152;
  gemm_r<18, 0>(acc, xb, wr, tl, [&](auto V_) AI {
    constexpr int v = decltype(V_)::v;
    if constexpr (v == 0) XWAIT6(xb, 0, 12);
    if constexpr (v < 5) part_store<v>(c, pk, p_off(WS_P2, mtH));
    if constexpr (v == 6) XWAIT6(xb, 6, 11);
    if constexpr (v == 9) {
      XWAIT6(xb, 12, 0); bar(); flag_store(c, F_P2 + ((mtH * 4 + nt) * 8 + sp) * 8);
      const rsrc_t rb = mk_rsrc(b2, DM * 2);
      const unsigned pr = lane >> 4, g = (lane >> 2) & 3;
      gld8(bb[0], (nt * 128 + pr * 32 + 4 * g) * 2, rb); gld8(bb[1], (nt * 128 + pr * 32 + 16 + 4 * g) * 2, rb); gld8(bb[2], (512 + nt * 16 + 4 * g) * 2, rb);
      res_load<true>(c, rd, c.rws, WS_H, mtH * 128 + frow, nt);
    }
    if constexpr (v == 14) flag_load(c, fl, F_P2 + (mtH * 4 + nt) * 64, 8);
    if constexpr (v == 16) {
      wait_vm<0>();
      flag_check(c, fl, F_P2 + (mtH * 4 + nt) * 64, 8);
      red_load(c, rd, WS_P2 + mtH * 256 * 4608 + fdo);
    }
  });
  pack_plain(pk, acc);
  static_for<5>([&](auto I) AI { part_store<decltype(I)::v>(c, pk, p_off(WS_P2, mtL)); });
  STAMP(21);
  RED_PIN(rd, 5);
  PIN(bb[0]); PIN(bb[1]); PIN(bb[2]);
  fin_job(c, rd, bb, ro, mtH * 128 + frow, nt);
  wait_vm<3>();
  bar();
  flag_store(c, F_P2 + ((mtL * 4 + nt) * 8 + sp) * 8);
  STAMP(22);
  if (w < 4) {
    res_load<true>(c, rd, c.rws, WS_H, mtL * 128 + frow, nt);
    poll_flags(c, F_P2 + (mtL * 4 + nt) * 64, 8, 1);
    red_load(c, rd, WS_P2 + mtL * 256 * 4608 + fdo);
    RED_PIN(rd, 0);
    fin_job(c, rd, bb, ro, mtL * 128 + frow, nt);
    if (b == 0 && w == 0) gst8c(u32x2{c.ep0, c.ep1}, c.rws, lane == 0 ? F_EPOCH : OOB, WS_FLAGS);
  }
}

__global__ __launch_bounds__(512, 2) void fused_block(const u16* __restrict__ x, const u16* __restrict__ ckv, const u16* __restrict__ wq,
                                                      const u16* __restrict__ wuk, const u16* __restrict__ wuv, const u16* __restrict__ wo,
                                                      const u16* __restrict__ w1, const u16* __restrict__ b1, const u16* __restrict__ w2,
                                                      const u16* __restrict__ b2, u16* __restrict__ out, char* __restrict__ ws) {
  extern __shared__ __attribute__((aligned(16))) char lds[];
  Ctx c;
  c.ldsb = (unsigned)(size_t)lds; c.lane = threadIdx.x & 63; c.w = rfl(threadIdx.x >> 6); c.b = blockIdx.x;
  c.rws = mk_rsrc(ws, WS_END);
  STAMP(0);
  QOut qo;
  u32x4 xa[18];
  phase_qkv(c, qo, x, ckv, wq, wuk, wuv);
  fresh(c);
  phase_attn(c, qo, xa, wo, w1, w2);
  fresh(c);
  phase_tail(c, xa, x, wo, w1, b1, w2, b2, out);
}

extern "C" void kernel_launch(void* const* d_in, const int* in_sizes, int n_in, void* d_out, int out_size, void* d_ws, size_t ws_size,
                              hipStream_t stream) {
  hipFuncSetAttribute((const void*)fused_block, hipFuncAttributeMaxDynamicSharedMemorySize, 163840);
  fused_block<<<256, 512, 163840, stream>>>((const u16*)d_in[0], (const u16*)d_in[1], (const u16*)d_in[2], (const u16*)d_in[3], (const u16*)d_in[4],
                                            (const u16*)d_in[5], (const u16*)d_in[7], (const u16*)d_in[8], (const u16*)d_in[9], (const u16*)d_in[10],
                                            (u16*)d_out, (char*)d_ws);
}
```

```cpp
#include <hip/hip_runtime.h>
#include <stdint.h>
#include <utility>

typedef __bf16 bf16x8 __attribute__((ext_vector_type(8)));
typedef __bf16 bf16x4 __attribute__((ext_vector_type(4)));
typedef __bf16 bf16x2 __attribute__((ext_vector_type(2)));
typedef float f32x16 __attribute__((ext_vector_type(16)));
typedef float f32x4 __attribute__((ext_vector_type(4)));
typedef float f32x2 __attribute__((ext_vector_type(2)));
typedef unsigned u32x4 __attribute__((ext_vector_type(4)));
typedef unsigned u32x2 __attribute__((ext_vector_type(2)));
typedef unsigned short u16;
typedef unsigned long long u64;
#define LAS __attribute__((address_space(3)))
#define DI __device__ __forceinline__
#define AI __attribute__((always_inline))
typedef __amdgpu_buffer_rsrc_t rsrc_t;

constexpr int SEQ = 2048, DM = 576, NH = 32, HD = 128, DC = 512, FF = 4608, NQ = 4096;

constexpr unsigned WS_FLAGS = 0;
constexpr unsigned WS_XN = 131072;
constexpr unsigned WS_K = WS_XN + SEQ * DM * 2;
constexpr unsigned WS_V = WS_K + SEQ * NQ * 2;
constexpr unsigned WS_CTX = WS_V + SEQ * NQ * 2;
constexpr unsigned WS_P1 = WS_CTX + SEQ * NQ * 2;
constexpr unsigned WS_H = WS_P1 + 8 * SEQ * DM * 2;
constexpr unsigned WS_HN = WS_H + SEQ * DM * 2;
constexpr unsigned WS_H1 = WS_HN + SEQ * DM * 2;
constexpr unsigned WS_P2 = WS_H1 + SEQ * FF * 2;
constexpr unsigned WS_END = WS_P2 + 8 * SEQ * DM * 2;
constexpr unsigned F_EPOCH = 0, F_XN = 4096, F_KV = 8192, F_CTX = 12288, F_P1 = 16384, F_HN = 32768, F_H1 = 49152, F_P2 = 65536;

template <int... Is> using iseq = std::integer_sequence<int, Is...>;
template <int N> using make_iseq = std::make_integer_sequence<int, N>;
template <int I> struct IC { static constexpr int v = I; };
template <class F, int... Is> DI void static_for_(F&& f, iseq<Is...>) { (f(IC<Is>{}), ...); }
template <int N, class F> DI void static_for(F&& f) { static_for_(f, make_iseq<N>{}); }

#define SB() __builtin_amdgcn_sched_barrier(0)
#define PIN(x) asm volatile("" : "+v"(x))
DI unsigned rfl(unsigned x) { return __builtin_amdgcn_readfirstlane(x); }
DI rsrc_t mk_rsrc(const void* p, unsigned bytes) { return __builtin_amdgcn_make_buffer_rsrc((void*)p, (short)0, (int)bytes, 0x00020000); }
constexpr unsigned OOB = 0xFFFFFF00u;
DI void dma16(unsigned lds_addr, unsigned voff, rsrc_t r, unsigned soff) {
  asm volatile("s_mov_b32 m0, %0\n\ts_nop 3\n\tbuffer_load_dwordx4 %1, %2, %3 offen lds" ::"s"(lds_addr), "v"(voff), "s"(r), "s"(soff) : "memory", "m0");
}
DI void dma16c(unsigned lds_addr, unsigned voff, rsrc_t r, unsigned soff) {
  asm volatile("s_mov_b32 m0, %0\n\ts_nop 3\n\tbuffer_load_dwordx4 %1, %2, %3 offen sc1 lds" ::"s"(lds_addr), "v"(voff), "s"(r), "s"(soff) : "memory", "m0");
}
template <int IMM> DI void gld16c(u32x4& d, unsigned voff, rsrc_t r, unsigned soff) {
  asm volatile("s_nop 4\n\tbuffer_load_dwordx4 %0, %1, %2, %3 offen offset:%4 sc1" : "=v"(d) : "v"(voff), "s"(r), "s"(soff), "i"(IMM) : "memory");
}
template <int IMM> DI void gld16(u32x4& d, unsigned voff, rsrc_t r, unsigned soff) {
  asm volatile("s_nop 4\n\tbuffer_load_dwordx4 %0, %1, %2, %3 offen offset:%4" : "=v"(d) : "v"(voff), "s"(r), "s"(soff), "i"(IMM) : "memory");
}
DI void gld8c(u32x2& d, unsigned voff, rsrc_t r, unsigned soff) {
  asm volatile("s_nop 4\n\tbuffer_load_dwordx2 %0, %1, %2, %3 offen sc1" : "=v"(d) : "v"(voff), "s"(r), "s"(soff) : "memory");
}
DI void gst16c(u32x4 v, rsrc_t r, unsigned voff, unsigned soff) {
  asm volatile("s_nop 4\n\tbuffer_store_dwordx4 %0, %1, %2, %3 offen sc1\n\ts_nop 1" ::"v"(v), "v"(voff), "s"(r), "s"(soff) : "memory");
}
DI void gst8c(u32x2 v, rsrc_t r, unsigned voff, unsigned soff) { __builtin_amdgcn_raw_buffer_store_b64(v, r, voff, soff, 16); }
template <int N> DI void wait_vm() { asm volatile("s_waitcnt vmcnt(%0)" ::"i"(N) : "memory"); }
DI void bar() { asm volatile("s_waitcnt lgkmcnt(0)" ::: "memory"); __builtin_amdgcn_s_barrier(); asm volatile("" ::: "memory"); }
DI unsigned cvtpk(float lo, float hi) { f32x2 v = {lo, hi}; bf16x2 b = __builtin_convertvector(v, bf16x2); return __builtin_bit_cast(unsigned, b); }
DI float bflo(unsigned x) { return __uint_as_float(x << 16); }
DI float bfhi(unsigned x) { return __uint_as_float(x & 0xffff0000u); }
DI bf16x8 lds_b128(unsigned a) { return *(LAS bf16x8*)(size_t)a; }
DI bf16x4 lds_tr(unsigned a) { return __builtin_amdgcn_ds_read_tr16_b64_v4bf16((LAS bf16x4*)(size_t)a); }
DI bf16x8 cat(bf16x4 a, bf16x4 b) { return __builtin_shufflevector(a, b, 0, 1, 2, 3, 4, 5, 6, 7); }
DI bf16x8 asbf(u32x4 x) { return __builtin_bit_cast(bf16x8, x); }
#define MFMA32(a, b, c) __builtin_amdgcn_mfma_f32_32x32x16_bf16((a), (b), (c), 0, 0, 0)
#define MFMA16(a, b, c) __builtin_amdgcn_mfma_f32_16x16x32_bf16((a), (b), (c), 0, 0, 0)
template <int S> DI u32x4 pack8(const f32x16& x, float sc) {
  u32x4 w;
  w[0] = cvtpk(x[8 * S + 0] * sc, x[8 * S + 1] * sc); w[1] = cvtpk(x[8 * S + 2] * sc, x[8 * S + 3] * sc);
  w[2] = cvtpk(x[8 * S + 4] * sc, x[8 * S + 5] * sc); w[3] = cvtpk(x[8 * S + 6] * sc, x[8 * S + 7] * sc);
  return w;
}
template <int S> DI u32x4 pack8(const f32x16& x) {
  u32x4 w;
  w[0] = cvtpk(x[8 * S + 0], x[8 * S + 1]); w[1] = cvtpk(x[8 * S + 2], x[8 * S + 3]);
  w[2] = cvtpk(x[8 * S + 4], x[8 * S + 5]); w[3] = cvtpk(x[8 * S + 6], x[8 * S + 7]);
  return w;
}
DI float max3f(float a, float b, float c) { float r; asm("v_max3_f32 %0, %1, %2, %3" : "=v"(r) : "v"(a), "v"(b), "v"(c)); return r; }
DI float max2f(float a, float b) { float r; asm("v_max_f32_e32 %0, %1, %2" : "=v"(r) : "v"(a), "v"(b)); return r; }
DI float fadd_s(float a, float b) { float r; asm("v_add_f32_e32 %0, %1, %2" : "=v"(r) : "v"(a), "v"(b)); return r; }
DI float xhalf_sum(float x) {
  auto r = __builtin_amdgcn_permlane32_swap(__float_as_uint(x), __float_as_uint(x), false, false);
  return __uint_as_float(r[0]) + __uint_as_float(r[1]);
}
DI float wave_sum(float x) {
#pragma unroll
  for (int o = 1; o < 64; o <<= 1) x += __shfl_xor(x, o);
  return x;
}

#define STAMP(i)

struct Ctx {
  unsigned ldsb, w, lane, b;
  unsigned ep0, ep1;
  rsrc_t rws;
};

DI void fresh(Ctx& c) {
  asm volatile("v_mbcnt_lo_u32_b32 %0, -1, 0\n\tv_mbcnt_hi_u32_b32 %0, -1, %0" : "=v"(c.lane));
  asm volatile("" : "+s"(c.w), "+s"(c.b), "+s"(c.ldsb));
}
DI void publish(Ctx& c, unsigned flag_off) {
  wait_vm<0>();
  bar();
  gst8c(u32x2{c.ep0, c.ep1}, c.rws, (c.w == 0 && c.lane == 0) ? flag_off : OOB, WS_FLAGS);
}
DI void poll_flags(Ctx& c, unsigned flag_off, unsigned n, unsigned stride) {
  const unsigned li = c.lane < n ? c.lane : 0;
  const unsigned voff = flag_off + li * 8 * stride;
  const u64 t0 = __builtin_amdgcn_s_memrealtime();
  for (;;) {
    u32x2 f;
    gld8c(f, voff, c.rws, WS_FLAGS);
    asm volatile("s_waitcnt vmcnt(0)" : "+v"(f)::"memory");
    if (__all(f[0] == c.ep0 && f[1] == c.ep1)) break;
    if (__builtin_amdgcn_s_memrealtime() - t0 > 5000000ull) break;
    __builtin_amdgcn_s_sleep(2);
  }
}
DI void wait_flags(Ctx& c, unsigned flag_off, unsigned n, unsigned stride) {
  if (c.w == 0) poll_flags(c, flag_off, n, stride);
  bar();
}

#define PRIO_H() __builtin_amdgcn_s_setprio(2)
#define DBG_DRAIN()
#define STAMPG(i)
DI void flag_store(Ctx& c, unsigned flag_off) {
  gst8c(u32x2{c.ep0, c.ep1}, c.rws, (c.w == 0 && c.lane == 0) ? flag_off : OOB, WS_FLAGS);
}
DI void flag_load(Ctx& c, u32x2& f, unsigned flag_off, unsigned n) { gld8c(f, flag_off + (c.lane < n ? c.lane : 0) * 8, c.rws, WS_FLAGS); }
DI void flag_check(Ctx& c, u32x2& f, unsigned flag_off, unsigned n) {
  PIN(f);
  if (__builtin_expect(!__all(f[0] == c.ep0 && f[1] == c.ep1), 0)) poll_flags(c, flag_off, n, 1);
}

constexpr float QSCALE = 0.08838834764831845f * 1.4426950408889634f;
constexpr unsigned LE = 131072;
constexpr int XR = 12;

constexpr int kv_wait(int t) {
  int seq[256] = {}; int n = 0;
  auto push = [&](int tag, int cnt) { for (int i = 0; i < cnt; ++i) seq[n++] = tag; };
  push(-1, 3);
  for (int s = 0; s < 4; ++s) push(s, 4);
  push(-2, 2);
  for (int s = 0;; ++s) {
    if (s >= 1) push(s + 3, 2);
    if (s == 6) push(-4, 2);
    if (s == t) break;
    if (s == 3) push(-3, 1);
    if (s == 10) push(-5, 1);
    push(s + 4, 2);
    if (s == 5) push(-4, 2);
  }
  int last = -1;
  for (int i = 0; i < n; ++i) if (seq[i] == t + 1) last = i;
  return n - 1 - last;
}
constexpr int q_wait(int ks) {
  int seq[256] = {}; int n = 0;
  for (int s = 0; s < ks; ++s) {
    if (s % 2 == 0 && s < 32) seq[n++] = -1;
    if (s >= 1 && s - 1 + XR < 36) seq[n++] = s - 1 + XR;
  }
  int last = -1;
  for (int i = 0; i < n; ++i) if (seq[i] == ks + 2) last = i;
  return n - 1 - last;
}

struct QOut { u32x4 q[8]; };

template <bool ISV>
DI void qkv_body(Ctx& c, QOut& qo, const rsrc_t rx, const rsrc_t rw, const rsrc_t rwq, const unsigned voffX, const unsigned voffW, const unsigned sX,
                 const unsigned sW, const unsigned sWq, const unsigned sWq4, const unsigned rot, const unsigned qmb, const unsigned head, const unsigned cb) {
  const unsigned w = c.w, lane = c.lane, ldsb = c.ldsb;
  const unsigned l31 = lane & 31, h = lane >> 5, g2 = (lane >> 4) & 1, q = (lane & 15) >> 2, pp = lane & 3;
  const unsigned wm = w >> 2, wn = w & 3, xsw = (l31 >> 2) & 3;
  const unsigned wrow = 8 * h + q, wch = (g2 * 2 + (pp >> 1)) ^ ((q >> 1) << 2);
  const unsigned wl0 = ldsb + wrow * 128 + (wch << 4) + 8 * (pp & 1), wl1 = ldsb + wrow * 128 + ((wch ^ 4) << 4) + 8 * (pp & 1);
  u32x4 pw[16], xq[36]; u32x2 xnf;
  const unsigned xso = WS_XN + qmb * 36 * 1024;
  auto ldx = [&](auto K_) AI { constexpr int K = decltype(K_)::v; gld16c<(K & 3) * 1024>(xq[K], lane * 16, c.rws, xso + (K & ~3) * 1024); };
  {
    unsigned xb[2][2], wb[2][2];
    xb[0][0] = ldsb + (wm * 128 + l31) * 64 + ((h ^ xsw) << 4); xb[0][1] = ldsb + (wm * 128 + l31) * 64 + (((2 + h) ^ xsw) << 4);
    wb[0][0] = wl0 + 16384 + wn * 4096; wb[0][1] = wl1 + 16384 + wn * 4096;
    xb[1][0] = xb[0][0] + 65536; xb[1][1] = xb[0][1] + 65536; wb[1][0] = wb[0][0] + 65536; wb[1][1] = wb[0][1] + 65536;
    PIN(xb[0][0]); PIN(xb[0][1]); PIN(xb[1][0]); PIN(xb[1][1]); PIN(wb[0][0]); PIN(wb[0][1]); PIN(wb[1][0]); PIN(wb[1][1]);
    const unsigned dX = ldsb + 2 * w * 1024, dW = ldsb + 16384 + 2 * w * 1024, dQ = ldsb + w * 4096;
    f32x16 acc[4][2];
#pragma unroll
    for (int i = 0; i < 4; ++i)
#pragma unroll
      for (int j = 0; j < 2; ++j) acc[i][j] = f32x16{};
    bf16x8 xf[2][4]; bf16x4 wl[2][4];
    auto rd = [&](auto T_, auto S_, auto R_) AI {
      constexpr int T = decltype(T_)::v, S = decltype(S_)::v, R = decltype(R_)::v;
      constexpr unsigned so = (T & 1) * 32768; constexpr int sp = (T >> 1) & 1;
      if constexpr (R < 2) wl[S][R] = lds_tr(wb[sp][0] + so + (16 * S + 4 * R) * 128);
      else if constexpr (R == 2) xf[S][0] = lds_b128(xb[sp][S] + so);
      else if constexpr (R < 5) wl[S][R - 1] = lds_tr(wb[sp][1] + so + (16 * S + 4 * (R - 3)) * 128);
      else xf[S][R - 4] = lds_b128(xb[sp][S] + so + (R - 4) * 2048);
    };
    auto issue = [&](auto U_, auto H_, auto P_) AI {
      constexpr int U = decltype(U_)::v, H = decltype(H_)::v, P = decltype(P_)::v;
      constexpr unsigned so = (U & 3) * 32768;
      if constexpr (U < 16) {
        const unsigned tr = (U + rot) & 15;
        if constexpr (H == 0) dma16(dX + so + P * 1024, voffX, rx, sX + 64 * tr + P * 16 * 1024);
        else dma16(dW + so + P * 1024, voffW, rw, sW + 32 * tr * 8192 + P * 8 * 8192);
      } else if constexpr (U < 19) {
        dma16(dQ + so + (2 * H + P) * 1024, voffW, rwq, sWq + (U - 15) * 128 * 8192 + (2 * H + P) * 8 * 8192);
      } else if constexpr (U == 19 && H == 0) {
        dma16(ldsb + so + (w >> 2) * 16384 + ((w & 3) * 2 + P) * 1024, voffW, rwq, sWq4 + P * 8 * 8192);
      }
    };
    static_for<8>([&](auto R) AI { rd(IC<0>{}, IC<0>{}, R); });
    SB();
    static_for<16>([&](auto T_) AI {
      constexpr int T = decltype(T_)::v;
      static_for<2>([&](auto S_) AI {
        constexpr int S = decltype(S_)::v;
        if constexpr (S == 1) {
          if constexpr (T < 15) wait_vm<kv_wait(T)>();
          bar();
          if constexpr (T == 3) flag_store(c, F_XN + c.b * 8);
          if constexpr (T == 10) flag_load(c, xnf, F_XN + qmb * 32, 4);
          if constexpr (T == 14) flag_check(c, xnf, F_XN + qmb * 32, 4);
          SB();
        }
        static_for<8>([&](auto M_) AI {
          constexpr int M = decltype(M_)::v, i = M >> 1, j = M & 1;
          const bf16x8 wf = cat(wl[S][2 * j], wl[S][2 * j + 1]);
          acc[i][j] = ISV ? MFMA32(xf[S][i], wf, acc[i][j]) : MFMA32(wf, xf[S][i], acc[i][j]);
          if constexpr (M < 4 && (S == 0 || T < 15)) {
            rd(IC<(S == 0 ? T : T + 1)>{}, IC<(S ^ 1)>{}, IC<2 * M>{});
            rd(IC<(S == 0 ? T : T + 1)>{}, IC<(S ^ 1)>{}, IC<2 * M + 1>{});
          }
          if constexpr (M == 4 || M == 6) {
            constexpr int P = (M - 4) >> 1;
            if constexpr (S == 1) issue(IC<T + 4>{}, IC<0>{}, IC<P>{});
            else if constexpr (T >= 1) issue(IC<T + 3>{}, IC<1>{}, IC<P>{});
          }
          if constexpr (M == 7 && ((S == 1 && T == 5) || (S == 0 && T == 6))) {
            constexpr int H = S == 1 ? 0 : 1;
            dma16(ldsb + LE + w * 4096 + (2 * H) * 1024, voffW, rwq, sWq + (2 * H) * 8 * 8192);
            dma16(ldsb + LE + w * 4096 + (2 * H + 1) * 1024, voffW, rwq, sWq + (2 * H + 1) * 8 * 8192);
          }
          if constexpr ((M & 1) && ((T == 14 && S == 1) || T == 15)) {
            constexpr int K = (T == 14 ? 0 : S == 0 ? 4 : 8) + (M >> 1);
            ldx(IC<K>{});
          }
          SB();
        });
      });
    });
#pragma unroll
    for (int i = 0; i < 4; ++i)
#pragma unroll
      for (int j = 0; j < 2; ++j) { pw[(i * 2 + j) * 2] = pack8<0>(acc[i][j]); pw[(i * 2 + j) * 2 + 1] = pack8<1>(acc[i][j]); }
  }
  STAMP(3);
  unsigned wbase[3][2];
  wbase[0][0] = wl0; wbase[0][1] = wl1; wbase[1][0] = wl0 + 65536; wbase[1][1] = wl1 + 65536; wbase[2][0] = wl0 + LE; wbase[2][1] = wl1 + LE;
  PIN(wbase[0][0]); PIN(wbase[0][1]); PIN(wbase[1][0]); PIN(wbase[1][1]); PIN(wbase[2][0]); PIN(wbase[2][1]);
  const unsigned sto = ISV ? WS_V + (((head * 32 + cb * 4 + wm * 2) * 4 + 2 * (wn & 1)) * 4) * 1024 : WS_K + ((head * 64 + cb * 8 + wm * 4) * 8 + 4 * (wn & 1)) * 1024;
  asm volatile("s_waitcnt vmcnt(12)" : "+v"(xq[0]), "+v"(xq[1]), "+v"(xq[2]), "+v"(xq[3])::"memory");
  STAMP(5);
  f32x16 acc[4];
#pragma unroll
  for (int j = 0; j < 4; ++j) acc[j] = f32x16{};
  bf16x4 wl[8][2];
  auto rdq = [&](auto N_) AI {
    constexpr int N = decltype(N_)::v, ks = N >> 2, j = N & 3, ci = ks >> 3;
    constexpr int reg = ci == 0 ? 2 : (ci - 1) >> 1;
    constexpr unsigned off = (ci == 0 ? 0 : ((ci - 1) & 1) * 32768) + (j >> 1) * 16384 + (16 * (ks & 7)) * 128;
    wl[N & 7][0] = lds_tr(wbase[reg][j & 1] + off);
    wl[N & 7][1] = lds_tr(wbase[reg][j & 1] + off + 512);
  };
  static_for<5>([&](auto N) AI { rdq(N); });
  SB();
  static_for<144>([&](auto N_) AI {
    constexpr int N = decltype(N_)::v, ks = N >> 2, j = N & 3;
    if constexpr (j == 0 && ks == 4) asm volatile("s_waitcnt vmcnt(11)" : "+v"(xq[4]), "+v"(xq[5]), "+v"(xq[6]), "+v"(xq[7])::"memory");
    if constexpr (j == 0 && ks == 6) {
      asm volatile("s_waitcnt vmcnt(8)" : "+v"(xq[8]), "+v"(xq[9]), "+v"(xq[10]), "+v"(xq[11])::"memory");
      bar(); SB();
    }
    if constexpr (j == 0 && ks >= XR && ks % 3 == 0)
      asm volatile("s_waitcnt vmcnt(%3)" : "+v"(xq[ks]), "+v"(xq[ks + 1]), "+v"(xq[ks + 2]) : "i"(q_wait(ks)) : "memory");
    acc[j] = MFMA32(cat(wl[N & 7][0], wl[N & 7][1]), asbf(xq[ks]), acc[j]);
    if constexpr (N + 5 < 144) rdq(IC<N + 5>{});
    if constexpr (j == 0 && ks % 2 == 0 && ks < 32) {
      constexpr int e = ks >> 1, i = e >> 2, jj = (e >> 1) & 1, s = e & 1;
      constexpr unsigned fo = ISV ? (((i >> 1) * 4 + jj) * 4 + 2 * (i & 1) + s) : (i * 8 + 2 * jj + s);
      gst16c(pw[e], c.rws, lane * 16, sto + fo * 1024);
    }
    if constexpr (j == 2 && ks >= 1 && ks - 1 + XR < 36) ldx(IC<ks - 1 + XR>{});
    SB();
  });
#pragma unroll
  for (int j = 0; j < 4; ++j) { qo.q[2 * j] = pack8<0>(acc[j], QSCALE); qo.q[2 * j + 1] = pack8<1>(acc[j], QSCALE); }
  STAMP(6);
}

DI void phase_qkv(Ctx& c, QOut& qo, const u16* x, const u16* ckv, const u16* wq, const u16* wuk, const u16* wuv) {
  const unsigned w = c.w, lane = c.lane, ldsb = c.ldsb, b = c.b;
  const unsigned xcd = b & 7, jj = b >> 3, head = xcd * 4 + (jj >> 3), cb = jj & 7;
  const rsrc_t rxx = mk_rsrc(x, SEQ * DM * 2);
  const rsrc_t rx = mk_rsrc(ckv, SEQ * DC * 2);
  const rsrc_t rw = mk_rsrc(w < 4 ? wuk : wuv, DC * NQ * 2);
  const rsrc_t rwq = mk_rsrc(wq, DM * NQ * 2);
  u32x2 ep; u32x4 xr0, xr1;
  gld8c(ep, F_EPOCH, c.rws, WS_FLAGS);
  {
    const unsigned vo = lane < 36 ? lane * 32 : OOB;
    gld16<0>(xr0, vo, rxx, (b * 8 + w) * (DM * 2));
    gld16<16>(xr1, vo, rxx, (b * 8 + w) * (DM * 2));
  }
  const unsigned voffX = (lane >> 2) * 1024 + (((lane & 3) ^ ((lane >> 4) & 3)) << 4);
  const unsigned voffW = (lane >> 3) * 8192 + (((lane & 7) ^ (((lane >> 4) & 1) << 2)) << 4);
  const unsigned sX = (cb * 256 + 32 * w) * 1024;
  const unsigned sW = ((2 * w & 3) * 8) * 8192 + (head * 128 + ((w >> 1) & 1) * 64) * 2;
  const unsigned sWq = ((w & 3) * 32) * 8192 + (head * 128 + (w >> 2) * 64) * 2;
  const unsigned sWq4 = (512 + (w & 3) * 16) * 8192 + (head * 128 + (w >> 2) * 64) * 2;
  const unsigned rot = (2 * cb + 4 * (head & 3)) & 15;
  {
    const unsigned dX = ldsb + 2 * w * 1024, dW = ldsb + 16384 + 2 * w * 1024;
#pragma unroll
    for (int u = 0; u < 4; ++u) {
      const unsigned tr = (u + rot) & 15;
      dma16(dX + u * 32768, voffX, rx, sX + 64 * tr);
      dma16(dX + u * 32768 + 1024, voffX, rx, sX + 64 * tr + 16 * 1024);
      dma16(dW + u * 32768, voffW, rw, sW + 32 * tr * 8192);
      dma16(dW + u * 32768 + 1024, voffW, rw, sW + 32 * tr * 8192 + 8 * 8192);
    }
  }
  asm volatile("s_waitcnt vmcnt(16)" : "+v"(ep), "+v"(xr0), "+v"(xr1)::"memory");
  STAMP(1);
  {
    u64 z = (((u64)rfl(ep[1]) << 32) | rfl(ep[0])) + 0x9E3779B97F4A7C15ull;
    z = (z ^ (z >> 30)) * 0xBF58476D1CE4E5B9ull; z = (z ^ (z >> 27)) * 0x94D049BB133111EBull; z ^= z >> 31;
    c.ep0 = (unsigned)z; c.ep1 = (unsigned)(z >> 32);
  }
  {
    float v[16];
#pragma unroll
    for (int e = 0; e < 4; ++e) { v[2 * e] = bflo(xr0[e]); v[2 * e + 1] = bfhi(xr0[e]); v[8 + 2 * e] = bflo(xr1[e]); v[9 + 2 * e] = bfhi(xr1[e]); }
    float s = 0.f;
#pragma unroll
    for (int e = 0; e < 16; ++e) s += v[e];
    const float mean = wave_sum(s) * (1.f / DM);
    float qq = 0.f;
#pragma unroll
    for (int e = 0; e < 16; ++e) { v[e] -= mean; qq += v[e] * v[e]; }
    qq = lane < 36 ? qq : 0.f;
    const float rs = rsqrtf(wave_sum(qq) * (1.f / DM) + 1e-5f);
    u32x4 o0, o1;
#pragma unroll
    for (int e = 0; e < 4; ++e) { o0[e] = cvtpk(v[2 * e] * rs, v[2 * e + 1] * rs); o1[e] = cvtpk(v[8 + 2 * e] * rs, v[9 + 2 * e] * rs); }
    if (lane < 36) {
      *(LAS u32x4*)(size_t)(ldsb + LE + ((2 * lane) * 8 + w) * 16) = o0;
      *(LAS u32x4*)(size_t)(ldsb + LE + ((2 * lane + 1) * 8 + w) * 16) = o1;
    }
  }
  wait_vm<12>();
  bar();
  STAMP(2);
  {
    const unsigned mb = b >> 2, r0 = (b & 3) * 8;
    const unsigned c0 = 9 * w + (lane >> 3), c1 = 9 * w + 8;
    const u32x4 a0 = *(LAS u32x4*)(size_t)(ldsb + LE + (9 * w * 8 + lane) * 16);
    const u32x4 a1 = *(LAS u32x4*)(size_t)(ldsb + LE + (c1 * 8 + (lane & 7)) * 16);
    gst16c(a0, c.rws, ((mb * 36 + (c0 >> 1)) * 64 + (c0 & 1) * 32 + r0 + (lane & 7)) * 16, WS_XN);
    gst16c(a1, c.rws, lane < 8 ? ((mb * 36 + (c1 >> 1)) * 64 + (c1 & 1) * 32 + r0 + lane) * 16 : OOB, WS_XN);
  }
  const unsigned kh = w >> 2, rg = kh ? 7 - w : w;
  const unsigned qmb = (kh ? cb : 15 - cb) * 4 + rg;
  if ((w & 3) >= 2) qkv_body<true>(c, qo, rx, rw, rwq, voffX, voffW, sX, sW, sWq, sWq4, rot, qmb, head, cb);
  else qkv_body<false>(c, qo, rx, rw, rwq, voffX, voffW, sX, sW, sWq, sWq4, rot, qmb, head, cb);
}

constexpr float THR = 32.f;

struct AttnState {
  bf16x8 qf[8];
  f32x16 o[4];
  f32x16 S[2][2];
  f32x16 negm;
  u32x4 pk[4];
  bf16x8 fr[8];
  float mhat, l0, l1, fct, mx0, mx1, t0, t1;
  bool pend;
  unsigned kb[2], vb[2];
  unsigned voff, ldsb, w, kbase, vbase;
};

constexpr int RD0[16] = {4, 5, 6, 7, 9, 10, 12, 13, 15, -1, -1, -1, -1, -1, -1, -1};
constexpr int RD1[16] = {-1, -1, -1, 8, -1, 11, -1, 14, -1, -1, -1, -1, -1, -1, -1, -1};

struct NoHook { DI void operator()() const {} };
template <int X, bool QK, bool PV, class Hook = NoHook>
DI void attn_iter(Ctx& c, AttnState& a, const unsigned s, const unsigned nhi, const bool first, const bool diag, const int dq, Hook&& hook = Hook{}) {
  constexpr int Y = X ^ 1;
  auto kaddr = [&](int slot, int n) AI { return a.kb[slot] + ((n & 1) * 8 + (n >> 1)) * 1024; };
  auto vaddr = [&](int slot, int n) AI { return a.vb[slot] + ((n & 3) * 4 + (n >> 2)) * 1024; };
  unsigned sk = s + 2; sk = sk > 16 ? 16 : sk; sk = sk < nhi ? sk : sk - nhi;
  unsigned sv = s > 16 ? 16 : s; sv = sv < nhi ? sv : sv - nhi;
  const unsigned sok = a.kbase + sk * 32768, sov = a.vbase + sv * 32768;
  const unsigned dk = a.ldsb + X * 32768 + a.w * 4096, dv = dk + 65536;
  static_for<16>([&](auto M_) AI {
    constexpr int M = decltype(M_)::v, d = M >> 1, kb = M & 1;
    if constexpr (QK) {
      if constexpr (d == 0) a.S[X][kb] = MFMA32(a.fr[M & 7], a.qf[d], a.negm);
      else a.S[X][kb] = MFMA32(a.fr[M & 7], a.qf[d], a.S[X][kb]);
      if constexpr (RD0[M] >= 0) a.fr[RD0[M] & 7] = lds_b128(kaddr(X, RD0[M]));
      if constexpr (RD1[M] >= 0) a.fr[RD1[M] & 7] = lds_b128(kaddr(X, RD1[M]));
      if constexpr (M == 0 || M == 2 || M == 6 || M == 9) { constexpr int P = M == 0 ? 0 : M == 2 ? 1 : M == 6 ? 2 : 3; dma16c(dv + P * 1024, a.voff, c.rws, sov + P * 1024); }
    }
    if constexpr (PV) {
      if constexpr (M < 8) {
        a.S[Y][1][2 * M] = __builtin_amdgcn_exp2f(a.S[Y][1][2 * M]);
        a.S[Y][1][2 * M + 1] = __builtin_amdgcn_exp2f(a.S[Y][1][2 * M + 1]);
        a.pk[M >> 2][M & 3] = cvtpk(a.S[Y][0][2 * M], a.S[Y][0][2 * M + 1]);
        PIN(a.pk[M >> 2][M & 3]);
        if constexpr (M == 0) { a.l0 = fadd_s(a.l0, a.S[Y][0][14]); a.l1 = fadd_s(a.l1, a.S[Y][0][15]); }
      } else {
        constexpr int e = M - 8;
        a.pk[2 + (e >> 2)][e & 3] = cvtpk(a.S[Y][1][2 * e], a.S[Y][1][2 * e + 1]);
        PIN(a.pk[2 + (e >> 2)][e & 3]);
        a.l0 = fadd_s(a.l0, a.S[Y][1][2 * e]); a.l1 = fadd_s(a.l1, a.S[Y][1][2 * e + 1]);
      }
    }
    if constexpr (M == 11) { SB(); if constexpr (QK) wait_vm<8>(); else wait_vm<0>(); bar(); hook(); SB(); }
    if constexpr (PV && M >= 12) a.fr[(M - 12) & 7] = lds_b128(vaddr(Y, M - 12));
    SB();
  });
  static_for<16>([&](auto M_) AI {
    constexpr int M = decltype(M_)::v, ks = M >> 2, db = M & 3;
    if constexpr (PV) {
      a.o[db] = MFMA32(a.fr[M & 7], asbf(a.pk[ks]), a.o[db]);
      if constexpr (RD0[M] >= 0) a.fr[RD0[M] & 7] = lds_b128(vaddr(Y, RD0[M]));
      if constexpr (RD1[M] >= 0) a.fr[RD1[M] & 7] = lds_b128(vaddr(Y, RD1[M]));
    }
    if constexpr (QK) {
      if constexpr (M == 0 || M == 1 || M == 6 || M == 9) { constexpr int P = M == 0 ? 0 : M == 1 ? 1 : M == 6 ? 2 : 3; dma16c(dk + P * 1024, a.voff, c.rws, sok + P * 1024); }
      if constexpr (M == 2) {
        asm volatile("s_nop 7\n\ts_nop 3" : "+v"(a.S[X][0]), "+v"(a.S[X][1]));
        if (__builtin_expect(diag, 0)) {
          int dq2 = dq; PIN(dq2);
#pragma unroll
          for (int r = 0; r < 16; ++r) {
            const int cr = (r & 3) + 8 * (r >> 2);
            if (cr > dq2) a.S[X][0][r] = -__builtin_inff();
            if (cr + 32 > dq2) a.S[X][1][r] = -__builtin_inff();
          }
        }
      }
      if constexpr (M >= 2 && M < 6) {
        constexpr int r = 4 * (M - 2);
        if constexpr (M == 2) {
          a.mx0 = max3f(a.S[X][0][0], a.S[X][0][1], a.S[X][1][0]); a.mx1 = max3f(a.S[X][0][2], a.S[X][0][3], a.S[X][1][1]);
          a.mx0 = max3f(a.mx0, a.S[X][1][2], a.S[X][1][3]);
        } else {
          a.mx0 = max3f(a.mx0, a.S[X][0][r], a.S[X][0][r + 1]); a.mx1 = max3f(a.mx1, a.S[X][0][r + 2], a.S[X][0][r + 3]);
          a.mx0 = max3f(a.mx0, a.S[X][1][r], a.S[X][1][r + 1]); a.mx1 = max3f(a.mx1, a.S[X][1][r + 2], a.S[X][1][r + 3]);
        }
      }
      if constexpr (M == 6) {
        const float mm = max2f(a.mx0, a.mx1);
        auto pr = __builtin_amdgcn_permlane32_swap(__float_as_uint(mm), __float_as_uint(mm), false, false);
        a.mx0 = max2f(__uint_as_float(pr[0]), __uint_as_float(pr[1]));
      }
      if constexpr (M == 7) {
        if (__builtin_expect(first || __any(a.mx0 > THR), 0)) {
          const float dl = first ? fmaxf(a.mx0, -1e30f) : fmaxf(a.mx0, 0.f);
          a.mhat = first ? dl : a.mhat + dl;
          a.fct = __builtin_amdgcn_exp2f(-dl);
#pragma unroll
          for (int r = 0; r < 16; ++r) { a.S[X][0][r] -= dl; a.S[X][1][r] -= dl; a.negm[r] = -a.mhat; }
          a.pend = !first;
        }
      }
      if constexpr (M >= 8) {
        constexpr int e = M - 8;
        a.S[X][0][2 * e] = __builtin_amdgcn_exp2f(a.S[X][0][2 * e]);
        a.S[X][0][2 * e + 1] = __builtin_amdgcn_exp2f(a.S[X][0][2 * e + 1]);
        PIN(a.S[X][0][2 * e]); PIN(a.S[X][0][2 * e + 1]);
        if constexpr (e > 0) {
          a.t0 = e == 1 ? a.S[X][0][0] : fadd_s(a.t0, a.S[X][0][2 * e - 2]); a.t1 = e == 1 ? a.S[X][0][1] : fadd_s(a.t1, a.S[X][0][2 * e - 1]);
          PIN(a.t0); PIN(a.t1);
        }
      }
      if constexpr (M == 11) { SB(); wait_vm<8>(); bar(); }
      if constexpr (M >= 12) a.fr[(M - 12) & 7] = lds_b128(kaddr(Y, M - 12));
    }
    SB();
  });
}

constexpr unsigned WP = 288;
constexpr unsigned LSYNC = 161280;
constexpr unsigned C_LAND = 0, C_FREE = 64, C_GB = 128, C_CTXD = 136, LSTAT = LSYNC + 256;
constexpr int XD = 8;
DI unsigned land(unsigned ph, unsigned q) { return LSYNC + C_LAND + (ph * 5 + q) * 4; }
DI unsigned fre(unsigned ph, unsigned q) { return LSYNC + C_FREE + (ph * 5 + q) * 4; }
DI void cnt_add(Ctx& c, unsigned off) {
  asm volatile("s_mov_b64 exec, 1\n\tds_add_u32 %0, %1\n\ts_mov_b64 exec, -1" ::"v"(c.ldsb + off), "v"(1u) : "memory");
}
DI unsigned cnt_add_rtn(Ctx& c, unsigned off) {
  unsigned r;
  asm volatile("s_mov_b64 exec, 1\n\tds_add_rtn_u32 %0, %1, %2\n\ts_mov_b64 exec, -1\n\ts_waitcnt lgkmcnt(0)" : "=&v"(r) : "v"(c.ldsb + off), "v"(1u) : "memory");
  return rfl(r);
}
DI unsigned cnt_read(Ctx& c, unsigned off) { return __hip_atomic_load((LAS unsigned*)(size_t)(c.ldsb + off), __ATOMIC_RELAXED, __HIP_MEMORY_SCOPE_WORKGROUP); }
DI void prio_lo() { __builtin_amdgcn_s_setprio(0); }
DI void prio_hi(Ctx& c) { if (c.w < 4) __builtin_amdgcn_s_setprio(2); else __builtin_amdgcn_s_setprio(1); }
DI void cnt_wait(Ctx& c, unsigned off, unsigned target) {
  asm volatile("" ::: "memory");
  unsigned spins = 0;
#pragma unroll 1
  while (rfl(cnt_read(c, off)) < target) { __builtin_amdgcn_s_sleep(1); if (++spins > (1u << 20)) break; }
  asm volatile("" ::: "memory");
}
DI void flag_w(Ctx& c, unsigned flag_off) {
  wait_vm<0>();
  gst8c(u32x2{c.ep0, c.ep1}, c.rws, c.lane == 0 ? flag_off : OOB, WS_FLAGS);
}
DI void ctx_arrive(Ctx& c) {
  if (cnt_add_rtn(c, LSYNC + C_CTXD) == 7) {
    const unsigned jj = c.b >> 3;
    gst8c(u32x2{c.ep0, c.ep1}, c.rws, c.lane == 0 ? F_CTX + ((jj & 7) * 32 + (c.b & 7) * 4 + (jj >> 3)) * 8 : OOB, WS_FLAGS);
  }
}

DI void dma16m(unsigned lds_addr, unsigned voff, rsrc_t r, unsigned soff, u64 mask) {
  asm volatile("s_mov_b32 m0, %0\n\ts_mov_b64 exec, %4\n\ts_nop 3\n\tbuffer_load_dwordx4 %1, %2, %3 offen lds\n\ts_mov_b64 exec, -1" ::"s"(lds_addr), "v"(voff), "s"(r), "s"(soff), "s"(mask)
               : "memory", "m0");
}
struct WSrc { unsigned voff[9]; rsrc_t r; unsigned main, tail, pitch; unsigned nx, sg, ph; };
DI void wsrc_setup(Ctx& c, WSrc& s, rsrc_t r, unsigned pitch, unsigned colA, unsigned colB, unsigned rowMain, unsigned rowTail, unsigned ph) {
#pragma unroll
  for (int p = 0; p < 9; ++p) {
    const unsigned ci = c.lane + 64 * p, rr = ci / 18, cc = ci - rr * 18;
    s.voff[p] = rr * pitch + (cc < 16 ? colA + cc * 16 : colB + (cc - 16) * 16);
  }
  s.r = r; s.pitch = pitch; s.main = rowMain * pitch; s.tail = rowTail * pitch;
  s.ph = ph; s.nx = s.sg = c.w < 4 ? 0 : 5;
}
DI void w_group(Ctx& c, const WSrc& s, unsigned q) {
  const unsigned r0 = 128 * q + 32 * (c.w & 3);
#pragma unroll
  for (int p = 0; p < 9; ++p) dma16(c.ldsb + r0 * WP + p * 1024, s.voff[p], s.r, s.main + r0 * s.pitch);
}
DI void w_group8(Ctx& c, const WSrc& s, unsigned q) {
  const unsigned r0 = 128 * q + 16 * c.w;
#pragma unroll
  for (int p = 0; p < 4; ++p) dma16(c.ldsb + r0 * WP + p * 1024, s.voff[p], s.r, s.main + r0 * s.pitch);
  dma16m(c.ldsb + r0 * WP + 4096, s.voff[4], s.r, s.main + r0 * s.pitch, 0xffffffffull);
}
DI void w_group4(Ctx& c, const WSrc& s) {
  const unsigned r0 = 12 * (c.w & 3), sk = (c.w & 3) == 3 ? 8 * s.pitch : 0;
  dma16(c.ldsb + (512 + r0) * WP, s.voff[0], s.r, s.tail + r0 * s.pitch);
  dma16(c.ldsb + (512 + r0) * WP + 1024, s.voff[1] + (c.lane >= 8 ? sk : 0), s.r, s.tail + r0 * s.pitch);
  dma16(c.ldsb + (512 + r0) * WP + 2048, s.voff[2], s.r, s.tail + r0 * s.pitch + sk);
  dma16m(c.ldsb + (512 + r0) * WP + 3072, s.voff[3], s.r, s.tail + r0 * s.pitch + sk, 0xffffffull);
}
DI void w_issue(Ctx& c, WSrc& s) { if (s.nx == 0) w_group4(c, s); else w_group(c, s, 4 - s.nx); ++s.nx; }
DI void w_service(Ctx& c, WSrc& s) {
#pragma unroll 1
  while (s.nx < 5 && rfl(cnt_read(c, fre(s.ph - 1, 4 - s.nx))) >= 8) w_issue(c, s);
}
DI void w_signal(Ctx& c, WSrc& s) {
#pragma unroll 1
  while (s.sg < s.nx) { cnt_add(c, land(s.ph, 4 - s.sg)); ++s.sg; }
}
DI void w_need(Ctx& c, WSrc& s, unsigned q) {
#pragma unroll 1
  while (s.nx <= 4 - q) { cnt_wait(c, fre(s.ph - 1, 4 - s.nx), 8); w_issue(c, s); }
  if (s.sg < s.nx) { wait_vm<0>(); w_signal(c, s); }
  cnt_wait(c, land(s.ph, q), 4);
}
template <bool SRV> DI void poll_srv(Ctx& c, WSrc& s, unsigned flag_off, unsigned n) {
  const unsigned voff = flag_off + (c.lane < n ? c.lane : 0) * 8;
  const u64 t0 = __builtin_amdgcn_s_memrealtime();
  prio_lo();
  for (;;) {
    u32x2 f;
    gld8c(f, voff, c.rws, WS_FLAGS);
    asm volatile("s_waitcnt vmcnt(0)" : "+v"(f)::"memory");
    if constexpr (SRV) w_signal(c, s);
    if (__all(f[0] == c.ep0 && f[1] == c.ep1)) break;
    if (__builtin_amdgcn_s_memrealtime() - t0 > 5000000ull) break;
    if constexpr (SRV) w_service(c, s);
    __builtin_amdgcn_s_sleep(1);
  }
  prio_hi(c);
}
struct Tail { unsigned e[9][4]; };
DI void tail_gather(Ctx& c, Tail& t, rsrc_t r, unsigned pitch, unsigned colA, unsigned colB, unsigned rowTail) {
  const unsigned g = c.lane >> 4, l15 = c.lane & 15;
  const unsigned vo = (rowTail + 40 + 16 * (g >> 1) + 4 * (g & 1)) * pitch + l15 * 2;
#pragma unroll
  for (int a = 0; a < 9; ++a) {
    const unsigned so = a < 8 ? colA + a * 32 : colB;
#pragma unroll
    for (int e = 0; e < 4; ++e)
      asm volatile("s_nop 4\n\tbuffer_load_ushort %0, %1, %2, %3 offen" : "=v"(t.e[a][e]) : "v"(vo), "s"(r), "s"(so + e * pitch) : "memory");
  }
}
DI void tail_pack(Tail& t, u32x2 (&tl)[9]) {
#pragma unroll
  for (int a = 0; a < 9; ++a) {
    asm volatile("" : "+v"(t.e[a][0]), "+v"(t.e[a][1]), "+v"(t.e[a][2]), "+v"(t.e[a][3]));
    tl[a] = u32x2{t.e[a][0] | (t.e[a][1] << 16), t.e[a][2] | (t.e[a][3] << 16)};
    PIN(tl[a]);
  }
}
constexpr int KK(int NK, int v) { return NK == 18 ? (v < 2 ? 16 + v : 4 * (3 - (v - 2) / 4) + (v - 2) % 4) : 4 * (3 - v / 4) + v % 4; }
constexpr bool QSTART(int NK, int v) { return v < NK && v > 0 && (KK(NK, v) & 3) == 0; }
constexpr bool QEND(int NK, int v) { return (KK(NK, v) & 3) == 3 || KK(NK, v) == 17; }
DI unsigned x_voff(Ctx& c) { const unsigned g = c.lane >> 4; return (g >> 1) * 1024 + ((g & 1) * 32 + (c.lane & 15)) * 16; }
template <int NK, int V> DI void x_ld(Ctx& c, u32x4 (&x)[2], unsigned voff, unsigned so, unsigned so2) {
  constexpr int k = KK(NK, V);
  const unsigned s = k < 16 ? so + (k >> 1) * 4096 : so2;
  gld16c<(k & 1) * 2048>(x[0], voff, c.rws, s); gld16c<(k & 1) * 2048 + 256>(x[1], voff, c.rws, s);
}
template <int NK> DI void x_pro(Ctx& c, u32x4 (&x)[XD][2], unsigned voff, unsigned so, unsigned so2) {
  static_for<XD>([&](auto V_) AI { x_ld<NK, decltype(V_)::v>(c, x[decltype(V_)::v], voff, so, so2); });
}
DI void x_ld4_masked(u32x4& a, u32x4& b, u32x4& d, u32x4& e, unsigned voff, rsrc_t r, unsigned so, unsigned mask) {
  asm volatile("s_mov_b32 exec_lo, %7\n\ts_mov_b32 exec_hi, %7\n\ts_nop 4\n\tbuffer_load_dwordx4 %0, %4, %5, %6 offen sc1\n\tbuffer_load_dwordx4 %1, %4, %5, %6 offen offset:256 sc1\n\t"
               "buffer_load_dwordx4 %2, %4, %5, %6 offen offset:2048 sc1\n\tbuffer_load_dwordx4 %3, %4, %5, %6 offen offset:2304 sc1\n\ts_mov_b64 exec, -1"
               : "+v"(a), "+v"(b), "+v"(d), "+v"(e) : "v"(voff), "s"(r), "s"(so), "s"(mask) : "memory");
}
DI void x_pro_masked16(Ctx& c, u32x4 (&x)[XD][2], unsigned so, unsigned mask) {
  const unsigned voff = x_voff(c);
#pragma unroll
  for (int p = 0; p < XD / 2; ++p) x_ld4_masked(x[2 * p][0], x[2 * p][1], x[2 * p + 1][0], x[2 * p + 1][1], voff, c.rws, so + (KK(16, 2 * p) >> 1) * 4096, mask);
}
template <int L> DI void wlane(unsigned& v, unsigned s) { asm volatile("v_writelane_b32 %0, %1, %2" : "+v"(v) : "s"(s), "i"(L)); }
template <int NK, int KORD, int PH, int GST>
DI void gemm_g(Ctx& c, f32x4 (&acc)[2][9], u32x4 (&x)[XD][2], u32x2 (&tl)[9], WSrc& ws, unsigned so, unsigned so2) {
  constexpr unsigned RT = KORD == 0 ? 16 : 8;
  constexpr int LA = 6;
  const unsigned g = c.lane >> 4, q = (c.lane & 15) >> 2, pp = c.lane & 3;
  const unsigned wrow1 = 16 * (g >> 1) + 4 * (g & 1) + q, wrow = KORD == 0 ? 4 * g + q : wrow1;
  unsigned wb[3], wt[2];
#pragma unroll
  for (int i = 0; i < 3; ++i) { wb[i] = c.ldsb + wrow * WP + pp * 8 + i * 7 * 32 * WP; PIN(wb[i]); }
  if constexpr (NK == 18) {
    wt[0] = c.ldsb + (512 + wrow1) * WP + pp * 8; wt[1] = c.ldsb + (544 + wrow1 - 8 * (g >> 1)) * WP + pp * 8;
    PIN(wt[0]); PIN(wt[1]);
  }
  unsigned voff = x_voff(c); PIN(voff);
  bf16x4 wl[8][2];
  auto rdw = [&](auto N_, auto H_) AI {
    constexpr int N = decltype(N_)::v, H = decltype(H_)::v, v = N / 9, a = N % 9, k = KK(NK, v);
    if constexpr (k < 16) wl[N & 7][H] = lds_tr(wb[k / 7] + ((k % 7) * 32 + H * RT) * WP + a * 32);
    else if constexpr (k == 16) wl[N & 7][H] = lds_tr(wt[0] + H * 8 * WP + a * 32);
    else if constexpr (H == 0) wl[N & 7][H] = lds_tr(wt[1] + a * 32);
    if constexpr (PH < 2 && H == 1 && a == 8 && QEND(NK, v)) { SB(); cnt_add(c, fre(PH, k >> 2)); }
  };
  if constexpr (PH > 0) { if (__builtin_expect(rfl(cnt_read(c, land(PH, 4))) < 4, 0)) w_need(c, ws, 4); }
  static_for<LA>([&](auto N) AI { rdw(N, IC<0>{}); rdw(N, IC<1>{}); });
  unsigned lc = 0;
  const unsigned nx0 = ws.nx;
  SB();
  static_for<NK * 9>([&](auto N_) AI {
    constexpr int N = decltype(N_)::v, v = N / 9, a = N % 9, k = KK(NK, v);
    if constexpr (a == 0) {
      constexpr int ahead = (XD - 1) < (NK - 1 - v) ? (XD - 1) : (NK - 1 - v);
      asm volatile("s_waitcnt vmcnt(%2)" : "+v"(x[v % XD][0]), "+v"(x[v % XD][1]) : "i"(2 * ahead) : "memory");
      if constexpr (v == 0 || v == 8) STAMPG(GST + (v >> 3));
      if constexpr (PH == 0 && v == 6) { if (c.w < 4) { wait_vm<12>(); ctx_arrive(c); } }
      if constexpr (PH > 0 && v == XD) {
#pragma unroll 1
        while (ws.sg < nx0) { cnt_add(c, land(PH, 4 - ws.sg)); ++ws.sg; }
      }
      constexpr bool chk2 = QSTART(NK, v + 2) && (PH > 0 || KK(NK, v + 2) < 8), chk1 = QSTART(NK, v + 1) && (PH > 0 || KK(NK, v + 1) < 8);
      if constexpr (chk1) {
        if (__builtin_expect(rfl(lc) < 4, 0)) { if constexpr (PH > 0) w_need(c, ws, KK(NK, v + 1) >> 2); else cnt_wait(c, land(0, KK(NK, v + 1) >> 2), 4); }
        if constexpr (PH > 0) { if (__builtin_expect(ws.nx < 5, 0)) w_service(c, ws); }
      }
      if constexpr (chk2) lc = cnt_read(c, land(PH, KK(NK, v + 2) >> 2));
      SB();
    }
    const bf16x8 wf = cat(wl[N & 7][0], k < 17 ? wl[N & 7][1] : __builtin_bit_cast(bf16x4, tl[a]));
    acc[0][a] = MFMA16(wf, asbf(x[v % XD][0]), acc[0][a]);
    SB();
    if constexpr (N + LA < NK * 9) rdw(IC<N + LA>{}, IC<0>{});
    SB();
    acc[1][a] = MFMA16(wf, asbf(x[v % XD][1]), acc[1][a]);
    SB();
    if constexpr (N + LA < NK * 9) rdw(IC<N + LA>{}, IC<1>{});
    if constexpr (a == 8 && v + XD < NK) x_ld<NK, v + XD>(c, x[v % XD], voff, so, so2);
    SB();
  });
}
DI void pack4(u32x4 (&p)[4], const f32x4 (&r)[9]) {
#pragma unroll
  for (int i = 0; i < 4; ++i)
    p[i] = u32x4{cvtpk(r[2 * i][0], r[2 * i][1]), cvtpk(r[2 * i][2], r[2 * i][3]), cvtpk(r[2 * i + 1][0], r[2 * i + 1][1]), cvtpk(r[2 * i + 1][2], r[2 * i + 1][3])};
}
DI u32x4 pack_single(const f32x4& r0, const f32x4& r1) {
  const auto a = __builtin_amdgcn_permlane32_swap(cvtpk(r0[0], r0[1]), cvtpk(r1[0], r1[1]), false, false);
  const auto b = __builtin_amdgcn_permlane32_swap(cvtpk(r0[2], r0[3]), cvtpk(r1[2], r1[3]), false, false);
  return u32x4{a[0], b[0], a[1], b[1]};
}
DI void part_store(Ctx& c, const f32x4 (&acc)[2][9], unsigned so) {
  const unsigned g = c.lane >> 4, l15 = c.lane & 15, base = (l15 >> 2) * 1152 + (l15 & 3) * 16;
#pragma unroll
  for (int u = 0; u < 2; ++u) {
    u32x4 p[4]; pack4(p, acc[u]);
#pragma unroll
    for (int i = 0; i < 4; ++i) gst16c(p[i], c.rws, base + i * 256 + g * 64, so + u * 4608);
  }
  gst16c(pack_single(acc[0][8], acc[1][8]), c.rws, (g >> 1) * 4608 + base + 1024 + (g & 1) * 64, so);
}
struct Red { u32x4 p[8]; u32x4 s[8]; };
DI void red_load(Ctx& c, Red& d, unsigned so) {
  const unsigned v0 = c.lane * 16, v1 = c.lane < 8 ? 1024 + c.lane * 16 : OOB;
#pragma unroll
  for (int s = 0; s < 8; ++s) { gld16c<0>(d.p[s], v0, c.rws, so + s * 36864); gld16c<0>(d.s[s], v1, c.rws, so + s * 36864); }
}
#define RED_PIN(d) asm volatile("s_waitcnt vmcnt(0)" : "+v"(d.p[0]), "+v"(d.p[1]), "+v"(d.p[2]), "+v"(d.p[3]), "+v"(d.p[4]), "+v"(d.p[5]), "+v"(d.p[6]), "+v"(d.p[7]), \
    "+v"(d.s[0]), "+v"(d.s[1]), "+v"(d.s[2]), "+v"(d.s[3]), "+v"(d.s[4]), "+v"(d.s[5]), "+v"(d.s[6]), "+v"(d.s[7])::"memory")
DI void gld8(u32x2& d, unsigned voff, rsrc_t r, unsigned soff) { asm volatile("s_nop 4\n\tbuffer_load_dwordx2 %0, %1, %2, %3 offen" : "=v"(d) : "v"(voff), "s"(r), "s"(soff) : "memory"); }
DI void job_offs(Ctx& c, unsigned row, unsigned& o0, unsigned& o2) {
  const unsigned pr = c.lane >> 4, g = (c.lane >> 2) & 3, nt = c.w & 3;
  o0 = row * (DM * 2) + (nt * 128 + pr * 32 + 4 * g) * 2;
  o2 = c.lane < 8 ? row * (DM * 2) + (512 + nt * 16 + 4 * g) * 2 : OOB;
}
DI void row_load(Ctx& c, u32x2 (&r)[4], rsrc_t rs, unsigned row) {
  unsigned o0, o2; job_offs(c, row, o0, o2);
  gld8(r[0], o0, rs, 0); gld8(r[1], o0, rs, 32); gld8(r[2], o2, rs, 0); gld8(r[3], o2, rs, 16);
}
DI void red_sum(const Red& d, float (&a)[16]) {
#pragma unroll
  for (int e = 0; e < 16; ++e) a[e] = 0.f;
#pragma unroll
  for (int s = 0; s < 8; ++s) {
#pragma unroll
    for (int e = 0; e < 4; ++e) { a[2 * e] += bflo(d.p[s][e]); a[2 * e + 1] += bfhi(d.p[s][e]); a[8 + 2 * e] += bflo(d.s[s][e]); a[9 + 2 * e] += bfhi(d.s[s][e]); }
  }
}
DI float sum_tok4(float x) {
  x += __uint_as_float(__builtin_amdgcn_update_dpp(0, __float_as_uint(x), 0x124, 0xf, 0xf, false));
  x += __uint_as_float(__builtin_amdgcn_update_dpp(0, __float_as_uint(x), 0x128, 0xf, 0xf, false));
  { auto r = __builtin_amdgcn_permlane16_swap(__float_as_uint(x), __float_as_uint(x), false, false); x = __uint_as_float(r[0]) + __uint_as_float(r[1]); }
  return xhalf_sum(x);
}
DI void ln2_job(Ctx& c, const Red& d, const u32x2 (&xr)[4], unsigned (&hw)[8], unsigned row0) {
  const unsigned lane = c.lane, tok = lane & 3, g = (lane >> 2) & 3, pr = lane >> 4, nt = c.w & 3;
  const unsigned stat = c.ldsb + LSTAT + (c.w >> 2) * 128;
  const bool sing = lane < 8;
  float a[16], v[16];
  red_sum(d, a);
#pragma unroll
  for (int e = 0; e < 8; ++e) {
    const unsigned xx = xr[e >> 1][e & 1];
    hw[e] = cvtpk(bflo(xx) + a[2 * e], bfhi(xx) + a[2 * e + 1]); v[2 * e] = bflo(hw[e]); v[2 * e + 1] = bfhi(hw[e]);
  }
  float s1 = 0.f;
#pragma unroll
  for (int e = 0; e < 16; ++e) s1 += v[e];
  const float mw = sum_tok4(s1) * (1.f / 144);
  float s2 = 0.f, s3 = 0.f;
#pragma unroll
  for (int e = 0; e < 8; ++e) { const float t = v[e] - mw, t2 = v[8 + e] - mw; s2 += t * t; s3 += t2 * t2; }
  s2 = sum_tok4(sing ? s2 + s3 : s2);
  if (lane < 4) *(LAS f32x2*)(size_t)(stat + (nt * 4 + lane) * 8) = f32x2{mw, s2};
  SB();
  cnt_add(c, LSYNC + C_GB + (c.w >> 2) * 4);
  cnt_wait(c, LSYNC + C_GB + (c.w >> 2) * 4, 4);
  f32x2 st[4];
#pragma unroll
  for (int i = 0; i < 4; ++i) st[i] = *(LAS f32x2*)(size_t)(stat + (i * 4 + tok) * 8);
  const float mean = (st[0][0] + st[1][0] + st[2][0] + st[3][0]) * 0.25f;
  float m2 = st[0][1] + st[1][1] + st[2][1] + st[3][1];
#pragma unroll
  for (int i = 0; i < 4; ++i) { const float t = st[i][0] - mean; m2 += 144.f * t * t; }
  const float rs = rsqrtf(m2 * (1.f / DM) + 1e-5f);
#pragma unroll
  for (int e = 0; e < 16; ++e) v[e] = (v[e] - mean) * rs;
  const unsigned row = row0 + tok, mb = row >> 5, r32 = row & 31;
  const unsigned o0 = ((mb * 36 + 2 * (nt * 4 + pr) + (g >> 1)) * 64 + (g & 1) * 32 + r32) * 16;
  const unsigned o1 = sing ? ((mb * 36 + 32 + nt) * 64 + g * 32 + r32) * 16 : OOB;
  gst16c(u32x4{cvtpk(v[0], v[1]), cvtpk(v[2], v[3]), cvtpk(v[4], v[5]), cvtpk(v[6], v[7])}, c.rws, o0, WS_HN);
  gst16c(u32x4{cvtpk(v[8], v[9]), cvtpk(v[10], v[11]), cvtpk(v[12], v[13]), cvtpk(v[14], v[15])}, c.rws, o1, WS_HN);
}

DI void phase_attn(Ctx& c, QOut& qo, WSrc& wso, u32x4 (&xr)[XD][2], const u16* wo, const u16* w1, const u16* w2) {
  AttnState a;
  const unsigned ldsb = c.ldsb, lane = c.lane, w = c.w, b = c.b;
  const unsigned xcd = b & 7, jj = b >> 3, head = xcd * 4 + (jj >> 3), pi = jj & 7;
  const unsigned l31 = lane & 31, h = lane >> 5;
  const unsigned kh = w >> 2, rg = kh ? 7 - w : w, partner = 7 - w;
  a.voff = lane * 16; a.ldsb = ldsb; a.w = w;
  a.kbase = WS_K + head * (SEQ * HD * 2) + w * 4096; a.vbase = WS_V + head * (SEQ * HD * 2) + w * 4096;
  a.kb[0] = ldsb + kh * 16384 + lane * 16; a.kb[1] = a.kb[0] + 32768; a.vb[0] = a.kb[0] + 65536; a.vb[1] = a.kb[0] + 98304;
  PIN(a.kb[0]); PIN(a.kb[1]); PIN(a.vb[0]); PIN(a.vb[1]);
  const unsigned nhi = 16 - pi;
  publish(c, F_KV + (head * 8 + pi) * 8);
  STAMP(7);
#pragma unroll
  for (int d = 0; d < 8; ++d) *(LAS u32x4*)(size_t)(ldsb + (kh ? LE : 98304) + rg * 8192 + d * 1024 + lane * 16) = qo.q[d];
  wait_flags(c, F_KV + head * 64, 8, 1);
  STAMP(8);
#pragma unroll
  for (int p = 0; p < 4; ++p) dma16c(ldsb + w * 4096 + p * 1024, a.voff, c.rws, a.kbase + p * 1024);
#pragma unroll
  for (int p = 0; p < 4; ++p) dma16c(ldsb + 32768 + w * 4096 + p * 1024, a.voff, c.rws, a.kbase + 32768 + p * 1024);
#pragma unroll
  for (int d = 0; d < 8; ++d) a.qf[d] = lds_b128(ldsb + 98304 + rg * 8192 + d * 1024 + lane * 16);
#pragma unroll
  for (int d = 0; d < 4; ++d) a.o[d] = f32x16{};
  a.negm = f32x16{}; a.mhat = 0.f; a.l0 = a.l1 = 0.f; a.fct = 1.f; a.pend = false;
  wait_vm<4>();
  bar();
  STAMP(9);
#pragma unroll
  for (int n = 0; n < 4; ++n) a.fr[n] = lds_b128(a.kb[0] + ((n & 1) * 8 + (n >> 1)) * 1024);
  const int dq = (int)(rg * 32 + l31) - (int)(kh * 64) - 4 * (int)h;
  float m_old = 0.f;
  unsigned dummy = 0;

  auto merge = [&](auto FIN_, unsigned sc0, unsigned sc1, unsigned qblk, float mo, float lo) AI {
    bar();
    if constexpr (decltype(FIN_)::v) w_group8(c, wso, 2);
    const float lt = xhalf_sum(lo);
    *(LAS f32x2*)(size_t)(sc0 + w * 512 + lane * 8) = f32x2{mo, lt};
    bar();
    const f32x2 ps = *(LAS f32x2*)(size_t)(sc0 + partner * 512 + lane * 8);
    const float mm = fmaxf(mo, ps[0]);
    const float fo = __builtin_amdgcn_exp2f(mo - mm), fp = __builtin_amdgcn_exp2f(ps[0] - mm);
    const float f = fo / (lt * fo + ps[1] * fp);
    bar();
    const unsigned mine = (kh ? sc1 : sc0) + (w & 3) * 8192 + lane * 16, theirs = (kh ? sc0 : sc1) + (partner & 3) * 8192 + lane * 16;
#pragma unroll
    for (int r = 0; r < 2; ++r) {
      const f32x16 x = kh ? a.o[r] : a.o[2 + r];
#pragma unroll
      for (int e = 0; e < 4; ++e)
        *(LAS f32x4*)(size_t)(mine + (r * 4 + e) * 1024) = f32x4{x[4 * e] * f, x[4 * e + 1] * f, x[4 * e + 2] * f, x[4 * e + 3] * f};
    }
    bar();
    const unsigned mb = qblk * 4 + rg;
#pragma unroll
    for (int r = 0; r < 2; ++r) {
      f32x16 x = kh ? a.o[2 + r] : a.o[r];
#pragma unroll
      for (int e = 0; e < 4; ++e) {
        const f32x4 y = *(LAS f32x4*)(size_t)(theirs + (r * 4 + e) * 1024);
#pragma unroll
        for (int k = 0; k < 4; ++k) x[4 * e + k] = x[4 * e + k] * f + y[k];
      }
      const unsigned so = WS_CTX + (mb * 256 + head * 8 + 2 * (kh * 2 + r)) * 1024;
      gst16c(pack8<0>(x), c.rws, lane * 16, so);
      gst16c(pack8<1>(x), c.rws, lane * 16, so + 1024);
    }
#pragma unroll
    for (int d = 0; d < 4; ++d) a.o[d] = f32x16{};
  };
  auto post = [&](auto X_, unsigned s) AI {
    constexpr int X = decltype(X_)::v;
    if (__builtin_expect(s == nhi, 0)) {
      merge(IC<0>{}, ldsb + 65536 + (X ^ 1) * 32768, ldsb + LE, 15 - pi, m_old, a.l0 + a.l1);
      a.l0 = a.l1 = 0.f;
      publish(c, F_CTX + ((15 - pi) * 32 + head) * 8);
    }
    if (__builtin_expect(a.pend, 0)) {
#pragma unroll
      for (int d = 0; d < 4; ++d)
#pragma unroll
        for (int r = 0; r < 16; ++r) a.o[d][r] *= a.fct;
      a.l0 *= a.fct; a.l1 *= a.fct; a.pend = false;
    }
    a.l0 = fadd_s(a.l0, a.t0); a.l1 = fadd_s(a.l1, a.t1);
    if (__builtin_expect(s + 1 == nhi, 0)) {
#pragma unroll
      for (int d = 0; d < 8; ++d) a.qf[d] = lds_b128(ldsb + LE + rg * 8192 + d * 1024 + lane * 16);
      a.negm = f32x16{};
      m_old = a.mhat;
    }
    if constexpr (X == 1) {
      const unsigned ph = s >= 11 ? s - 10 : s;
      if (__builtin_expect(ph <= 5, 0)) {
        const unsigned li = w * 21 + lane;
        unsigned off;
        if (ph == 3) off = (jj * 18 + li / 9) * (FF * 2) + (li % 9 < 8 ? xcd * 1024 + (li % 9) * 128 : 8192 + xcd * 128);
        else off = li < 144 ? xcd * 589824 + (jj * 144 + li) * 128 : ph == 5 ? (4096 + 64 * xcd) * (DM * 2) + (jj * 18 + li - 144) * 128 : OOB;
        const size_t p1 = (size_t)wo, p3 = (size_t)w1, p5 = (size_t)w2;
        const rsrc_t rp = mk_rsrc((const void*)(ph == 1 ? p1 + 0 : ph == 3 ? p3 + 0 : p5 + 0), ph == 1 ? NQ * DM * 2 : FF * DM * 2);
        asm volatile("s_nop 4\n\tbuffer_load_dword %0, %1, %2, 0 offen" : "+v"(dummy) : "v"((lane < 21 && li < 162) ? off : OOB), "s"(rp) : "memory");
      }
    }
    SB();
  };
  attn_iter<0, true, false>(c, a, 0, nhi, true, false, dq); post(IC<0>{}, 0);
  for (unsigned s = 1; s < 17; s += 2) {
    attn_iter<1, true, true>(c, a, s, nhi, s == nhi, s + 1 == nhi, dq); post(IC<1>{}, s);
    attn_iter<0, true, true>(c, a, s + 1, nhi, s + 1 == nhi, s + 2 == nhi || s + 1 == 16, dq); post(IC<0>{}, s + 1);
  }
  const unsigned mtH = 8 + (jj >> 2);
  u32x2 fl;
  flag_load(c, fl, F_CTX + (mtH * 32 + xcd * 4) * 8, 4);
  if (w == 0) *(LAS unsigned*)(size_t)(ldsb + LSYNC + lane * 4) = lane * 4 == fre(0, 4) - LSYNC ? 8 : 0;
  attn_iter<1, false, true>(c, a, 17, nhi, false, false, dq, [&]() AI {
    asm volatile("" ::"v"(dummy));
    wsrc_setup(c, wso, mk_rsrc(wo, NQ * DM * 2), DM * 2, (jj & 3) * 256, 1024 + (jj & 3) * 32, 512 * xcd, 0, 0);
    w_group8(c, wso, 3);
    flag_check(c, fl, F_CTX + (mtH * 32 + xcd * 4) * 8, 4);
    x_pro<16>(c, xr, kh == 0 ? x_voff(c) : OOB, WS_CTX + ((mtH * 4 + (w & 3)) * 256 + xcd * 32) * 1024, 0);
  });
  STAMP(10);
  merge(IC<1>{}, ldsb, ldsb + 32768, pi, a.mhat, a.l0 + a.l1);
  wait_vm<4>();
  bar();
  STAMP(11);
}

struct TId { unsigned sp, nt, mt, nt32, mb, own, wq; bool isH; };
DI TId tids(Ctx& c) {
  fresh(c);
  TId t;
  const unsigned xcd = c.b & 7, jj = c.b >> 3;
  t.isH = c.w < 4; t.wq = c.w & 3; t.sp = xcd; t.nt = jj & 3; t.mt = (t.isH ? 8 : 0) + (jj >> 2);
  t.nt32 = xcd * 4 + t.nt; t.mb = t.mt * 4 + t.wq; t.own = t.nt * 8 + t.sp;
  return t;
}
DI void acc_zero(f32x4 (&acc)[2][9]) {
#pragma unroll
  for (int u = 0; u < 2; ++u)
#pragma unroll
    for (int a = 0; a < 9; ++a) acc[u][a] = f32x4{};
}
DI void phase_tail(Ctx& c, WSrc& wso, u32x4 (&xr)[XD][2], const u16* x, const u16* w1, const u16* b1, const u16* w2, const u16* b2, u16* out) {
  f32x4 acc[2][9]; u32x2 tl[9]; unsigned hw[8];
  {
    const TId t = tids(c);
    const unsigned xo = WS_CTX + (t.mb * 256 + t.sp * 32) * 1024;
    if (!t.isH) {
      w_group(c, wso, 1); w_group(c, wso, 0);
      wait_vm<18>(); ctx_arrive(c);
      wait_vm<9>(); cnt_add(c, land(0, 1));
      wait_vm<0>(); cnt_add(c, land(0, 0));
      poll_srv<false>(c, wso, F_CTX + (t.mt * 32 + t.sp * 4) * 8, 4);
    }
    prio_hi(c);
    STAMPG(12);
    x_pro_masked16(c, xr, xo, 0u - (c.w >> 2));
    acc_zero(acc);
    gemm_g<16, 1, 0, 13>(c, acc, xr, tl, wso, xo, 0);
    STAMPG(15);
  }
  {
    const TId t = tids(c);
    part_store(c, acc, WS_P1 + (((t.mt * 4 + t.nt) * 8 + t.sp) * 8 + 2 * t.wq) * 4608);
    STAMPG(16);
    flag_w(c, F_P1 + (t.mb * 32 + t.own) * 8);
    STAMPG(17);
    const rsrc_t rw1 = mk_rsrc(w1, DM * FF * 2);
    u32x2 bias[9], xres[4]; Red rd; WSrc ws; Tail tg;
    wsrc_setup(c, ws, rw1, FF * 2, t.nt32 * 256, 8192 + t.nt32 * 32, 0, 512, 1);
    w_service(c, ws);
    {
      const rsrc_t rb = mk_rsrc(b1, FF * 2);
#pragma unroll
      for (int a = 0; a < 9; ++a) gld8(bias[a], 8 * (c.lane >> 4), rb, (a < 8 ? t.nt32 * 128 + a * 16 : 4096 + t.nt32 * 16) * 2);
    }
    row_load(c, xres, mk_rsrc(x, SEQ * DM * 2), t.mt * 128 + t.own * 4 + (c.lane & 3));
    tail_gather(c, tg, rw1, FF * 2, t.nt32 * 256, 8192 + t.nt32 * 32, 512);
    STAMPG(18);
    poll_srv<true>(c, ws, F_P1 + ((t.mt * 4 + (t.own >> 3)) * 32 + t.wq * 8) * 8, 8);
    red_load(c, rd, WS_P1 + (t.mt * 256 + t.wq * 64 + (t.own >> 2)) * 4608 + (t.own & 3) * 1152);
    STAMPG(19);
    tail_pack(tg, tl);
    PIN(xres[0]); PIN(xres[1]); PIN(xres[2]); PIN(xres[3]);
    RED_PIN(rd);
    STAMPG(20);
    ln2_job(c, rd, xres, hw, t.mt * 128 + t.own * 4);
    STAMPG(21);
    flag_w(c, F_HN + ((t.mt * 32 + t.own) * 4 + t.wq) * 8);
    STAMPG(22);
    w_service(c, ws);
    poll_srv<true>(c, ws, F_HN + t.mb * 256, 32);
    STAMPG(23);
    const unsigned xh = WS_HN + t.mb * 36 * 1024;
    x_pro<18>(c, xr, x_voff(c), xh, xh + 32768);
    w_service(c, ws);
#pragma unroll
    for (int a = 0; a < 9; ++a) {
      PIN(bias[a]);
      acc[0][a] = acc[1][a] = f32x4{bflo(bias[a][0]), bfhi(bias[a][0]), bflo(bias[a][1]), bfhi(bias[a][1])};
    }
    gemm_g<18, 0, 1, 24>(c, acc, xr, tl, ws, xh, xh + 32768);
    STAMPG(26);
  }
  {
    const TId t = tids(c);
    const unsigned g = c.lane >> 4, vo = ((g >> 1) * 64 + (g & 1) * 32 + (c.lane & 15)) * 16;
#pragma unroll
    for (int u = 0; u < 2; ++u) {
#pragma unroll
      for (int a = 0; a < 9; ++a) {
        const float r0 = fmaxf(acc[u][a][0], 0.f), r1 = fmaxf(acc[u][a][1], 0.f), r2 = fmaxf(acc[u][a][2], 0.f), r3 = fmaxf(acc[u][a][3], 0.f);
        acc[u][a] = f32x4{r0 * r0, r1 * r1, r2 * r2, r3 * r3};
      }
      u32x4 p[4]; pack4(p, acc[u]);
#pragma unroll
      for (int i = 0; i < 4; ++i) gst16c(p[i], c.rws, vo + u * 256, WS_H1 + (t.mb * 288 + 2 * (t.nt32 * 4 + i)) * 1024);
    }
    gst16c(pack_single(acc[0][8], acc[1][8]), c.rws, ((g & 1) * 32 + (g >> 1) * 16 + (c.lane & 15)) * 16, WS_H1 + (t.mb * 288 + 256 + t.nt32) * 1024);
    STAMPG(27);
    flag_w(c, F_H1 + (t.mb * 32 + t.nt32) * 8);
    STAMPG(28);
    const rsrc_t rw2 = mk_rsrc(w2, FF * DM * 2);
    WSrc ws; Tail tg;
    wsrc_setup(c, ws, rw2, DM * 2, t.nt * 256, 1024 + t.nt * 32, 512 * t.sp, 4096 + 64 * t.sp, 2);
    w_service(c, ws);
    tail_gather(c, tg, rw2, DM * 2, t.nt * 256, 1024 + t.nt * 32, 4096 + 64 * t.sp);
    STAMPG(29);
    poll_srv<true>(c, ws, F_H1 + (t.mb * 32 + t.sp * 4) * 8, 4);
    STAMPG(30);
    const unsigned x1 = WS_H1 + (t.mb * 288 + t.sp * 32) * 1024, x1t = WS_H1 + (t.mb * 288 + 256 + t.sp * 4) * 1024;
    x_pro<18>(c, xr, x_voff(c), x1, x1t);
    w_service(c, ws);
    tail_pack(tg, tl);
    acc_zero(acc);
    gemm_g<18, 0, 2, 31>(c, acc, xr, tl, ws, x1, x1t);
    STAMPG(33);
  }
  {
    const TId t = tids(c);
    part_store(c, acc, WS_P2 + (((t.mt * 4 + t.nt) * 8 + t.sp) * 8 + 2 * t.wq) * 4608);
    STAMPG(34);
    flag_w(c, F_P2 + (t.mb * 32 + t.own) * 8);
    STAMPG(35);
    u32x2 bb[4]; Red rd;
    row_load(c, bb, mk_rsrc(b2, DM * 2), 0);
    poll_srv<false>(c, wso, F_P2 + ((t.mt * 4 + (t.own >> 3)) * 32 + t.wq * 8) * 8, 8);
    STAMPG(36);
    red_load(c, rd, WS_P2 + (t.mt * 256 + t.wq * 64 + (t.own >> 2)) * 4608 + (t.own & 3) * 1152);
    PIN(bb[0]); PIN(bb[1]); PIN(bb[2]); PIN(bb[3]);
    RED_PIN(rd);
    const rsrc_t ro = mk_rsrc(out, SEQ * DM * 2);
    float a[16]; unsigned ow[8];
    red_sum(rd, a);
#pragma unroll
    for (int e = 0; e < 8; ++e) {
      const unsigned bv = bb[e >> 1][e & 1];
      ow[e] = cvtpk(a[2 * e] + bflo(bv) + bflo(hw[e]), a[2 * e + 1] + bfhi(bv) + bfhi(hw[e]));
    }
    unsigned o0, o2; job_offs(c, t.mt * 128 + t.own * 4 + (c.lane & 3), o0, o2);
    __builtin_amdgcn_raw_buffer_store_b64(u32x2{ow[0], ow[1]}, ro, o0, 0, 0);
    __builtin_amdgcn_raw_buffer_store_b64(u32x2{ow[2], ow[3]}, ro, o0, 32, 0);
    __builtin_amdgcn_raw_buffer_store_b64(u32x2{ow[4], ow[5]}, ro, o2, 0, 0);
    __builtin_amdgcn_raw_buffer_store_b64(u32x2{ow[6], ow[7]}, ro, o2, 16, 0);
    STAMPG(37);
    if (c.b == 0 && c.w == 0) gst8c(u32x2{c.ep0, c.ep1}, c.rws, c.lane == 0 ? F_EPOCH : OOB, WS_FLAGS);
  }
}

__global__ __launch_bounds__(512, 2) void fused_block(const u16* __restrict__ x, const u16* __restrict__ ckv, const u16* __restrict__ wq,
                                                      const u16* __restrict__ wuk, const u16* __restrict__ wuv, const u16* __restrict__ wo,
                                                      const u16* __restrict__ w1, const u16* __restrict__ b1, const u16* __restrict__ w2,
                                                      const u16* __restrict__ b2, u16* __restrict__ out, char* __restrict__ ws) {
  extern __shared__ __attribute__((aligned(16))) char lds[];
  Ctx c;
  c.ldsb = (unsigned)(size_t)lds; c.lane = threadIdx.x & 63; c.w = rfl(threadIdx.x >> 6); c.b = blockIdx.x;
  c.rws = mk_rsrc(ws, WS_END);
  STAMP(0);
  QOut qo;
  WSrc wso; u32x4 xr[XD][2];
  phase_qkv(c, qo, x, ckv, wq, wuk, wuv);
  fresh(c);
  phase_attn(c, qo, wso, xr, wo, w1, w2);
  fresh(c);
  phase_tail(c, wso, xr, x, w1, b1, w2, b2, out);
}

extern "C" void kernel_launch(void* const* d_in, const int* in_sizes, int n_in, void* d_out, int out_size, void* d_ws, size_t ws_size,
                              hipStream_t stream) {
  hipFuncSetAttribute((const void*)fused_block, hipFuncAttributeMaxDynamicSharedMemorySize, 163840);
  fused_block<<<256, 512, 163840, stream>>>((const u16*)d_in[0], (const u16*)d_in[1], (const u16*)d_in[2], (const u16*)d_in[3], (const u16*)d_in[4],
                                            (const u16*)d_in[5], (const u16*)d_in[7], (const u16*)d_in[8], (const u16*)d_in[9], (const u16*)d_in[10],
                                            (u16*)d_out, (char*)d_ws);
}
```

```cpp
#include <hip/hip_runtime.h>
#include <stdint.h>
#include <utility>

typedef __bf16 bf16x8 __attribute__((ext_vector_type(8)));
typedef __bf16 bf16x4 __attribute__((ext_vector_type(4)));
typedef __bf16 bf16x2 __attribute__((ext_vector_type(2)));
typedef float f32x16 __attribute__((ext_vector_type(16)));
typedef float f32x4 __attribute__((ext_vector_type(4)));
typedef float f32x2 __attribute__((ext_vector_type(2)));
typedef unsigned u32x4 __attribute__((ext_vector_type(4)));
typedef unsigned u32x2 __attribute__((ext_vector_type(2)));
typedef unsigned short u16;
typedef unsigned long long u64;
#define LAS __attribute__((address_space(3)))
#define DI __device__ __forceinline__
#define AI __attribute__((always_inline))
typedef __amdgpu_buffer_rsrc_t rsrc_t;

constexpr int SEQ = 2048, DM = 576, NH = 32, HD = 128, DC = 512, FF = 4608, NQ = 4096;

constexpr unsigned WS_FLAGS = 0;
constexpr unsigned WS_XN = 131072;
constexpr unsigned WS_K = WS_XN + SEQ * DM * 2;
constexpr unsigned WS_V = WS_K + SEQ * NQ * 2;
constexpr unsigned WS_CTX = WS_V + SEQ * NQ * 2;
constexpr unsigned WS_P1 = WS_CTX + SEQ * NQ * 2;
constexpr unsigned WS_H = WS_P1 + 8 * SEQ * DM * 2;
constexpr unsigned WS_HN = WS_H + SEQ * DM * 2;
constexpr unsigned WS_H1 = WS_HN + SEQ * DM * 2;
constexpr unsigned WS_P2 = WS_H1 + SEQ * FF * 2;
constexpr unsigned WS_END = WS_P2 + 8 * SEQ * DM * 2;
constexpr unsigned F_EPOCH = 0, F_XN = 4096, F_KV = 8192, F_CTX = 12288, F_P1 = 16384, F_HN = 32768, F_H1 = 49152, F_P2 = 65536;

template <int... Is> using iseq = std::integer_sequence<int, Is...>;
template <int N> using make_iseq = std::make_integer_sequence<int, N>;
template <int I> struct IC { static constexpr int v = I; };
template <class F, int... Is> DI void static_for_(F&& f, iseq<Is...>) { (f(IC<Is>{}), ...); }
template <int N, class F> DI void static_for(F&& f) { static_for_(f, make_iseq<N>{}); }

#define SB() __builtin_amdgcn_sched_barrier(0)
#define PIN(x) asm volatile("" : "+v"(x))
DI unsigned rfl(unsigned x) { return __builtin_amdgcn_readfirstlane(x); }
DI rsrc_t mk_rsrc(const void* p, unsigned bytes) { return __builtin_amdgcn_make_buffer_rsrc((void*)p, (short)0, (int)bytes, 0x00020000); }
constexpr unsigned OOB = 0xFFFFFF00u;
DI void dma16(unsigned lds_addr, unsigned voff, rsrc_t r, unsigned soff) {
  asm volatile("s_mov_b32 m0, %0\n\ts_nop 3\n\tbuffer_load_dwordx4 %1, %2, %3 offen lds" ::"s"(lds_addr), "v"(voff), "s"(r), "s"(soff) : "memory", "m0");
}
DI void dma16c(unsigned lds_addr, unsigned voff, rsrc_t r, unsigned soff) {
  asm volatile("s_mov_b32 m0, %0\n\ts_nop 3\n\tbuffer_load_dwordx4 %1, %2, %3 offen sc1 lds" ::"s"(lds_addr), "v"(voff), "s"(r), "s"(soff) : "memory", "m0");
}
template <int IMM> DI void gld16c(u32x4& d, unsigned voff, rsrc_t r, unsigned soff) {
  asm volatile("s_nop 4\n\tbuffer_load_dwordx4 %0, %1, %2, %3 offen offset:%4 sc1" : "=v"(d) : "v"(voff), "s"(r), "s"(soff), "i"(IMM) : "memory");
}
template <int IMM> DI void gld16(u32x4& d, unsigned voff, rsrc_t r, unsigned soff) {
  asm volatile("s_nop 4\n\tbuffer_load_dwordx4 %0, %1, %2, %3 offen offset:%4" : "=v"(d) : "v"(voff), "s"(r), "s"(soff), "i"(IMM) : "memory");
}
DI void gld8c(u32x2& d, unsigned voff, rsrc_t r, unsigned soff) {
  asm volatile("s_nop 4\n\tbuffer_load_dwordx2 %0, %1, %2, %3 offen sc1" : "=v"(d) : "v"(voff), "s"(r), "s"(soff) : "memory");
}
DI void gst16c(u32x4 v, rsrc_t r, unsigned voff, unsigned soff) {
  asm volatile("s_nop 4\n\tbuffer_store_dwordx4 %0, %1, %2, %3 offen sc1\n\ts_nop 1" ::"v"(v), "v"(voff), "s"(r), "s"(soff) : "memory");
}
DI void gst8c(u32x2 v, rsrc_t r, unsigned voff, unsigned soff) { __builtin_amdgcn_raw_buffer_store_b64(v, r, voff, soff, 16); }
DI void fput(u32x2 v, rsrc_t r, unsigned voff) { asm volatile("s_nop 4\n\tbuffer_atomic_swap_x2 %0, %1, %2, 0 offen sc1\n\ts_nop 1" ::"v"(v), "v"(voff), "s"(r) : "memory"); }
DI void fget(u32x2& d, rsrc_t r, unsigned voff) {
  d = u32x2{0u, 0u};
  asm volatile("s_nop 4\n\tbuffer_atomic_add_x2 %0, %1, %2, 0 offen sc0 sc1" : "+v"(d) : "v"(voff), "s"(r) : "memory");
}
template <int N> DI void wait_vm() { asm volatile("s_waitcnt vmcnt(%0)" ::"i"(N) : "memory"); }
DI void bar() { asm volatile("s_waitcnt lgkmcnt(0)" ::: "memory"); __builtin_amdgcn_s_barrier(); asm volatile("" ::: "memory"); }
DI unsigned cvtpk(float lo, float hi) { f32x2 v = {lo, hi}; bf16x2 b = __builtin_convertvector(v, bf16x2); return __builtin_bit_cast(unsigned, b); }
DI float bflo(unsigned x) { return __uint_as_float(x << 16); }
DI float bfhi(unsigned x) { return __uint_as_float(x & 0xffff0000u); }
DI bf16x8 lds_b128(unsigned a) { return *(LAS bf16x8*)(size_t)a; }
DI bf16x4 lds_tr(unsigned a) { return __builtin_amdgcn_ds_read_tr16_b64_v4bf16((LAS bf16x4*)(size_t)a); }
DI bf16x8 cat(bf16x4 a, bf16x4 b) { return __builtin_shufflevector(a, b, 0, 1, 2, 3, 4, 5, 6, 7); }
DI bf16x8 asbf(u32x4 x) { return __builtin_bit_cast(bf16x8, x); }
#define MFMA32(a, b, c) __builtin_amdgcn_mfma_f32_32x32x16_bf16((a), (b), (c), 0, 0, 0)
#define MFMA16(a, b, c) __builtin_amdgcn_mfma_f32_16x16x32_bf16((a), (b), (c), 0, 0, 0)
template <int S> DI u32x4 pack8(const f32x16& x, float sc) {
  u32x4 w;
  w[0] = cvtpk(x[8 * S + 0] * sc, x[8 * S + 1] * sc); w[1] = cvtpk(x[8 * S + 2] * sc, x[8 * S + 3] * sc);
  w[2] = cvtpk(x[8 * S + 4] * sc, x[8 * S + 5] * sc); w[3] = cvtpk(x[8 * S + 6] * sc, x[8 * S + 7] * sc);
  return w;
}
template <int S> DI u32x4 pack8(const f32x16& x) {
  u32x4 w;
  w[0] = cvtpk(x[8 * S + 0], x[8 * S + 1]); w[1] = cvtpk(x[8 * S + 2], x[8 * S + 3]);
  w[2] = cvtpk(x[8 * S + 4], x[8 * S + 5]); w[3] = cvtpk(x[8 * S + 6], x[8 * S + 7]);
  return w;
}
DI float max3f(float a, float b, float c) { float r; asm("v_max3_f32 %0, %1, %2, %3" : "=v"(r) : "v"(a), "v"(b), "v"(c)); return r; }
DI float max2f(float a, float b) { float r; asm("v_max_f32_e32 %0, %1, %2" : "=v"(r) : "v"(a), "v"(b)); return r; }
DI float fadd_s(float a, float b) { float r; asm("v_add_f32_e32 %0, %1, %2" : "=v"(r) : "v"(a), "v"(b)); return r; }
DI float xhalf_sum(float x) {
  auto r = __builtin_amdgcn_permlane32_swap(__float_as_uint(x), __float_as_uint(x), false, false);
  return __uint_as_float(r[0]) + __uint_as_float(r[1]);
}
DI float wave_sum(float x) {
#pragma unroll
  for (int o = 1; o < 64; o <<= 1) x += __shfl_xor(x, o);
  return x;
}

#define STAMP(i)

struct Ctx {
  unsigned ldsb, w, lane, b;
  unsigned ep0, ep1;
  rsrc_t rws;
  u64 pc0;
};

DI void fresh(Ctx& c) {
  asm volatile("v_mbcnt_lo_u32_b32 %0, -1, 0\n\tv_mbcnt_hi_u32_b32 %0, -1, %0" : "=v"(c.lane));
  asm volatile("" : "+s"(c.w), "+s"(c.b), "+s"(c.ldsb));
}
DI void code_touch(Ctx& c, unsigned& dummy) {
  unsigned bytes;
  asm volatile("s_mov_b32 %0, .Lfb_end-.Lfb_begin" : "=s"(bytes));
  const rsrc_t r = mk_rsrc((const void*)c.pc0, bytes);
  asm volatile("s_nop 4\n\tbuffer_load_dword %0, %1, %2, 0 offen" : "+v"(dummy) : "v"(c.w == 0 ? (c.lane * 32 + (c.b >> 3)) * 128 : OOB), "s"(r) : "memory");
}
DI void publish(Ctx& c, unsigned flag_off) {
  wait_vm<0>();
  bar();
  fput(u32x2{c.ep0, c.ep1}, c.rws, (c.w == 0 && c.lane == 0) ? flag_off : OOB);
}
DI void poll_flags(Ctx& c, unsigned flag_off, unsigned n, unsigned stride) {
  const unsigned voff = c.lane < n ? flag_off + c.lane * 8 * stride : OOB;
  const u64 t0 = __builtin_amdgcn_s_memrealtime();
  for (;;) {
    u32x2 f;
    fget(f, c.rws, voff);
    asm volatile("s_waitcnt vmcnt(0)" : "+v"(f)::"memory");
    if (__all((f[0] == c.ep0 && f[1] == c.ep1) || c.lane >= n)) break;
    if (__builtin_amdgcn_s_memrealtime() - t0 > 5000000ull) break;
    __builtin_amdgcn_s_sleep(2);
  }
}
DI void wait_flags(Ctx& c, unsigned flag_off, unsigned n, unsigned stride) {
  if (c.w == 0) poll_flags(c, flag_off, n, stride);
  bar();
}

#define DBG_DRAIN()
#define DBG_NX(x)
#define STAMPG(i)
DI void flag_store(Ctx& c, unsigned flag_off) {
  fput(u32x2{c.ep0, c.ep1}, c.rws, (c.w == 0 && c.lane == 0) ? flag_off : OOB);
}
DI void flag_load(Ctx& c, u32x2& f, unsigned flag_off, unsigned n) { fget(f, c.rws, c.lane < n ? flag_off + c.lane * 8 : OOB); }
DI void flag_check(Ctx& c, u32x2& f, unsigned flag_off, unsigned n) {
  PIN(f);
  if (__builtin_expect(!__all((f[0] == c.ep0 && f[1] == c.ep1) || c.lane >= n), 0)) poll_flags(c, flag_off, n, 1);
}

constexpr float QSCALE = 0.08838834764831845f * 1.4426950408889634f;
constexpr unsigned LE = 131072;
constexpr int XR = 12;

constexpr int kv_wait(int t) {
  int seq[256] = {}; int n = 0;
  auto push = [&](int tag, int cnt) { for (int i = 0; i < cnt; ++i) seq[n++] = tag; };
  push(-1, 3);
  for (int s = 0; s < 4; ++s) push(s, 4);
  push(-2, 2);
  for (int s = 0;; ++s) {
    if (s >= 1) push(s + 3, 2);
    if (s == 6) push(-4, 2);
    if (s == t) break;
    if (s == 3) push(-3, 1);
    if (s == 10) push(-5, 1);
    push(s + 4, 2);
    if (s == 5) push(-4, 2);
  }
  int last = -1;
  for (int i = 0; i < n; ++i) if (seq[i] == t + 1) last = i;
  return n - 1 - last;
}
constexpr int q_wait(int ks) {
  int seq[256] = {}; int n = 0;
  for (int s = 0; s < ks; ++s) {
    if (s % 2 == 0 && s < 32) seq[n++] = -1;
    if (s >= 1 && s - 1 + XR < 36) seq[n++] = s - 1 + XR;
  }
  int last = -1;
  for (int i = 0; i < n; ++i) if (seq[i] == ks + 2) last = i;
  return n - 1 - last;
}

struct QOut { u32x4 q[8]; };

template <bool ISV>
DI void qkv_body(Ctx& c, QOut& qo, const rsrc_t rx, const rsrc_t rw, const rsrc_t rwq, const unsigned voffX, const unsigned voffW, const unsigned sX,
                 const unsigned sW, const unsigned sWq, const unsigned sWq4, const unsigned rot, const unsigned qmb, const unsigned head, const unsigned cb) {
  const unsigned w = c.w, lane = c.lane, ldsb = c.ldsb;
  const unsigned l31 = lane & 31, h = lane >> 5, g2 = (lane >> 4) & 1, q = (lane & 15) >> 2, pp = lane & 3;
  const unsigned wm = w >> 2, wn = w & 3, xsw = (l31 >> 2) & 3;
  const unsigned wrow = 8 * h + q, wch = (g2 * 2 + (pp >> 1)) ^ ((q >> 1) << 2);
  const unsigned wl0 = ldsb + wrow * 128 + (wch << 4) + 8 * (pp & 1), wl1 = ldsb + wrow * 128 + ((wch ^ 4) << 4) + 8 * (pp & 1);
  u32x4 pw[16], xq[36]; u32x2 xnf;
  const unsigned xso = WS_XN + qmb * 36 * 1024;
  auto ldx = [&](auto K_) AI { constexpr int K = decltype(K_)::v; gld16c<(K & 3) * 1024>(xq[K], lane * 16, c.rws, xso + (K & ~3) * 1024); };
  {
    unsigned xb[2][2], wb[2][2];
    xb[0][0] = ldsb + (wm * 128 + l31) * 64 + ((h ^ xsw) << 4); xb[0][1] = ldsb + (wm * 128 + l31) * 64 + (((2 + h) ^ xsw) << 4);
    wb[0][0] = wl0 + 16384 + wn * 4096; wb[0][1] = wl1 + 16384 + wn * 4096;
    xb[1][0] = xb[0][0] + 65536; xb[1][1] = xb[0][1] + 65536; wb[1][0] = wb[0][0] + 65536; wb[1][1] = wb[0][1] + 65536;
    PIN(xb[0][0]); PIN(xb[0][1]); PIN(xb[1][0]); PIN(xb[1][1]); PIN(wb[0][0]); PIN(wb[0][1]); PIN(wb[1][0]); PIN(wb[1][1]);
    const unsigned dX = ldsb + 2 * w * 1024, dW = ldsb + 16384 + 2 * w * 1024, dQ = ldsb + w * 4096;
    f32x16 acc[4][2];
#pragma unroll
    for (int i = 0; i < 4; ++i)
#pragma unroll
      for (int j = 0; j < 2; ++j) acc[i][j] = f32x16{};
    bf16x8 xf[2][4]; bf16x4 wl[2][4];
    auto rd = [&](auto T_, auto S_, auto R_) AI {
      constexpr int T = decltype(T_)::v, S = decltype(S_)::v, R = decltype(R_)::v;
      constexpr unsigned so = (T & 1) * 32768; constexpr int sp = (T >> 1) & 1;
      if constexpr (R < 2) wl[S][R] = lds_tr(wb[sp][0] + so + (16 * S + 4 * R) * 128);
      else if constexpr (R == 2) xf[S][0] = lds_b128(xb[sp][S] + so);
      else if constexpr (R < 5) wl[S][R - 1] = lds_tr(wb[sp][1] + so + (16 * S + 4 * (R - 3)) * 128);
      else xf[S][R - 4] = lds_b128(xb[sp][S] + so + (R - 4) * 2048);
    };
    auto issue = [&](auto U_, auto H_, auto P_) AI {
      constexpr int U = decltype(U_)::v, H = decltype(H_)::v, P = decltype(P_)::v;
      constexpr unsigned so = (U & 3) * 32768;
      if constexpr (U < 16) {
        const unsigned tr = (U + rot) & 15;
        if constexpr (H == 0) dma16(dX + so + P * 1024, voffX, rx, sX + 64 * tr + P * 16 * 1024);
        else dma16(dW + so + P * 1024, voffW, rw, sW + 32 * tr * 8192 + P * 8 * 8192);
      } else if constexpr (U < 19) {
        dma16(dQ + so + (2 * H + P) * 1024, voffW, rwq, sWq + (U - 15) * 128 * 8192 + (2 * H + P) * 8 * 8192);
      } else if constexpr (U == 19 && H == 0) {
        dma16(ldsb + so + (w >> 2) * 16384 + ((w & 3) * 2 + P) * 1024, voffW, rwq, sWq4 + P * 8 * 8192);
      }
    };
    static_for<8>([&](auto R) AI { rd(IC<0>{}, IC<0>{}, R); });
    SB();
    static_for<16>([&](auto T_) AI {
      constexpr int T = decltype(T_)::v;
      static_for<2>([&](auto S_) AI {
        constexpr int S = decltype(S_)::v;
        if constexpr (S == 1) {
          if constexpr (T < 15) wait_vm<kv_wait(T)>();
          bar();
          if constexpr (T == 3) flag_store(c, F_XN + c.b * 8);
          if constexpr (T == 10) flag_load(c, xnf, F_XN + qmb * 32, 4);
          if constexpr (T == 14) flag_check(c, xnf, F_XN + qmb * 32, 4);
          SB();
        }
        static_for<8>([&](auto M_) AI {
          constexpr int M = decltype(M_)::v, i = M >> 1, j = M & 1;
          const bf16x8 wf = cat(wl[S][2 * j], wl[S][2 * j + 1]);
          acc[i][j] = ISV ? MFMA32(xf[S][i], wf, acc[i][j]) : MFMA32(wf, xf[S][i], acc[i][j]);
          if constexpr (M < 4 && (S == 0 || T < 15)) {
            rd(IC<(S == 0 ? T : T + 1)>{}, IC<(S ^ 1)>{}, IC<2 * M>{});
            rd(IC<(S == 0 ? T : T + 1)>{}, IC<(S ^ 1)>{}, IC<2 * M + 1>{});
          }
          if constexpr (M == 4 || M == 6) {
            constexpr int P = (M - 4) >> 1;
            if constexpr (S == 1) issue(IC<T + 4>{}, IC<0>{}, IC<P>{});
            else if constexpr (T >= 1) issue(IC<T + 3>{}, IC<1>{}, IC<P>{});
          }
          if constexpr (M == 7 && ((S == 1 && T == 5) || (S == 0 && T == 6))) {
            constexpr int H = S == 1 ? 0 : 1;
            dma16(ldsb + LE + w * 4096 + (2 * H) * 1024, voffW, rwq, sWq + (2 * H) * 8 * 8192);
            dma16(ldsb + LE + w * 4096 + (2 * H + 1) * 1024, voffW, rwq, sWq + (2 * H + 1) * 8 * 8192);
          }
          if constexpr ((M & 1) && ((T == 14 && S == 1) || T == 15)) {
            constexpr int K = (T == 14 ? 0 : S == 0 ? 4 : 8) + (M >> 1);
            ldx(IC<K>{});
          }
          SB();
        });
      });
    });
#pragma unroll
    for (int i = 0; i < 4; ++i)
#pragma unroll
      for (int j = 0; j < 2; ++j) { pw[(i * 2 + j) * 2] = pack8<0>(acc[i][j]); pw[(i * 2 + j) * 2 + 1] = pack8<1>(acc[i][j]); }
  }
  STAMP(3);
  unsigned wbase[3][2];
  wbase[0][0] = wl0; wbase[0][1] = wl1; wbase[1][0] = wl0 + 65536; wbase[1][1] = wl1 + 65536; wbase[2][0] = wl0 + LE; wbase[2][1] = wl1 + LE;
  PIN(wbase[0][0]); PIN(wbase[0][1]); PIN(wbase[1][0]); PIN(wbase[1][1]); PIN(wbase[2][0]); PIN(wbase[2][1]);
  const unsigned sto = ISV ? WS_V + (((head * 32 + cb * 4 + wm * 2) * 4 + 2 * (wn & 1)) * 4) * 1024 : WS_K + ((head * 64 + cb * 8 + wm * 4) * 8 + 4 * (wn & 1)) * 1024;
  asm volatile("s_waitcnt vmcnt(12)" : "+v"(xq[0]), "+v"(xq[1]), "+v"(xq[2]), "+v"(xq[3])::"memory");
  STAMP(5);
  f32x16 acc[4];
#pragma unroll
  for (int j = 0; j < 4; ++j) acc[j] = f32x16{};
  bf16x4 wl[8][2];
  auto rdq = [&](auto N_) AI {
    constexpr int N = decltype(N_)::v, ks = N >> 2, j = N & 3, ci = ks >> 3;
    constexpr int reg = ci == 0 ? 2 : (ci - 1) >> 1;
    constexpr unsigned off = (ci == 0 ? 0 : ((ci - 1) & 1) * 32768) + (j >> 1) * 16384 + (16 * (ks & 7)) * 128;
    wl[N & 7][0] = lds_tr(wbase[reg][j & 1] + off);
    wl[N & 7][1] = lds_tr(wbase[reg][j & 1] + off + 512);
  };
  static_for<5>([&](auto N) AI { rdq(N); });
  SB();
  static_for<144>([&](auto N_) AI {
    constexpr int N = decltype(N_)::v, ks = N >> 2, j = N & 3;
    if constexpr (j == 0 && ks == 4) asm volatile("s_waitcnt vmcnt(11)" : "+v"(xq[4]), "+v"(xq[5]), "+v"(xq[6]), "+v"(xq[7])::"memory");
    if constexpr (j == 0 && ks == 6) {
      asm volatile("s_waitcnt vmcnt(8)" : "+v"(xq[8]), "+v"(xq[9]), "+v"(xq[10]), "+v"(xq[11])::"memory");
      bar(); SB();
    }
    if constexpr (j == 0 && ks >= XR && ks % 3 == 0)
      asm volatile("s_waitcnt vmcnt(%3)" : "+v"(xq[ks]), "+v"(xq[ks + 1]), "+v"(xq[ks + 2]) : "i"(q_wait(ks)) : "memory");
    acc[j] = MFMA32(cat(wl[N & 7][0], wl[N & 7][1]), asbf(xq[ks]), acc[j]);
    if constexpr (N + 5 < 144) rdq(IC<N + 5>{});
    if constexpr (j == 0 && ks % 2 == 0 && ks < 32) {
      constexpr int e = ks >> 1, i = e >> 2, jj = (e >> 1) & 1, s = e & 1;
      constexpr unsigned fo = ISV ? (((i >> 1) * 4 + jj) * 4 + 2 * (i & 1) + s) : (i * 8 + 2 * jj + s);
      gst16c(pw[e], c.rws, lane * 16, sto + fo * 1024);
    }
    if constexpr (j == 2 && ks >= 1 && ks - 1 + XR < 36) ldx(IC<ks - 1 + XR>{});
    SB();
  });
#pragma unroll
  for (int j = 0; j < 4; ++j) { qo.q[2 * j] = pack8<0>(acc[j], QSCALE); qo.q[2 * j + 1] = pack8<1>(acc[j], QSCALE); }
  STAMP(6);
}

DI void phase_qkv(Ctx& c, QOut& qo, const u16* x, const u16* ckv, const u16* wq, const u16* wuk, const u16* wuv) {
  const unsigned w = c.w, lane = c.lane, ldsb = c.ldsb, b = c.b;
  const unsigned xcd = b & 7, jj = b >> 3, head = xcd * 4 + (jj >> 3), cb = jj & 7;
  const rsrc_t rxx = mk_rsrc(x, SEQ * DM * 2);
  const rsrc_t rx = mk_rsrc(ckv, SEQ * DC * 2);
  const rsrc_t rw = mk_rsrc(w < 4 ? wuk : wuv, DC * NQ * 2);
  const rsrc_t rwq = mk_rsrc(wq, DM * NQ * 2);
  u32x2 ep; u32x4 xr0, xr1;
  unsigned dummy = 0;
  code_touch(c, dummy);
  gld8c(ep, F_EPOCH, c.rws, WS_FLAGS);
  {
    const unsigned vo = lane < 36 ? lane * 32 : OOB;
    gld16<0>(xr0, vo, rxx, (b * 8 + w) * (DM * 2));
    gld16<16>(xr1, vo, rxx, (b * 8 + w) * (DM * 2));
  }
  const unsigned voffX = (lane >> 2) * 1024 + (((lane & 3) ^ ((lane >> 4) & 3)) << 4);
  const unsigned voffW = (lane >> 3) * 8192 + (((lane & 7) ^ (((lane >> 4) & 1) << 2)) << 4);
  const unsigned sX = (cb * 256 + 32 * w) * 1024;
  const unsigned sW = ((2 * w & 3) * 8) * 8192 + (head * 128 + ((w >> 1) & 1) * 64) * 2;
  const unsigned sWq = ((w & 3) * 32) * 8192 + (head * 128 + (w >> 2) * 64) * 2;
  const unsigned sWq4 = (512 + (w & 3) * 16) * 8192 + (head * 128 + (w >> 2) * 64) * 2;
#define ROT 3
  const unsigned rot = ROT == 0 ? 4 * (head & 3) : ROT == 1 ? (2 * cb + 4 * (head & 3)) & 15 : ROT == 2 ? 4 * (head & 3) + 2 * (cb & 1) : 4 * (cb & 3);
  {
    const unsigned dX = ldsb + 2 * w * 1024, dW = ldsb + 16384 + 2 * w * 1024;
#pragma unroll
    for (int u = 0; u < 4; ++u) {
      const unsigned tr = (u + rot) & 15;
      dma16(dX + u * 32768, voffX, rx, sX + 64 * tr);
      dma16(dX + u * 32768 + 1024, voffX, rx, sX + 64 * tr + 16 * 1024);
      dma16(dW + u * 32768, voffW, rw, sW + 32 * tr * 8192);
      dma16(dW + u * 32768 + 1024, voffW, rw, sW + 32 * tr * 8192 + 8 * 8192);
    }
  }
  asm volatile("s_waitcnt vmcnt(16)" : "+v"(ep), "+v"(xr0), "+v"(xr1)::"memory");
  asm volatile("" ::"v"(dummy));
  STAMP(1);
  {
    u64 z = (((u64)rfl(ep[1]) << 32) | rfl(ep[0])) + 0x9E3779B97F4A7C15ull;
    z = (z ^ (z >> 30)) * 0xBF58476D1CE4E5B9ull; z = (z ^ (z >> 27)) * 0x94D049BB133111EBull; z ^= z >> 31;
    c.ep0 = (unsigned)z; c.ep1 = (unsigned)(z >> 32);
  }
  {
    float v[16];
#pragma unroll
    for (int e = 0; e < 4; ++e) { v[2 * e] = bflo(xr0[e]); v[2 * e + 1] = bfhi(xr0[e]); v[8 + 2 * e] = bflo(xr1[e]); v[9 + 2 * e] = bfhi(xr1[e]); }
    float s = 0.f;
#pragma unroll
    for (int e = 0; e < 16; ++e) s += v[e];
    const float mean = wave_sum(s) * (1.f / DM);
    float qq = 0.f;
#pragma unroll
    for (int e = 0; e < 16; ++e) { v[e] -= mean; qq += v[e] * v[e]; }
    qq = lane < 36 ? qq : 0.f;
    const float rs = rsqrtf(wave_sum(qq) * (1.f / DM) + 1e-5f);
    u32x4 o0, o1;
#pragma unroll
    for (int e = 0; e < 4; ++e) { o0[e] = cvtpk(v[2 * e] * rs, v[2 * e + 1] * rs); o1[e] = cvtpk(v[8 + 2 * e] * rs, v[9 + 2 * e] * rs); }
    if (lane < 36) {
      *(LAS u32x4*)(size_t)(ldsb + LE + ((2 * lane) * 8 + w) * 16) = o0;
      *(LAS u32x4*)(size_t)(ldsb + LE + ((2 * lane + 1) * 8 + w) * 16) = o1;
    }
  }
  wait_vm<12>();
  bar();
  STAMP(2);
  {
    const unsigned mb = b >> 2, r0 = (b & 3) * 8;
    const unsigned c0 = 9 * w + (lane >> 3), c1 = 9 * w + 8;
    const u32x4 a0 = *(LAS u32x4*)(size_t)(ldsb + LE + (9 * w * 8 + lane) * 16);
    const u32x4 a1 = *(LAS u32x4*)(size_t)(ldsb + LE + (c1 * 8 + (lane & 7)) * 16);
    gst16c(a0, c.rws, ((mb * 36 + (c0 >> 1)) * 64 + (c0 & 1) * 32 + r0 + (lane & 7)) * 16, WS_XN);
    gst16c(a1, c.rws, lane < 8 ? ((mb * 36 + (c1 >> 1)) * 64 + (c1 & 1) * 32 + r0 + lane) * 16 : OOB, WS_XN);
  }
  const unsigned kh = w >> 2, rg = kh ? 7 - w : w;
  const unsigned qmb = (kh ? cb : 15 - cb) * 4 + rg;
  if ((w & 3) >= 2) qkv_body<true>(c, qo, rx, rw, rwq, voffX, voffW, sX, sW, sWq, sWq4, rot, qmb, head, cb);
  else qkv_body<false>(c, qo, rx, rw, rwq, voffX, voffW, sX, sW, sWq, sWq4, rot, qmb, head, cb);
}

constexpr float THR = 32.f;

struct AttnState {
  bf16x8 qf[8];
  f32x16 o[4];
  f32x16 S[2][2];
  f32x16 negm;
  u32x4 pk[4];
  bf16x8 fr[8];
  float mhat, l0, l1, fct, mx0, mx1, t0, t1;
  bool pend;
  unsigned kb[2], vb[2];
  unsigned voff, ldsb, w, kbase, vbase;
};

constexpr int RD0[16] = {4, 5, 6, 7, 9, 10, 12, 13, 15, -1, -1, -1, -1, -1, -1, -1};
constexpr int RD1[16] = {-1, -1, -1, 8, -1, 11, -1, 14, -1, -1, -1, -1, -1, -1, -1, -1};

struct NoHook { DI void operator()() const {} };
template <int X, bool QK, bool PV, class Hook = NoHook>
DI void attn_iter(Ctx& c, AttnState& a, const unsigned s, const unsigned nhi, const bool first, const bool diag, const int dq, Hook&& hook = Hook{}) {
  constexpr int Y = X ^ 1;
  auto kaddr = [&](int slot, int n) AI { return a.kb[slot] + ((n & 1) * 8 + (n >> 1)) * 1024; };
  auto vaddr = [&](int slot, int n) AI { return a.vb[slot] + ((n & 3) * 4 + (n >> 2)) * 1024; };
  unsigned sk = s + 2; sk = sk > 16 ? 16 : sk; sk = sk < nhi ? sk : sk - nhi;
  unsigned sv = s > 16 ? 16 : s; sv = sv < nhi ? sv : sv - nhi;
  const unsigned sok = a.kbase + sk * 32768, sov = a.vbase + sv * 32768;
  const unsigned dk = a.ldsb + X * 32768 + a.w * 4096, dv = dk + 65536;
  static_for<16>([&](auto M_) AI {
    constexpr int M = decltype(M_)::v, d = M >> 1, kb = M & 1;
    if constexpr (QK) {
      if constexpr (d == 0) a.S[X][kb] = MFMA32(a.fr[M & 7], a.qf[d], a.negm);
      else a.S[X][kb] = MFMA32(a.fr[M & 7], a.qf[d], a.S[X][kb]);
      if constexpr (RD0[M] >= 0) a.fr[RD0[M] & 7] = lds_b128(kaddr(X, RD0[M]));
      if constexpr (RD1[M] >= 0) a.fr[RD1[M] & 7] = lds_b128(kaddr(X, RD1[M]));
      if constexpr (M == 0 || M == 2 || M == 6 || M == 9) { constexpr int P = M == 0 ? 0 : M == 2 ? 1 : M == 6 ? 2 : 3; dma16c(dv + P * 1024, a.voff, c.rws, sov + P * 1024); }
    }
    if constexpr (PV) {
      if constexpr (M < 8) {
        a.S[Y][1][2 * M] = __builtin_amdgcn_exp2f(a.S[Y][1][2 * M]);
        a.S[Y][1][2 * M + 1] = __builtin_amdgcn_exp2f(a.S[Y][1][2 * M + 1]);
        a.pk[M >> 2][M & 3] = cvtpk(a.S[Y][0][2 * M], a.S[Y][0][2 * M + 1]);
        PIN(a.pk[M >> 2][M & 3]);
        if constexpr (M == 0) { a.l0 = fadd_s(a.l0, a.S[Y][0][14]); a.l1 = fadd_s(a.l1, a.S[Y][0][15]); }
      } else {
        constexpr int e = M - 8;
        a.pk[2 + (e >> 2)][e & 3] = cvtpk(a.S[Y][1][2 * e], a.S[Y][1][2 * e + 1]);
        PIN(a.pk[2 + (e >> 2)][e & 3]);
        a.l0 = fadd_s(a.l0, a.S[Y][1][2 * e]); a.l1 = fadd_s(a.l1, a.S[Y][1][2 * e + 1]);
      }
    }
    if constexpr (M == 11) { SB(); if constexpr (QK) wait_vm<8>(); else wait_vm<0>(); bar(); hook(); SB(); }
    if constexpr (PV && M >= 12) a.fr[(M - 12) & 7] = lds_b128(vaddr(Y, M - 12));
    SB();
  });
  static_for<16>([&](auto M_) AI {
    constexpr int M = decltype(M_)::v, ks = M >> 2, db = M & 3;
    if constexpr (PV) {
      a.o[db] = MFMA32(a.fr[M & 7], asbf(a.pk[ks]), a.o[db]);
      if constexpr (RD0[M] >= 0) a.fr[RD0[M] & 7] = lds_b128(vaddr(Y, RD0[M]));
      if constexpr (RD1[M] >= 0) a.fr[RD1[M] & 7] = lds_b128(vaddr(Y, RD1[M]));
    }
    if constexpr (QK) {
      if constexpr (M == 0 || M == 1 || M == 6 || M == 9) { constexpr int P = M == 0 ? 0 : M == 1 ? 1 : M == 6 ? 2 : 3; dma16c(dk + P * 1024, a.voff, c.rws, sok + P * 1024); }
      if constexpr (M == 2) {
        asm volatile("s_nop 7\n\ts_nop 3" : "+v"(a.S[X][0]), "+v"(a.S[X][1]));
        if (__builtin_expect(diag, 0)) {
          int dq2 = dq; PIN(dq2);
#pragma unroll
          for (int r = 0; r < 16; ++r) {
            const int cr = (r & 3) + 8 * (r >> 2);
            if (cr > dq2) a.S[X][0][r] = -__builtin_inff();
            if (cr + 32 > dq2) a.S[X][1][r] = -__builtin_inff();
          }
        }
      }
      if constexpr (M >= 2 && M < 6) {
        constexpr int r = 4 * (M - 2);
        if constexpr (M == 2) {
          a.mx0 = max3f(a.S[X][0][0], a.S[X][0][1], a.S[X][1][0]); a.mx1 = max3f(a.S[X][0][2], a.S[X][0][3], a.S[X][1][1]);
          a.mx0 = max3f(a.mx0, a.S[X][1][2], a.S[X][1][3]);
        } else {
          a.mx0 = max3f(a.mx0, a.S[X][0][r], a.S[X][0][r + 1]); a.mx1 = max3f(a.mx1, a.S[X][0][r + 2], a.S[X][0][r + 3]);
          a.mx0 = max3f(a.mx0, a.S[X][1][r], a.S[X][1][r + 1]); a.mx1 = max3f(a.mx1, a.S[X][1][r + 2], a.S[X][1][r + 3]);
        }
      }
      if constexpr (M == 6) {
        const float mm = max2f(a.mx0, a.mx1);
        auto pr = __builtin_amdgcn_permlane32_swap(__float_as_uint(mm), __float_as_uint(mm), false, false);
        a.mx0 = max2f(__uint_as_float(pr[0]), __uint_as_float(pr[1]));
      }
      if constexpr (M == 7) {
        if (__builtin_expect(first || __any(a.mx0 > THR), 0)) {
          const float dl = first ? fmaxf(a.mx0, -1e30f) : fmaxf(a.mx0, 0.f);
          a.mhat = first ? dl : a.mhat + dl;
          a.fct = __builtin_amdgcn_exp2f(-dl);
#pragma unroll
          for (int r = 0; r < 16; ++r) { a.S[X][0][r] -= dl; a.S[X][1][r] -= dl; a.negm[r] = -a.mhat; }
          a.pend = !first;
        }
      }
      if constexpr (M >= 8) {
        constexpr int e = M - 8;
        a.S[X][0][2 * e] = __builtin_amdgcn_exp2f(a.S[X][0][2 * e]);
        a.S[X][0][2 * e + 1] = __builtin_amdgcn_exp2f(a.S[X][0][2 * e + 1]);
        PIN(a.S[X][0][2 * e]); PIN(a.S[X][0][2 * e + 1]);
        if constexpr (e > 0) {
          a.t0 = e == 1 ? a.S[X][0][0] : fadd_s(a.t0, a.S[X][0][2 * e - 2]); a.t1 = e == 1 ? a.S[X][0][1] : fadd_s(a.t1, a.S[X][0][2 * e - 1]);
          PIN(a.t0); PIN(a.t1);
        }
      }
      if constexpr (M == 11) { SB(); wait_vm<8>(); bar(); }
      if constexpr (M >= 12) a.fr[(M - 12) & 7] = lds_b128(kaddr(Y, M - 12));
    }
    SB();
  });
}

constexpr unsigned WP = 288;
constexpr unsigned LSYNC = 161280;
constexpr unsigned C_LAND = 0, C_FREE = 64, C_GB = 128, C_CTXD = 136, LSTAT = LSYNC + 256;
#define ARRV 4
constexpr int XD = 8;
DI unsigned land(unsigned ph, unsigned q) { return LSYNC + C_LAND + (ph * 5 + q) * 4; }
DI unsigned fre(unsigned ph, unsigned q) { return LSYNC + C_FREE + (ph * 5 + q) * 4; }
DI void cnt_add(Ctx& c, unsigned off) {
  asm volatile("s_mov_b64 exec, 1\n\tds_add_u32 %0, %1\n\ts_mov_b64 exec, -1" ::"v"(c.ldsb + off), "v"(1u) : "memory");
}
DI unsigned cnt_add_rtn(Ctx& c, unsigned off) {
  unsigned r;
  asm volatile("s_mov_b64 exec, 1\n\tds_add_rtn_u32 %0, %1, %2\n\ts_mov_b64 exec, -1\n\ts_waitcnt lgkmcnt(0)" : "=&v"(r) : "v"(c.ldsb + off), "v"(1u) : "memory");
  return rfl(r);
}
DI unsigned cnt_read(Ctx& c, unsigned off) { return __hip_atomic_load((LAS unsigned*)(size_t)(c.ldsb + off), __ATOMIC_RELAXED, __HIP_MEMORY_SCOPE_WORKGROUP); }
DI void prio_lo() { __builtin_amdgcn_s_setprio(0); }
#define PRIO_L
DI void prio_hi(Ctx& c) { if (c.w < 4) __builtin_amdgcn_s_setprio(1); else __builtin_amdgcn_s_setprio(2); }
DI void cnt_wait(Ctx& c, unsigned off, unsigned target) {
  asm volatile("" ::: "memory");
  unsigned spins = 0;
#pragma unroll 1
  while (rfl(cnt_read(c, off)) < target) { __builtin_amdgcn_s_sleep(1); if (++spins > (1u << 20)) break; }
  asm volatile("" ::: "memory");
}
DI void flag_w(Ctx& c, unsigned flag_off) {
  wait_vm<0>();
  fput(u32x2{c.ep0, c.ep1}, c.rws, c.lane == 0 ? flag_off : OOB);
}
DI void ctx_arrive(Ctx& c) {
  if (cnt_add_rtn(c, LSYNC + C_CTXD) == 7) {
    const unsigned jj = c.b >> 3;
    fput(u32x2{c.ep0, c.ep1}, c.rws, c.lane == 0 ? F_CTX + ((jj & 7) * 32 + (c.b & 7) * 4 + (jj >> 3)) * 8 : OOB);
  }
}

DI void dma16m(unsigned lds_addr, unsigned voff, rsrc_t r, unsigned soff, u64 mask) {
  asm volatile("s_mov_b32 m0, %0\n\ts_mov_b64 exec, %4\n\ts_nop 3\n\tbuffer_load_dwordx4 %1, %2, %3 offen lds\n\ts_mov_b64 exec, -1" ::"s"(lds_addr), "v"(voff), "s"(r), "s"(soff), "s"(mask)
               : "memory", "m0");
}
DI void w_touch(Ctx& c, unsigned& dummy, unsigned ph, const void* wp, unsigned l2) {
  const unsigned xcd = c.b & 7, jj = c.b >> 3;
  unsigned off;
  if (ph == 3) off = (jj * 18 + l2 / 9) * (FF * 2) + (l2 % 9 < 8 ? xcd * 1024 + (l2 % 9) * 128 : 8192 + xcd * 128);
  else off = l2 < 144 ? xcd * 589824 + (jj * 144 + l2) * 128 : ph == 5 ? (4096 + 64 * xcd) * (DM * 2) + (jj * 18 + l2 - 144) * 128 : OOB;
  const rsrc_t rp = mk_rsrc(wp, ph == 1 ? NQ * DM * 2 : FF * DM * 2);
  asm volatile("s_nop 4\n\tbuffer_load_dword %0, %1, %2, 0 offen" : "+v"(dummy) : "v"(l2 < 162 ? off : OOB), "s"(rp) : "memory");
}
struct WSrc { unsigned voff[9]; rsrc_t r; unsigned main, tail, pitch, colA, colB; unsigned nx, sg, ph; };
template <int NV = 5> DI void wsrc_voff(WSrc& s) {
  unsigned lane;
  asm volatile("v_mbcnt_lo_u32_b32 %0, -1, 0\n\tv_mbcnt_hi_u32_b32 %0, -1, %0" : "=v"(lane));
#pragma unroll
  for (int p = 0; p < NV; ++p) {
    const unsigned ci = lane + 64 * p, rr = ci / 18, cc = ci - rr * 18;
    s.voff[p] = rr * s.pitch + (cc < 16 ? s.colA + cc * 16 : s.colB + (cc - 16) * 16);
  }
}
template <int NV = 5> DI void wsrc_setup(Ctx& c, WSrc& s, rsrc_t r, unsigned pitch, unsigned colA, unsigned colB, unsigned rowMain, unsigned rowTail, unsigned ph) {
  s.r = r; s.pitch = pitch; s.main = rowMain * pitch; s.tail = rowTail * pitch;
  s.colA = colA; s.colB = colB; s.ph = ph; s.nx = s.sg = 0;
  wsrc_voff<NV>(s);
}
DI void w_group(Ctx& c, const WSrc& s, unsigned q) {
  const unsigned r0 = 128 * q + 32 * (c.w & 3);
#pragma unroll
  for (int p = 0; p < 9; ++p) dma16(c.ldsb + r0 * WP + p * 1024, s.voff[p], s.r, s.main + r0 * s.pitch);
}
DI void w_group8(Ctx& c, const WSrc& s, unsigned q) {
  const unsigned r0 = 128 * q + 16 * c.w;
#pragma unroll
  for (int p = 0; p < 4; ++p) dma16(c.ldsb + r0 * WP + p * 1024, s.voff[p], s.r, s.main + r0 * s.pitch);
  dma16m(c.ldsb + r0 * WP + 4096, s.voff[4], s.r, s.main + r0 * s.pitch, 0xffffffffull);
}
DI void poll_w(Ctx& c, unsigned flag_off, unsigned n) {
  prio_lo();
  poll_flags(c, flag_off, n, 1);
  prio_hi(c);
}
DI void w_group4(Ctx& c, const WSrc& s) {
  const unsigned r0 = 6 * c.w, sk = 8 * s.pitch;
  dma16(c.ldsb + (512 + r0) * WP, s.voff[0], s.r, s.tail + r0 * s.pitch + (c.w == 7 ? sk : 0));
  dma16m(c.ldsb + (512 + r0) * WP + 1024, s.voff[1] + ((c.w == 6 && c.lane >= 8) ? sk : 0), s.r, s.tail + r0 * s.pitch + (c.w == 7 ? sk : 0), 0xfffffffffffull);
}
DI void w_issue(Ctx& c, WSrc& s) { if (s.nx == 0) w_group4(c, s); else w_group8(c, s, 4 - s.nx); ++s.nx; }
DI bool w_ready(Ctx& c, const WSrc& s) { return s.nx < 5 && rfl(cnt_read(c, fre(s.ph - 1, 4 - s.nx))) >= 8; }
DI void w_service(Ctx& c, WSrc& s) {
#pragma unroll 1
  while (w_ready(c, s)) w_issue(c, s);
}
DI void w_signal(Ctx& c, WSrc& s, unsigned nx) {
#pragma unroll 1
  while (s.sg < nx) { cnt_add(c, land(s.ph, 4 - s.sg)); ++s.sg; }
}
DI void w_need(Ctx& c, WSrc& s, unsigned q) {
  wsrc_voff(s);
#pragma unroll 1
  while (s.nx <= 4 - q) { cnt_wait(c, fre(s.ph - 1, 4 - s.nx), 8); w_issue(c, s); }
  if (s.sg < s.nx) { wait_vm<0>(); w_signal(c, s, s.nx); }
  cnt_wait(c, land(s.ph, q), 8);
}
DI void poll_srv(Ctx& c, WSrc& s, unsigned flag_off, unsigned n) {
  const unsigned voff = c.lane < n ? flag_off + c.lane * 8 : OOB;
  const u64 t0 = __builtin_amdgcn_s_memrealtime();
  prio_lo();
  for (;;) {
    u32x2 f;
    fget(f, c.rws, voff);
    const unsigned nx = s.nx;
    const bool more = w_ready(c, s);
    if (more) { w_issue(c, s); if (nx == 0) wait_vm<2>(); else wait_vm<5>(); } else wait_vm<0>();
    w_signal(c, s, nx);
    PIN(f);
    if (__all((f[0] == c.ep0 && f[1] == c.ep1) || c.lane >= n)) break;
    if (__builtin_amdgcn_s_memrealtime() - t0 > 5000000ull) break;
    if (!more) __builtin_amdgcn_s_sleep(1);
  }
  prio_hi(c);
}
constexpr unsigned LB1 = LSTAT + 256, LB2 = LB1 + 320;
DI void park_loads(Ctx& c, const u16* w1, const u16* b1, const u16* w2, const u16* b2) {
  const unsigned xcd = c.b & 7, nt = (c.b >> 3) & 3, nt32 = xcd * 4 + nt, w = c.w;
  const unsigned rr = c.lane >= 18 ? 1 : 0, cc = c.lane - 18 * rr, r0 = (w < 4 ? 40 : 48) + 2 * w;
  const unsigned c1 = cc < 16 ? nt32 * 256 + cc * 16 : 8192 + nt32 * 32 + (cc - 16) * 16, c2 = cc < 16 ? nt * 256 + cc * 16 : 1024 + nt * 32 + (cc - 16) * 16;
  dma16m(c.ldsb + (512 + 2 * w) * WP, rr * (FF * 2) + c1, mk_rsrc(w1, DM * FF * 2), (512 + r0) * (FF * 2), 0xfffffffffull);
  dma16m(c.ldsb + (528 + 2 * w) * WP, rr * (DM * 2) + c2, mk_rsrc(w2, FF * DM * 2), (4096 + 64 * xcd + r0) * (DM * 2), 0xfffffffffull);
  if (w == 0) dma16m(c.ldsb + LB1, c1, mk_rsrc(b1, FF * 2), 0, 0x3ffffull);
  if (w == 1) dma16(c.ldsb + LB2, c.lane * 16, mk_rsrc(b2, DM * 2), 0);
  if (w == 2) dma16m(c.ldsb + LB2 + 1024, c.lane * 16, mk_rsrc(b2, DM * 2), 1024, 0xffull);
}
DI void park_read(Ctx& c, u32x2 (&tl)[9], u32x2 (&tl2)[9]) {
  const unsigned ad = c.ldsb + (512 + (c.lane >> 2)) * WP + (c.lane & 3) * 8;
#pragma unroll
  for (int a = 0; a < 9; ++a) { tl[a] = __builtin_bit_cast(u32x2, lds_tr(ad + a * 32)); tl2[a] = __builtin_bit_cast(u32x2, lds_tr(ad + 16 * WP + a * 32)); }
  SB();
  cnt_add(c, fre(0, 4));
#pragma unroll
  for (int a = 0; a < 9; ++a) { PIN(tl[a]); PIN(tl2[a]); }
}
constexpr int KK(int NK, int v) { return NK == 18 ? (v < 2 ? 16 + v : 4 * (3 - (v - 2) / 4) + (v - 2) % 4) : 4 * (3 - v / 4) + v % 4; }
constexpr bool QSTART(int NK, int v) { return v < NK && v > 0 && (KK(NK, v) & 3) == 0; }
constexpr bool QEND(int NK, int v) { return (KK(NK, v) & 3) == 3 || KK(NK, v) == 17; }
DI unsigned x_voff(Ctx& c) { const unsigned g = c.lane >> 4; return (g >> 1) * 1024 + ((g & 1) * 32 + (c.lane & 15)) * 16; }
template <int IMM> DI void gld16c2(u32x4& d0, u32x4& d1, unsigned voff, rsrc_t r, unsigned soff) {
  asm volatile("s_nop 4\n\tbuffer_load_dwordx4 %0, %2, %3, %4 offen offset:%5 sc1\n\tbuffer_load_dwordx4 %1, %2, %3, %4 offen offset:%6 sc1"
               : "=&v"(d0), "=&v"(d1) : "v"(voff), "s"(r), "s"(soff), "i"(IMM), "i"(IMM + 256) : "memory");
}
template <int NK, int V> DI void x_ld(Ctx& c, u32x4 (&x)[2], unsigned voff, unsigned so, unsigned so2) {
  constexpr int k = KK(NK, V);
  gld16c2<(k & 1) * 2048>(x[0], x[1], voff, c.rws, k < 16 ? so + (k >> 1) * 4096 : so2);
}
template <int NK> DI void x_pro(Ctx& c, u32x4 (&x)[XD][2], unsigned voff, unsigned so, unsigned so2) {
  static_for<XD>([&](auto V_) AI { x_ld<NK, decltype(V_)::v>(c, x[decltype(V_)::v], voff, so, so2); });
}
DI void x_ld4_masked(u32x4& a, u32x4& b, u32x4& d, u32x4& e, unsigned voff, rsrc_t r, unsigned so, unsigned mask) {
  asm volatile("s_mov_b32 exec_lo, %7\n\ts_mov_b32 exec_hi, %7\n\ts_nop 4\n\tbuffer_load_dwordx4 %0, %4, %5, %6 offen sc1\n\tbuffer_load_dwordx4 %1, %4, %5, %6 offen offset:256 sc1\n\t"
               "buffer_load_dwordx4 %2, %4, %5, %6 offen offset:2048 sc1\n\tbuffer_load_dwordx4 %3, %4, %5, %6 offen offset:2304 sc1\n\ts_mov_b64 exec, -1"
               : "+v"(a), "+v"(b), "+v"(d), "+v"(e) : "v"(voff), "s"(r), "s"(so), "s"(mask) : "memory");
}
DI void x_pro_masked16(Ctx& c, u32x4 (&x)[XD][2], unsigned so, unsigned mask) {
  const unsigned voff = x_voff(c);
#pragma unroll
  for (int p = 0; p < XD / 2; ++p) x_ld4_masked(x[2 * p][0], x[2 * p][1], x[2 * p + 1][0], x[2 * p + 1][1], voff, c.rws, so + (KK(16, 2 * p) >> 1) * 4096, mask);
}
template <int L> DI void wlane(unsigned& v, unsigned s) { asm volatile("v_writelane_b32 %0, %1, %2" : "+v"(v) : "s"(s), "i"(L)); }
struct Sched {
  int NK;
  constexpr int T0() const { return 9 * (NK - 4); }
  constexpr int sv(int n) const { return n < T0() ? n / 9 : NK - 4 + (n - T0()) % 4; }
  constexpr int sa(int n) const { return n < T0() ? n % 9 : ((n - T0()) / 4 == 0 ? 8 : (n - T0()) / 4 - 1); }
  constexpr int g0(int G) const { return G == 3 ? T0() + 8 : 9 * (NK - 16 + 1 + 4 * G) + 1; }
  constexpr int gn(int G) const { return G == 0 ? 4 : 9; }
  constexpr int wg(int n) const { for (int G = 0; G < 4; ++G) if (n >= g0(G) && (n - g0(G)) % 3 == 0 && (n - g0(G)) / 3 < gn(G)) return G; return -1; }
  constexpr int wp(int n) const { return (n - g0(wg(n))) / 3; }
  constexpr int gsig(int n) const { for (int G = 0; G < 3; ++G) if (n == g0(G) + 3 * (gn(G) - 1) + 27) return G; return -1; }
  constexpr int st(int n) const {
    const int m = n - T0();
    if (m == 6) return 0;
    if (m >= 13 && (m - 13) % 8 == 0) return 1 + 2 * ((m - 13) / 8);
    if (m >= 15 && (m - 15) % 8 == 0) return 2 + 2 * ((m - 15) / 8);
    return -1;
  }
  constexpr bool sp(int n) const { return n < T0() ? (n % 18 == 0 && n >= 18) : ((n - T0()) % 18 == 0); }
  constexpr int between(int n0, int n1) const {
    int cnt = 0;
    for (int i = n0; i < n1; ++i) { if (i < T0() && sa(i) == 8 && sv(i) + XD < NK) cnt += 2; if (st(i) >= 0) cnt += 1; }
    return cnt;
  }
  constexpr int younger(int tag, int n, bool ldr) const {
    int cnt = 0, last = -1;
    for (int v = 0; v < XD; ++v) { if (100 + v == tag) last = cnt + 1; cnt += 2; }
    for (int i = 0; i < n; ++i) {
      if (i < T0() && sa(i) == 8 && sv(i) + XD < NK) { if (100 + sv(i) + XD == tag) last = cnt + 1; cnt += 2; }
      if (ldr && wg(i) >= 0) { if (200 + wg(i) == tag) last = cnt; cnt += 1; }
      if (st(i) >= 0) cnt += 1;
    }
    return cnt - 1 - last;
  }
};
template <int NK, int KORD, int PH, int GST, class St>
DI void gemm_g(Ctx& c, f32x4 (&acc)[2][9], u32x4 (&x)[XD][2], u32x2 (&tl)[9], WSrc& ws, WSrc& wn, unsigned so, unsigned so2, unsigned flag_off, St&& st) {
  constexpr unsigned RT = KORD == 0 ? 16 : 8;
  constexpr int LA = 6, NS = NK * 9;
  constexpr Sched S{NK};
  constexpr bool LD = PH < 2;
  const unsigned g = c.lane >> 4, q = (c.lane & 15) >> 2, pp = c.lane & 3;
  const unsigned wrow1 = 16 * (g >> 1) + 4 * (g & 1) + q, wrow = KORD == 0 ? 4 * g + q : wrow1;
  unsigned wb[3], wt[2];
#pragma unroll
  for (int i = 0; i < 3; ++i) { wb[i] = c.ldsb + wrow * WP + pp * 8 + i * 7 * 32 * WP; PIN(wb[i]); }
  if constexpr (NK == 18) {
    wt[0] = c.ldsb + (512 + wrow1) * WP + pp * 8; wt[1] = c.ldsb + (544 + wrow1 - 8 * (g >> 1)) * WP + pp * 8;
    PIN(wt[0]); PIN(wt[1]);
  }
  unsigned voff = x_voff(c); PIN(voff);
  bf16x4 wl[8][2];
  auto rdw = [&](auto N_, auto H_) AI {
    constexpr int N = decltype(N_)::v, H = decltype(H_)::v, v = S.sv(N), a = S.sa(N), k = KK(NK, v);
    if constexpr (k < 16) wl[N & 7][H] = lds_tr(wb[k / 7] + ((k % 7) * 32 + H * RT) * WP + a * 32);
    else if constexpr (k == 16) wl[N & 7][H] = lds_tr(wt[0] + H * 8 * WP + a * 32);
    else if constexpr (H == 0) wl[N & 7][H] = lds_tr(wt[1] + a * 32);
    else {
      u32x2 t;
      asm volatile("v_mov_b64 %0, %1" : "=v"(t) : "v"(tl[a]));
      wl[N & 7][1] = __builtin_bit_cast(bf16x4, t);
    }
    if constexpr (LD && H == 1 && ((N < S.T0() && a == 8 && QEND(NK, v)) || N == NS - 1)) { SB(); cnt_add(c, fre(PH, k >> 2)); }
  };
  if constexpr (PH > 0) { if (__builtin_expect(rfl(cnt_read(c, land(PH, 4))) < 8, 0)) w_need(c, ws, 4); }
  static_for<LA>([&](auto N) AI { rdw(N, IC<0>{}); rdw(N, IC<1>{}); });
  unsigned lc = 0, lf = 0;
  SB();
  static_for<NS>([&](auto N_) AI {
    constexpr int N = decltype(N_)::v, v = S.sv(N), a = S.sa(N);
    if constexpr (N < S.T0() ? a == 0 : N < S.T0() + 4) {
      wait_vm<S.younger(100 + v, N, false)>();
      PIN(x[v % XD][0]); PIN(x[v % XD][1]);
    }
    if constexpr (N < S.T0() && a == 0) {
      if constexpr (v == 0 || v == 8) STAMPG(GST + (v >> 3));
      if constexpr (PH == 0 && v == ARRV) { if (c.w < 4) { wait_vm<2 * ARRV>(); ctx_arrive(c); } }
      constexpr bool chk2 = QSTART(NK, v + 2) && (PH > 0 || KK(NK, v + 2) < 8), chk1 = QSTART(NK, v + 1) && (PH > 0 || KK(NK, v + 1) < 8);
      if constexpr (chk1) {
        if (__builtin_expect(rfl(lc) < (PH > 0 ? 8 : 4), 0)) { if constexpr (PH > 0) w_need(c, ws, KK(NK, v + 1) >> 2); else cnt_wait(c, land(0, KK(NK, v + 1) >> 2), 4); }
        if constexpr (PH > 0 && v < 12) { if (__builtin_expect(ws.nx < 5, 0)) { wsrc_voff(ws); w_service(c, ws); } }
      }
      if constexpr (chk2) lc = cnt_read(c, land(PH, KK(NK, v + 2) >> 2));
    }
    if constexpr (LD && S.sp(N)) {
      if (wn.sg < wn.nx) { wait_vm<S.between(N - 18, N)>(); w_signal(c, wn, wn.nx); }
      if (wn.nx < 5 && rfl(lf) >= 8) w_issue(c, wn);
    }
    if constexpr (LD && S.sp(N + 5)) lf = cnt_read(c, fre(PH, 4) - 4 * (wn.nx < 4 ? wn.nx : 4));
    SB();
    const bf16x8 wf = cat(wl[N & 7][0], wl[N & 7][1]);
    acc[0][a] = MFMA16(wf, asbf(x[v % XD][0]), acc[0][a]);
    SB();
    if constexpr (N + LA < NS) rdw(IC<N + LA>{}, IC<0>{});
    SB();
    acc[1][a] = MFMA16(wf, asbf(x[v % XD][1]), acc[1][a]);
    SB();
    if constexpr (N + LA < NS) rdw(IC<N + LA>{}, IC<1>{});
    if constexpr (N < S.T0() && a == 8 && v + XD < NK) x_ld<NK, v + XD>(c, x[v % XD], voff, so, so2);
    if constexpr (S.st(N) >= 0) st(IC<S.st(N)>{});
    SB();
  });
  st(IC<7>{}); st(IC<8>{});
  STAMPG(GST + 2);
  flag_w(c, flag_off);
  if constexpr (LD) w_signal(c, wn, wn.nx);
}
DI u32x4 pack_pair(const f32x4& r0, const f32x4& r1) { return u32x4{cvtpk(r0[0], r0[1]), cvtpk(r0[2], r0[3]), cvtpk(r1[0], r1[1]), cvtpk(r1[2], r1[3])}; }
DI f32x4 relu2(const f32x4& a) {
  const float r0 = fmaxf(a[0], 0.f), r1 = fmaxf(a[1], 0.f), r2 = fmaxf(a[2], 0.f), r3 = fmaxf(a[3], 0.f);
  return f32x4{r0 * r0, r1 * r1, r2 * r2, r3 * r3};
}
DI u32x4 pack_single(const f32x4& r0, const f32x4& r1) {
  const auto a = __builtin_amdgcn_permlane32_swap(cvtpk(r0[0], r0[1]), cvtpk(r1[0], r1[1]), false, false);
  const auto b = __builtin_amdgcn_permlane32_swap(cvtpk(r0[2], r0[3]), cvtpk(r1[2], r1[3]), false, false);
  return u32x4{a[0], b[0], a[1], b[1]};
}
template <int I> DI void part_store(Ctx& c, const f32x4 (&acc)[2][9], unsigned base, unsigned so) {
  const unsigned g = c.lane >> 4;
  if constexpr (I == 0) gst16c(pack_single(acc[0][8], acc[1][8]), c.rws, (g >> 1) * (4608 - 128) + base + 1024, so);
  else { constexpr int i = (I - 1) >> 1, u = (I - 1) & 1; gst16c(pack_pair(acc[u][2 * i], acc[u][2 * i + 1]), c.rws, base, so + u * 4608 + i * 256); }
}
DI unsigned part_base(Ctx& c) { const unsigned l15 = c.lane & 15; return (l15 >> 2) * 1152 + (l15 & 3) * 16 + (c.lane >> 4) * 64; }
struct Red { u32x4 p[8]; u32x4 s; };
DI void red_load(Ctx& c, Red& d, unsigned so) {
  gld16c<1024>(d.s, (c.lane >> 3) * 36864 + (c.lane & 7) * 16, c.rws, so);
#pragma unroll
  for (int s = 0; s < 8; ++s) gld16c<0>(d.p[s], c.lane * 16, c.rws, so + s * 36864);
}
#define RED_PIN(d) asm volatile("s_waitcnt vmcnt(0)" : "+v"(d.p[0]), "+v"(d.p[1]), "+v"(d.p[2]), "+v"(d.p[3]), "+v"(d.p[4]), "+v"(d.p[5]), "+v"(d.p[6]), "+v"(d.p[7]), "+v"(d.s)::"memory")
DI void gld8(u32x2& d, unsigned voff, rsrc_t r, unsigned soff) { asm volatile("s_nop 4\n\tbuffer_load_dwordx2 %0, %1, %2, %3 offen" : "=v"(d) : "v"(voff), "s"(r), "s"(soff) : "memory"); }
DI void job_offs(Ctx& c, unsigned row, unsigned& o0, unsigned& o2) {
  const unsigned pr = c.lane >> 4, g = (c.lane >> 2) & 3, nt = c.w & 3;
  o0 = row * (DM * 2) + (nt * 128 + pr * 32 + 4 * g) * 2;
  o2 = c.lane < 8 ? row * (DM * 2) + (512 + nt * 16 + 4 * g) * 2 : OOB;
}
DI void row_load(Ctx& c, u32x2 (&r)[4], rsrc_t rs, unsigned row) {
  unsigned o0, o2; job_offs(c, row, o0, o2);
  gld8(r[0], o0, rs, 0); gld8(r[1], o0, rs, 32); gld8(r[2], o2, rs, 0); gld8(r[3], o2, rs, 16);
}
DI float sum_hi3(float x) {
  x += __uint_as_float(__builtin_amdgcn_update_dpp(0, __float_as_uint(x), 0x128, 0xf, 0xf, false));
  { auto r = __builtin_amdgcn_permlane16_swap(__float_as_uint(x), __float_as_uint(x), false, false); x = __uint_as_float(r[0]) + __uint_as_float(r[1]); }
  return xhalf_sum(x);
}
DI void red_sum(const Red& d, float (&a)[16]) {
#pragma unroll
  for (int e = 0; e < 8; ++e) a[e] = 0.f;
#pragma unroll
  for (int s = 0; s < 8; ++s) {
#pragma unroll
    for (int e = 0; e < 4; ++e) { a[2 * e] += bflo(d.p[s][e]); a[2 * e + 1] += bfhi(d.p[s][e]); }
  }
#pragma unroll
  for (int e = 0; e < 4; ++e) { a[8 + 2 * e] = sum_hi3(bflo(d.s[e])); a[9 + 2 * e] = sum_hi3(bfhi(d.s[e])); }
}
DI float sum_tok4(float x) {
  x += __uint_as_float(__builtin_amdgcn_update_dpp(0, __float_as_uint(x), 0x124, 0xf, 0xf, false));
  x += __uint_as_float(__builtin_amdgcn_update_dpp(0, __float_as_uint(x), 0x128, 0xf, 0xf, false));
  { auto r = __builtin_amdgcn_permlane16_swap(__float_as_uint(x), __float_as_uint(x), false, false); x = __uint_as_float(r[0]) + __uint_as_float(r[1]); }
  return xhalf_sum(x);
}
DI void ln2_job(Ctx& c, const Red& d, const u32x2 (&xr)[4], unsigned (&hw)[8], unsigned row0) {
  const unsigned lane = c.lane, tok = lane & 3, g = (lane >> 2) & 3, pr = lane >> 4, nt = c.w & 3;
  const unsigned stat = c.ldsb + LSTAT + (c.w >> 2) * 128;
  const bool sing = lane < 8;
  float a[16], v[16];
  red_sum(d, a);
#pragma unroll
  for (int e = 0; e < 8; ++e) {
    const unsigned xx = xr[e >> 1][e & 1];
    hw[e] = cvtpk(bflo(xx) + a[2 * e], bfhi(xx) + a[2 * e + 1]); v[2 * e] = bflo(hw[e]); v[2 * e + 1] = bfhi(hw[e]);
  }
  float s1 = 0.f, s0 = 0.f;
#pragma unroll
  for (int e = 0; e < 8; ++e) { s1 += v[e]; s0 += v[8 + e]; }
  const float mw = sum_tok4(sing ? s1 + s0 : s1) * (1.f / 144);
  float s2 = 0.f, s3 = 0.f;
#pragma unroll
  for (int e = 0; e < 8; ++e) { const float t = v[e] - mw, t2 = v[8 + e] - mw; s2 += t * t; s3 += t2 * t2; }
  s2 = sum_tok4(sing ? s2 + s3 : s2);
  if (lane < 4) *(LAS f32x2*)(size_t)(stat + (nt * 4 + lane) * 8) = f32x2{mw, s2};
  SB();
  cnt_add(c, LSYNC + C_GB + (c.w >> 2) * 4);
  cnt_wait(c, LSYNC + C_GB + (c.w >> 2) * 4, 4);
  f32x2 st[4];
#pragma unroll
  for (int i = 0; i < 4; ++i) st[i] = *(LAS f32x2*)(size_t)(stat + (i * 4 + tok) * 8);
  const float mean = (st[0][0] + st[1][0] + st[2][0] + st[3][0]) * 0.25f;
  float m2 = st[0][1] + st[1][1] + st[2][1] + st[3][1];
#pragma unroll
  for (int i = 0; i < 4; ++i) { const float t = st[i][0] - mean; m2 += 144.f * t * t; }
  const float rs = rsqrtf(m2 * (1.f / DM) + 1e-5f);
#pragma unroll
  for (int e = 0; e < 16; ++e) v[e] = (v[e] - mean) * rs;
  const unsigned row = row0 + tok, mb = row >> 5, r32 = row & 31;
  const unsigned o0 = ((mb * 36 + 2 * (nt * 4 + pr) + (g >> 1)) * 64 + (g & 1) * 32 + r32) * 16;
  const unsigned o1 = sing ? ((mb * 36 + 32 + nt) * 64 + g * 32 + r32) * 16 : OOB;
  gst16c(u32x4{cvtpk(v[0], v[1]), cvtpk(v[2], v[3]), cvtpk(v[4], v[5]), cvtpk(v[6], v[7])}, c.rws, o0, WS_HN);
  gst16c(u32x4{cvtpk(v[8], v[9]), cvtpk(v[10], v[11]), cvtpk(v[12], v[13]), cvtpk(v[14], v[15])}, c.rws, o1, WS_HN);
}

DI void phase_attn(Ctx& c, QOut& qo, WSrc& wso, u32x4 (&xr)[XD][2], const u16* wo, const u16* w1, const u16* b1, const u16* w2, const u16* b2) {
  AttnState a;
  const unsigned ldsb = c.ldsb, lane = c.lane, w = c.w, b = c.b;
  const unsigned xcd = b & 7, jj = b >> 3, head = xcd * 4 + (jj >> 3), pi = jj & 7;
  const unsigned l31 = lane & 31, h = lane >> 5;
  const unsigned kh = w >> 2, rg = kh ? 7 - w : w, partner = 7 - w;
  a.voff = lane * 16; a.ldsb = ldsb; a.w = w;
  a.kbase = WS_K + head * (SEQ * HD * 2) + w * 4096; a.vbase = WS_V + head * (SEQ * HD * 2) + w * 4096;
  a.kb[0] = ldsb + kh * 16384 + lane * 16; a.kb[1] = a.kb[0] + 32768; a.vb[0] = a.kb[0] + 65536; a.vb[1] = a.kb[0] + 98304;
  PIN(a.kb[0]); PIN(a.kb[1]); PIN(a.vb[0]); PIN(a.vb[1]);
  const unsigned nhi = 16 - pi;
  publish(c, F_KV + (head * 8 + pi) * 8);
  STAMP(7);
#pragma unroll
  for (int d = 0; d < 8; ++d) *(LAS u32x4*)(size_t)(ldsb + (kh ? LE : 98304) + rg * 8192 + d * 1024 + lane * 16) = qo.q[d];
  wait_flags(c, F_KV + head * 64, 8, 1);
  STAMP(8);
#pragma unroll
  for (int p = 0; p < 4; ++p) dma16c(ldsb + w * 4096 + p * 1024, a.voff, c.rws, a.kbase + p * 1024);
#pragma unroll
  for (int p = 0; p < 4; ++p) dma16c(ldsb + 32768 + w * 4096 + p * 1024, a.voff, c.rws, a.kbase + 32768 + p * 1024);
#pragma unroll
  for (int d = 0; d < 8; ++d) a.qf[d] = lds_b128(ldsb + 98304 + rg * 8192 + d * 1024 + lane * 16);
#pragma unroll
  for (int d = 0; d < 4; ++d) a.o[d] = f32x16{};
  a.negm = f32x16{}; a.mhat = 0.f; a.l0 = a.l1 = 0.f; a.fct = 1.f; a.pend = false;
  wait_vm<4>();
  bar();
  STAMP(9);
#pragma unroll
  for (int n = 0; n < 4; ++n) a.fr[n] = lds_b128(a.kb[0] + ((n & 1) * 8 + (n >> 1)) * 1024);
  const int dq = (int)(rg * 32 + l31) - (int)(kh * 64) - 4 * (int)h;
  float m_old = 0.f;
  unsigned dummy = 0;

  auto merge = [&](auto FIN_, unsigned sc0, unsigned sc1, unsigned qblk, float mo, float lo) AI {
    bar();
    if constexpr (decltype(FIN_)::v) w_group8(c, wso, 2);
    const float lt = xhalf_sum(lo);
    *(LAS f32x2*)(size_t)(sc0 + w * 512 + lane * 8) = f32x2{mo, lt};
    bar();
    const f32x2 ps = *(LAS f32x2*)(size_t)(sc0 + partner * 512 + lane * 8);
    const float mm = fmaxf(mo, ps[0]);
    const float fo = __builtin_amdgcn_exp2f(mo - mm), fp = __builtin_amdgcn_exp2f(ps[0] - mm);
    const float f = fo / (lt * fo + ps[1] * fp);
    bar();
    const unsigned mine = (kh ? sc1 : sc0) + (w & 3) * 8192 + lane * 16, theirs = (kh ? sc0 : sc1) + (partner & 3) * 8192 + lane * 16;
#pragma unroll
    for (int r = 0; r < 2; ++r) {
      const f32x16 x = kh ? a.o[r] : a.o[2 + r];
#pragma unroll
      for (int e = 0; e < 4; ++e)
        *(LAS f32x4*)(size_t)(mine + (r * 4 + e) * 1024) = f32x4{x[4 * e] * f, x[4 * e + 1] * f, x[4 * e + 2] * f, x[4 * e + 3] * f};
    }
    bar();
    const unsigned mb = qblk * 4 + rg;
#pragma unroll
    for (int r = 0; r < 2; ++r) {
      f32x16 x = kh ? a.o[2 + r] : a.o[r];
#pragma unroll
      for (int e = 0; e < 4; ++e) {
        const f32x4 y = *(LAS f32x4*)(size_t)(theirs + (r * 4 + e) * 1024);
#pragma unroll
        for (int k = 0; k < 4; ++k) x[4 * e + k] = x[4 * e + k] * f + y[k];
      }
      const unsigned so = WS_CTX + (mb * 256 + head * 8 + 2 * (kh * 2 + r)) * 1024;
      gst16c(pack8<0>(x), c.rws, lane * 16, so);
      gst16c(pack8<1>(x), c.rws, lane * 16, so + 1024);
    }
#pragma unroll
    for (int d = 0; d < 4; ++d) a.o[d] = f32x16{};
  };
  auto post = [&](auto X_, unsigned s) AI {
    constexpr int X = decltype(X_)::v;
    if (__builtin_expect(s == nhi, 0)) {
      merge(IC<0>{}, ldsb + 65536 + (X ^ 1) * 32768, ldsb + LE, 15 - pi, m_old, a.l0 + a.l1);
      a.l0 = a.l1 = 0.f;
      publish(c, F_CTX + ((15 - pi) * 32 + head) * 8);
    }
    if (__builtin_expect(a.pend, 0)) {
#pragma unroll
      for (int d = 0; d < 4; ++d)
#pragma unroll
        for (int r = 0; r < 16; ++r) a.o[d][r] *= a.fct;
      a.l0 *= a.fct; a.l1 *= a.fct; a.pend = false;
    }
    a.l0 = fadd_s(a.l0, a.t0); a.l1 = fadd_s(a.l1, a.t1);
    if (__builtin_expect(s + 1 == nhi, 0)) {
#pragma unroll
      for (int d = 0; d < 8; ++d) a.qf[d] = lds_b128(ldsb + LE + rg * 8192 + d * 1024 + lane * 16);
      a.negm = f32x16{};
      m_old = a.mhat;
    }
    if constexpr (X == 1) {
      const unsigned ph = s >= 13 ? s - 12 : s;
      if (__builtin_expect(s == 11, 0)) code_touch(c, dummy);
      if (__builtin_expect(ph <= 5, 0)) {
        const size_t p1 = (size_t)wo, p3 = (size_t)w1, p5 = (size_t)w2;
        w_touch(c, dummy, ph, (const void*)(ph == 1 ? p1 + 0 : ph == 3 ? p3 + 0 : p5 + 0), lane < 21 ? w * 21 + lane : 162);
      }
    }
    SB();
  };
  attn_iter<0, true, false>(c, a, 0, nhi, true, false, dq); post(IC<0>{}, 0);
  for (unsigned s = 1; s < 17; s += 2) {
    attn_iter<1, true, true>(c, a, s, nhi, s == nhi, s + 1 == nhi, dq); post(IC<1>{}, s);
    attn_iter<0, true, true>(c, a, s + 1, nhi, s + 1 == nhi, s + 2 == nhi || s + 1 == 16, dq); post(IC<0>{}, s + 1);
  }
  const unsigned mtH = 8 + (jj >> 2);
  u32x2 fl;
  flag_load(c, fl, F_CTX + (mtH * 32 + xcd * 4) * 8, 4);
  if (w == 0) *(LAS unsigned*)(size_t)(ldsb + LSYNC + lane * 4) = 0;
  attn_iter<1, false, true>(c, a, 17, nhi, false, false, dq, [&]() AI {
    asm volatile("" ::"v"(dummy));
    wsrc_setup<9>(c, wso, mk_rsrc(wo, NQ * DM * 2), DM * 2, (jj & 3) * 256, 1024 + (jj & 3) * 32, 512 * xcd, 0, 0);
    w_group8(c, wso, 3);
    park_loads(c, w1, b1, w2, b2);
    flag_check(c, fl, F_CTX + (mtH * 32 + xcd * 4) * 8, 4);
    x_pro<16>(c, xr, kh == 0 ? x_voff(c) : OOB, WS_CTX + ((mtH * 4 + (w & 3)) * 256 + xcd * 32) * 1024, 0);
  });
  STAMP(10);
  merge(IC<1>{}, ldsb, ldsb + 32768, pi, a.mhat, a.l0 + a.l1);
  wait_vm<4>();
  bar();
  STAMP(11);
}

struct TId { unsigned sp, nt, mt, nt32, mb, own, wq; bool isH; };
DI TId tids(Ctx& c) {
  fresh(c);
  TId t;
  const unsigned xcd = c.b & 7, jj = c.b >> 3;
  t.isH = c.w < 4; t.wq = c.w & 3; t.sp = xcd; t.nt = jj & 3; t.mt = (t.isH ? 8 : 0) + (jj >> 2);
  t.nt32 = xcd * 4 + t.nt; t.mb = t.mt * 4 + t.wq; t.own = t.nt * 8 + t.sp;
  return t;
}
DI void acc_zero(f32x4 (&acc)[2][9]) {
#pragma unroll
  for (int u = 0; u < 2; ++u)
#pragma unroll
    for (int a = 0; a < 9; ++a) acc[u][a] = f32x4{};
}
DI void phase_tail(Ctx& c, WSrc& wso, u32x4 (&xr)[XD][2], const u16* x, const u16* w1, const u16* b1, const u16* w2, const u16* b2, u16* out) {
  f32x4 acc[2][9]; u32x2 tl[9], tl2[9]; unsigned hw[8]; WSrc ws, wt;
  {
    const TId t = tids(c);
    const unsigned xo = WS_CTX + (t.mb * 256 + t.sp * 32) * 1024;
    if (!t.isH) { w_group(c, wso, 1); w_group(c, wso, 0); }
    park_read(c, tl, tl2);
    if (!t.isH) {
      wait_vm<18>(); ctx_arrive(c);
      wait_vm<9>(); cnt_add(c, land(0, 1));
      wait_vm<0>(); cnt_add(c, land(0, 0));
      poll_w(c, F_CTX + (t.mt * 32 + t.sp * 4) * 8, 4);
    }
    prio_hi(c);
    STAMPG(12);
    x_pro_masked16(c, xr, xo, 0u - (c.w >> 2));
    wsrc_setup(c, ws, mk_rsrc(w1, DM * FF * 2), FF * 2, t.nt32 * 256, 8192 + t.nt32 * 32, 0, 512, 1);
    acc_zero(acc);
    const unsigned pb = part_base(c), po = WS_P1 + (((t.mt * 4 + t.nt) * 8 + t.sp) * 8 + 2 * t.wq) * 4608;
    gemm_g<16, 1, 0, 13>(c, acc, xr, tl, wso, ws, xo, 0, F_P1 + (t.mb * 32 + t.own) * 8, [&](auto I) AI { part_store<decltype(I)::v>(c, acc, pb, po); });
    STAMPG(16);
  }
  {
    const TId t = tids(c);
    u32x2 xres[4]; Red rd;
    row_load(c, xres, mk_rsrc(x, SEQ * DM * 2), t.mt * 128 + t.own * 4 + (c.lane & 3));
    unsigned dummy = 0;
    w_touch(c, dummy, 5, w2, c.lane < 21 ? c.w * 21 + c.lane : 162);
    poll_srv(c, ws, F_P1 + ((t.mt * 4 + (t.own >> 3)) * 32 + t.wq * 8) * 8, 8);
    red_load(c, rd, WS_P1 + (t.mt * 256 + t.wq * 64 + (t.own >> 2)) * 4608 + (t.own & 3) * 1152);
    STAMPG(17);
    asm volatile("" ::"v"(dummy));
    PIN(xres[0]); PIN(xres[1]); PIN(xres[2]); PIN(xres[3]);
    RED_PIN(rd);
    STAMPG(18);
    ln2_job(c, rd, xres, hw, t.mt * 128 + t.own * 4);
#pragma unroll
    for (int e = 0; e < 8; ++e) PIN(hw[e]);
    STAMPG(19);
    flag_w(c, F_HN + ((t.mt * 32 + t.own) * 4 + t.wq) * 8);
    STAMPG(20);
    w_signal(c, ws, ws.nx);
    poll_srv(c, ws, F_HN + t.mb * 256, 32);
    STAMPG(21);
    const unsigned xh = WS_HN + t.mb * 36 * 1024;
    x_pro<18>(c, xr, x_voff(c), xh, xh + 32768);
    w_service(c, ws);
    wsrc_setup(c, wt, mk_rsrc(w2, FF * DM * 2), DM * 2, t.nt * 256, 1024 + t.nt * 32, 512 * t.sp, 4096 + 64 * t.sp, 2);
#pragma unroll
    for (int a = 0; a < 9; ++a) {
      const u32x2 bias = *(LAS u32x2*)(size_t)(c.ldsb + LB1 + a * 32 + (c.lane >> 4) * 8);
      acc[0][a] = acc[1][a] = f32x4{bflo(bias[0]), bfhi(bias[0]), bflo(bias[1]), bfhi(bias[1])};
    }
    const unsigned vo = x_voff(c), ho = WS_H1 + (t.mb * 288 + 8 * t.nt32) * 1024, hs = WS_H1 + (t.mb * 288 + 256 + t.nt32) * 1024;
    gemm_g<18, 0, 1, 22>(c, acc, xr, tl, ws, wt, xh, xh + 32768, F_H1 + (t.mb * 32 + t.nt32) * 8, [&](auto I_) AI {
      constexpr int I = decltype(I_)::v;
      if constexpr (I == 0) {
        const unsigned g = c.lane >> 4;
        gst16c(pack_single(relu2(acc[0][8]), relu2(acc[1][8])), c.rws, ((g & 1) * 32 + (g >> 1) * 16 + (c.lane & 15)) * 16, hs);
      } else {
        constexpr int i = (I - 1) >> 1, u = (I - 1) & 1;
        gst16c(pack_pair(relu2(acc[u][2 * i]), relu2(acc[u][2 * i + 1])), c.rws, vo, ho + i * 2048 + u * 256);
      }
    });
    STAMPG(25);
  }
  {
    const TId t = tids(c);
    poll_srv(c, wt, F_H1 + (t.mb * 32 + t.sp * 4) * 8, 4);
    STAMPG(26);
    const unsigned x1 = WS_H1 + (t.mb * 288 + t.sp * 32) * 1024, x1t = WS_H1 + (t.mb * 288 + 256 + t.sp * 4) * 1024;
    x_pro<18>(c, xr, x_voff(c), x1, x1t);
    w_service(c, wt);
    acc_zero(acc);
    const unsigned pb = part_base(c), po = WS_P2 + (((t.mt * 4 + t.nt) * 8 + t.sp) * 8 + 2 * t.wq) * 4608;
    gemm_g<18, 0, 2, 27>(c, acc, xr, tl2, wt, wt, x1, x1t, F_P2 + (t.mb * 32 + t.own) * 8, [&](auto I) AI { part_store<decltype(I)::v>(c, acc, pb, po); });
    STAMPG(30);
  }
  {
    const TId t = tids(c);
    u32x2 bb[4]; Red rd;
    {
      unsigned o0, o2; job_offs(c, 0, o0, o2); o2 = c.lane < 8 ? o2 : 0;
      bb[0] = *(LAS u32x2*)(size_t)(c.ldsb + LB2 + o0); bb[1] = *(LAS u32x2*)(size_t)(c.ldsb + LB2 + o0 + 32);
      bb[2] = *(LAS u32x2*)(size_t)(c.ldsb + LB2 + o2); bb[3] = *(LAS u32x2*)(size_t)(c.ldsb + LB2 + o2 + 16);
    }
    poll_w(c, F_P2 + ((t.mt * 4 + (t.own >> 3)) * 32 + t.wq * 8) * 8, 8);
    STAMPG(31);
    red_load(c, rd, WS_P2 + (t.mt * 256 + t.wq * 64 + (t.own >> 2)) * 4608 + (t.own & 3) * 1152);
    RED_PIN(rd);
    const rsrc_t ro = mk_rsrc(out, SEQ * DM * 2);
    float a[16]; unsigned ow[8];
    red_sum(rd, a);
#pragma unroll
    for (int e = 0; e < 8; ++e) {
      const unsigned bv = bb[e >> 1][e & 1];
      ow[e] = cvtpk(a[2 * e] + bflo(bv) + bflo(hw[e]), a[2 * e + 1] + bfhi(bv) + bfhi(hw[e]));
    }
    unsigned o0, o2; job_offs(c, t.mt * 128 + t.own * 4 + (c.lane & 3), o0, o2);
    __builtin_amdgcn_raw_buffer_store_b64(u32x2{ow[0], ow[1]}, ro, o0, 0, 0);
    __builtin_amdgcn_raw_buffer_store_b64(u32x2{ow[2], ow[3]}, ro, o0, 32, 0);
    __builtin_amdgcn_raw_buffer_store_b64(u32x2{ow[4], ow[5]}, ro, o2, 0, 0);
    __builtin_amdgcn_raw_buffer_store_b64(u32x2{ow[6], ow[7]}, ro, o2, 16, 0);
    STAMPG(32);
    if (c.b == 0 && c.w == 0) gst8c(u32x2{c.ep0, c.ep1}, c.rws, c.lane == 0 ? F_EPOCH : OOB, WS_FLAGS);
  }
}

__global__ __launch_bounds__(512, 2) void fused_block(const u16* __restrict__ x, const u16* __restrict__ ckv, const u16* __restrict__ wq,
                                                      const u16* __restrict__ wuk, const u16* __restrict__ wuv, const u16* __restrict__ wo,
                                                      const u16* __restrict__ w1, const u16* __restrict__ b1, const u16* __restrict__ w2,
                                                      const u16* __restrict__ b2, u16* __restrict__ out, char* __restrict__ ws) {
  extern __shared__ __attribute__((aligned(16))) char lds[];
  Ctx c;
  asm volatile("s_getpc_b64 %0\n.Lfb_begin:" : "=s"(c.pc0));
  c.ldsb = (unsigned)(size_t)lds; c.lane = threadIdx.x & 63; c.w = rfl(threadIdx.x >> 6); c.b = blockIdx.x;
  c.rws = mk_rsrc(ws, WS_END);
  STAMP(0);
  QOut qo;
  WSrc wso; u32x4 xr[XD][2];
  phase_qkv(c, qo, x, ckv, wq, wuk, wuv);
  fresh(c);
  phase_attn(c, qo, wso, xr, wo, w1, b1, w2, b2);
  fresh(c);
  phase_tail(c, wso, xr, x, w1, b1, w2, b2, out);
  asm volatile(".Lfb_end:");
}

extern "C" void kernel_launch(void* const* d_in, const int* in_sizes, int n_in, void* d_out, int out_size, void* d_ws, size_t ws_size,
                              hipStream_t stream) {
  hipFuncSetAttribute((const void*)fused_block, hipFuncAttributeMaxDynamicSharedMemorySize, 163840);
  fused_block<<<256, 512, 163840, stream>>>((const u16*)d_in[0], (const u16*)d_in[1], (const u16*)d_in[2], (const u16*)d_in[3], (const u16*)d_in[4],
                                            (const u16*)d_in[5], (const u16*)d_in[7], (const u16*)d_in[8], (const u16*)d_in[9], (const u16*)d_in[10],
                                            (u16*)d_out, (char*)d_ws);
}
```

```cpp
#include <hip/hip_runtime.h>
#include <stdint.h>
#include <utility>

typedef __bf16 bf16x8 __attribute__((ext_vector_type(8)));
typedef __bf16 bf16x4 __attribute__((ext_vector_type(4)));
typedef __bf16 bf16x2 __attribute__((ext_vector_type(2)));
typedef float f32x16 __attribute__((ext_vector_type(16)));
typedef float f32x4 __attribute__((ext_vector_type(4)));
typedef float f32x2 __attribute__((ext_vector_type(2)));
typedef unsigned u32x4 __attribute__((ext_vector_type(4)));
typedef unsigned u32x2 __attribute__((ext_vector_type(2)));
typedef unsigned short u16;
typedef unsigned long long u64;
#define LAS __attribute__((address_space(3)))
#define DI __device__ __forceinline__
#define AI __attribute__((always_inline))
typedef __amdgpu_buffer_rsrc_t rsrc_t;

constexpr int SEQ = 2048, DM = 576, NH = 32, HD = 128, DC = 512, FF = 4608, NQ = 4096;

constexpr unsigned WS_FLAGS = 0;
constexpr unsigned WS_XN = 131072;
constexpr unsigned WS_K = WS_XN + SEQ * DM * 2;
constexpr unsigned WS_V = WS_K + SEQ * NQ * 2;
constexpr unsigned WS_CTX = WS_V + SEQ * NQ * 2;
constexpr unsigned WS_P1 = WS_CTX + SEQ * NQ * 2;
constexpr unsigned WS_H = WS_P1 + 8 * SEQ * DM * 2;
constexpr unsigned WS_HN = WS_H + SEQ * DM * 2;
constexpr unsigned WS_H1 = WS_HN + SEQ * DM * 2;
constexpr unsigned WS_P2 = WS_H1 + SEQ * FF * 2;
constexpr unsigned WS_END = WS_P2 + 8 * SEQ * DM * 2;
constexpr unsigned F_EPOCH = 0, F_XN = 4096, F_KV = 8192, F_CTX = 12288, F_P1 = 16384, F_HN = 32768, F_H1 = 49152, F_P2 = 65536, F_K = 81920, F_V = 90112;

template <int... Is> using iseq = std::integer_sequence<int, Is...>;
template <int N> using make_iseq = std::make_integer_sequence<int, N>;
template <int I> struct IC { static constexpr int v = I; };
template <class F, int... Is> DI void static_for_(F&& f, iseq<Is...>) { (f(IC<Is>{}), ...); }
template <int N, class F> DI void static_for(F&& f) { static_for_(f, make_iseq<N>{}); }

#define SB() __builtin_amdgcn_sched_barrier(0)
#define PIN(x) asm volatile("" : "+v"(x))
DI unsigned rfl(unsigned x) { return __builtin_amdgcn_readfirstlane(x); }
DI rsrc_t mk_rsrc(const void* p, unsigned bytes) { return __builtin_amdgcn_make_buffer_rsrc((void*)p, (short)0, (int)bytes, 0x00020000); }
constexpr unsigned OOB = 0xFFFFFF00u;
DI void dma16(unsigned lds_addr, unsigned voff, rsrc_t r, unsigned soff) {
  asm volatile("s_mov_b32 m0, %0\n\ts_nop 3\n\tbuffer_load_dwordx4 %1, %2, %3 offen lds" ::"s"(lds_addr), "v"(voff), "s"(r), "s"(soff) : "memory", "m0");
}
DI void dma16c(unsigned lds_addr, unsigned voff, rsrc_t r, unsigned soff) {
  asm volatile("s_mov_b32 m0, %0\n\ts_nop 3\n\tbuffer_load_dwordx4 %1, %2, %3 offen sc1 lds" ::"s"(lds_addr), "v"(voff), "s"(r), "s"(soff) : "memory", "m0");
}
template <int IMM> DI void gld16c(u32x4& d, unsigned voff, rsrc_t r, unsigned soff) {
  asm volatile("s_nop 4\n\tbuffer_load_dwordx4 %0, %1, %2, %3 offen offset:%4 sc1" : "=v"(d) : "v"(voff), "s"(r), "s"(soff), "i"(IMM) : "memory");
}
template <int IMM> DI void gld16(u32x4& d, unsigned voff, rsrc_t r, unsigned soff) {
  asm volatile("s_nop 4\n\tbuffer_load_dwordx4 %0, %1, %2, %3 offen offset:%4" : "=v"(d) : "v"(voff), "s"(r), "s"(soff), "i"(IMM) : "memory");
}
DI void gld8c(u32x2& d, unsigned voff, rsrc_t r, unsigned soff) {
  asm volatile("s_nop 4\n\tbuffer_load_dwordx2 %0, %1, %2, %3 offen sc1" : "=v"(d) : "v"(voff), "s"(r), "s"(soff) : "memory");
}
DI void gst16c(u32x4 v, rsrc_t r, unsigned voff, unsigned soff) {
  asm volatile("s_nop 4\n\tbuffer_store_dwordx4 %0, %1, %2, %3 offen sc1\n\ts_nop 1" ::"v"(v), "v"(voff), "s"(r), "s"(soff) : "memory");
}
DI void gst8c(u32x2 v, rsrc_t r, unsigned voff, unsigned soff) { __builtin_amdgcn_raw_buffer_store_b64(v, r, voff, soff, 16); }
DI void fput(u32x2 v, rsrc_t r, unsigned voff) { asm volatile("s_nop 4\n\tbuffer_atomic_swap_x2 %0, %1, %2, 0 offen sc1\n\ts_nop 1" ::"v"(v), "v"(voff), "s"(r) : "memory"); }
DI void fget(u32x2& d, rsrc_t r, unsigned voff) {
  d = u32x2{0u, 0u};
  asm volatile("s_nop 4\n\tbuffer_atomic_add_x2 %0, %1, %2, 0 offen sc0 sc1" : "+v"(d) : "v"(voff), "s"(r) : "memory");
}
template <int N> DI void wait_vm() { asm volatile("s_waitcnt vmcnt(%0)" ::"i"(N) : "memory"); }
DI void bar() { asm volatile("s_waitcnt lgkmcnt(0)" ::: "memory"); __builtin_amdgcn_s_barrier(); asm volatile("" ::: "memory"); }
DI unsigned cvtpk(float lo, float hi) { f32x2 v = {lo, hi}; bf16x2 b = __builtin_convertvector(v, bf16x2); return __builtin_bit_cast(unsigned, b); }
DI float bflo(unsigned x) { return __uint_as_float(x << 16); }
DI float bfhi(unsigned x) { return __uint_as_float(x & 0xffff0000u); }
DI bf16x8 lds_b128(unsigned a) { return *(LAS bf16x8*)(size_t)a; }
DI bf16x4 lds_tr(unsigned a) { return __builtin_amdgcn_ds_read_tr16_b64_v4bf16((LAS bf16x4*)(size_t)a); }
DI bf16x8 cat(bf16x4 a, bf16x4 b) { return __builtin_shufflevector(a, b, 0, 1, 2, 3, 4, 5, 6, 7); }
DI bf16x8 asbf(u32x4 x) { return __builtin_bit_cast(bf16x8, x); }
#define MFMA32(a, b, c) __builtin_amdgcn_mfma_f32_32x32x16_bf16((a), (b), (c), 0, 0, 0)
#define MFMA16(a, b, c) __builtin_amdgcn_mfma_f32_16x16x32_bf16((a), (b), (c), 0, 0, 0)
template <int S> DI u32x4 pack8(const f32x16& x, float sc) {
  u32x4 w;
  w[0] = cvtpk(x[8 * S + 0] * sc, x[8 * S + 1] * sc); w[1] = cvtpk(x[8 * S + 2] * sc, x[8 * S + 3] * sc);
  w[2] = cvtpk(x[8 * S + 4] * sc, x[8 * S + 5] * sc); w[3] = cvtpk(x[8 * S + 6] * sc, x[8 * S + 7] * sc);
  return w;
}
template <int S> DI u32x4 pack8(const f32x16& x) {
  u32x4 w;
  w[0] = cvtpk(x[8 * S + 0], x[8 * S + 1]); w[1] = cvtpk(x[8 * S + 2], x[8 * S + 3]);
  w[2] = cvtpk(x[8 * S + 4], x[8 * S + 5]); w[3] = cvtpk(x[8 * S + 6], x[8 * S + 7]);
  return w;
}
DI float max3f(float a, float b, float c) { float r; asm("v_max3_f32 %0, %1, %2, %3" : "=v"(r) : "v"(a), "v"(b), "v"(c)); return r; }
DI float max2f(float a, float b) { float r; asm("v_max_f32_e32 %0, %1, %2" : "=v"(r) : "v"(a), "v"(b)); return r; }
DI float fadd_s(float a, float b) { float r; asm("v_add_f32_e32 %0, %1, %2" : "=v"(r) : "v"(a), "v"(b)); return r; }
DI float xhalf_sum(float x) {
  auto r = __builtin_amdgcn_permlane32_swap(__float_as_uint(x), __float_as_uint(x), false, false);
  return __uint_as_float(r[0]) + __uint_as_float(r[1]);
}
DI float wave_sum(float x) {
#pragma unroll
  for (int o = 1; o < 64; o <<= 1) x += __shfl_xor(x, o);
  return x;
}

#define STAMP(i)

struct Ctx {
  unsigned ldsb, w, lane, b;
  unsigned ep0, ep1;
  rsrc_t rws;
  u64 pc0;
};

DI void fresh(Ctx& c) {
  asm volatile("v_mbcnt_lo_u32_b32 %0, -1, 0\n\tv_mbcnt_hi_u32_b32 %0, -1, %0" : "=v"(c.lane));
  asm volatile("" : "+s"(c.w), "+s"(c.b), "+s"(c.ldsb));
}
DI void code_touch(Ctx& c, unsigned& dummy) {
  unsigned bytes;
  asm volatile("s_mov_b32 %0, .Lfb_end-.Lfb_begin" : "=s"(bytes));
  const rsrc_t r = mk_rsrc((const void*)c.pc0, bytes);
  asm volatile("s_nop 4\n\tbuffer_load_dword %0, %1, %2, 0 offen" : "+v"(dummy) : "v"(c.w == 0 ? (c.lane * 32 + (c.b >> 3)) * 128 : OOB), "s"(r) : "memory");
}
DI void publish(Ctx& c, unsigned flag_off) {
  wait_vm<0>();
  bar();
  fput(u32x2{c.ep0, c.ep1}, c.rws, (c.w == 0 && c.lane == 0) ? flag_off : OOB);
}
DI void poll_flags(Ctx& c, unsigned flag_off, unsigned n, unsigned stride) {
  const unsigned voff = c.lane < n ? flag_off + c.lane * 8 * stride : OOB;
  const u64 t0 = __builtin_amdgcn_s_memrealtime();
  for (;;) {
    u32x2 f;
    fget(f, c.rws, voff);
    asm volatile("s_waitcnt vmcnt(0)" : "+v"(f)::"memory");
    if (__all((f[0] == c.ep0 && f[1] == c.ep1) || c.lane >= n)) break;
    if (__builtin_amdgcn_s_memrealtime() - t0 > 5000000ull) break;
    __builtin_amdgcn_s_sleep(2);
  }
}
DI void wait_flags(Ctx& c, unsigned flag_off, unsigned n, unsigned stride) {
  if (c.w == 0) poll_flags(c, flag_off, n, stride);
  bar();
}

#define DBG_DRAIN()
#define DBG_NX(x)
#define STAMPG(i)
DI void flag_store(Ctx& c, unsigned flag_off) {
  fput(u32x2{c.ep0, c.ep1}, c.rws, (c.w == 0 && c.lane == 0) ? flag_off : OOB);
}
DI void flag_load(Ctx& c, u32x2& f, unsigned flag_off, unsigned n) { fget(f, c.rws, c.lane < n ? flag_off + c.lane * 8 : OOB); }
DI void flag_check(Ctx& c, u32x2& f, unsigned flag_off, unsigned n) {
  PIN(f);
  if (__builtin_expect(!__all((f[0] == c.ep0 && f[1] == c.ep1) || c.lane >= n), 0)) poll_flags(c, flag_off, n, 1);
}

constexpr float QSCALE = 0.08838834764831845f * 1.4426950408889634f;
constexpr unsigned LE = 131072;
constexpr unsigned LX = 147456;
constexpr unsigned KW = LX + 12288;
constexpr unsigned SLOT = 49152;
constexpr int slot_of(int S) { return (S + 2) % 3; }
constexpr int XQ = 6;
constexpr int LAQ = 6;
constexpr int QB1 = 40, QB2 = 88, QB3 = 116;
constexpr int Q_KPOLL = 70, Q_VPOLL = 134, Q_VST = 12;
constexpr int vm_wait(int pos, int tag) {
  int seq[512] = {}; int n = 0;
  auto push = [&](int t, int cnt) { for (int i = 0; i < cnt; ++i) seq[n++] = t; };
  push(0, 6); push(-1, 3); push(1, 6); push(2, 6);
  bool done = false;
  for (int T = 0; T < 32 && !done; ++T) {
    if (pos == T) { done = true; break; }
    if (T == 1) push(-6, 2);
    if (T == 3) push(-2, 2);
    if (T == 7) push(-3, 1);
    if (T == 19) push(-5, 1);
    if (T == 27) push(-8, 1);
    if (T >= 16 && T < 24) push(-7, 1);
    if (T >= 1) push((T - 1) / 2 + 3, 3);
    if (T >= 29) push(200 + 2 * (T - 29), 2), push(201 + 2 * (T - 29), 2);
  }
  for (int q = 0; q < 144 && !done; ++q) {
    if (pos == 100 + q) break;
    if (q == QB3) push(-11, 1);
    if (q == 1 || q == 4 || q == 6) push(18, 1);
    if (tag == -9 && q == Q_KPOLL) push(-9, 1);
    if (q % Q_VST == 2 && q / Q_VST < 8) push(-10, 1);
    if ((q & 7) == 7 && (q >> 3) + XQ < 18) push(200 + (q >> 3) + XQ, 2);
  }
  int last = -1;
  for (int i = 0; i < n; ++i) if (seq[i] == tag) last = i;
  return n - 1 - last;
}
constexpr int imin(int a, int b) { return a < b ? a : b; }
DI unsigned lds_add_rtn(unsigned addr) {
  unsigned r;
  asm volatile("s_mov_b64 exec, 1\n\tds_add_rtn_u32 %0, %1, %2\n\ts_mov_b64 exec, -1\n\ts_waitcnt lgkmcnt(0)" : "=&v"(r) : "v"(addr), "v"(1u) : "memory");
  return rfl(r);
}
DI unsigned lds_word(unsigned addr) { return __hip_atomic_load((LAS unsigned*)(size_t)addr, __ATOMIC_RELAXED, __HIP_MEMORY_SCOPE_WORKGROUP); }
DI void lds_set(unsigned addr, unsigned v) { __hip_atomic_store((LAS unsigned*)(size_t)addr, v, __ATOMIC_RELAXED, __HIP_MEMORY_SCOPE_WORKGROUP); }
DI float wave_sum64(float x) {
  x += __uint_as_float(__builtin_amdgcn_update_dpp(0, __float_as_uint(x), 0xB1, 0xf, 0xf, false));
  x += __uint_as_float(__builtin_amdgcn_update_dpp(0, __float_as_uint(x), 0x4E, 0xf, 0xf, false));
  x += __uint_as_float(__builtin_amdgcn_update_dpp(0, __float_as_uint(x), 0x124, 0xf, 0xf, false));
  x += __uint_as_float(__builtin_amdgcn_update_dpp(0, __float_as_uint(x), 0x128, 0xf, 0xf, false));
  { auto r = __builtin_amdgcn_permlane16_swap(__float_as_uint(x), __float_as_uint(x), false, false); x = __uint_as_float(r[0]) + __uint_as_float(r[1]); }
  return xhalf_sum(x);
}
DI void ln1_row(Ctx& c, const u32x4& xr0, const u32x4& xr1) {
  const unsigned lane = c.lane;
  float v[16];
#pragma unroll
  for (int e = 0; e < 4; ++e) { v[2 * e] = bflo(xr0[e]); v[2 * e + 1] = bfhi(xr0[e]); v[8 + 2 * e] = bflo(xr1[e]); v[9 + 2 * e] = bfhi(xr1[e]); }
  float s = 0.f;
#pragma unroll
  for (int e = 0; e < 16; ++e) s += v[e];
  const float mean = wave_sum64(s) * (1.f / DM);
  float qq = 0.f;
#pragma unroll
  for (int e = 0; e < 16; ++e) { v[e] -= mean; qq += v[e] * v[e]; }
  qq = lane < 36 ? qq : 0.f;
  const float rs = rsqrtf(wave_sum64(qq) * (1.f / DM) + 1e-5f);
  u32x4 o0, o1;
#pragma unroll
  for (int e = 0; e < 4; ++e) { o0[e] = cvtpk(v[2 * e] * rs, v[2 * e + 1] * rs); o1[e] = cvtpk(v[8 + 2 * e] * rs, v[9 + 2 * e] * rs); }
  const unsigned ad = c.ldsb + LX + (lane < 36 ? (2 * lane * 8 + c.w) * 16 : 9216 + (lane - 36) * 16);
  *(LAS u32x4*)(size_t)ad = o0;
  *(LAS u32x4*)(size_t)(ad + 128) = o1;
}
DI void xn_store(Ctx& c) {
  const unsigned w = c.w, lane = c.lane, mb = c.b >> 2, r0 = (c.b & 3) * 8;
  const unsigned c0 = 9 * w + (lane >> 3), c1 = 9 * w + 8;
  const u32x4 a0 = *(LAS u32x4*)(size_t)(c.ldsb + LX + (9 * w * 8 + lane) * 16);
  const u32x4 a1 = *(LAS u32x4*)(size_t)(c.ldsb + LX + (c1 * 8 + (lane & 7)) * 16);
  gst16c(a0, c.rws, ((mb * 36 + (c0 >> 1)) * 64 + (c0 & 1) * 32 + r0 + (lane & 7)) * 16, WS_XN);
  gst16c(a1, c.rws, lane < 8 ? ((mb * 36 + (c1 >> 1)) * 64 + (c1 & 1) * 32 + r0 + lane) * 16 : OOB, WS_XN);
}

struct QOut { u32x4 q[8]; bool kdone; };
DI u32x4 pack_pair(const f32x4& r0, const f32x4& r1) { return u32x4{cvtpk(r0[0], r0[1]), cvtpk(r0[2], r0[3]), cvtpk(r1[0], r1[1]), cvtpk(r1[2], r1[3])}; }
template <int IMM> DI void gld16c2(u32x4& d0, u32x4& d1, unsigned voff, rsrc_t r, unsigned soff) {
  asm volatile("s_nop 4\n\tbuffer_load_dwordx4 %0, %2, %3, %4 offen offset:%5 sc1\n\tbuffer_load_dwordx4 %1, %2, %3, %4 offen offset:%6 sc1"
               : "=&v"(d0), "=&v"(d1) : "v"(voff), "s"(r), "s"(soff), "i"(IMM), "i"(IMM + 256) : "memory");
}
DI unsigned x_voff(Ctx& c) { const unsigned g = c.lane >> 4; return (g >> 1) * 1024 + ((g & 1) * 32 + (c.lane & 15)) * 16; }

DI void phase_qkv(Ctx& c, QOut& qo, const u16* x, const u16* ckv, const u16* wq, const u16* wuk, const u16* wuv) {
  const unsigned w = c.w, lane = c.lane, ldsb = c.ldsb, b = c.b;
  const unsigned xcd = b & 7, jj = b >> 3, head = xcd * 4 + (jj >> 3), cb = jj & 7;
  const rsrc_t rxx = mk_rsrc(x, SEQ * DM * 2), rx = mk_rsrc(ckv, SEQ * DC * 2);
  const rsrc_t rwk = mk_rsrc(wuk, DC * NQ * 2), rwv = mk_rsrc(wuv, DC * NQ * 2), rwq = mk_rsrc(wq, DM * NQ * 2);
  unsigned dummy = 0;
  code_touch(c, dummy);
  const unsigned voffX0 = (lane >> 3) * 1024 + (((lane & 7) ^ (lane >> 4)) << 4), voffX1 = voffX0 ^ 64;
  const unsigned voffW = (lane >> 3) * 8192 + (((((lane >> 1) & 3) ^ (((lane >> 4) & 1) | ((w & 1) << 1))) << 5) | ((lane & 1) << 4));
  const unsigned sX = (cb * 256 + 32 * w) * 1024, sW = (8 * (w & 3)) * 8192 + (head * 128 + (w >> 2) * 64) * 2;
  const unsigned rot = 2 * (cb & 3);
  const unsigned dX = ldsb + 4 * w * 1024, dW = ldsb + w * 1024;
  auto issue = [&](auto U_, auto P_) AI {
    constexpr int U = decltype(U_)::v, P = decltype(P_)::v;
    constexpr unsigned so = slot_of(U) * SLOT;
    if constexpr (U < 16) {
      const unsigned tr = (U + rot) & 7;
      constexpr int p = P < 2 ? P : P - 1;
      if constexpr (P % 3 != 2) dma16(dX + so + p * 1024, (p & 1) ? voffX1 : voffX0, rx, sX + 128 * tr + (U < 8 ? 8 * p : 16 * (p & 1) + 8 * (p >> 1)) * 1024);
      else dma16(dW + so + 32768 + (P / 3) * 8192, voffW, U < 8 ? rwk : rwv, sW + (64 * tr + 32 * (P / 3)) * 8192);
    } else dma16(dW + so + (U == 18 ? 5 - P : P) * 8192, voffW, rwq, sW + 32 * (6 * (U - 16) + P) * 8192);
  };
  u32x2 ep; u32x4 xr0, xr1;
  static_for<3>([&](auto U_) AI {
    static_for<6>([&](auto P_) AI { issue(U_, P_); });
    if constexpr (decltype(U_)::v == 0) {
      const unsigned vo = lane < 36 ? lane * 32 : OOB;
      gld8c(ep, F_EPOCH, c.rws, WS_FLAGS);
      gld16<0>(xr0, vo, rxx, (b * 8 + w) * (DM * 2));
      gld16<16>(xr1, vo, rxx, (b * 8 + w) * (DM * 2));
    }
  });
  const unsigned kh = w >> 2, rg = kh ? 7 - w : w;
  const unsigned qmb = (kh ? cb : 15 - cb) * 4 + rg;
  const unsigned wm = w >> 1, wn = w & 1, g = lane >> 4, l15 = lane & 15, q = l15 >> 2, pp = lane & 3;
  unsigned xb[3][2], wb[3][4], wqb[3][4];
  {
    const unsigned sg = ((q >> 1) & 1) | ((g & 1) << 1);
#pragma unroll
    for (int i = 0; i < 3; ++i) {
      xb[i][0] = ldsb + i * SLOT + (wm * 64 + l15) * 128 + ((g ^ (l15 >> 1)) << 4); xb[i][1] = xb[i][0] ^ 64; PIN(xb[i][0]); PIN(xb[i][1]);
#pragma unroll
      for (int j = 0; j < 4; ++j) { wb[i][j] = ldsb + i * SLOT + wn * 4096 + (8 * g + q) * 128 + ((j ^ sg) << 5) + pp * 8; PIN(wb[i][j]); }
    }
  }
  wait_vm<vm_wait(0, 0)>();
  asm volatile("" ::"v"(dummy));
  bar();
  STAMP(2);
  f32x4 acc[4][4];
  bf16x8 xf[2][4]; bf16x4 wl[2][4][2];
  u32x4 pk[8], pv[8], xq[XQ][2]; u32x2 xnf; unsigned td0, td1;
  auto rd = [&](auto T_, auto R_) AI {
    constexpr int T = decltype(T_)::v, R = decltype(R_)::v, B = T & 1, sl = slot_of(T >> 1);
    constexpr int K = R % 3, I = R / 3;
    if constexpr (K == 0) xf[B][I] = lds_b128(xb[sl][B] + I * 2048);
    else wl[B][I][K - 1] = lds_tr(wb[sl][I] + 32768 + B * 8192 + (K - 1) * 512);
  };
  bf16x4 wq8[8][2];
  auto rdq = [&](auto N_, auto H_) AI {
    constexpr int N = decltype(N_)::v, H = decltype(H_)::v, v = N >> 3, a = N & 7, sl = slot_of(16 + v / 6);
    wq8[N & 7][H] = lds_tr(wqb[sl][a & 3] + (v >= 12 ? 17 - v : v % 6) * 8192 + (a >> 2) * 4096 + H * 512);
  };
  const unsigned xvo = x_voff(c), xso = WS_XN + qmb * 36 * 1024;
  auto ldx = [&](auto V_) AI { constexpr int v = decltype(V_)::v; gld16c2<(v & 1) * 2048>(xq[v % XQ][0], xq[v % XQ][1], xvo, c.rws, xso + (v >> 1) * 4096); };
  const unsigned kst = WS_K + ((head * 64 + cb * 8 + wm * 2) * 8 + 4 * wn) * 1024, vst = WS_V + (((head * 32 + cb * 4 + wm) * 4 + 2 * wn) * 4) * 1024;
  static_for<12>([&](auto R) AI { rd(IC<0>{}, R); });
  SB();
  static_for<32>([&](auto T_) AI {
    constexpr int T = decltype(T_)::v, B = T & 1;
    if constexpr (B) { wait_vm<vm_wait(T, (T >> 1) + 1)>(); bar(); }
    if constexpr (T == 1) {
      PIN(ep); PIN(xr0); PIN(xr1);
      u64 z = (((u64)rfl(ep[1]) << 32) | rfl(ep[0])) + 0x9E3779B97F4A7C15ull;
      z = (z ^ (z >> 30)) * 0xBF58476D1CE4E5B9ull; z = (z ^ (z >> 27)) * 0x94D049BB133111EBull; z ^= z >> 31;
      c.ep0 = (unsigned)z; c.ep1 = (unsigned)(z >> 32);
      asm volatile("s_nop 4\n\tbuffer_load_dword %0, %1, %2, 0 offen" : "=v"(td0) : "v"(lane < 16 ? (b * 128 + w * 16 + lane) * 128 : OOB), "s"(rwv) : "memory");
      asm volatile("s_nop 4\n\tbuffer_load_dword %0, %1, %2, 0 offen" : "=v"(td1) : "v"(lane < 18 ? (b * 144 + w * 18 + lane) * 128 : OOB), "s"(rwq) : "memory");
      if (w == 0) lds_set(ldsb + KW + (lane & 3) * 4, 0);
    }
    if constexpr (T == 1 || T == 2) { if (kh == T - 1) ln1_row(c, xr0, xr1); }
    if constexpr (T == 3) xn_store(c);
    if constexpr (T == 7) { asm volatile("" ::"v"(td0), "v"(td1)); flag_store(c, F_XN + b * 8); }
    if constexpr (T == 19) flag_load(c, xnf, F_XN + qmb * 32, 4);
    if constexpr (T == 27) { flag_store(c, F_K + (head * 8 + cb) * 8); flag_check(c, xnf, F_XN + qmb * 32, 4); }
    if constexpr (T == 31) {
#pragma unroll
      for (int i = 0; i < 3; ++i)
#pragma unroll
        for (int j = 0; j < 4; ++j) { wqb[i][j] = wb[i][j] - wn * 4096; PIN(wqb[i][j]); }
    }
    if constexpr (T == 8 || T == 16 || T == 24) STAMP(100 + T / 2);
    SB();
    static_for<16>([&](auto M_) AI {
      constexpr int M = decltype(M_)::v, i = M >> 2, j = M & 3;
      const bf16x8 wf = cat(wl[B][j][0], wl[B][j][1]);
      const f32x4 cin = (T & 15) == 0 ? f32x4{} : acc[i][j];
      acc[i][j] = T < 16 ? MFMA16(wf, xf[B][i], cin) : MFMA16(xf[B][i], wf, cin);
      SB();
      if constexpr (M < 12) {
        if constexpr (T < 31) rd(IC<T + 1>{}, IC<M>{});
        else rdq(IC<(M >> 1)>{}, IC<(M & 1)>{});
      }
      if constexpr (M == 1 && T >= 16 && T < 24) {
        constexpr int e = T - 16;
        gst16c(pk[e], c.rws, xvo, kst + (e >> 2) * 8192 + (e & 1) * 2048 + ((e >> 1) & 1) * 256);
      }
      if constexpr (T >= 1 && (M == 4 || M == 8 || M == 12)) issue(IC<(T - 1) / 2 + 3>{}, IC<(B ? 0 : 3) + (M >> 2) - 1>{});
      if constexpr (T >= 29 && (M == 13 || M == 15)) ldx(IC<2 * (T - 29) + (M == 15)>{});
      SB();
    });
    if constexpr (T == 15) {
#pragma unroll
      for (int e = 0; e < 8; ++e) { pk[e] = pack_pair(acc[e >> 1][2 * (e & 1)], acc[e >> 1][2 * (e & 1) + 1]); PIN(pk[e]); }
    }
    if constexpr (T == 31) {
#pragma unroll
      for (int e = 0; e < 8; ++e) { pv[e] = pack_pair(acc[2 * (e >> 2)][e & 3], acc[2 * (e >> 2) + 1][e & 3]); PIN(pv[e]); }
    }
  });
  STAMP(3);
  f32x4 qa[2][8];
  u32x2 kf; bool kdone = false;
  const unsigned kso = WS_K + head * (SEQ * HD * 2) + w * 4096;
  static_for<144>([&](auto N_) AI {
    constexpr int N = decltype(N_)::v, v = N >> 3, a = N & 7;
    if constexpr (a == 0) { wait_vm<vm_wait(100 + N, 200 + v)>(); PIN(xq[v % XQ][0]); PIN(xq[v % XQ][1]); }
    if constexpr (N == QB1) { wait_vm<vm_wait(100 + N, 17)>(); bar(); SB(); }
    if constexpr (N == QB2) {
      wait_vm<vm_wait(100 + N, 18)>();
      if (w == 0) wait_vm<vm_wait(100 + N, -9)>();
      PIN(kf);
      if (w == 0 && __all((kf[0] == c.ep0 && kf[1] == c.ep1) || lane >= 8)) lds_set(ldsb + KW + 8, 1);
      bar();
      kdone = rfl(lds_word(ldsb + KW + 8)) != 0;
      SB();
    }
    if constexpr (N == QB3) { wait_vm<vm_wait(100 + N, -10)>(); bar(); flag_store(c, F_V + (head * 8 + cb) * 8); SB(); }
    const bf16x8 wf = cat(wq8[N & 7][0], wq8[N & 7][1]);
    qa[0][a] = MFMA16(wf, asbf(xq[v % XQ][0]), v == 0 ? f32x4{} : qa[0][a]);
    SB();
    if constexpr (N + LAQ < 144) rdq(IC<N + LAQ>{}, IC<0>{});
    SB();
    qa[1][a] = MFMA16(wf, asbf(xq[v % XQ][1]), v == 0 ? f32x4{} : qa[1][a]);
    SB();
    if constexpr (N + LAQ < 144) rdq(IC<N + LAQ>{}, IC<1>{});
    if constexpr (N == 1 || N == 4 || N == 6) issue(IC<18>{}, IC<(N == 1 ? 3 : N == 4 ? 4 : 5)>{});
    if constexpr (N == Q_KPOLL || N == Q_VPOLL) {
      kf = u32x2{0u, 0u};
      asm volatile("s_mov_b32 exec_lo, %3\n\ts_mov_b32 exec_hi, 0\n\ts_nop 4\n\tbuffer_atomic_add_x2 %0, %1, %2, 0 offen sc0 sc1\n\ts_mov_b64 exec, -1"
                   : "+v"(kf) : "v"((N == Q_KPOLL ? F_K : F_V) + (head * 8 + lane) * 8), "s"(c.rws), "s"(w == 0 ? 0xffu : 0u) : "memory");
    }
    if constexpr (N % Q_VST == 2 && N / Q_VST < 8) {
      constexpr int e = N / Q_VST;
      gst16c(pv[e], c.rws, xvo, vst + ((e >> 1) & 1) * 4096 + (e >> 2) * 2048 + (e & 1) * 256);
    }
    if constexpr (a == 7 && v + XQ < 18) ldx(IC<v + XQ>{});
    if constexpr (N > QB3 && N <= QB3 + 16 && (N - QB3) % 2 == 0) {
      constexpr int p = (N - QB3) / 2 - 1;
      if (kdone) dma16c(ldsb + (p >> 2) * 32768 + w * 4096 + (p & 3) * 1024, lane * 16, c.rws, kso + (p >> 2) * 32768 + (p & 3) * 1024);
    }
    SB();
  });
#pragma unroll
  for (int A = 0; A < 4; ++A) {
    u32x4 p0, p1;
#pragma unroll
    for (int e = 0; e < 4; ++e) {
      const int t = 2 * A + (e >> 1), k = 2 * (e & 1);
      const unsigned u0 = cvtpk(qa[0][t][k] * QSCALE, qa[0][t][k + 1] * QSCALE), u1 = cvtpk(qa[1][t][k] * QSCALE, qa[1][t][k + 1] * QSCALE);
      const auto s = __builtin_amdgcn_permlane16_swap(u0, u1, false, false);
      const auto r = __builtin_amdgcn_permlane32_swap(s[0], s[1], false, false);
      p0[e] = r[0]; p1[e] = r[1];
    }
    qo.q[2 * A] = p0; qo.q[2 * A + 1] = p1;
  }
  qo.kdone = kdone;
  STAMP(6);
  if (w == 0) wait_vm<0>();
  flag_check(c, kf, F_V + head * 64, w == 0 ? 8 : 0);
}

constexpr float THR = 32.f;

struct AttnState {
  bf16x8 qf[8];
  f32x16 o[4];
  f32x16 S[2][2];
  f32x16 negm;
  u32x4 pk[4];
  bf16x8 fr[8];
  float mhat, l0, l1, fct, mx0, mx1, t0, t1;
  bool pend;
  unsigned kb[2], vb[2];
  unsigned voff, ldsb, w, kbase, vbase;
};

constexpr int RD0[16] = {4, 5, 6, 7, 9, 10, 12, 13, 15, -1, -1, -1, -1, -1, -1, -1};
constexpr int RD1[16] = {-1, -1, -1, 8, -1, 11, -1, 14, -1, -1, -1, -1, -1, -1, -1, -1};

struct NoHook { template <class T> DI void operator()(T) const {} };
template <int X, bool QK, bool PV, bool VDMA = true, class Hook = NoHook>
DI void attn_iter(Ctx& c, AttnState& a, const unsigned s, const unsigned nhi, const bool first, const bool diag, const int dq, Hook&& hook = Hook{}) {
  constexpr int Y = X ^ 1;
  auto kaddr = [&](int slot, int n) AI { return a.kb[slot] + ((n & 1) * 8 + (n >> 1)) * 1024; };
  auto vaddr = [&](int slot, int n) AI { return a.vb[slot] + ((n & 3) * 4 + (n >> 2)) * 1024; };
  unsigned sk = s + 2; sk = sk > 16 ? 16 : sk; sk = sk < nhi ? sk : sk - nhi;
  unsigned sv = s > 16 ? 16 : s; sv = sv < nhi ? sv : sv - nhi;
  const unsigned sok = a.kbase + sk * 32768, sov = a.vbase + sv * 32768;
  const unsigned dk = a.ldsb + X * 32768 + a.w * 4096, dv = dk + 65536;
  static_for<16>([&](auto M_) AI {
    constexpr int M = decltype(M_)::v, d = M >> 1, kb = M & 1;
    if constexpr (QK) {
      if constexpr (d == 0) a.S[X][kb] = MFMA32(a.fr[M & 7], a.qf[d], a.negm);
      else a.S[X][kb] = MFMA32(a.fr[M & 7], a.qf[d], a.S[X][kb]);
      if constexpr (RD0[M] >= 0) a.fr[RD0[M] & 7] = lds_b128(kaddr(X, RD0[M]));
      if constexpr (RD1[M] >= 0) a.fr[RD1[M] & 7] = lds_b128(kaddr(X, RD1[M]));
      if constexpr (VDMA && (M == 0 || M == 2 || M == 6 || M == 9)) { constexpr int P = M == 0 ? 0 : M == 2 ? 1 : M == 6 ? 2 : 3; dma16c(dv + P * 1024, a.voff, c.rws, sov + P * 1024); }
    }
    if constexpr (PV) {
      if constexpr (M < 8) {
        a.S[Y][1][2 * M] = __builtin_amdgcn_exp2f(a.S[Y][1][2 * M]);
        a.S[Y][1][2 * M + 1] = __builtin_amdgcn_exp2f(a.S[Y][1][2 * M + 1]);
        a.pk[M >> 2][M & 3] = cvtpk(a.S[Y][0][2 * M], a.S[Y][0][2 * M + 1]);
        PIN(a.pk[M >> 2][M & 3]);
        if constexpr (M == 0) { a.l0 = fadd_s(a.l0, a.S[Y][0][14]); a.l1 = fadd_s(a.l1, a.S[Y][0][15]); }
      } else {
        constexpr int e = M - 8;
        a.pk[2 + (e >> 2)][e & 3] = cvtpk(a.S[Y][1][2 * e], a.S[Y][1][2 * e + 1]);
        PIN(a.pk[2 + (e >> 2)][e & 3]);
        a.l0 = fadd_s(a.l0, a.S[Y][1][2 * e]); a.l1 = fadd_s(a.l1, a.S[Y][1][2 * e + 1]);
      }
    }
    if constexpr (M == 11) { SB(); if constexpr (QK) wait_vm<8>(); else wait_vm<0>(); hook(IC<0>{}); bar(); hook(IC<1>{}); SB(); }
    if constexpr (PV && M >= 12) a.fr[(M - 12) & 7] = lds_b128(vaddr(Y, M - 12));
    SB();
  });
  static_for<16>([&](auto M_) AI {
    constexpr int M = decltype(M_)::v, ks = M >> 2, db = M & 3;
    if constexpr (PV) {
      a.o[db] = MFMA32(a.fr[M & 7], asbf(a.pk[ks]), a.o[db]);
      if constexpr (RD0[M] >= 0) a.fr[RD0[M] & 7] = lds_b128(vaddr(Y, RD0[M]));
      if constexpr (RD1[M] >= 0) a.fr[RD1[M] & 7] = lds_b128(vaddr(Y, RD1[M]));
    }
    if constexpr (QK) {
      if constexpr (M == 0 || M == 1 || M == 6 || M == 9) { constexpr int P = M == 0 ? 0 : M == 1 ? 1 : M == 6 ? 2 : 3; dma16c(dk + P * 1024, a.voff, c.rws, sok + P * 1024); }
      if constexpr (M == 2) {
        asm volatile("s_nop 7\n\ts_nop 3" : "+v"(a.S[X][0]), "+v"(a.S[X][1]));
        if (__builtin_expect(diag, 0)) {
          int dq2 = dq; PIN(dq2);
#pragma unroll
          for (int r = 0; r < 16; ++r) {
            const int cr = (r & 3) + 8 * (r >> 2);
            if (cr > dq2) a.S[X][0][r] = -__builtin_inff();
            if (cr + 32 > dq2) a.S[X][1][r] = -__builtin_inff();
          }
        }
      }
      if constexpr (M >= 2 && M < 6) {
        constexpr int r = 4 * (M - 2);
        if constexpr (M == 2) {
          a.mx0 = max3f(a.S[X][0][0], a.S[X][0][1], a.S[X][1][0]); a.mx1 = max3f(a.S[X][0][2], a.S[X][0][3], a.S[X][1][1]);
          a.mx0 = max3f(a.mx0, a.S[X][1][2], a.S[X][1][3]);
        } else {
          a.mx0 = max3f(a.mx0, a.S[X][0][r], a.S[X][0][r + 1]); a.mx1 = max3f(a.mx1, a.S[X][0][r + 2], a.S[X][0][r + 3]);
          a.mx0 = max3f(a.mx0, a.S[X][1][r], a.S[X][1][r + 1]); a.mx1 = max3f(a.mx1, a.S[X][1][r + 2], a.S[X][1][r + 3]);
        }
      }
      if constexpr (M == 6) {
        const float mm = max2f(a.mx0, a.mx1);
        auto pr = __builtin_amdgcn_permlane32_swap(__float_as_uint(mm), __float_as_uint(mm), false, false);
        a.mx0 = max2f(__uint_as_float(pr[0]), __uint_as_float(pr[1]));
      }
      if constexpr (M == 7) {
        if (__builtin_expect(first || __any(a.mx0 > THR), 0)) {
          const float dl = first ? fmaxf(a.mx0, -1e30f) : fmaxf(a.mx0, 0.f);
          a.mhat = first ? dl : a.mhat + dl;
          a.fct = __builtin_amdgcn_exp2f(-dl);
#pragma unroll
          for (int r = 0; r < 16; ++r) { a.S[X][0][r] -= dl; a.S[X][1][r] -= dl; a.negm[r] = -a.mhat; }
          a.pend = !first;
        }
      }
      if constexpr (M >= 8) {
        constexpr int e = M - 8;
        a.S[X][0][2 * e] = __builtin_amdgcn_exp2f(a.S[X][0][2 * e]);
        a.S[X][0][2 * e + 1] = __builtin_amdgcn_exp2f(a.S[X][0][2 * e + 1]);
        PIN(a.S[X][0][2 * e]); PIN(a.S[X][0][2 * e + 1]);
        if constexpr (e > 0) {
          a.t0 = e == 1 ? a.S[X][0][0] : fadd_s(a.t0, a.S[X][0][2 * e - 2]); a.t1 = e == 1 ? a.S[X][0][1] : fadd_s(a.t1, a.S[X][0][2 * e - 1]);
          PIN(a.t0); PIN(a.t1);
        }
      }
      if constexpr (M == 11) { SB(); wait_vm<8>(); bar(); }
      if constexpr (M >= 12) a.fr[(M - 12) & 7] = lds_b128(kaddr(Y, M - 12));
    }
    SB();
  });
}

constexpr unsigned WP = 288;
constexpr unsigned LSYNC = 161280;
constexpr unsigned C_LAND = 0, C_FREE = 64, C_GB = 128, C_CTXD = 136, LSTAT = LSYNC + 256;
#define ARRV 4
constexpr int XD = 8;
DI unsigned land(unsigned ph, unsigned q) { return LSYNC + C_LAND + (ph * 5 + q) * 4; }
DI unsigned fre(unsigned ph, unsigned q) { return LSYNC + C_FREE + (ph * 5 + q) * 4; }
DI void cnt_add(Ctx& c, unsigned off) {
  asm volatile("s_mov_b64 exec, 1\n\tds_add_u32 %0, %1\n\ts_mov_b64 exec, -1" ::"v"(c.ldsb + off), "v"(1u) : "memory");
}
DI unsigned cnt_add_rtn(Ctx& c, unsigned off) {
  unsigned r;
  asm volatile("s_mov_b64 exec, 1\n\tds_add_rtn_u32 %0, %1, %2\n\ts_mov_b64 exec, -1\n\ts_waitcnt lgkmcnt(0)" : "=&v"(r) : "v"(c.ldsb + off), "v"(1u) : "memory");
  return rfl(r);
}
DI unsigned cnt_read(Ctx& c, unsigned off) { return __hip_atomic_load((LAS unsigned*)(size_t)(c.ldsb + off), __ATOMIC_RELAXED, __HIP_MEMORY_SCOPE_WORKGROUP); }
DI void prio_lo() { __builtin_amdgcn_s_setprio(0); }
#define PRIO_L
DI void prio_hi(Ctx& c) { if (c.w < 4) __builtin_amdgcn_s_setprio(1); else __builtin_amdgcn_s_setprio(2); }
DI void cnt_wait(Ctx& c, unsigned off, unsigned target) {
  asm volatile("" ::: "memory");
  unsigned spins = 0;
#pragma unroll 1
  while (rfl(cnt_read(c, off)) < target) { __builtin_amdgcn_s_sleep(1); if (++spins > (1u << 20)) break; }
  asm volatile("" ::: "memory");
}
DI void flag_w(Ctx& c, unsigned flag_off) {
  wait_vm<0>();
  fput(u32x2{c.ep0, c.ep1}, c.rws, c.lane == 0 ? flag_off : OOB);
}
DI void ctx_arrive(Ctx& c) {
  if (cnt_add_rtn(c, LSYNC + C_CTXD) == 7) {
    const unsigned jj = c.b >> 3;
    fput(u32x2{c.ep0, c.ep1}, c.rws, c.lane == 0 ? F_CTX + ((jj & 7) * 32 + (c.b & 7) * 4 + (jj >> 3)) * 8 : OOB);
  }
}

DI void dma16m(unsigned lds_addr, unsigned voff, rsrc_t r, unsigned soff, u64 mask) {
  asm volatile("s_mov_b32 m0, %0\n\ts_mov_b64 exec, %4\n\ts_nop 3\n\tbuffer_load_dwordx4 %1, %2, %3 offen lds\n\ts_mov_b64 exec, -1" ::"s"(lds_addr), "v"(voff), "s"(r), "s"(soff), "s"(mask)
               : "memory", "m0");
}
DI void w_touch(Ctx& c, unsigned& dummy, unsigned ph, const void* wp, unsigned l2) {
  const unsigned xcd = c.b & 7, jj = c.b >> 3;
  unsigned off;
  if (ph == 3) off = (jj * 18 + l2 / 9) * (FF * 2) + (l2 % 9 < 8 ? xcd * 1024 + (l2 % 9) * 128 : 8192 + xcd * 128);
  else off = l2 < 144 ? xcd * 589824 + (jj * 144 + l2) * 128 : ph == 5 ? (4096 + 64 * xcd) * (DM * 2) + (jj * 18 + l2 - 144) * 128 : OOB;
  const rsrc_t rp = mk_rsrc(wp, ph == 1 ? NQ * DM * 2 : FF * DM * 2);
  asm volatile("s_nop 4\n\tbuffer_load_dword %0, %1, %2, 0 offen" : "+v"(dummy) : "v"(l2 < 162 ? off : OOB), "s"(rp) : "memory");
}
struct WSrc { unsigned voff[9]; rsrc_t r; unsigned main, tail, pitch, colA, colB; unsigned nx, sg, ph; };
template <int NV = 5> DI void wsrc_voff(WSrc& s) {
  unsigned lane;
  asm volatile("v_mbcnt_lo_u32_b32 %0, -1, 0\n\tv_mbcnt_hi_u32_b32 %0, -1, %0" : "=v"(lane));
#pragma unroll
  for (int p = 0; p < NV; ++p) {
    const unsigned ci = lane + 64 * p, rr = ci / 18, cc = ci - rr * 18;
    s.voff[p] = rr * s.pitch + (cc < 16 ? s.colA + cc * 16 : s.colB + (cc - 16) * 16);
  }
}
template <int NV = 5> DI void wsrc_setup(Ctx& c, WSrc& s, rsrc_t r, unsigned pitch, unsigned colA, unsigned colB, unsigned rowMain, unsigned rowTail, unsigned ph) {
  s.r = r; s.pitch = pitch; s.main = rowMain * pitch; s.tail = rowTail * pitch;
  s.colA = colA; s.colB = colB; s.ph = ph; s.nx = s.sg = 0;
  wsrc_voff<NV>(s);
}
DI void w_group(Ctx& c, const WSrc& s, unsigned q) {
  const unsigned r0 = 128 * q + 32 * (c.w & 3);
#pragma unroll
  for (int p = 0; p < 9; ++p) dma16(c.ldsb + r0 * WP + p * 1024, s.voff[p], s.r, s.main + r0 * s.pitch);
}
DI void w_group8(Ctx& c, const WSrc& s, unsigned q) {
  const unsigned r0 = 128 * q + 16 * c.w;
#pragma unroll
  for (int p = 0; p < 4; ++p) dma16(c.ldsb + r0 * WP + p * 1024, s.voff[p], s.r, s.main + r0 * s.pitch);
  dma16m(c.ldsb + r0 * WP + 4096, s.voff[4], s.r, s.main + r0 * s.pitch, 0xffffffffull);
}
DI void poll_w(Ctx& c, unsigned flag_off, unsigned n) {
  prio_lo();
  poll_flags(c, flag_off, n, 1);
  prio_hi(c);
}
DI void w_group4(Ctx& c, const WSrc& s) {
  const unsigned r0 = 6 * c.w, sk = 8 * s.pitch;
  dma16(c.ldsb + (512 + r0) * WP, s.voff[0], s.r, s.tail + r0 * s.pitch + (c.w == 7 ? sk : 0));
  dma16m(c.ldsb + (512 + r0) * WP + 1024, s.voff[1] + ((c.w == 6 && c.lane >= 8) ? sk : 0), s.r, s.tail + r0 * s.pitch + (c.w == 7 ? sk : 0), 0xfffffffffffull);
}
DI void w_issue(Ctx& c, WSrc& s) { if (s.nx == 0) w_group4(c, s); else w_group8(c, s, 4 - s.nx); ++s.nx; }
DI bool w_ready(Ctx& c, const WSrc& s) { return s.nx < 5 && rfl(cnt_read(c, fre(s.ph - 1, 4 - s.nx))) >= 8; }
DI void w_service(Ctx& c, WSrc& s) {
#pragma unroll 1
  while (w_ready(c, s)) w_issue(c, s);
}
DI void w_signal(Ctx& c, WSrc& s, unsigned nx) {
#pragma unroll 1
  while (s.sg < nx) { cnt_add(c, land(s.ph, 4 - s.sg)); ++s.sg; }
}
DI void w_need(Ctx& c, WSrc& s, unsigned q) {
  wsrc_voff(s);
#pragma unroll 1
  while (s.nx <= 4 - q) { cnt_wait(c, fre(s.ph - 1, 4 - s.nx), 8); w_issue(c, s); }
  if (s.sg < s.nx) { wait_vm<0>(); w_signal(c, s, s.nx); }
  cnt_wait(c, land(s.ph, q), 8);
}
DI void poll_srv(Ctx& c, WSrc& s, unsigned flag_off, unsigned n) {
  const unsigned voff = c.lane < n ? flag_off + c.lane * 8 : OOB;
  const u64 t0 = __builtin_amdgcn_s_memrealtime();
  prio_lo();
  for (;;) {
    u32x2 f;
    fget(f, c.rws, voff);
    const unsigned nx = s.nx;
    const bool more = w_ready(c, s);
    if (more) { w_issue(c, s); if (nx == 0) wait_vm<2>(); else wait_vm<5>(); } else wait_vm<0>();
    w_signal(c, s, nx);
    PIN(f);
    if (__all((f[0] == c.ep0 && f[1] == c.ep1) || c.lane >= n)) break;
    if (__builtin_amdgcn_s_memrealtime() - t0 > 5000000ull) break;
    if (!more) __builtin_amdgcn_s_sleep(1);
  }
  prio_hi(c);
}
constexpr unsigned LB1 = LSTAT + 256, LB2 = LB1 + 320;
DI void park_loads(Ctx& c, const u16* w1, const u16* b1, const u16* w2, const u16* b2) {
  const unsigned xcd = c.b & 7, nt = (c.b >> 3) & 3, nt32 = xcd * 4 + nt, w = c.w;
  const unsigned rr = c.lane >= 18 ? 1 : 0, cc = c.lane - 18 * rr, r0 = (w < 4 ? 40 : 48) + 2 * w;
  const unsigned c1 = cc < 16 ? nt32 * 256 + cc * 16 : 8192 + nt32 * 32 + (cc - 16) * 16, c2 = cc < 16 ? nt * 256 + cc * 16 : 1024 + nt * 32 + (cc - 16) * 16;
  dma16m(c.ldsb + (512 + 2 * w) * WP, rr * (FF * 2) + c1, mk_rsrc(w1, DM * FF * 2), (512 + r0) * (FF * 2), 0xfffffffffull);
  dma16m(c.ldsb + (528 + 2 * w) * WP, rr * (DM * 2) + c2, mk_rsrc(w2, FF * DM * 2), (4096 + 64 * xcd + r0) * (DM * 2), 0xfffffffffull);
  if (w == 0) dma16m(c.ldsb + LB1, c1, mk_rsrc(b1, FF * 2), 0, 0x3ffffull);
  if (w == 1) dma16(c.ldsb + LB2, c.lane * 16, mk_rsrc(b2, DM * 2), 0);
  if (w == 2) dma16m(c.ldsb + LB2 + 1024, c.lane * 16, mk_rsrc(b2, DM * 2), 1024, 0xffull);
}
DI void park_read(Ctx& c, u32x2 (&tl)[9], u32x2 (&tl2)[9]) {
  const unsigned ad = c.ldsb + (512 + (c.lane >> 2)) * WP + (c.lane & 3) * 8;
#pragma unroll
  for (int a = 0; a < 9; ++a) { tl[a] = __builtin_bit_cast(u32x2, lds_tr(ad + a * 32)); tl2[a] = __builtin_bit_cast(u32x2, lds_tr(ad + 16 * WP + a * 32)); }
  SB();
  cnt_add(c, fre(0, 4));
#pragma unroll
  for (int a = 0; a < 9; ++a) { PIN(tl[a]); PIN(tl2[a]); }
}
constexpr int KK(int NK, int v) { return NK == 18 ? (v < 2 ? 16 + v : 4 * (3 - (v - 2) / 4) + (v - 2) % 4) : 4 * (3 - v / 4) + v % 4; }
constexpr bool QSTART(int NK, int v) { return v < NK && v > 0 && (KK(NK, v) & 3) == 0; }
constexpr bool QEND(int NK, int v) { return (KK(NK, v) & 3) == 3 || KK(NK, v) == 17; }
template <int NK, int V> DI void x_ld(Ctx& c, u32x4 (&x)[2], unsigned voff, unsigned so, unsigned so2) {
  constexpr int k = KK(NK, V);
  gld16c2<(k & 1) * 2048>(x[0], x[1], voff, c.rws, k < 16 ? so + (k >> 1) * 4096 : so2);
}
template <int NK> DI void x_pro(Ctx& c, u32x4 (&x)[XD][2], unsigned voff, unsigned so, unsigned so2) {
  static_for<XD>([&](auto V_) AI { x_ld<NK, decltype(V_)::v>(c, x[decltype(V_)::v], voff, so, so2); });
}
DI void x_ld4_masked(u32x4& a, u32x4& b, u32x4& d, u32x4& e, unsigned voff, rsrc_t r, unsigned so, unsigned mask) {
  asm volatile("s_mov_b32 exec_lo, %7\n\ts_mov_b32 exec_hi, %7\n\ts_nop 4\n\tbuffer_load_dwordx4 %0, %4, %5, %6 offen sc1\n\tbuffer_load_dwordx4 %1, %4, %5, %6 offen offset:256 sc1\n\t"
               "buffer_load_dwordx4 %2, %4, %5, %6 offen offset:2048 sc1\n\tbuffer_load_dwordx4 %3, %4, %5, %6 offen offset:2304 sc1\n\ts_mov_b64 exec, -1"
               : "+v"(a), "+v"(b), "+v"(d), "+v"(e) : "v"(voff), "s"(r), "s"(so), "s"(mask) : "memory");
}
DI void x_pro_masked16(Ctx& c, u32x4 (&x)[XD][2], unsigned so, unsigned mask) {
  const unsigned voff = x_voff(c);
#pragma unroll
  for (int p = 0; p < XD / 2; ++p) x_ld4_masked(x[2 * p][0], x[2 * p][1], x[2 * p + 1][0], x[2 * p + 1][1], voff, c.rws, so + (KK(16, 2 * p) >> 1) * 4096, mask);
}
template <int L> DI void wlane(unsigned& v, unsigned s) { asm volatile("v_writelane_b32 %0, %1, %2" : "+v"(v) : "s"(s), "i"(L)); }
struct Sched {
  int NK;
  constexpr int T0() const { return 9 * (NK - 4); }
  constexpr int sv(int n) const { return n < T0() ? n / 9 : NK - 4 + (n - T0()) % 4; }
  constexpr int sa(int n) const { return n < T0() ? n % 9 : ((n - T0()) / 4 == 0 ? 8 : (n - T0()) / 4 - 1); }
  constexpr int g0(int G) const { return G == 3 ? T0() + 8 : 9 * (NK - 16 + 1 + 4 * G) + 1; }
  constexpr int gn(int G) const { return G == 0 ? 4 : 9; }
  constexpr int wg(int n) const { for (int G = 0; G < 4; ++G) if (n >= g0(G) && (n - g0(G)) % 3 == 0 && (n - g0(G)) / 3 < gn(G)) return G; return -1; }
  constexpr int wp(int n) const { return (n - g0(wg(n))) / 3; }
  constexpr int gsig(int n) const { for (int G = 0; G < 3; ++G) if (n == g0(G) + 3 * (gn(G) - 1) + 27) return G; return -1; }
  constexpr int st(int n) const {
    const int m = n - T0();
    if (m == 6) return 0;
    if (m >= 13 && (m - 13) % 8 == 0) return 1 + 2 * ((m - 13) / 8);
    if (m >= 15 && (m - 15) % 8 == 0) return 2 + 2 * ((m - 15) / 8);
    return -1;
  }
  constexpr bool sp(int n) const { return n < T0() ? (n % 18 == 0 && n >= 18) : ((n - T0()) % 18 == 0); }
  constexpr int between(int n0, int n1) const {
    int cnt = 0;
    for (int i = n0; i < n1; ++i) { if (i < T0() && sa(i) == 8 && sv(i) + XD < NK) cnt += 2; if (st(i) >= 0) cnt += 1; }
    return cnt;
  }
  constexpr int younger(int tag, int n, bool ldr) const {
    int cnt = 0, last = -1;
    for (int v = 0; v < XD; ++v) { if (100 + v == tag) last = cnt + 1; cnt += 2; }
    for (int i = 0; i < n; ++i) {
      if (i < T0() && sa(i) == 8 && sv(i) + XD < NK) { if (100 + sv(i) + XD == tag) last = cnt + 1; cnt += 2; }
      if (ldr && wg(i) >= 0) { if (200 + wg(i) == tag) last = cnt; cnt += 1; }
      if (st(i) >= 0) cnt += 1;
    }
    return cnt - 1 - last;
  }
};
template <int NK, int KORD, int PH, int GST, class St>
DI void gemm_g(Ctx& c, f32x4 (&acc)[2][9], u32x4 (&x)[XD][2], u32x2 (&tl)[9], WSrc& ws, WSrc& wn, unsigned so, unsigned so2, unsigned flag_off, St&& st) {
  constexpr unsigned RT = KORD == 0 ? 16 : 8;
  constexpr int LA = 6, NS = NK * 9;
  constexpr Sched S{NK};
  constexpr bool LD = PH < 2;
  const unsigned g = c.lane >> 4, q = (c.lane & 15) >> 2, pp = c.lane & 3;
  const unsigned wrow1 = 16 * (g >> 1) + 4 * (g & 1) + q, wrow = KORD == 0 ? 4 * g + q : wrow1;
  unsigned wb[3], wt[2];
#pragma unroll
  for (int i = 0; i < 3; ++i) { wb[i] = c.ldsb + wrow * WP + pp * 8 + i * 7 * 32 * WP; PIN(wb[i]); }
  if constexpr (NK == 18) {
    wt[0] = c.ldsb + (512 + wrow1) * WP + pp * 8; wt[1] = c.ldsb + (544 + wrow1 - 8 * (g >> 1)) * WP + pp * 8;
    PIN(wt[0]); PIN(wt[1]);
  }
  unsigned voff = x_voff(c); PIN(voff);
  bf16x4 wl[8][2];
  auto rdw = [&](auto N_, auto H_) AI {
    constexpr int N = decltype(N_)::v, H = decltype(H_)::v, v = S.sv(N), a = S.sa(N), k = KK(NK, v);
    if constexpr (k < 16) wl[N & 7][H] = lds_tr(wb[k / 7] + ((k % 7) * 32 + H * RT) * WP + a * 32);
    else if constexpr (k == 16) wl[N & 7][H] = lds_tr(wt[0] + H * 8 * WP + a * 32);
    else if constexpr (H == 0) wl[N & 7][H] = lds_tr(wt[1] + a * 32);
    else {
      u32x2 t;
      asm volatile("v_mov_b64 %0, %1" : "=v"(t) : "v"(tl[a]));
      wl[N & 7][1] = __builtin_bit_cast(bf16x4, t);
    }
    if constexpr (LD && H == 1 && ((N < S.T0() && a == 8 && QEND(NK, v)) || N == NS - 1)) { SB(); cnt_add(c, fre(PH, k >> 2)); }
  };
  if constexpr (PH > 0) { if (__builtin_expect(rfl(cnt_read(c, land(PH, 4))) < 8, 0)) w_need(c, ws, 4); }
  static_for<LA>([&](auto N) AI { rdw(N, IC<0>{}); rdw(N, IC<1>{}); });
  unsigned lc = 0, lf = 0;
  SB();
  static_for<NS>([&](auto N_) AI {
    constexpr int N = decltype(N_)::v, v = S.sv(N), a = S.sa(N);
    if constexpr (N < S.T0() ? a == 0 : N < S.T0() + 4) {
      wait_vm<S.younger(100 + v, N, false)>();
      PIN(x[v % XD][0]); PIN(x[v % XD][1]);
    }
    if constexpr (N < S.T0() && a == 0) {
      if constexpr (v == 0 || v == 8) STAMPG(GST + (v >> 3));
      if constexpr (PH == 0 && v == ARRV) { if (c.w < 4) { wait_vm<2 * ARRV>(); ctx_arrive(c); } }
      constexpr bool chk2 = QSTART(NK, v + 2) && (PH > 0 || KK(NK, v + 2) < 8), chk1 = QSTART(NK, v + 1) && (PH > 0 || KK(NK, v + 1) < 8);
      if constexpr (chk1) {
        if (__builtin_expect(rfl(lc) < (PH > 0 ? 8 : 4), 0)) { if constexpr (PH > 0) w_need(c, ws, KK(NK, v + 1) >> 2); else cnt_wait(c, land(0, KK(NK, v + 1) >> 2), 4); }
        if constexpr (PH > 0 && v < 12) { if (__builtin_expect(ws.nx < 5, 0)) { wsrc_voff(ws); w_service(c, ws); } }
      }
      if constexpr (chk2) lc = cnt_read(c, land(PH, KK(NK, v + 2) >> 2));
    }
    if constexpr (LD && S.sp(N)) {
      if (wn.sg < wn.nx) { wait_vm<S.between(N - 18, N)>(); w_signal(c, wn, wn.nx); }
      if (wn.nx < 5 && rfl(lf) >= 8) w_issue(c, wn);
    }
    if constexpr (LD && S.sp(N + 5)) lf = cnt_read(c, fre(PH, 4) - 4 * (wn.nx < 4 ? wn.nx : 4));
    SB();
    const bf16x8 wf = cat(wl[N & 7][0], wl[N & 7][1]);
    acc[0][a] = MFMA16(wf, asbf(x[v % XD][0]), acc[0][a]);
    SB();
    if constexpr (N + LA < NS) rdw(IC<N + LA>{}, IC<0>{});
    SB();
    acc[1][a] = MFMA16(wf, asbf(x[v % XD][1]), acc[1][a]);
    SB();
    if constexpr (N + LA < NS) rdw(IC<N + LA>{}, IC<1>{});
    if constexpr (N < S.T0() && a == 8 && v + XD < NK) x_ld<NK, v + XD>(c, x[v % XD], voff, so, so2);
    if constexpr (S.st(N) >= 0) st(IC<S.st(N)>{});
    SB();
  });
  st(IC<7>{}); st(IC<8>{});
  STAMPG(GST + 2);
  flag_w(c, flag_off);
  if constexpr (LD) w_signal(c, wn, wn.nx);
}
DI f32x4 relu2(const f32x4& a) {
  const float r0 = fmaxf(a[0], 0.f), r1 = fmaxf(a[1], 0.f), r2 = fmaxf(a[2], 0.f), r3 = fmaxf(a[3], 0.f);
  return f32x4{r0 * r0, r1 * r1, r2 * r2, r3 * r3};
}
DI u32x4 pack_single(const f32x4& r0, const f32x4& r1) {
  const auto a = __builtin_amdgcn_permlane32_swap(cvtpk(r0[0], r0[1]), cvtpk(r1[0], r1[1]), false, false);
  const auto b = __builtin_amdgcn_permlane32_swap(cvtpk(r0[2], r0[3]), cvtpk(r1[2], r1[3]), false, false);
  return u32x4{a[0], b[0], a[1], b[1]};
}
template <int I> DI void part_store(Ctx& c, const f32x4 (&acc)[2][9], unsigned base, unsigned so) {
  const unsigned g = c.lane >> 4;
  if constexpr (I == 0) gst16c(pack_single(acc[0][8], acc[1][8]), c.rws, (g >> 1) * (4608 - 128) + base + 1024, so);
  else { constexpr int i = (I - 1) >> 1, u = (I - 1) & 1; gst16c(pack_pair(acc[u][2 * i], acc[u][2 * i + 1]), c.rws, base, so + u * 4608 + i * 256); }
}
DI unsigned part_base(Ctx& c) { const unsigned l15 = c.lane & 15; return (l15 >> 2) * 1152 + (l15 & 3) * 16 + (c.lane >> 4) * 64; }
struct Red { u32x4 p[8]; u32x4 s; };
DI void red_load(Ctx& c, Red& d, unsigned so) {
  gld16c<1024>(d.s, (c.lane >> 3) * 36864 + (c.lane & 7) * 16, c.rws, so);
#pragma unroll
  for (int s = 0; s < 8; ++s) gld16c<0>(d.p[s], c.lane * 16, c.rws, so + s * 36864);
}
#define RED_PIN(d) asm volatile("s_waitcnt vmcnt(0)" : "+v"(d.p[0]), "+v"(d.p[1]), "+v"(d.p[2]), "+v"(d.p[3]), "+v"(d.p[4]), "+v"(d.p[5]), "+v"(d.p[6]), "+v"(d.p[7]), "+v"(d.s)::"memory")
DI void gld8(u32x2& d, unsigned voff, rsrc_t r, unsigned soff) { asm volatile("s_nop 4\n\tbuffer_load_dwordx2 %0, %1, %2, %3 offen" : "=v"(d) : "v"(voff), "s"(r), "s"(soff) : "memory"); }
DI void job_offs(Ctx& c, unsigned row, unsigned& o0, unsigned& o2) {
  const unsigned pr = c.lane >> 4, g = (c.lane >> 2) & 3, nt = c.w & 3;
  o0 = row * (DM * 2) + (nt * 128 + pr * 32 + 4 * g) * 2;
  o2 = c.lane < 8 ? row * (DM * 2) + (512 + nt * 16 + 4 * g) * 2 : OOB;
}
DI void row_load(Ctx& c, u32x2 (&r)[4], rsrc_t rs, unsigned row) {
  unsigned o0, o2; job_offs(c, row, o0, o2);
  gld8(r[0], o0, rs, 0); gld8(r[1], o0, rs, 32); gld8(r[2], o2, rs, 0); gld8(r[3], o2, rs, 16);
}
DI float sum_hi3(float x) {
  x += __uint_as_float(__builtin_amdgcn_update_dpp(0, __float_as_uint(x), 0x128, 0xf, 0xf, false));
  { auto r = __builtin_amdgcn_permlane16_swap(__float_as_uint(x), __float_as_uint(x), false, false); x = __uint_as_float(r[0]) + __uint_as_float(r[1]); }
  return xhalf_sum(x);
}
DI void red_sum(const Red& d, float (&a)[16]) {
#pragma unroll
  for (int e = 0; e < 8; ++e) a[e] = 0.f;
#pragma unroll
  for (int s = 0; s < 8; ++s) {
#pragma unroll
    for (int e = 0; e < 4; ++e) { a[2 * e] += bflo(d.p[s][e]); a[2 * e + 1] += bfhi(d.p[s][e]); }
  }
#pragma unroll
  for (int e = 0; e < 4; ++e) { a[8 + 2 * e] = sum_hi3(bflo(d.s[e])); a[9 + 2 * e] = sum_hi3(bfhi(d.s[e])); }
}
DI float sum_tok4(float x) {
  x += __uint_as_float(__builtin_amdgcn_update_dpp(0, __float_as_uint(x), 0x124, 0xf, 0xf, false));
  x += __uint_as_float(__builtin_amdgcn_update_dpp(0, __float_as_uint(x), 0x128, 0xf, 0xf, false));
  { auto r = __builtin_amdgcn_permlane16_swap(__float_as_uint(x), __float_as_uint(x), false, false); x = __uint_as_float(r[0]) + __uint_as_float(r[1]); }
  return xhalf_sum(x);
}
DI void ln2_job(Ctx& c, const Red& d, const u32x2 (&xr)[4], unsigned (&hw)[8], unsigned row0) {
  const unsigned lane = c.lane, tok = lane & 3, g = (lane >> 2) & 3, pr = lane >> 4, nt = c.w & 3;
  const unsigned stat = c.ldsb + LSTAT + (c.w >> 2) * 128;
  const bool sing = lane < 8;
  float a[16], v[16];
  red_sum(d, a);
#pragma unroll
  for (int e = 0; e < 8; ++e) {
    const unsigned xx = xr[e >> 1][e & 1];
    hw[e] = cvtpk(bflo(xx) + a[2 * e], bfhi(xx) + a[2 * e + 1]); v[2 * e] = bflo(hw[e]); v[2 * e + 1] = bfhi(hw[e]);
  }
  float s1 = 0.f, s0 = 0.f;
#pragma unroll
  for (int e = 0; e < 8; ++e) { s1 += v[e]; s0 += v[8 + e]; }
  const float mw = sum_tok4(sing ? s1 + s0 : s1) * (1.f / 144);
  float s2 = 0.f, s3 = 0.f;
#pragma unroll
  for (int e = 0; e < 8; ++e) { const float t = v[e] - mw, t2 = v[8 + e] - mw; s2 += t * t; s3 += t2 * t2; }
  s2 = sum_tok4(sing ? s2 + s3 : s2);
  if (lane < 4) *(LAS f32x2*)(size_t)(stat + (nt * 4 + lane) * 8) = f32x2{mw, s2};
  SB();
  cnt_add(c, LSYNC + C_GB + (c.w >> 2) * 4);
  cnt_wait(c, LSYNC + C_GB + (c.w >> 2) * 4, 4);
  f32x2 st[4];
#pragma unroll
  for (int i = 0; i < 4; ++i) st[i] = *(LAS f32x2*)(size_t)(stat + (i * 4 + tok) * 8);
  const float mean = (st[0][0] + st[1][0] + st[2][0] + st[3][0]) * 0.25f;
  float m2 = st[0][1] + st[1][1] + st[2][1] + st[3][1];
#pragma unroll
  for (int i = 0; i < 4; ++i) { const float t = st[i][0] - mean; m2 += 144.f * t * t; }
  const float rs = rsqrtf(m2 * (1.f / DM) + 1e-5f);
#pragma unroll
  for (int e = 0; e < 16; ++e) v[e] = (v[e] - mean) * rs;
  const unsigned row = row0 + tok, mb = row >> 5, r32 = row & 31;
  const unsigned o0 = ((mb * 36 + 2 * (nt * 4 + pr) + (g >> 1)) * 64 + (g & 1) * 32 + r32) * 16;
  const unsigned o1 = sing ? ((mb * 36 + 32 + nt) * 64 + g * 32 + r32) * 16 : OOB;
  gst16c(u32x4{cvtpk(v[0], v[1]), cvtpk(v[2], v[3]), cvtpk(v[4], v[5]), cvtpk(v[6], v[7])}, c.rws, o0, WS_HN);
  gst16c(u32x4{cvtpk(v[8], v[9]), cvtpk(v[10], v[11]), cvtpk(v[12], v[13]), cvtpk(v[14], v[15])}, c.rws, o1, WS_HN);
}

DI void phase_attn(Ctx& c, QOut& qo, WSrc& wso, u32x4 (&xr)[XD][2], const u16* wo, const u16* w1, const u16* b1, const u16* w2, const u16* b2) {
  AttnState a;
  const unsigned ldsb = c.ldsb, lane = c.lane, w = c.w, b = c.b;
  const unsigned xcd = b & 7, jj = b >> 3, head = xcd * 4 + (jj >> 3), pi = jj & 7;
  const unsigned l31 = lane & 31, h = lane >> 5;
  const unsigned kh = w >> 2, rg = kh ? 7 - w : w, partner = 7 - w;
  a.voff = lane * 16; a.ldsb = ldsb; a.w = w;
  a.kbase = WS_K + head * (SEQ * HD * 2) + w * 4096; a.vbase = WS_V + head * (SEQ * HD * 2) + w * 4096;
  a.kb[0] = ldsb + kh * 16384 + lane * 16; a.kb[1] = a.kb[0] + 32768; a.vb[0] = a.kb[0] + 65536; a.vb[1] = a.kb[0] + 98304;
  PIN(a.kb[0]); PIN(a.kb[1]); PIN(a.vb[0]); PIN(a.vb[1]);
  const unsigned nhi = 16 - pi;
  if (kh == 0) {
#pragma unroll
    for (int d = 0; d < 8; ++d) *(LAS u32x4*)(size_t)(ldsb + LE + rg * 8192 + d * 1024 + lane * 16) = qo.q[d];
  }
  if (__builtin_expect(!qo.kdone, 0)) {
    wait_flags(c, F_K + head * 64, 8, 1);
#pragma unroll
    for (int p = 0; p < 8; ++p) dma16c(ldsb + (p >> 2) * 32768 + w * 4096 + (p & 3) * 1024, a.voff, c.rws, a.kbase + (p >> 2) * 32768 + (p & 3) * 1024);
  }
  STAMP(8);
#pragma unroll
  for (int d = 0; d < 4; ++d) a.o[d] = f32x16{};
  a.negm = f32x16{}; a.mhat = 0.f; a.l0 = a.l1 = 0.f; a.fct = 1.f; a.pend = false;
  wait_vm<4>();
  bar();
  STAMP(9);
#pragma unroll
  for (int d = 0; d < 8; ++d) a.qf[d] = lds_b128(ldsb + LE + rg * 8192 + d * 1024 + lane * 16);
  SB();
  {
    u32x4 t[8];
#pragma unroll
    for (int d = 0; d < 8; ++d) { t[d] = __builtin_bit_cast(u32x4, a.qf[d]); PIN(t[d]); a.qf[d] = asbf(kh ? t[d] : qo.q[d]); }
  }
  if (kh) {
#pragma unroll
    for (int d = 0; d < 8; ++d) *(LAS u32x4*)(size_t)(ldsb + LE + rg * 8192 + d * 1024 + lane * 16) = qo.q[d];
  }
#pragma unroll
  for (int n = 0; n < 4; ++n) a.fr[n] = lds_b128(a.kb[0] + ((n & 1) * 8 + (n >> 1)) * 1024);
  const int dq = (int)(rg * 32 + l31) - (int)(kh * 64) - 4 * (int)h;
  float m_old = 0.f;
  unsigned dummy = 0;

  auto merge = [&](auto FIN_, unsigned sc0, unsigned sc1, unsigned qblk, float mo, float lo) AI {
    bar();
    if constexpr (decltype(FIN_)::v) w_group8(c, wso, 2);
    const float lt = xhalf_sum(lo);
    *(LAS f32x2*)(size_t)(sc0 + w * 512 + lane * 8) = f32x2{mo, lt};
    bar();
    const f32x2 ps = *(LAS f32x2*)(size_t)(sc0 + partner * 512 + lane * 8);
    const float mm = fmaxf(mo, ps[0]);
    const float fo = __builtin_amdgcn_exp2f(mo - mm), fp = __builtin_amdgcn_exp2f(ps[0] - mm);
    const float f = fo / (lt * fo + ps[1] * fp);
    bar();
    const unsigned mine = (kh ? sc1 : sc0) + (w & 3) * 8192 + lane * 16, theirs = (kh ? sc0 : sc1) + (partner & 3) * 8192 + lane * 16;
#pragma unroll
    for (int r = 0; r < 2; ++r) {
      const f32x16 x = kh ? a.o[r] : a.o[2 + r];
#pragma unroll
      for (int e = 0; e < 4; ++e)
        *(LAS f32x4*)(size_t)(mine + (r * 4 + e) * 1024) = f32x4{x[4 * e] * f, x[4 * e + 1] * f, x[4 * e + 2] * f, x[4 * e + 3] * f};
    }
    bar();
    const unsigned mb = qblk * 4 + rg;
#pragma unroll
    for (int r = 0; r < 2; ++r) {
      f32x16 x = kh ? a.o[2 + r] : a.o[r];
#pragma unroll
      for (int e = 0; e < 4; ++e) {
        const f32x4 y = *(LAS f32x4*)(size_t)(theirs + (r * 4 + e) * 1024);
#pragma unroll
        for (int k = 0; k < 4; ++k) x[4 * e + k] = x[4 * e + k] * f + y[k];
      }
      const unsigned so = WS_CTX + (mb * 256 + head * 8 + 2 * (kh * 2 + r)) * 1024;
      gst16c(pack8<0>(x), c.rws, lane * 16, so);
      gst16c(pack8<1>(x), c.rws, lane * 16, so + 1024);
    }
#pragma unroll
    for (int d = 0; d < 4; ++d) a.o[d] = f32x16{};
  };
  auto post = [&](auto X_, unsigned s) AI {
    constexpr int X = decltype(X_)::v;
    if (__builtin_expect(s == nhi, 0)) {
      merge(IC<0>{}, ldsb + 65536 + (X ^ 1) * 32768, ldsb + LE, 15 - pi, m_old, a.l0 + a.l1);
      a.l0 = a.l1 = 0.f;
      publish(c, F_CTX + ((15 - pi) * 32 + head) * 8);
    }
    if (__builtin_expect(a.pend, 0)) {
#pragma unroll
      for (int d = 0; d < 4; ++d)
#pragma unroll
        for (int r = 0; r < 16; ++r) a.o[d][r] *= a.fct;
      a.l0 *= a.fct; a.l1 *= a.fct; a.pend = false;
    }
    a.l0 = fadd_s(a.l0, a.t0); a.l1 = fadd_s(a.l1, a.t1);
    if (__builtin_expect(s + 1 == nhi, 0)) {
#pragma unroll
      for (int d = 0; d < 8; ++d) a.qf[d] = lds_b128(ldsb + LE + rg * 8192 + d * 1024 + lane * 16);
      a.negm = f32x16{};
      m_old = a.mhat;
    }
    if constexpr (X == 1) {
      const unsigned ph = s >= 13 ? s - 12 : s;
      if (__builtin_expect(s == 11, 0)) code_touch(c, dummy);
      if (__builtin_expect(ph <= 5, 0)) {
        const size_t p1 = (size_t)wo, p3 = (size_t)w1, p5 = (size_t)w2;
        w_touch(c, dummy, ph, (const void*)(ph == 1 ? p1 + 0 : ph == 3 ? p3 + 0 : p5 + 0), lane < 21 ? w * 21 + lane : 162);
      }
    }
    SB();
  };
  attn_iter<0, true, false>(c, a, 0, nhi, true, false, dq);
  post(IC<0>{}, 0);
  for (unsigned s = 1; s < 17; s += 2) {
    attn_iter<1, true, true>(c, a, s, nhi, s == nhi, s + 1 == nhi, dq); post(IC<1>{}, s);
    attn_iter<0, true, true>(c, a, s + 1, nhi, s + 1 == nhi, s + 2 == nhi || s + 1 == 16, dq); post(IC<0>{}, s + 1);
  }
  const unsigned mtH = 8 + (jj >> 2);
  u32x2 fl;
  flag_load(c, fl, F_CTX + (mtH * 32 + xcd * 4) * 8, 4);
  if (w == 0) *(LAS unsigned*)(size_t)(ldsb + LSYNC + lane * 4) = 0;
  attn_iter<1, false, true, true>(c, a, 17, nhi, false, false, dq, [&](auto P_) AI {
    if constexpr (decltype(P_)::v == 0) return;
    asm volatile("" ::"v"(dummy));
    wsrc_setup<9>(c, wso, mk_rsrc(wo, NQ * DM * 2), DM * 2, (jj & 3) * 256, 1024 + (jj & 3) * 32, 512 * xcd, 0, 0);
    w_group8(c, wso, 3);
    park_loads(c, w1, b1, w2, b2);
    flag_check(c, fl, F_CTX + (mtH * 32 + xcd * 4) * 8, 4);
    x_pro<16>(c, xr, kh == 0 ? x_voff(c) : OOB, WS_CTX + ((mtH * 4 + (w & 3)) * 256 + xcd * 32) * 1024, 0);
  });
  STAMP(10);
  merge(IC<1>{}, ldsb, ldsb + 32768, pi, a.mhat, a.l0 + a.l1);
  wait_vm<4>();
  bar();
  STAMP(11);
}

struct TId { unsigned sp, nt, mt, nt32, mb, own, wq; bool isH; };
DI TId tids(Ctx& c) {
  fresh(c);
  TId t;
  const unsigned xcd = c.b & 7, jj = c.b >> 3;
  t.isH = c.w < 4; t.wq = c.w & 3; t.sp = xcd; t.nt = jj & 3; t.mt = (t.isH ? 8 : 0) + (jj >> 2);
  t.nt32 = xcd * 4 + t.nt; t.mb = t.mt * 4 + t.wq; t.own = t.nt * 8 + t.sp;
  return t;
}
DI void acc_zero(f32x4 (&acc)[2][9]) {
#pragma unroll
  for (int u = 0; u < 2; ++u)
#pragma unroll
    for (int a = 0; a < 9; ++a) acc[u][a] = f32x4{};
}
DI void phase_tail(Ctx& c, WSrc& wso, u32x4 (&xr)[XD][2], const u16* x, const u16* w1, const u16* b1, const u16* w2, const u16* b2, u16* out) {
  f32x4 acc[2][9]; u32x2 tl[9], tl2[9]; unsigned hw[8]; WSrc ws, wt;
  {
    const TId t = tids(c);
    const unsigned xo = WS_CTX + (t.mb * 256 + t.sp * 32) * 1024;
    if (!t.isH) { w_group(c, wso, 1); w_group(c, wso, 0); }
    park_read(c, tl, tl2);
    if (!t.isH) {
      wait_vm<18>(); ctx_arrive(c);
      wait_vm<9>(); cnt_add(c, land(0, 1));
      wait_vm<0>(); cnt_add(c, land(0, 0));
      poll_w(c, F_CTX + (t.mt * 32 + t.sp * 4) * 8, 4);
    }
    prio_hi(c);
    STAMPG(12);
    x_pro_masked16(c, xr, xo, 0u - (c.w >> 2));
    wsrc_setup(c, ws, mk_rsrc(w1, DM * FF * 2), FF * 2, t.nt32 * 256, 8192 + t.nt32 * 32, 0, 512, 1);
    acc_zero(acc);
    const unsigned pb = part_base(c), po = WS_P1 + (((t.mt * 4 + t.nt) * 8 + t.sp) * 8 + 2 * t.wq) * 4608;
    gemm_g<16, 1, 0, 13>(c, acc, xr, tl, wso, ws, xo, 0, F_P1 + (t.mb * 32 + t.own) * 8, [&](auto I) AI { part_store<decltype(I)::v>(c, acc, pb, po); });
    STAMPG(16);
  }
  {
    const TId t = tids(c);
    u32x2 xres[4]; Red rd;
    row_load(c, xres, mk_rsrc(x, SEQ * DM * 2), t.mt * 128 + t.own * 4 + (c.lane & 3));
    unsigned dummy = 0;
    w_touch(c, dummy, 5, w2, c.lane < 21 ? c.w * 21 + c.lane : 162);
    poll_srv(c, ws, F_P1 + ((t.mt * 4 + (t.own >> 3)) * 32 + t.wq * 8) * 8, 8);
    red_load(c, rd, WS_P1 + (t.mt * 256 + t.wq * 64 + (t.own >> 2)) * 4608 + (t.own & 3) * 1152);
    STAMPG(17);
    asm volatile("" ::"v"(dummy));
    PIN(xres[0]); PIN(xres[1]); PIN(xres[2]); PIN(xres[3]);
    RED_PIN(rd);
    STAMPG(18);
    ln2_job(c, rd, xres, hw, t.mt * 128 + t.own * 4);
#pragma unroll
    for (int e = 0; e < 8; ++e) PIN(hw[e]);
    STAMPG(19);
    flag_w(c, F_HN + ((t.mt * 32 + t.own) * 4 + t.wq) * 8);
    STAMPG(20);
    w_signal(c, ws, ws.nx);
    poll_srv(c, ws, F_HN + t.mb * 256, 32);
    STAMPG(21);
    const unsigned xh = WS_HN + t.mb * 36 * 1024;
    x_pro<18>(c, xr, x_voff(c), xh, xh + 32768);
    w_service(c, ws);
    wsrc_setup(c, wt, mk_rsrc(w2, FF * DM * 2), DM * 2, t.nt * 256, 1024 + t.nt * 32, 512 * t.sp, 4096 + 64 * t.sp, 2);
#pragma unroll
    for (int a = 0; a < 9; ++a) {
      const u32x2 bias = *(LAS u32x2*)(size_t)(c.ldsb + LB1 + a * 32 + (c.lane >> 4) * 8);
      acc[0][a] = acc[1][a] = f32x4{bflo(bias[0]), bfhi(bias[0]), bflo(bias[1]), bfhi(bias[1])};
    }
    const unsigned vo = x_voff(c), ho = WS_H1 + (t.mb * 288 + 8 * t.nt32) * 1024, hs = WS_H1 + (t.mb * 288 + 256 + t.nt32) * 1024;
    gemm_g<18, 0, 1, 22>(c, acc, xr, tl, ws, wt, xh, xh + 32768, F_H1 + (t.mb * 32 + t.nt32) * 8, [&](auto I_) AI {
      constexpr int I = decltype(I_)::v;
      if constexpr (I == 0) {
        const unsigned g = c.lane >> 4;
        gst16c(pack_single(relu2(acc[0][8]), relu2(acc[1][8])), c.rws, ((g & 1) * 32 + (g >> 1) * 16 + (c.lane & 15)) * 16, hs);
      } else {
        constexpr int i = (I - 1) >> 1, u = (I - 1) & 1;
        gst16c(pack_pair(relu2(acc[u][2 * i]), relu2(acc[u][2 * i + 1])), c.rws, vo, ho + i * 2048 + u * 256);
      }
    });
    STAMPG(25);
  }
  {
    const TId t = tids(c);
    poll_srv(c, wt, F_H1 + (t.mb * 32 + t.sp * 4) * 8, 4);
    STAMPG(26);
    const unsigned x1 = WS_H1 + (t.mb * 288 + t.sp * 32) * 1024, x1t = WS_H1 + (t.mb * 288 + 256 + t.sp * 4) * 1024;
    x_pro<18>(c, xr, x_voff(c), x1, x1t);
    w_service(c, wt);
    acc_zero(acc);
    const unsigned pb = part_base(c), po = WS_P2 + (((t.mt * 4 + t.nt) * 8 + t.sp) * 8 + 2 * t.wq) * 4608;
    gemm_g<18, 0, 2, 27>(c, acc, xr, tl2, wt, wt, x1, x1t, F_P2 + (t.mb * 32 + t.own) * 8, [&](auto I) AI { part_store<decltype(I)::v>(c, acc, pb, po); });
    STAMPG(30);
  }
  {
    const TId t = tids(c);
    u32x2 bb[4]; Red rd;
    {
      unsigned o0, o2; job_offs(c, 0, o0, o2); o2 = c.lane < 8 ? o2 : 0;
      bb[0] = *(LAS u32x2*)(size_t)(c.ldsb + LB2 + o0); bb[1] = *(LAS u32x2*)(size_t)(c.ldsb + LB2 + o0 + 32);
      bb[2] = *(LAS u32x2*)(size_t)(c.ldsb + LB2 + o2); bb[3] = *(LAS u32x2*)(size_t)(c.ldsb + LB2 + o2 + 16);
    }
    poll_w(c, F_P2 + ((t.mt * 4 + (t.own >> 3)) * 32 + t.wq * 8) * 8, 8);
    STAMPG(31);
    red_load(c, rd, WS_P2 + (t.mt * 256 + t.wq * 64 + (t.own >> 2)) * 4608 + (t.own & 3) * 1152);
    RED_PIN(rd);
    const rsrc_t ro = mk_rsrc(out, SEQ * DM * 2);
    float a[16]; unsigned ow[8];
    red_sum(rd, a);
#pragma unroll
    for (int e = 0; e < 8; ++e) {
      const unsigned bv = bb[e >> 1][e & 1];
      ow[e] = cvtpk(a[2 * e] + bflo(bv) + bflo(hw[e]), a[2 * e + 1] + bfhi(bv) + bfhi(hw[e]));
    }
    unsigned o0, o2; job_offs(c, t.mt * 128 + t.own * 4 + (c.lane & 3), o0, o2);
    __builtin_amdgcn_raw_buffer_store_b64(u32x2{ow[0], ow[1]}, ro, o0, 0, 0);
    __builtin_amdgcn_raw_buffer_store_b64(u32x2{ow[2], ow[3]}, ro, o0, 32, 0);
    __builtin_amdgcn_raw_buffer_store_b64(u32x2{ow[4], ow[5]}, ro, o2, 0, 0);
    __builtin_amdgcn_raw_buffer_store_b64(u32x2{ow[6], ow[7]}, ro, o2, 16, 0);
    STAMPG(32);
    if (c.b == 0 && c.w == 0) gst8c(u32x2{c.ep0, c.ep1}, c.rws, c.lane == 0 ? F_EPOCH : OOB, WS_FLAGS);
  }
}

__global__ __launch_bounds__(512, 2) void fused_block(const u16* __restrict__ x, const u16* __restrict__ ckv, const u16* __restrict__ wq,
                                                      const u16* __restrict__ wuk, const u16* __restrict__ wuv, const u16* __restrict__ wo,
                                                      const u16* __restrict__ w1, const u16* __restrict__ b1, const u16* __restrict__ w2,
                                                      const u16* __restrict__ b2, u16* __restrict__ out, char* __restrict__ ws) {
  extern __shared__ __attribute__((aligned(16))) char lds[];
  Ctx c;
  asm volatile("s_getpc_b64 %0\n.Lfb_begin:" : "=s"(c.pc0));
  c.ldsb = (unsigned)(size_t)lds; c.lane = threadIdx.x & 63; c.w = rfl(threadIdx.x >> 6); c.b = blockIdx.x;
  c.rws = mk_rsrc(ws, WS_END);
  STAMP(0);
  QOut qo;
  WSrc wso; u32x4 xr[XD][2];
  phase_qkv(c, qo, x, ckv, wq, wuk, wuv);
  fresh(c);
  phase_attn(c, qo, wso, xr, wo, w1, b1, w2, b2);
  fresh(c);
  phase_tail(c, wso, xr, x, w1, b1, w2, b2, out);
  asm volatile(".Lfb_end:");
}

extern "C" void kernel_launch(void* const* d_in, const int* in_sizes, int n_in, void* d_out, int out_size, void* d_ws, size_t ws_size,
                              hipStream_t stream) {
  hipFuncSetAttribute((const void*)fused_block, hipFuncAttributeMaxDynamicSharedMemorySize, 163840);
  fused_block<<<256, 512, 163840, stream>>>((const u16*)d_in[0], (const u16*)d_in[1], (const u16*)d_in[2], (const u16*)d_in[3], (const u16*)d_in[4],
                                            (const u16*)d_in[5], (const u16*)d_in[7], (const u16*)d_in[8], (const u16*)d_in[9], (const u16*)d_in[10],
                                            (u16*)d_out, (char*)d_ws);
}
```

```cpp
#include <hip/hip_runtime.h>
#include <stdint.h>
#include <utility>

typedef __bf16 bf16x8 __attribute__((ext_vector_type(8)));
typedef __bf16 bf16x4 __attribute__((ext_vector_type(4)));
typedef __bf16 bf16x2 __attribute__((ext_vector_type(2)));
typedef float f32x16 __attribute__((ext_vector_type(16)));
typedef float f32x4 __attribute__((ext_vector_type(4)));
typedef float f32x2 __attribute__((ext_vector_type(2)));
typedef unsigned u32x4 __attribute__((ext_vector_type(4)));
typedef unsigned u32x2 __attribute__((ext_vector_type(2)));
typedef unsigned short u16;
typedef unsigned long long u64;
#define LAS __attribute__((address_space(3)))
#define DI __device__ __forceinline__
#define AI __attribute__((always_inline))
typedef __amdgpu_buffer_rsrc_t rsrc_t;

constexpr int SEQ = 2048, DM = 576, NH = 32, HD = 128, DC = 512, FF = 4608, NQ = 4096;

constexpr unsigned WS_FLAGS = 0;
constexpr unsigned WS_XN = 131072;
constexpr unsigned WS_K = WS_XN + SEQ * DM * 2;
constexpr unsigned WS_V = WS_K + SEQ * NQ * 2;
constexpr unsigned WS_CTX = WS_V + SEQ * NQ * 2;
constexpr unsigned WS_P1 = WS_CTX + SEQ * NQ * 2;
constexpr unsigned WS_H = WS_P1 + 8 * SEQ * DM * 2;
constexpr unsigned WS_HN = WS_H + SEQ * DM * 2;
constexpr unsigned WS_H1 = WS_HN + SEQ * DM * 2;
constexpr unsigned WS_P2 = WS_H1 + SEQ * FF * 2;
constexpr unsigned WS_END = WS_P2 + 8 * SEQ * DM * 2;
constexpr unsigned F_EPOCH = 0, F_XN = 4096, F_KV = 8192, F_CTX = 12288, F_P1 = 16384, F_HN = 32768, F_H1 = 49152, F_P2 = 65536, F_K = 81920, F_V = 90112;

template <int... Is> using iseq = std::integer_sequence<int, Is...>;
template <int N> using make_iseq = std::make_integer_sequence<int, N>;
template <int I> struct IC { static constexpr int v = I; };
template <class F, int... Is> DI void static_for_(F&& f, iseq<Is...>) { (f(IC<Is>{}), ...); }
template <int N, class F> DI void static_for(F&& f) { static_for_(f, make_iseq<N>{}); }

#define SB() __builtin_amdgcn_sched_barrier(0)
#define PIN(x) asm volatile("" : "+v"(x))
DI unsigned rfl(unsigned x) { return __builtin_amdgcn_readfirstlane(x); }
DI rsrc_t mk_rsrc(const void* p, unsigned bytes) { return __builtin_amdgcn_make_buffer_rsrc((void*)p, (short)0, (int)bytes, 0x00020000); }
constexpr unsigned OOB = 0xFFFFFF00u;
DI void dma16(unsigned lds_addr, unsigned voff, rsrc_t r, unsigned soff) {
  asm volatile("s_mov_b32 m0, %0\n\ts_nop 3\n\tbuffer_load_dwordx4 %1, %2, %3 offen lds" ::"s"(lds_addr), "v"(voff), "s"(r), "s"(soff) : "memory", "m0");
}
DI void dma16c(unsigned lds_addr, unsigned voff, rsrc_t r, unsigned soff) {
  asm volatile("s_mov_b32 m0, %0\n\ts_nop 3\n\tbuffer_load_dwordx4 %1, %2, %3 offen sc1 lds" ::"s"(lds_addr), "v"(voff), "s"(r), "s"(soff) : "memory", "m0");
}
template <int IMM> DI void gld16c(u32x4& d, unsigned voff, rsrc_t r, unsigned soff) {
  asm volatile("s_nop 4\n\tbuffer_load_dwordx4 %0, %1, %2, %3 offen offset:%4 sc1" : "=v"(d) : "v"(voff), "s"(r), "s"(soff), "i"(IMM) : "memory");
}
template <int IMM> DI void gld16(u32x4& d, unsigned voff, rsrc_t r, unsigned soff) {
  asm volatile("s_nop 4\n\tbuffer_load_dwordx4 %0, %1, %2, %3 offen offset:%4" : "=v"(d) : "v"(voff), "s"(r), "s"(soff), "i"(IMM) : "memory");
}
DI void gld8c(u32x2& d, unsigned voff, rsrc_t r, unsigned soff) {
  asm volatile("s_nop 4\n\tbuffer_load_dwordx2 %0, %1, %2, %3 offen sc1" : "=v"(d) : "v"(voff), "s"(r), "s"(soff) : "memory");
}
DI void gst16c(u32x4 v, rsrc_t r, unsigned voff, unsigned soff) {
  asm volatile("s_nop 4\n\tbuffer_store_dwordx4 %0, %1, %2, %3 offen sc1\n\ts_nop 1" ::"v"(v), "v"(voff), "s"(r), "s"(soff) : "memory");
}
DI void gst8c(u32x2 v, rsrc_t r, unsigned voff, unsigned soff) { __builtin_amdgcn_raw_buffer_store_b64(v, r, voff, soff, 16); }
DI void fput(u32x2 v, rsrc_t r, unsigned voff) { asm volatile("s_nop 4\n\tbuffer_atomic_swap_x2 %0, %1, %2, 0 offen sc1\n\ts_nop 1" ::"v"(v), "v"(voff), "s"(r) : "memory"); }
DI void fget(u32x2& d, rsrc_t r, unsigned voff) {
  d = u32x2{0u, 0u};
  asm volatile("s_nop 4\n\tbuffer_atomic_add_x2 %0, %1, %2, 0 offen sc0 sc1" : "+v"(d) : "v"(voff), "s"(r) : "memory");
}
template <int N> DI void wait_vm() { asm volatile("s_waitcnt vmcnt(%0)" ::"i"(N) : "memory"); }
DI void bar() { asm volatile("s_waitcnt lgkmcnt(0)" ::: "memory"); __builtin_amdgcn_s_barrier(); asm volatile("" ::: "memory"); }
DI unsigned cvtpk(float lo, float hi) { f32x2 v = {lo, hi}; bf16x2 b = __builtin_convertvector(v, bf16x2); return __builtin_bit_cast(unsigned, b); }
DI float bflo(unsigned x) { return __uint_as_float(x << 16); }
DI float bfhi(unsigned x) { return __uint_as_float(x & 0xffff0000u); }
DI bf16x8 lds_b128(unsigned a) { return *(LAS bf16x8*)(size_t)a; }
DI bf16x4 lds_tr(unsigned a) { return __builtin_amdgcn_ds_read_tr16_b64_v4bf16((LAS bf16x4*)(size_t)a); }
DI bf16x8 cat(bf16x4 a, bf16x4 b) { return __builtin_shufflevector(a, b, 0, 1, 2, 3, 4, 5, 6, 7); }
DI bf16x8 asbf(u32x4 x) { return __builtin_bit_cast(bf16x8, x); }
#define MFMA32(a, b, c) __builtin_amdgcn_mfma_f32_32x32x16_bf16((a), (b), (c), 0, 0, 0)
#define MFMA16(a, b, c) __builtin_amdgcn_mfma_f32_16x16x32_bf16((a), (b), (c), 0, 0, 0)
template <int S> DI u32x4 pack8(const f32x16& x, float sc) {
  u32x4 w;
  w[0] = cvtpk(x[8 * S + 0] * sc, x[8 * S + 1] * sc); w[1] = cvtpk(x[8 * S + 2] * sc, x[8 * S + 3] * sc);
  w[2] = cvtpk(x[8 * S + 4] * sc, x[8 * S + 5] * sc); w[3] = cvtpk(x[8 * S + 6] * sc, x[8 * S + 7] * sc);
  return w;
}
template <int S> DI u32x4 pack8(const f32x16& x) {
  u32x4 w;
  w[0] = cvtpk(x[8 * S + 0], x[8 * S + 1]); w[1] = cvtpk(x[8 * S + 2], x[8 * S + 3]);
  w[2] = cvtpk(x[8 * S + 4], x[8 * S + 5]); w[3] = cvtpk(x[8 * S + 6], x[8 * S + 7]);
  return w;
}
DI float max3f(float a, float b, float c) { float r; asm("v_max3_f32 %0, %1, %2, %3" : "=v"(r) : "v"(a), "v"(b), "v"(c)); return r; }
DI float max2f(float a, float b) { float r; asm("v_max_f32_e32 %0, %1, %2" : "=v"(r) : "v"(a), "v"(b)); return r; }
DI float fadd_s(float a, float b) { float r; asm("v_add_f32_e32 %0, %1, %2" : "=v"(r) : "v"(a), "v"(b)); return r; }
DI float xhalf_sum(float x) {
  auto r = __builtin_amdgcn_permlane32_swap(__float_as_uint(x), __float_as_uint(x), false, false);
  return __uint_as_float(r[0]) + __uint_as_float(r[1]);
}
DI float wave_sum(float x) {
#pragma unroll
  for (int o = 1; o < 64; o <<= 1) x += __shfl_xor(x, o);
  return x;
}

#define STAMP(i)

struct Ctx {
  unsigned ldsb, w, lane, b;
  unsigned ep0, ep1;
  rsrc_t rws;
  u64 pc0;
};

DI void fresh(Ctx& c) {
  asm volatile("v_mbcnt_lo_u32_b32 %0, -1, 0\n\tv_mbcnt_hi_u32_b32 %0, -1, %0" : "=v"(c.lane));
  asm volatile("" : "+s"(c.w), "+s"(c.b), "+s"(c.ldsb));
}
DI void code_touch(Ctx& c, unsigned& dummy) {
  unsigned bytes;
  asm volatile("s_mov_b32 %0, .Lfb_end-.Lfb_begin" : "=s"(bytes));
  const rsrc_t r = mk_rsrc((const void*)c.pc0, bytes);
  asm volatile("s_nop 4\n\tbuffer_load_dword %0, %1, %2, 0 offen" : "+v"(dummy) : "v"(c.w == 0 ? (c.lane * 32 + (c.b >> 3)) * 128 : OOB), "s"(r) : "memory");
}
DI void publish(Ctx& c, unsigned flag_off) {
  wait_vm<0>();
  bar();
  fput(u32x2{c.ep0, c.ep1}, c.rws, (c.w == 0 && c.lane == 0) ? flag_off : OOB);
}
DI void poll_flags(Ctx& c, unsigned flag_off, unsigned n, unsigned stride) {
  const unsigned voff = c.lane < n ? flag_off + c.lane * 8 * stride : OOB;
  const u64 t0 = __builtin_amdgcn_s_memrealtime();
  for (;;) {
    u32x2 f;
    fget(f, c.rws, voff);
    asm volatile("s_waitcnt vmcnt(0)" : "+v"(f)::"memory");
    if (__all((f[0] == c.ep0 && f[1] == c.ep1) || c.lane >= n)) break;
    if (__builtin_amdgcn_s_memrealtime() - t0 > 5000000ull) break;
    __builtin_amdgcn_s_sleep(2);
  }
}
DI void wait_flags(Ctx& c, unsigned flag_off, unsigned n, unsigned stride) {
  if (c.w == 0) poll_flags(c, flag_off, n, stride);
  bar();
}

#define DBG_DRAIN()
#define DBG_NX(x)
#define STAMPG(i)
DI void flag_store(Ctx& c, unsigned flag_off) {
  fput(u32x2{c.ep0, c.ep1}, c.rws, (c.w == 0 && c.lane == 0) ? flag_off : OOB);
}
DI void flag_load(Ctx& c, u32x2& f, unsigned flag_off, unsigned n) { fget(f, c.rws, c.lane < n ? flag_off + c.lane * 8 : OOB); }
DI void flag_check(Ctx& c, u32x2& f, unsigned flag_off, unsigned n) {
  PIN(f);
  if (__builtin_expect(!__all((f[0] == c.ep0 && f[1] == c.ep1) || c.lane >= n), 0)) poll_flags(c, flag_off, n, 1);
}

constexpr float QSCALE = 0.08838834764831845f * 1.4426950408889634f;
constexpr unsigned LE = 131072;
constexpr unsigned LX = 147456;
constexpr unsigned KW = LX + 12288;
#define ROT 0
constexpr unsigned SLOT = 49152;
constexpr int slot_of(int S) { return (S + 2) % 3; }
constexpr int XQ = 6;
constexpr int LAQ = 6;
constexpr int QB1 = 40, QB2 = 88, QB3 = 116;
constexpr int Q_KPOLL = 70, Q_VPOLL = 134, Q_VST = 12;
constexpr int vm_wait(int pos, int tag) {
  int seq[512] = {}; int n = 0;
  auto push = [&](int t, int cnt) { for (int i = 0; i < cnt; ++i) seq[n++] = t; };
  push(0, 6); push(-1, 3); push(1, 6); push(2, 6);
  if (ROT == 0) push(-12, 1);
  bool done = false;
  for (int T = 0; T < 32 && !done; ++T) {
    if (pos == T) { done = true; break; }
    if (T == 1) push(-6, 2);
    if (T == 3) push(-2, 2);
    if (T == 7) push(-3, 1);
    if (T == 19) push(-5, 1);
    if (T == 27) push(-8, 1);
    if (T >= 16 && T < 24) push(-7, 1);
    if (T >= 1) push((T - 1) / 2 + 3, 3);
    if (T >= 29) push(200 + 2 * (T - 29), 2), push(201 + 2 * (T - 29), 2);
  }
  for (int q = 0; q < 144 && !done; ++q) {
    if (pos == 100 + q) break;
    if (q == QB3) push(-11, 1);
    if (q == 1 || q == 4 || q == 6) push(18, 1);
    if (tag == -9 && q == Q_KPOLL) push(-9, 1);
    if (q % Q_VST == 2 && q / Q_VST < 8) push(-10, 1);
    if ((q & 7) == 7 && (q >> 3) + XQ < 18) push(200 + (q >> 3) + XQ, 2);
  }
  int last = -1;
  for (int i = 0; i < n; ++i) if (seq[i] == tag) last = i;
  return n - 1 - last;
}
constexpr int imin(int a, int b) { return a < b ? a : b; }
DI unsigned lds_add_rtn(unsigned addr) {
  unsigned r;
  asm volatile("s_mov_b64 exec, 1\n\tds_add_rtn_u32 %0, %1, %2\n\ts_mov_b64 exec, -1\n\ts_waitcnt lgkmcnt(0)" : "=&v"(r) : "v"(addr), "v"(1u) : "memory");
  return rfl(r);
}
DI unsigned lds_word(unsigned addr) { return __hip_atomic_load((LAS unsigned*)(size_t)addr, __ATOMIC_RELAXED, __HIP_MEMORY_SCOPE_WORKGROUP); }
DI void lds_set(unsigned addr, unsigned v) { __hip_atomic_store((LAS unsigned*)(size_t)addr, v, __ATOMIC_RELAXED, __HIP_MEMORY_SCOPE_WORKGROUP); }
DI float wave_sum64(float x) {
  x += __uint_as_float(__builtin_amdgcn_update_dpp(0, __float_as_uint(x), 0xB1, 0xf, 0xf, false));
  x += __uint_as_float(__builtin_amdgcn_update_dpp(0, __float_as_uint(x), 0x4E, 0xf, 0xf, false));
  x += __uint_as_float(__builtin_amdgcn_update_dpp(0, __float_as_uint(x), 0x124, 0xf, 0xf, false));
  x += __uint_as_float(__builtin_amdgcn_update_dpp(0, __float_as_uint(x), 0x128, 0xf, 0xf, false));
  { auto r = __builtin_amdgcn_permlane16_swap(__float_as_uint(x), __float_as_uint(x), false, false); x = __uint_as_float(r[0]) + __uint_as_float(r[1]); }
  return xhalf_sum(x);
}
DI void ln1_row(Ctx& c, const u32x4& xr0, const u32x4& xr1) {
  const unsigned lane = c.lane;
  float v[16];
#pragma unroll
  for (int e = 0; e < 4; ++e) { v[2 * e] = bflo(xr0[e]); v[2 * e + 1] = bfhi(xr0[e]); v[8 + 2 * e] = bflo(xr1[e]); v[9 + 2 * e] = bfhi(xr1[e]); }
  float s = 0.f;
#pragma unroll
  for (int e = 0; e < 16; ++e) s += v[e];
  const float mean = wave_sum64(s) * (1.f / DM);
  float qq = 0.f;
#pragma unroll
  for (int e = 0; e < 16; ++e) { v[e] -= mean; qq += v[e] * v[e]; }
  qq = lane < 36 ? qq : 0.f;
  const float rs = rsqrtf(wave_sum64(qq) * (1.f / DM) + 1e-5f);
  u32x4 o0, o1;
#pragma unroll
  for (int e = 0; e < 4; ++e) { o0[e] = cvtpk(v[2 * e] * rs, v[2 * e + 1] * rs); o1[e] = cvtpk(v[8 + 2 * e] * rs, v[9 + 2 * e] * rs); }
  const unsigned ad = c.ldsb + LX + (lane < 36 ? (2 * lane * 8 + c.w) * 16 : 9216 + (lane - 36) * 16);
  *(LAS u32x4*)(size_t)ad = o0;
  *(LAS u32x4*)(size_t)(ad + 128) = o1;
}
DI void xn_store(Ctx& c) {
  const unsigned w = c.w, lane = c.lane, mb = c.b >> 2, r0 = (c.b & 3) * 8;
  const unsigned c0 = 9 * w + (lane >> 3), c1 = 9 * w + 8;
  const u32x4 a0 = *(LAS u32x4*)(size_t)(c.ldsb + LX + (9 * w * 8 + lane) * 16);
  const u32x4 a1 = *(LAS u32x4*)(size_t)(c.ldsb + LX + (c1 * 8 + (lane & 7)) * 16);
  gst16c(a0, c.rws, ((mb * 36 + (c0 >> 1)) * 64 + (c0 & 1) * 32 + r0 + (lane & 7)) * 16, WS_XN);
  gst16c(a1, c.rws, lane < 8 ? ((mb * 36 + (c1 >> 1)) * 64 + (c1 & 1) * 32 + r0 + lane) * 16 : OOB, WS_XN);
}

struct QOut { u32x4 q[8]; bool kdone; };
DI u32x4 pack_pair(const f32x4& r0, const f32x4& r1) { return u32x4{cvtpk(r0[0], r0[1]), cvtpk(r0[2], r0[3]), cvtpk(r1[0], r1[1]), cvtpk(r1[2], r1[3])}; }
template <int IMM> DI void gld16c2(u32x4& d0, u32x4& d1, unsigned voff, rsrc_t r, unsigned soff) {
  asm volatile("s_nop 4\n\tbuffer_load_dwordx4 %0, %2, %3, %4 offen offset:%5 sc1\n\tbuffer_load_dwordx4 %1, %2, %3, %4 offen offset:%6 sc1"
               : "=&v"(d0), "=&v"(d1) : "v"(voff), "s"(r), "s"(soff), "i"(IMM), "i"(IMM + 256) : "memory");
}
DI unsigned x_voff(Ctx& c) { const unsigned g = c.lane >> 4; return (g >> 1) * 1024 + ((g & 1) * 32 + (c.lane & 15)) * 16; }

DI void phase_qkv(Ctx& c, QOut& qo, const u16* x, const u16* ckv, const u16* wq, const u16* wuk, const u16* wuv) {
  const unsigned w = c.w, lane = c.lane, ldsb = c.ldsb, b = c.b;
  const unsigned xcd = b & 7, jj = b >> 3, head = xcd * 4 + (jj >> 3), cb = jj & 7;
  const rsrc_t rxx = mk_rsrc(x, SEQ * DM * 2), rx = mk_rsrc(ckv, SEQ * DC * 2);
  const rsrc_t rwk = mk_rsrc(wuk, DC * NQ * 2), rwv = mk_rsrc(wuv, DC * NQ * 2), rwq = mk_rsrc(wq, DM * NQ * 2);
  unsigned dummy = 0;
  code_touch(c, dummy);
  const unsigned voffX0 = (lane >> 3) * 1024 + (((lane & 7) ^ (lane >> 4)) << 4), voffX1 = voffX0 ^ 64;
  const unsigned voffW = (lane >> 3) * 8192 + (((((lane >> 1) & 3) ^ (((lane >> 4) & 1) | ((w & 1) << 1))) << 5) | ((lane & 1) << 4));
  const unsigned sX = (cb * 256 + 32 * w) * 1024, sW = (8 * (w & 3)) * 8192 + (head * 128 + (w >> 2) * 64) * 2;
  const unsigned rot = ROT == 0 ? 2 * (cb & 3) : ROT == 1 ? 2 * ((cb + head) & 3) : ROT == 2 ? 2 * (cb & 3) + 4 * (head & 1) : (cb + 2 * head) & 7;
  const unsigned dX = ldsb + 4 * w * 1024, dW = ldsb + w * 1024;
  auto issue = [&](auto U_, auto P_) AI {
    constexpr int U = decltype(U_)::v, P = decltype(P_)::v;
    constexpr unsigned so = slot_of(U) * SLOT;
    if constexpr (U < 16) {
      const unsigned tr = (U + rot) & 7;
      constexpr int p = P < 2 ? P : P - 1;
      if constexpr (P % 3 != 2) dma16(dX + so + p * 1024, (p & 1) ? voffX1 : voffX0, rx, sX + 128 * tr + (U < 8 ? 8 * p : 16 * (p & 1) + 8 * (p >> 1)) * 1024);
      else dma16(dW + so + 32768 + (P / 3) * 8192, voffW, U < 8 ? rwk : rwv, sW + (64 * tr + 32 * (P / 3)) * 8192);
    } else dma16(dW + so + (U == 18 ? 5 - P : P) * 8192, voffW, rwq, sW + 32 * (6 * (U - 16) + P) * 8192);
  };
  u32x2 ep; u32x4 xr0, xr1;
  static_for<3>([&](auto U_) AI {
    static_for<6>([&](auto P_) AI { issue(U_, P_); });
    if constexpr (decltype(U_)::v == 0) {
      const unsigned vo = lane < 36 ? lane * 32 : OOB;
      gld8c(ep, F_EPOCH, c.rws, WS_FLAGS);
      gld16<0>(xr0, vo, rxx, (b * 8 + w) * (DM * 2));
      gld16<16>(xr1, vo, rxx, (b * 8 + w) * (DM * 2));
    }
  });
  unsigned td2 = 0;
  if constexpr (ROT == 0) {
    const unsigned row = cb * 256 + (head & 3) * 64 + w * 8 + lane / 5, ch = (rot + 3 + lane % 5) & 7;
    asm volatile("s_nop 4\n\tbuffer_load_dword %0, %1, %2, 0 offen" : "+v"(td2) : "v"(lane < 40 ? row * 1024 + ch * 128 : OOB), "s"(rx) : "memory");
  }
  const unsigned kh = w >> 2, rg = kh ? 7 - w : w;
  const unsigned qmb = (kh ? cb : 15 - cb) * 4 + rg;
  const unsigned wm = w >> 1, wn = w & 1, g = lane >> 4, l15 = lane & 15, q = l15 >> 2, pp = lane & 3;
  unsigned xb[3][2], wb[3][4], wqb[3][4];
  {
    const unsigned sg = ((q >> 1) & 1) | ((g & 1) << 1);
#pragma unroll
    for (int i = 0; i < 3; ++i) {
      xb[i][0] = ldsb + i * SLOT + (wm * 64 + l15) * 128 + ((g ^ (l15 >> 1)) << 4); xb[i][1] = xb[i][0] ^ 64; PIN(xb[i][0]); PIN(xb[i][1]);
#pragma unroll
      for (int j = 0; j < 4; ++j) { wb[i][j] = ldsb + i * SLOT + wn * 4096 + (8 * g + q) * 128 + ((j ^ sg) << 5) + pp * 8; PIN(wb[i][j]); }
    }
  }
  wait_vm<vm_wait(0, 0)>();
  asm volatile("" ::"v"(dummy));
  bar();
  STAMP(2);
  f32x4 acc[4][4];
  bf16x8 xf[2][4]; bf16x4 wl[2][4][2];
  u32x4 pk[8], pv[8], xq[XQ][2]; u32x2 xnf; unsigned td0, td1;
  auto rd = [&](auto T_, auto R_) AI {
    constexpr int T = decltype(T_)::v, R = decltype(R_)::v, B = T & 1, sl = slot_of(T >> 1);
    constexpr int K = R % 3, I = R / 3;
    if constexpr (K == 0) xf[B][I] = lds_b128(xb[sl][B] + I * 2048);
    else wl[B][I][K - 1] = lds_tr(wb[sl][I] + 32768 + B * 8192 + (K - 1) * 512);
  };
  bf16x4 wq8[8][2];
  auto rdq = [&](auto N_, auto H_) AI {
    constexpr int N = decltype(N_)::v, H = decltype(H_)::v, v = N >> 3, a = N & 7, sl = slot_of(16 + v / 6);
    wq8[N & 7][H] = lds_tr(wqb[sl][a & 3] + (v >= 12 ? 17 - v : v % 6) * 8192 + (a >> 2) * 4096 + H * 512);
  };
  const unsigned xvo = x_voff(c), xso = WS_XN + qmb * 36 * 1024;
  auto ldx = [&](auto V_) AI { constexpr int v = decltype(V_)::v; gld16c2<(v & 1) * 2048>(xq[v % XQ][0], xq[v % XQ][1], xvo, c.rws, xso + (v >> 1) * 4096); };
  const unsigned kst = WS_K + ((head * 64 + cb * 8 + wm * 2) * 8 + 4 * wn) * 1024, vst = WS_V + (((head * 32 + cb * 4 + wm) * 4 + 2 * wn) * 4) * 1024;
  static_for<12>([&](auto R) AI { rd(IC<0>{}, R); });
  SB();
  static_for<32>([&](auto T_) AI {
    constexpr int T = decltype(T_)::v, B = T & 1;
    if constexpr (B) { wait_vm<vm_wait(T, (T >> 1) + 1)>(); bar(); }
    if constexpr (T == 1) {
      PIN(ep); PIN(xr0); PIN(xr1);
      u64 z = (((u64)rfl(ep[1]) << 32) | rfl(ep[0])) + 0x9E3779B97F4A7C15ull;
      z = (z ^ (z >> 30)) * 0xBF58476D1CE4E5B9ull; z = (z ^ (z >> 27)) * 0x94D049BB133111EBull; z ^= z >> 31;
      c.ep0 = (unsigned)z; c.ep1 = (unsigned)(z >> 32);
      asm volatile("s_nop 4\n\tbuffer_load_dword %0, %1, %2, 0 offen" : "=v"(td0) : "v"(lane < 16 ? (b * 128 + w * 16 + lane) * 128 : OOB), "s"(rwv) : "memory");
      asm volatile("s_nop 4\n\tbuffer_load_dword %0, %1, %2, 0 offen" : "=v"(td1) : "v"(lane < 18 ? (b * 144 + w * 18 + lane) * 128 : OOB), "s"(rwq) : "memory");
      if (w == 0) lds_set(ldsb + KW + (lane & 3) * 4, 0);
    }
    if constexpr (T == 1 || T == 2) { if (kh == T - 1) ln1_row(c, xr0, xr1); }
    if constexpr (T == 3) xn_store(c);
    if constexpr (T == 7) { asm volatile("" ::"v"(td0), "v"(td1), "v"(td2)); flag_store(c, F_XN + b * 8); }
    if constexpr (T == 19) flag_load(c, xnf, F_XN + qmb * 32, 4);
    if constexpr (T == 27) { flag_store(c, F_K + (head * 8 + cb) * 8); flag_check(c, xnf, F_XN + qmb * 32, 4); }
    if constexpr (T == 31) {
#pragma unroll
      for (int i = 0; i < 3; ++i)
#pragma unroll
        for (int j = 0; j < 4; ++j) { wqb[i][j] = wb[i][j] - wn * 4096; PIN(wqb[i][j]); }
    }
    if constexpr (T == 8 || T == 16 || T == 24) STAMP(100 + T / 2);
    SB();
    static_for<16>([&](auto M_) AI {
      constexpr int M = decltype(M_)::v, i = M >> 2, j = M & 3;
      const bf16x8 wf = cat(wl[B][j][0], wl[B][j][1]);
      const f32x4 cin = (T & 15) == 0 ? f32x4{} : acc[i][j];
      acc[i][j] = T < 16 ? MFMA16(wf, xf[B][i], cin) : MFMA16(xf[B][i], wf, cin);
      SB();
      if constexpr (M < 12) {
        if constexpr (T < 31) rd(IC<T + 1>{}, IC<M>{});
        else rdq(IC<(M >> 1)>{}, IC<(M & 1)>{});
      }
      if constexpr (M == 1 && T >= 16 && T < 24) {
        constexpr int e = T - 16;
        gst16c(pk[e], c.rws, xvo, kst + (e >> 2) * 8192 + (e & 1) * 2048 + ((e >> 1) & 1) * 256);
      }
      if constexpr (T >= 1 && (M == 4 || M == 8 || M == 12)) issue(IC<(T - 1) / 2 + 3>{}, IC<(B ? 0 : 3) + (M >> 2) - 1>{});
      if constexpr (T >= 29 && (M == 13 || M == 15)) ldx(IC<2 * (T - 29) + (M == 15)>{});
      SB();
    });
    if constexpr (T == 15) {
#pragma unroll
      for (int e = 0; e < 8; ++e) { pk[e] = pack_pair(acc[e >> 1][2 * (e & 1)], acc[e >> 1][2 * (e & 1) + 1]); PIN(pk[e]); }
    }
    if constexpr (T == 31) {
#pragma unroll
      for (int e = 0; e < 8; ++e) { pv[e] = pack_pair(acc[2 * (e >> 2)][e & 3], acc[2 * (e >> 2) + 1][e & 3]); PIN(pv[e]); }
    }
  });
  STAMP(3);
  f32x4 qa[2][8];
  u32x2 kf; bool kdone = false;
  const unsigned kso = WS_K + head * (SEQ * HD * 2) + w * 4096;
  static_for<144>([&](auto N_) AI {
    constexpr int N = decltype(N_)::v, v = N >> 3, a = N & 7;
    if constexpr (a == 0) { wait_vm<vm_wait(100 + N, 200 + v)>(); PIN(xq[v % XQ][0]); PIN(xq[v % XQ][1]); }
    if constexpr (N == QB1) { wait_vm<vm_wait(100 + N, 17)>(); bar(); SB(); }
    if constexpr (N == QB2) {
      wait_vm<vm_wait(100 + N, 18)>();
      if (w == 0) wait_vm<vm_wait(100 + N, -9)>();
      PIN(kf);
      if (w == 0 && __all((kf[0] == c.ep0 && kf[1] == c.ep1) || lane >= 8)) lds_set(ldsb + KW + 8, 1);
      bar();
      kdone = rfl(lds_word(ldsb + KW + 8)) != 0;
      SB();
    }
    if constexpr (N == QB3) { wait_vm<vm_wait(100 + N, -10)>(); bar(); flag_store(c, F_V + (head * 8 + cb) * 8); SB(); }
    const bf16x8 wf = cat(wq8[N & 7][0], wq8[N & 7][1]);
    qa[0][a] = MFMA16(wf, asbf(xq[v % XQ][0]), v == 0 ? f32x4{} : qa[0][a]);
    SB();
    if constexpr (N + LAQ < 144) rdq(IC<N + LAQ>{}, IC<0>{});
    SB();
    qa[1][a] = MFMA16(wf, asbf(xq[v % XQ][1]), v == 0 ? f32x4{} : qa[1][a]);
    SB();
    if constexpr (N + LAQ < 144) rdq(IC<N + LAQ>{}, IC<1>{});
    if constexpr (N == 1 || N == 4 || N == 6) issue(IC<18>{}, IC<(N == 1 ? 3 : N == 4 ? 4 : 5)>{});
    if constexpr (N == Q_KPOLL || N == Q_VPOLL) {
      kf = u32x2{0u, 0u};
      asm volatile("s_mov_b32 exec_lo, %3\n\ts_mov_b32 exec_hi, 0\n\ts_nop 4\n\tbuffer_atomic_add_x2 %0, %1, %2, 0 offen sc0 sc1\n\ts_mov_b64 exec, -1"
                   : "+v"(kf) : "v"((N == Q_KPOLL ? F_K : F_V) + (head * 8 + lane) * 8), "s"(c.rws), "s"(w == 0 ? 0xffu : 0u) : "memory");
    }
    if constexpr (N % Q_VST == 2 && N / Q_VST < 8) {
      constexpr int e = N / Q_VST;
      gst16c(pv[e], c.rws, xvo, vst + ((e >> 1) & 1) * 4096 + (e >> 2) * 2048 + (e & 1) * 256);
    }
    if constexpr (a == 7 && v + XQ < 18) ldx(IC<v + XQ>{});
    if constexpr (N > QB3 && N <= QB3 + 16 && (N - QB3) % 2 == 0) {
      constexpr int p = (N - QB3) / 2 - 1;
      if (kdone) dma16c(ldsb + (p >> 2) * 32768 + w * 4096 + (p & 3) * 1024, lane * 16, c.rws, kso + (p >> 2) * 32768 + (p & 3) * 1024);
    }
    SB();
  });
#pragma unroll
  for (int A = 0; A < 4; ++A) {
    u32x4 p0, p1;
#pragma unroll
    for (int e = 0; e < 4; ++e) {
      const int t = 2 * A + (e >> 1), k = 2 * (e & 1);
      const unsigned u0 = cvtpk(qa[0][t][k] * QSCALE, qa[0][t][k + 1] * QSCALE), u1 = cvtpk(qa[1][t][k] * QSCALE, qa[1][t][k + 1] * QSCALE);
      const auto s = __builtin_amdgcn_permlane16_swap(u0, u1, false, false);
      const auto r = __builtin_amdgcn_permlane32_swap(s[0], s[1], false, false);
      p0[e] = r[0]; p1[e] = r[1];
    }
    qo.q[2 * A] = p0; qo.q[2 * A + 1] = p1;
  }
  qo.kdone = kdone;
  STAMP(6);
  if (w == 0) wait_vm<0>();
  flag_check(c, kf, F_V + head * 64, w == 0 ? 8 : 0);
}

constexpr float THR = 32.f;

struct AttnState {
  bf16x8 qf[8];
  f32x16 o[4];
  f32x16 S[2][2];
  f32x16 negm;
  u32x4 pk[4];
  bf16x8 fr[8];
  float mhat, l0, l1, fct, mx0, mx1, t0, t1;
  bool pend;
  unsigned kb[2], vb[2];
  unsigned voff, ldsb, w, kbase, vbase;
};

constexpr int RD0[16] = {4, 5, 6, 7, 9, 10, 12, 13, 15, -1, -1, -1, -1, -1, -1, -1};
constexpr int RD1[16] = {-1, -1, -1, 8, -1, 11, -1, 14, -1, -1, -1, -1, -1, -1, -1, -1};

struct NoHook { template <class T> DI void operator()(T) const {} };
template <int X, bool QK, bool PV, bool VDMA = true, class Hook = NoHook>
DI void attn_iter(Ctx& c, AttnState& a, const unsigned s, const unsigned nhi, const bool first, const bool diag, const int dq, Hook&& hook = Hook{}) {
  constexpr int Y = X ^ 1;
  auto kaddr = [&](int slot, int n) AI { return a.kb[slot] + ((n & 1) * 8 + (n >> 1)) * 1024; };
  auto vaddr = [&](int slot, int n) AI { return a.vb[slot] + ((n & 3) * 4 + (n >> 2)) * 1024; };
  unsigned sk = s + 2; sk = sk > 16 ? 16 : sk; sk = sk < nhi ? sk : sk - nhi;
  unsigned sv = s > 16 ? 16 : s; sv = sv < nhi ? sv : sv - nhi;
  const unsigned sok = a.kbase + sk * 32768, sov = a.vbase + sv * 32768;
  const unsigned dk = a.ldsb + X * 32768 + a.w * 4096, dv = dk + 65536;
  static_for<16>([&](auto M_) AI {
    constexpr int M = decltype(M_)::v, d = M >> 1, kb = M & 1;
    if constexpr (QK) {
      if constexpr (d == 0) a.S[X][kb] = MFMA32(a.fr[M & 7], a.qf[d], a.negm);
      else a.S[X][kb] = MFMA32(a.fr[M & 7], a.qf[d], a.S[X][kb]);
      if constexpr (RD0[M] >= 0) a.fr[RD0[M] & 7] = lds_b128(kaddr(X, RD0[M]));
      if constexpr (RD1[M] >= 0) a.fr[RD1[M] & 7] = lds_b128(kaddr(X, RD1[M]));
      if constexpr (VDMA && (M == 0 || M == 2 || M == 6 || M == 9)) { constexpr int P = M == 0 ? 0 : M == 2 ? 1 : M == 6 ? 2 : 3; dma16c(dv + P * 1024, a.voff, c.rws, sov + P * 1024); }
    }
    if constexpr (PV) {
      if constexpr (M < 8) {
        a.S[Y][1][2 * M] = __builtin_amdgcn_exp2f(a.S[Y][1][2 * M]);
        a.S[Y][1][2 * M + 1] = __builtin_amdgcn_exp2f(a.S[Y][1][2 * M + 1]);
        a.pk[M >> 2][M & 3] = cvtpk(a.S[Y][0][2 * M], a.S[Y][0][2 * M + 1]);
        PIN(a.pk[M >> 2][M & 3]);
        if constexpr (M == 0) { a.l0 = fadd_s(a.l0, a.S[Y][0][14]); a.l1 = fadd_s(a.l1, a.S[Y][0][15]); }
      } else {
        constexpr int e = M - 8;
        a.pk[2 + (e >> 2)][e & 3] = cvtpk(a.S[Y][1][2 * e], a.S[Y][1][2 * e + 1]);
        PIN(a.pk[2 + (e >> 2)][e & 3]);
        a.l0 = fadd_s(a.l0, a.S[Y][1][2 * e]); a.l1 = fadd_s(a.l1, a.S[Y][1][2 * e + 1]);
      }
    }
    if constexpr (M == 11) { SB(); if constexpr (QK) wait_vm<8>(); else wait_vm<0>(); hook(IC<0>{}); bar(); hook(IC<1>{}); SB(); }
    if constexpr (PV && M >= 12) a.fr[(M - 12) & 7] = lds_b128(vaddr(Y, M - 12));
    SB();
  });
  static_for<16>([&](auto M_) AI {
    constexpr int M = decltype(M_)::v, ks = M >> 2, db = M & 3;
    if constexpr (PV) {
      a.o[db] = MFMA32(a.fr[M & 7], asbf(a.pk[ks]), a.o[db]);
      if constexpr (RD0[M] >= 0) a.fr[RD0[M] & 7] = lds_b128(vaddr(Y, RD0[M]));
      if constexpr (RD1[M] >= 0) a.fr[RD1[M] & 7] = lds_b128(vaddr(Y, RD1[M]));
    }
    if constexpr (QK) {
      if constexpr (M == 0 || M == 1 || M == 6 || M == 9) { constexpr int P = M == 0 ? 0 : M == 1 ? 1 : M == 6 ? 2 : 3; dma16c(dk + P * 1024, a.voff, c.rws, sok + P * 1024); }
      if constexpr (M == 2) {
        asm volatile("s_nop 7\n\ts_nop 3" : "+v"(a.S[X][0]), "+v"(a.S[X][1]));
        if (__builtin_expect(diag, 0)) {
          int dq2 = dq; PIN(dq2);
#pragma unroll
          for (int r = 0; r < 16; ++r) {
            const int cr = (r & 3) + 8 * (r >> 2);
            if (cr > dq2) a.S[X][0][r] = -__builtin_inff();
            if (cr + 32 > dq2) a.S[X][1][r] = -__builtin_inff();
          }
        }
      }
      if constexpr (M >= 2 && M < 6) {
        constexpr int r = 4 * (M - 2);
        if constexpr (M == 2) {
          a.mx0 = max3f(a.S[X][0][0], a.S[X][0][1], a.S[X][1][0]); a.mx1 = max3f(a.S[X][0][2], a.S[X][0][3], a.S[X][1][1]);
          a.mx0 = max3f(a.mx0, a.S[X][1][2], a.S[X][1][3]);
        } else {
          a.mx0 = max3f(a.mx0, a.S[X][0][r], a.S[X][0][r + 1]); a.mx1 = max3f(a.mx1, a.S[X][0][r + 2], a.S[X][0][r + 3]);
          a.mx0 = max3f(a.mx0, a.S[X][1][r], a.S[X][1][r + 1]); a.mx1 = max3f(a.mx1, a.S[X][1][r + 2], a.S[X][1][r + 3]);
        }
      }
      if constexpr (M == 6) {
        const float mm = max2f(a.mx0, a.mx1);
        auto pr = __builtin_amdgcn_permlane32_swap(__float_as_uint(mm), __float_as_uint(mm), false, false);
        a.mx0 = max2f(__uint_as_float(pr[0]), __uint_as_float(pr[1]));
      }
      if constexpr (M == 7) {
        if (__builtin_expect(first || __any(a.mx0 > THR), 0)) {
          const float dl = first ? fmaxf(a.mx0, -1e30f) : fmaxf(a.mx0, 0.f);
          a.mhat = first ? dl : a.mhat + dl;
          a.fct = __builtin_amdgcn_exp2f(-dl);
#pragma unroll
          for (int r = 0; r < 16; ++r) { a.S[X][0][r] -= dl; a.S[X][1][r] -= dl; a.negm[r] = -a.mhat; }
          a.pend = !first;
        }
      }
      if constexpr (M >= 8) {
        constexpr int e = M - 8;
        a.S[X][0][2 * e] = __builtin_amdgcn_exp2f(a.S[X][0][2 * e]);
        a.S[X][0][2 * e + 1] = __builtin_amdgcn_exp2f(a.S[X][0][2 * e + 1]);
        PIN(a.S[X][0][2 * e]); PIN(a.S[X][0][2 * e + 1]);
        if constexpr (e > 0) {
          a.t0 = e == 1 ? a.S[X][0][0] : fadd_s(a.t0, a.S[X][0][2 * e - 2]); a.t1 = e == 1 ? a.S[X][0][1] : fadd_s(a.t1, a.S[X][0][2 * e - 1]);
          PIN(a.t0); PIN(a.t1);
        }
      }
      if constexpr (M == 11) { SB(); wait_vm<8>(); bar(); }
      if constexpr (M >= 12) a.fr[(M - 12) & 7] = lds_b128(kaddr(Y, M - 12));
    }
    SB();
  });
}

constexpr unsigned WP = 288;
constexpr unsigned LSYNC = 161280;
constexpr unsigned C_LAND = 0, C_FREE = 64, C_GB = 128, C_CTXD = 136, C_MRG = 140, LSTAT = LSYNC + 256;
#define ARRV 5
constexpr int XM = 12;
constexpr int XDEP(int NK) { return NK == 16 ? XM : 8; }
DI unsigned land(unsigned ph, unsigned q) { return LSYNC + C_LAND + (ph * 5 + q) * 4; }
DI unsigned fre(unsigned ph, unsigned q) { return LSYNC + C_FREE + (ph * 5 + q) * 4; }
DI void cnt_add(Ctx& c, unsigned off) {
  asm volatile("s_mov_b64 exec, 1\n\tds_add_u32 %0, %1\n\ts_mov_b64 exec, -1" ::"v"(c.ldsb + off), "v"(1u) : "memory");
}
DI unsigned cnt_add_rtn(Ctx& c, unsigned off) {
  unsigned r;
  asm volatile("s_mov_b64 exec, 1\n\tds_add_rtn_u32 %0, %1, %2\n\ts_mov_b64 exec, -1\n\ts_waitcnt lgkmcnt(0)" : "=&v"(r) : "v"(c.ldsb + off), "v"(1u) : "memory");
  return rfl(r);
}
DI unsigned cnt_read(Ctx& c, unsigned off) { return __hip_atomic_load((LAS unsigned*)(size_t)(c.ldsb + off), __ATOMIC_RELAXED, __HIP_MEMORY_SCOPE_WORKGROUP); }
DI void prio_lo() { __builtin_amdgcn_s_setprio(0); }
DI void prio_hi(Ctx& c) { __builtin_amdgcn_s_setprio(3); }
DI void prio_gemm(Ctx& c, bool behind) {
  if (c.w >= 4) __builtin_amdgcn_s_setprio(1); else if (behind) __builtin_amdgcn_s_setprio(0); else __builtin_amdgcn_s_setprio(2);
}
DI void cnt_wait(Ctx& c, unsigned off, unsigned target) {
  asm volatile("" ::: "memory");
  unsigned spins = 0;
#pragma unroll 1
  while (rfl(cnt_read(c, off)) < target) { __builtin_amdgcn_s_sleep(1); if (++spins > (1u << 20)) break; }
  asm volatile("" ::: "memory");
}
DI void flag_w(Ctx& c, unsigned flag_off) {
  wait_vm<0>();
  fput(u32x2{c.ep0, c.ep1}, c.rws, c.lane == 0 ? flag_off : OOB);
}
DI void ctx_arrive(Ctx& c) {
  if (cnt_add_rtn(c, LSYNC + C_CTXD) == 7) {
    const unsigned jj = c.b >> 3;
    fput(u32x2{c.ep0, c.ep1}, c.rws, c.lane == 0 ? F_CTX + ((jj & 7) * 32 + (c.b & 7) * 4 + (jj >> 3)) * 8 : OOB);
  }
}

DI void dma16m(unsigned lds_addr, unsigned voff, rsrc_t r, unsigned soff, u64 mask) {
  asm volatile("s_mov_b32 m0, %0\n\ts_mov_b64 exec, %4\n\ts_nop 3\n\tbuffer_load_dwordx4 %1, %2, %3 offen lds\n\ts_mov_b64 exec, -1" ::"s"(lds_addr), "v"(voff), "s"(r), "s"(soff), "s"(mask)
               : "memory", "m0");
}
DI void w_touch(Ctx& c, unsigned& dummy, unsigned ph, const void* wp, unsigned l2) {
  const unsigned xcd = c.b & 7, jj = c.b >> 3;
  unsigned off;
  if (ph == 3) off = (jj * 18 + l2 / 9) * (FF * 2) + (l2 % 9 < 8 ? xcd * 1024 + (l2 % 9) * 128 : 8192 + xcd * 128);
  else off = l2 < 144 ? xcd * 589824 + (jj * 144 + l2) * 128 : ph == 5 ? (4096 + 64 * xcd) * (DM * 2) + (jj * 18 + l2 - 144) * 128 : OOB;
  const rsrc_t rp = mk_rsrc(wp, ph == 1 ? NQ * DM * 2 : FF * DM * 2);
  asm volatile("s_nop 4\n\tbuffer_load_dword %0, %1, %2, 0 offen" : "+v"(dummy) : "v"(l2 < 162 ? off : OOB), "s"(rp) : "memory");
}
struct WSrc { unsigned voff[9]; rsrc_t r; unsigned main, tail, pitch, colA, colB; unsigned nx, sg, ph; };
template <int NV = 5> DI void wsrc_voff(WSrc& s) {
  unsigned lane;
  asm volatile("v_mbcnt_lo_u32_b32 %0, -1, 0\n\tv_mbcnt_hi_u32_b32 %0, -1, %0" : "=v"(lane));
#pragma unroll
  for (int p = 0; p < NV; ++p) {
    const unsigned ci = lane + 64 * p, rr = ci / 18, cc = ci - rr * 18;
    s.voff[p] = rr * s.pitch + (cc < 16 ? s.colA + cc * 16 : s.colB + (cc - 16) * 16);
  }
}
template <int NV = 5> DI void wsrc_setup(Ctx& c, WSrc& s, rsrc_t r, unsigned pitch, unsigned colA, unsigned colB, unsigned rowMain, unsigned rowTail, unsigned ph) {
  s.r = r; s.pitch = pitch; s.main = rowMain * pitch; s.tail = rowTail * pitch;
  s.colA = colA; s.colB = colB; s.ph = ph; s.nx = s.sg = 0;
  wsrc_voff<NV>(s);
}
DI void w_group(Ctx& c, const WSrc& s, unsigned q) {
  const unsigned r0 = 128 * q + 32 * (c.w & 3);
#pragma unroll
  for (int p = 0; p < 9; ++p) dma16(c.ldsb + r0 * WP + p * 1024, s.voff[p], s.r, s.main + r0 * s.pitch);
}
DI void w_group8(Ctx& c, const WSrc& s, unsigned q) {
  const unsigned r0 = 128 * q + 16 * c.w;
#pragma unroll
  for (int p = 0; p < 4; ++p) dma16(c.ldsb + r0 * WP + p * 1024, s.voff[p], s.r, s.main + r0 * s.pitch);
  dma16m(c.ldsb + r0 * WP + 4096, s.voff[4], s.r, s.main + r0 * s.pitch, 0xffffffffull);
}
DI void poll_w(Ctx& c, unsigned flag_off, unsigned n) {
  prio_lo();
  poll_flags(c, flag_off, n, 1);
  prio_hi(c);
}
DI void w_group4(Ctx& c, const WSrc& s) {
  const unsigned r0 = 6 * c.w, sk = 8 * s.pitch;
  dma16(c.ldsb + (512 + r0) * WP, s.voff[0], s.r, s.tail + r0 * s.pitch + (c.w == 7 ? sk : 0));
  dma16m(c.ldsb + (512 + r0) * WP + 1024, s.voff[1] + ((c.w == 6 && c.lane >= 8) ? sk : 0), s.r, s.tail + r0 * s.pitch + (c.w == 7 ? sk : 0), 0xfffffffffffull);
}
DI void w_issue(Ctx& c, WSrc& s) { if (s.nx == 0) w_group4(c, s); else w_group8(c, s, 4 - s.nx); ++s.nx; }
DI bool w_ready(Ctx& c, const WSrc& s) { return s.nx < 5 && rfl(cnt_read(c, fre(s.ph - 1, 4 - s.nx))) >= 8; }
DI void w_service(Ctx& c, WSrc& s) {
#pragma unroll 1
  while (w_ready(c, s)) w_issue(c, s);
}
DI void w_signal(Ctx& c, WSrc& s, unsigned nx) {
#pragma unroll 1
  while (s.sg < nx) { cnt_add(c, land(s.ph, 4 - s.sg)); ++s.sg; }
}
DI void w_need(Ctx& c, WSrc& s, unsigned q) {
  wsrc_voff(s);
#pragma unroll 1
  while (s.nx <= 4 - q) { cnt_wait(c, fre(s.ph - 1, 4 - s.nx), 8); w_issue(c, s); }
  if (s.sg < s.nx) { wait_vm<0>(); w_signal(c, s, s.nx); }
  cnt_wait(c, land(s.ph, q), 8);
}
DI void poll_srv(Ctx& c, WSrc& s, unsigned flag_off, unsigned n) {
  const unsigned voff = c.lane < n ? flag_off + c.lane * 8 : OOB;
  const u64 t0 = __builtin_amdgcn_s_memrealtime();
  prio_lo();
  for (;;) {
    u32x2 f;
    fget(f, c.rws, voff);
    const unsigned nx = s.nx;
    const bool more = w_ready(c, s);
    if (more) { w_issue(c, s); if (nx == 0) wait_vm<2>(); else wait_vm<5>(); } else wait_vm<0>();
    w_signal(c, s, nx);
    PIN(f);
    if (__all((f[0] == c.ep0 && f[1] == c.ep1) || c.lane >= n)) break;
    if (__builtin_amdgcn_s_memrealtime() - t0 > 5000000ull) break;
    if (!more) __builtin_amdgcn_s_sleep(1);
  }
  prio_hi(c);
}
constexpr unsigned LB1 = LSTAT + 256, LB2 = LB1 + 320;
DI void park_loads(Ctx& c, const u16* w1, const u16* b1, const u16* w2, const u16* b2) {
  const unsigned xcd = c.b & 7, nt = (c.b >> 3) & 3, nt32 = xcd * 4 + nt, w = c.w;
  const unsigned rr = c.lane >= 18 ? 1 : 0, cc = c.lane - 18 * rr, r0 = (w < 4 ? 40 : 48) + 2 * w;
  const unsigned c1 = cc < 16 ? nt32 * 256 + cc * 16 : 8192 + nt32 * 32 + (cc - 16) * 16, c2 = cc < 16 ? nt * 256 + cc * 16 : 1024 + nt * 32 + (cc - 16) * 16;
  dma16m(c.ldsb + (512 + 2 * w) * WP, rr * (FF * 2) + c1, mk_rsrc(w1, DM * FF * 2), (512 + r0) * (FF * 2), 0xfffffffffull);
  dma16m(c.ldsb + (528 + 2 * w) * WP, rr * (DM * 2) + c2, mk_rsrc(w2, FF * DM * 2), (4096 + 64 * xcd + r0) * (DM * 2), 0xfffffffffull);
  if (w == 0) dma16m(c.ldsb + LB1, c1, mk_rsrc(b1, FF * 2), 0, 0x3ffffull);
  if (w == 1) dma16(c.ldsb + LB2, c.lane * 16, mk_rsrc(b2, DM * 2), 0);
  if (w == 2) dma16m(c.ldsb + LB2 + 1024, c.lane * 16, mk_rsrc(b2, DM * 2), 1024, 0xffull);
}
DI void park_read(Ctx& c, u32x2 (&tl)[9], u32x2 (&tl2)[9]) {
  const unsigned ad = c.ldsb + (512 + (c.lane >> 2)) * WP + (c.lane & 3) * 8;
#pragma unroll
  for (int a = 0; a < 9; ++a) { tl[a] = __builtin_bit_cast(u32x2, lds_tr(ad + a * 32)); tl2[a] = __builtin_bit_cast(u32x2, lds_tr(ad + 16 * WP + a * 32)); }
  SB();
  cnt_add(c, fre(0, 4));
#pragma unroll
  for (int a = 0; a < 9; ++a) { PIN(tl[a]); PIN(tl2[a]); }
}
constexpr int KK(int NK, int v) { return NK == 18 ? (v < 2 ? 16 + v : 4 * (3 - (v - 2) / 4) + (v - 2) % 4) : 4 * (3 - v / 4) + v % 4; }
constexpr bool QSTART(int NK, int v) { return v < NK && v > 0 && (KK(NK, v) & 3) == 0; }
constexpr bool QEND(int NK, int v) { return (KK(NK, v) & 3) == 3 || KK(NK, v) == 17; }
template <int NK, int V> DI void x_ld(Ctx& c, u32x4 (&x)[2], unsigned voff, unsigned so, unsigned so2) {
  constexpr int k = KK(NK, V);
  gld16c2<(k & 1) * 2048>(x[0], x[1], voff, c.rws, k < 16 ? so + (k >> 1) * 4096 : so2);
}
template <int NK, int V0 = 0, int NV = 8> DI void x_pro(Ctx& c, u32x4 (&x)[XM][2], unsigned voff, unsigned so, unsigned so2) {
  static_for<NV>([&](auto V_) AI { x_ld<NK, V0 + decltype(V_)::v>(c, x[V0 + decltype(V_)::v], voff, so, so2); });
}
DI void x_ld4_masked(u32x4& a, u32x4& b, u32x4& d, u32x4& e, unsigned voff, rsrc_t r, unsigned so, unsigned mask) {
  asm volatile("s_mov_b32 exec_lo, %7\n\ts_mov_b32 exec_hi, %7\n\ts_nop 4\n\tbuffer_load_dwordx4 %0, %4, %5, %6 offen sc1\n\tbuffer_load_dwordx4 %1, %4, %5, %6 offen offset:256 sc1\n\t"
               "buffer_load_dwordx4 %2, %4, %5, %6 offen offset:2048 sc1\n\tbuffer_load_dwordx4 %3, %4, %5, %6 offen offset:2304 sc1\n\ts_mov_b64 exec, -1"
               : "+v"(a), "+v"(b), "+v"(d), "+v"(e) : "v"(voff), "s"(r), "s"(so), "s"(mask) : "memory");
}
DI void x_pro_masked16(Ctx& c, u32x4 (&x)[XM][2], unsigned so, unsigned mask) {
  const unsigned voff = x_voff(c);
#pragma unroll
  for (int p = 0; p < XM / 2; ++p) x_ld4_masked(x[2 * p][0], x[2 * p][1], x[2 * p + 1][0], x[2 * p + 1][1], voff, c.rws, so + (KK(16, 2 * p) >> 1) * 4096, mask);
}
template <int L> DI void wlane(unsigned& v, unsigned s) { asm volatile("v_writelane_b32 %0, %1, %2" : "+v"(v) : "s"(s), "i"(L)); }
struct Sched {
  int NK, XD;
  constexpr int T0() const { return 9 * (NK - 4); }
  constexpr int sv(int n) const { return n < T0() ? n / 9 : NK - 4 + (n - T0()) % 4; }
  constexpr int sa(int n) const { return n < T0() ? n % 9 : ((n - T0()) / 4 == 0 ? 8 : (n - T0()) / 4 - 1); }
  constexpr int g0(int G) const { return G == 3 ? T0() + 8 : 9 * (NK - 16 + 1 + 4 * G) + 1; }
  constexpr int gn(int G) const { return G == 0 ? 4 : 9; }
  constexpr int wg(int n) const { for (int G = 0; G < 4; ++G) if (n >= g0(G) && (n - g0(G)) % 3 == 0 && (n - g0(G)) / 3 < gn(G)) return G; return -1; }
  constexpr int wp(int n) const { return (n - g0(wg(n))) / 3; }
  constexpr int gsig(int n) const { for (int G = 0; G < 3; ++G) if (n == g0(G) + 3 * (gn(G) - 1) + 27) return G; return -1; }
  constexpr int st(int n) const {
    const int m = n - T0();
    if (m == 6) return 0;
    if (m >= 13 && (m - 13) % 8 == 0) return 1 + 2 * ((m - 13) / 8);
    if (m >= 15 && (m - 15) % 8 == 0) return 2 + 2 * ((m - 15) / 8);
    return -1;
  }
  constexpr bool sp(int n) const { return n < T0() ? (n % 18 == 0 && n >= 18) : ((n - T0()) % 18 == 0); }
  constexpr int between(int n0, int n1) const {
    int cnt = 0;
    for (int i = n0; i < n1; ++i) { if (i < T0() && sa(i) == 8 && sv(i) + XD < NK) cnt += 2; if (st(i) >= 0) cnt += 1; }
    return cnt;
  }
  constexpr int younger(int tag, int n, bool ldr) const {
    int cnt = 0, last = -1;
    for (int v = 0; v < XD; ++v) { if (100 + v == tag) last = cnt + 1; cnt += 2; }
    for (int i = 0; i < n; ++i) {
      if (i < T0() && sa(i) == 8 && sv(i) + XD < NK) { if (100 + sv(i) + XD == tag) last = cnt + 1; cnt += 2; }
      if (ldr && wg(i) >= 0) { if (200 + wg(i) == tag) last = cnt; cnt += 1; }
      if (st(i) >= 0) cnt += 1;
    }
    return cnt - 1 - last;
  }
};
template <int NK, int KORD, int PH, int GST, class St>
DI void gemm_g(Ctx& c, f32x4 (&acc)[2][9], u32x4 (&x)[XM][2], u32x2 (&tl)[9], u32x2 (&tl2)[9], WSrc& ws, WSrc& wn, unsigned so, unsigned so2, unsigned flag_off, St&& st) {
  constexpr unsigned RT = KORD == 0 ? 16 : 8;
  constexpr int LA = 6, NS = NK * 9;
  constexpr int XD = XDEP(NK);
  constexpr Sched S{NK, XD};
  constexpr bool LD = PH < 2;
  const unsigned g = c.lane >> 4, q = (c.lane & 15) >> 2, pp = c.lane & 3;
  const unsigned wrow1 = 16 * (g >> 1) + 4 * (g & 1) + q, wrow = KORD == 0 ? 4 * g + q : wrow1;
  unsigned wb[3], wt[2];
#pragma unroll
  for (int i = 0; i < 3; ++i) { wb[i] = c.ldsb + wrow * WP + pp * 8 + i * 7 * 32 * WP; PIN(wb[i]); }
  if constexpr (NK == 18) {
    wt[0] = c.ldsb + (512 + wrow1) * WP + pp * 8; wt[1] = c.ldsb + (544 + wrow1 - 8 * (g >> 1)) * WP + pp * 8;
    PIN(wt[0]); PIN(wt[1]);
  }
  unsigned voff = x_voff(c); PIN(voff);
  bf16x4 wl[8][2];
  auto rdw = [&](auto N_, auto H_) AI {
    constexpr int N = decltype(N_)::v, H = decltype(H_)::v, v = S.sv(N), a = S.sa(N), k = KK(NK, v);
    if constexpr (k < 16) wl[N & 7][H] = lds_tr(wb[k / 7] + ((k % 7) * 32 + H * RT) * WP + a * 32);
    else if constexpr (k == 16) wl[N & 7][H] = lds_tr(wt[0] + H * 8 * WP + a * 32);
    else if constexpr (H == 0) wl[N & 7][H] = lds_tr(wt[1] + a * 32);
    else {
      u32x2 t;
      asm volatile("v_mov_b64 %0, %1" : "=v"(t) : "v"(tl[a]));
      wl[N & 7][1] = __builtin_bit_cast(bf16x4, t);
    }
    if constexpr (LD && H == 1 && ((N < S.T0() && a == 8 && QEND(NK, v)) || N == NS - 1)) { SB(); cnt_add(c, fre(PH, k >> 2)); }
  };
  if constexpr (PH > 0) { if (__builtin_expect(rfl(cnt_read(c, land(PH, 4))) < 8, 0)) w_need(c, ws, 4); }
  prio_gemm(c, PH > 0 && ws.nx < 5);
  static_for<LA>([&](auto N) AI { rdw(N, IC<0>{}); rdw(N, IC<1>{}); });
  unsigned lc = 0, lf = 0;
  SB();
  static_for<NS>([&](auto N_) AI {
    constexpr int N = decltype(N_)::v, v = S.sv(N), a = S.sa(N);
    if constexpr (N < S.T0() ? a == 0 : N < S.T0() + 4) {
      wait_vm<S.younger(100 + v, N, false)>();
      PIN(x[v % XD][0]); PIN(x[v % XD][1]);
    }
    if constexpr (N < S.T0() && a == 0) {
      if constexpr (v == 0 || v == 8) STAMPG(GST + (v >> 3));
      if constexpr (PH == 0 && v == ARRV) { if (c.w < 4) { wait_vm<S.between(0, N)>(); ctx_arrive(c); } }
      if constexpr (PH == 0 && v == 7) park_read(c, tl, tl2);
      constexpr bool chk2 = QSTART(NK, v + 2), chk1 = QSTART(NK, v + 1);
      if constexpr (chk1) {
        if (__builtin_expect(rfl(lc) < (PH > 0 ? 8 : 4), 0)) { if constexpr (PH > 0) { w_need(c, ws, KK(NK, v + 1) >> 2); prio_gemm(c, ws.nx < 5); } else cnt_wait(c, land(0, KK(NK, v + 1) >> 2), 4); }
      }
      if constexpr (chk2) lc = cnt_read(c, land(PH, KK(NK, v + 2) >> 2));
    }
    if constexpr (PH > 0 && S.sp(N) && N < S.T0()) {
      if (__builtin_expect(ws.sg < 5, 0)) {
        if (ws.sg < ws.nx) { wait_vm<S.between(N - 18, N)>(); w_signal(c, ws, ws.nx); }
        if (ws.nx < 5) { wsrc_voff(ws); w_service(c, ws); }
        prio_gemm(c, ws.nx < 5);
      }
    }
    if constexpr (LD && S.sp(N)) {
      const bool go = (wn.nx < 5) & (rfl(lf) >= 8);
      if (__builtin_expect((wn.sg < wn.nx) | go, 0)) {
        if (wn.sg < wn.nx) { wait_vm<S.between(N - 18, N)>(); w_signal(c, wn, wn.nx); }
        if (go) w_issue(c, wn);
      }
    }
    if constexpr (LD && S.sp(N + 5)) lf = cnt_read(c, fre(PH, 4) - 4 * (wn.nx < 4 ? wn.nx : 4));
    SB();
    const bf16x8 wf = cat(wl[N & 7][0], wl[N & 7][1]);
    acc[0][a] = MFMA16(wf, asbf(x[v % XD][0]), acc[0][a]);
    SB();
    if constexpr (N + LA < NS) rdw(IC<N + LA>{}, IC<0>{});
    SB();
    acc[1][a] = MFMA16(wf, asbf(x[v % XD][1]), acc[1][a]);
    SB();
    if constexpr (N + LA < NS) rdw(IC<N + LA>{}, IC<1>{});
    if constexpr (N < S.T0() && a == 8 && v + XD < NK) x_ld<NK, v + XD>(c, x[v % XD], voff, so, so2);
    if constexpr (S.st(N) >= 0) st(IC<S.st(N)>{});
    SB();
  });
  prio_hi(c);
  st(IC<7>{}); st(IC<8>{});
  STAMPG(GST + 2);
  flag_w(c, flag_off);
  if constexpr (LD) w_signal(c, wn, wn.nx);
}
DI f32x4 relu2(const f32x4& a) {
  const float inf = 3.4028234664e38f;
  const float r0 = __builtin_amdgcn_fmed3f(a[0], 0.f, inf), r1 = __builtin_amdgcn_fmed3f(a[1], 0.f, inf), r2 = __builtin_amdgcn_fmed3f(a[2], 0.f, inf), r3 = __builtin_amdgcn_fmed3f(a[3], 0.f, inf);
  return f32x4{r0 * r0, r1 * r1, r2 * r2, r3 * r3};
}
DI u32x4 pack_single(const f32x4& r0, const f32x4& r1) {
  const auto a = __builtin_amdgcn_permlane32_swap(cvtpk(r0[0], r0[1]), cvtpk(r1[0], r1[1]), false, false);
  const auto b = __builtin_amdgcn_permlane32_swap(cvtpk(r0[2], r0[3]), cvtpk(r1[2], r1[3]), false, false);
  return u32x4{a[0], b[0], a[1], b[1]};
}
template <int I> DI void part_store(Ctx& c, const f32x4 (&acc)[2][9], unsigned base, unsigned so) {
  const unsigned g = c.lane >> 4;
  if constexpr (I == 0) gst16c(pack_single(acc[0][8], acc[1][8]), c.rws, (g >> 1) * (4608 - 128) + base + 1024, so);
  else { constexpr int i = (I - 1) >> 1, u = (I - 1) & 1; gst16c(pack_pair(acc[u][2 * i], acc[u][2 * i + 1]), c.rws, base, so + u * 4608 + i * 256); }
}
DI unsigned part_base(Ctx& c) { const unsigned l15 = c.lane & 15; return (l15 >> 2) * 1152 + (l15 & 3) * 16 + (c.lane >> 4) * 64; }
struct Red { u32x4 p[8]; u32x4 s; };
DI void red_load(Ctx& c, Red& d, unsigned so) {
  gld16c<1024>(d.s, (c.lane >> 3) * 36864 + (c.lane & 7) * 16, c.rws, so);
#pragma unroll
  for (int s = 0; s < 8; ++s) gld16c<0>(d.p[s], c.lane * 16, c.rws, so + s * 36864);
}
#define RED_PIN(d) asm volatile("s_waitcnt vmcnt(0)" : "+v"(d.p[0]), "+v"(d.p[1]), "+v"(d.p[2]), "+v"(d.p[3]), "+v"(d.p[4]), "+v"(d.p[5]), "+v"(d.p[6]), "+v"(d.p[7]), "+v"(d.s)::"memory")
DI void gld8(u32x2& d, unsigned voff, rsrc_t r, unsigned soff) { asm volatile("s_nop 4\n\tbuffer_load_dwordx2 %0, %1, %2, %3 offen" : "=v"(d) : "v"(voff), "s"(r), "s"(soff) : "memory"); }
DI void job_offs(Ctx& c, unsigned row, unsigned& o0, unsigned& o2) {
  const unsigned pr = c.lane >> 4, g = (c.lane >> 2) & 3, nt = c.w & 3;
  o0 = row * (DM * 2) + (nt * 128 + pr * 32 + 4 * g) * 2;
  o2 = c.lane < 8 ? row * (DM * 2) + (512 + nt * 16 + 4 * g) * 2 : OOB;
}
DI void row_load(Ctx& c, u32x2 (&r)[4], rsrc_t rs, unsigned row) {
  unsigned o0, o2; job_offs(c, row, o0, o2);
  gld8(r[0], o0, rs, 0); gld8(r[1], o0, rs, 32); gld8(r[2], o2, rs, 0); gld8(r[3], o2, rs, 16);
}
DI float sum_hi3(float x) {
  x += __uint_as_float(__builtin_amdgcn_update_dpp(0, __float_as_uint(x), 0x128, 0xf, 0xf, false));
  { auto r = __builtin_amdgcn_permlane16_swap(__float_as_uint(x), __float_as_uint(x), false, false); x = __uint_as_float(r[0]) + __uint_as_float(r[1]); }
  return xhalf_sum(x);
}
DI void red_sum(const Red& d, float (&a)[16]) {
#pragma unroll
  for (int e = 0; e < 8; ++e) a[e] = 0.f;
#pragma unroll
  for (int s = 0; s < 8; ++s) {
#pragma unroll
    for (int e = 0; e < 4; ++e) { a[2 * e] += bflo(d.p[s][e]); a[2 * e + 1] += bfhi(d.p[s][e]); }
  }
#pragma unroll
  for (int e = 0; e < 4; ++e) { a[8 + 2 * e] = sum_hi3(bflo(d.s[e])); a[9 + 2 * e] = sum_hi3(bfhi(d.s[e])); }
}
DI float sum_tok4(float x) {
  x += __uint_as_float(__builtin_amdgcn_update_dpp(0, __float_as_uint(x), 0x124, 0xf, 0xf, false));
  x += __uint_as_float(__builtin_amdgcn_update_dpp(0, __float_as_uint(x), 0x128, 0xf, 0xf, false));
  { auto r = __builtin_amdgcn_permlane16_swap(__float_as_uint(x), __float_as_uint(x), false, false); x = __uint_as_float(r[0]) + __uint_as_float(r[1]); }
  return xhalf_sum(x);
}
DI void ln2_job(Ctx& c, const Red& d, const u32x2 (&xr)[4], unsigned (&hw)[8], unsigned row0) {
  const unsigned lane = c.lane, tok = lane & 3, g = (lane >> 2) & 3, pr = lane >> 4, nt = c.w & 3;
  const unsigned stat = c.ldsb + LSTAT + (c.w >> 2) * 128;
  const bool sing = lane < 8;
  float a[16], v[16];
  red_sum(d, a);
#pragma unroll
  for (int e = 0; e < 8; ++e) {
    const unsigned xx = xr[e >> 1][e & 1];
    hw[e] = cvtpk(bflo(xx) + a[2 * e], bfhi(xx) + a[2 * e + 1]); v[2 * e] = bflo(hw[e]); v[2 * e + 1] = bfhi(hw[e]);
  }
  float s1 = 0.f, s0 = 0.f;
#pragma unroll
  for (int e = 0; e < 8; ++e) { s1 += v[e]; s0 += v[8 + e]; }
  const float mw = sum_tok4(sing ? s1 + s0 : s1) * (1.f / 144);
  float s2 = 0.f, s3 = 0.f;
#pragma unroll
  for (int e = 0; e < 8; ++e) { const float t = v[e] - mw, t2 = v[8 + e] - mw; s2 += t * t; s3 += t2 * t2; }
  s2 = sum_tok4(sing ? s2 + s3 : s2);
  if (lane < 4) *(LAS f32x2*)(size_t)(stat + (nt * 4 + lane) * 8) = f32x2{mw, s2};
  SB();
  cnt_add(c, LSYNC + C_GB + (c.w >> 2) * 4);
  cnt_wait(c, LSYNC + C_GB + (c.w >> 2) * 4, 4);
  f32x2 st[4];
#pragma unroll
  for (int i = 0; i < 4; ++i) st[i] = *(LAS f32x2*)(size_t)(stat + (i * 4 + tok) * 8);
  const float mean = (st[0][0] + st[1][0] + st[2][0] + st[3][0]) * 0.25f;
  float m2 = st[0][1] + st[1][1] + st[2][1] + st[3][1];
#pragma unroll
  for (int i = 0; i < 4; ++i) { const float t = st[i][0] - mean; m2 += 144.f * t * t; }
  const float rs = rsqrtf(m2 * (1.f / DM) + 1e-5f);
#pragma unroll
  for (int e = 0; e < 16; ++e) v[e] = (v[e] - mean) * rs;
  const unsigned row = row0 + tok, mb = row >> 5, r32 = row & 31;
  const unsigned o0 = ((mb * 36 + 2 * (nt * 4 + pr) + (g >> 1)) * 64 + (g & 1) * 32 + r32) * 16;
  const unsigned o1 = sing ? ((mb * 36 + 32 + nt) * 64 + g * 32 + r32) * 16 : OOB;
  gst16c(u32x4{cvtpk(v[0], v[1]), cvtpk(v[2], v[3]), cvtpk(v[4], v[5]), cvtpk(v[6], v[7])}, c.rws, o0, WS_HN);
  gst16c(u32x4{cvtpk(v[8], v[9]), cvtpk(v[10], v[11]), cvtpk(v[12], v[13]), cvtpk(v[14], v[15])}, c.rws, o1, WS_HN);
}

DI void phase_attn(Ctx& c, QOut& qo, WSrc& wso, u32x4 (&xr)[XM][2], const u16* wo, const u16* w1, const u16* b1, const u16* w2, const u16* b2) {
  AttnState a;
  const unsigned ldsb = c.ldsb, lane = c.lane, w = c.w, b = c.b;
  const unsigned xcd = b & 7, jj = b >> 3, head = xcd * 4 + (jj >> 3), pi = jj & 7;
  const unsigned l31 = lane & 31, h = lane >> 5;
  const unsigned kh = w >> 2, rg = kh ? 7 - w : w, partner = 7 - w;
  a.voff = lane * 16; a.ldsb = ldsb; a.w = w;
  a.kbase = WS_K + head * (SEQ * HD * 2) + w * 4096; a.vbase = WS_V + head * (SEQ * HD * 2) + w * 4096;
  a.kb[0] = ldsb + kh * 16384 + lane * 16; a.kb[1] = a.kb[0] + 32768; a.vb[0] = a.kb[0] + 65536; a.vb[1] = a.kb[0] + 98304;
  PIN(a.kb[0]); PIN(a.kb[1]); PIN(a.vb[0]); PIN(a.vb[1]);
  const unsigned nhi = 16 - pi;
  if (kh == 0) {
#pragma unroll
    for (int d = 0; d < 8; ++d) *(LAS u32x4*)(size_t)(ldsb + LE + rg * 8192 + d * 1024 + lane * 16) = qo.q[d];
  }
  if (__builtin_expect(!qo.kdone, 0)) {
    wait_flags(c, F_K + head * 64, 8, 1);
#pragma unroll
    for (int p = 0; p < 8; ++p) dma16c(ldsb + (p >> 2) * 32768 + w * 4096 + (p & 3) * 1024, a.voff, c.rws, a.kbase + (p >> 2) * 32768 + (p & 3) * 1024);
  }
  STAMP(8);
#pragma unroll
  for (int d = 0; d < 4; ++d) a.o[d] = f32x16{};
  a.negm = f32x16{}; a.mhat = 0.f; a.l0 = a.l1 = 0.f; a.fct = 1.f; a.pend = false;
  wait_vm<4>();
  bar();
  STAMP(9);
#pragma unroll
  for (int d = 0; d < 8; ++d) a.qf[d] = lds_b128(ldsb + LE + rg * 8192 + d * 1024 + lane * 16);
  SB();
  {
    u32x4 t[8];
#pragma unroll
    for (int d = 0; d < 8; ++d) { t[d] = __builtin_bit_cast(u32x4, a.qf[d]); PIN(t[d]); a.qf[d] = asbf(kh ? t[d] : qo.q[d]); }
  }
  if (kh) {
#pragma unroll
    for (int d = 0; d < 8; ++d) *(LAS u32x4*)(size_t)(ldsb + LE + rg * 8192 + d * 1024 + lane * 16) = qo.q[d];
  }
#pragma unroll
  for (int n = 0; n < 4; ++n) a.fr[n] = lds_b128(a.kb[0] + ((n & 1) * 8 + (n >> 1)) * 1024);
  const int dq = (int)(rg * 32 + l31) - (int)(kh * 64) - 4 * (int)h;
  float m_old = 0.f;
  unsigned dummy = 0;

  auto merge = [&](auto FIN_, unsigned sc0, unsigned sc1, unsigned qblk, float mo, float lo) AI {
    constexpr bool FIN = decltype(FIN_)::v;
    const unsigned stat = FIN ? ldsb + 544 * WP : sc0;
    const float lt = xhalf_sum(lo);
    *(LAS f32x2*)(size_t)(stat + w * 512 + lane * 8) = f32x2{mo, lt};
    float f = 1.f, f2 = 1.f;
    auto factors = [&]() AI {
      const f32x2 ps = *(LAS f32x2*)(size_t)(stat + partner * 512 + lane * 8);
      const float mm = fmaxf(mo, ps[0]);
      const float fo = __builtin_amdgcn_exp2f(mo - mm), fp = __builtin_amdgcn_exp2f(ps[0] - mm);
      const float inv = 1.f / (lt * fo + ps[1] * fp);
      f = fo * inv; f2 = fp * inv;
    };
    if constexpr (!FIN) { bar(); factors(); bar(); }
    const unsigned mine = (kh ? sc1 : sc0) + (w & 3) * 8192 + lane * 16, theirs = (kh ? sc0 : sc1) + (partner & 3) * 8192 + lane * 16;
    auto put = [&](const f32x16 x, int r) AI {
#pragma unroll
      for (int e = 0; e < 4; ++e) *(LAS f32x4*)(size_t)(mine + (r * 4 + e) * 1024) = f32x4{x[4 * e], x[4 * e + 1], x[4 * e + 2], x[4 * e + 3]};
    };
    if (kh) { put(a.o[0], 0); put(a.o[1], 1); SB(); a.o[0] = a.o[2]; a.o[1] = a.o[3]; } else { put(a.o[2], 0); put(a.o[3], 1); SB(); }
    if constexpr (FIN) {
      x_pro<16, 8, XM - 8>(c, xr, kh == 0 ? x_voff(c) : OOB, WS_CTX + (((8 + (jj >> 2)) * 4 + (w & 3)) * 256 + xcd * 32) * 1024, 0);
      wait_vm<2 * (XM - 8)>();
    }
    bar();
    if constexpr (FIN) { if (kh) w_group(c, wso, 2); factors(); }
    const unsigned mb = qblk * 4 + rg;
#pragma unroll
    for (int r = 0; r < 2; ++r) {
      f32x16 x = a.o[r];
#pragma unroll
      for (int e = 0; e < 4; ++e) {
        const f32x4 y = *(LAS f32x4*)(size_t)(theirs + (r * 4 + e) * 1024);
#pragma unroll
        for (int k = 0; k < 4; ++k) x[4 * e + k] = x[4 * e + k] * f + y[k] * f2;
      }
      if constexpr (FIN) { if (r == 1) { SB(); cnt_add(c, LSYNC + C_MRG); } }
      const unsigned so = WS_CTX + (mb * 256 + head * 8 + 2 * (kh * 2 + r)) * 1024;
      gst16c(pack8<0>(x), c.rws, lane * 16, so);
      gst16c(pack8<1>(x), c.rws, lane * 16, so + 1024);
    }
#pragma unroll
    for (int d = 0; d < 4; ++d) a.o[d] = f32x16{};
  };
  auto post = [&](auto X_, unsigned s) AI {
    constexpr int X = decltype(X_)::v;
    if (__builtin_expect(s == nhi, 0)) {
      merge(IC<0>{}, ldsb + 65536 + (X ^ 1) * 32768, ldsb + LE, 15 - pi, m_old, a.l0 + a.l1);
      a.l0 = a.l1 = 0.f;
      bar();
    }
    if (__builtin_expect(s == nhi + 1, 0)) flag_store(c, F_CTX + ((15 - pi) * 32 + head) * 8);
    if (__builtin_expect(a.pend, 0)) {
#pragma unroll
      for (int d = 0; d < 4; ++d)
#pragma unroll
        for (int r = 0; r < 16; ++r) a.o[d][r] *= a.fct;
      a.l0 *= a.fct; a.l1 *= a.fct; a.pend = false;
    }
    a.l0 = fadd_s(a.l0, a.t0); a.l1 = fadd_s(a.l1, a.t1);
    if (__builtin_expect(s + 1 == nhi, 0)) {
#pragma unroll
      for (int d = 0; d < 8; ++d) a.qf[d] = lds_b128(ldsb + LE + rg * 8192 + d * 1024 + lane * 16);
      a.negm = f32x16{};
      m_old = a.mhat;
    }
    if constexpr (X == 1) {
      const unsigned ph = s >= 13 ? s - 12 : s;
      if (__builtin_expect(s == 11, 0)) code_touch(c, dummy);
      if (__builtin_expect(ph <= 5, 0)) {
        const size_t p1 = (size_t)wo, p3 = (size_t)w1, p5 = (size_t)w2;
        w_touch(c, dummy, ph, (const void*)(ph == 1 ? p1 + 0 : ph == 3 ? p3 + 0 : p5 + 0), lane < 21 ? w * 21 + lane : 162);
      }
    }
    SB();
  };
  attn_iter<0, true, false>(c, a, 0, nhi, true, false, dq);
  post(IC<0>{}, 0);
  for (unsigned s = 1; s < 17; s += 2) {
    attn_iter<1, true, true>(c, a, s, nhi, s == nhi, s + 1 == nhi, dq); post(IC<1>{}, s);
    attn_iter<0, true, true>(c, a, s + 1, nhi, s + 1 == nhi, s + 2 == nhi || s + 1 == 16, dq); post(IC<0>{}, s + 1);
  }
  const unsigned mtH = 8 + (jj >> 2);
  u32x2 fl;
  flag_load(c, fl, F_CTX + (mtH * 32 + xcd * 4) * 8, 4);
  if (w == 0) *(LAS unsigned*)(size_t)(ldsb + LSYNC + lane * 4) = 0;
  attn_iter<1, false, true, true>(c, a, 17, nhi, false, false, dq, [&](auto P_) AI {
    if constexpr (decltype(P_)::v == 0) return;
    asm volatile("" ::"v"(dummy));
    if (nhi == 16) flag_store(c, F_CTX + (15 * 32 + head) * 8);
    wsrc_setup<9>(c, wso, mk_rsrc(wo, NQ * DM * 2), DM * 2, (jj & 3) * 256, 1024 + (jj & 3) * 32, 512 * xcd, 0, 0);
    w_group8(c, wso, 3);
    park_loads(c, w1, b1, w2, b2);
    flag_check(c, fl, F_CTX + (mtH * 32 + xcd * 4) * 8, 4);
    x_pro<16>(c, xr, kh == 0 ? x_voff(c) : OOB, WS_CTX + ((mtH * 4 + (w & 3)) * 256 + xcd * 32) * 1024, 0);
  });
  STAMP(10);
  merge(IC<1>{}, ldsb, ldsb + 32768, pi, a.mhat, a.l0 + a.l1);
  STAMP(11);
}

struct TId { unsigned sp, nt, mt, nt32, mb, own, wq; bool isH; };
DI TId tids(Ctx& c) {
  fresh(c);
  TId t;
  const unsigned xcd = c.b & 7, jj = c.b >> 3;
  t.isH = c.w < 4; t.wq = c.w & 3; t.sp = xcd; t.nt = jj & 3; t.mt = (t.isH ? 8 : 0) + (jj >> 2);
  t.nt32 = xcd * 4 + t.nt; t.mb = t.mt * 4 + t.wq; t.own = t.nt * 8 + t.sp;
  return t;
}
DI void acc_zero(f32x4 (&acc)[2][9]) {
#pragma unroll
  for (int u = 0; u < 2; ++u)
#pragma unroll
    for (int a = 0; a < 9; ++a) acc[u][a] = f32x4{};
}
DI void phase_tail(Ctx& c, WSrc& wso, u32x4 (&xr)[XM][2], const u16* x, const u16* w1, const u16* b1, const u16* w2, const u16* b2, u16* out) {
  f32x4 acc[2][9]; u32x2 tl[9], tl2[9]; unsigned hw[8]; WSrc ws, wt;
  {
    const TId t = tids(c);
    const unsigned xo = WS_CTX + (t.mb * 256 + t.sp * 32) * 1024;
    if (!t.isH) { cnt_wait(c, LSYNC + C_MRG, 8); w_group(c, wso, 1); wait_vm<13>(); cnt_add(c, land(0, 2)); w_group(c, wso, 0); }
    if (!t.isH) {
      wait_vm<18>(); ctx_arrive(c);
      wait_vm<9>(); cnt_add(c, land(0, 1));
      wait_vm<0>(); cnt_add(c, land(0, 0));
      poll_w(c, F_CTX + (t.mt * 32 + t.sp * 4) * 8, 4);
    }
    prio_hi(c);
    STAMPG(12);
    x_pro_masked16(c, xr, xo, 0u - (c.w >> 2));
    wsrc_setup(c, ws, mk_rsrc(w1, DM * FF * 2), FF * 2, t.nt32 * 256, 8192 + t.nt32 * 32, 0, 512, 1);
    acc_zero(acc);
    const unsigned pb = part_base(c), po = WS_P1 + (((t.mt * 4 + t.nt) * 8 + t.sp) * 8 + 2 * t.wq) * 4608;
    gemm_g<16, 1, 0, 13>(c, acc, xr, tl, tl2, wso, ws, xo, 0, F_P1 + (t.mb * 32 + t.own) * 8, [&](auto I) AI { part_store<decltype(I)::v>(c, acc, pb, po); });
    STAMPG(16);
  }
  {
    const TId t = tids(c);
    u32x2 xres[4]; Red rd;
    row_load(c, xres, mk_rsrc(x, SEQ * DM * 2), t.mt * 128 + t.own * 4 + (c.lane & 3));
    unsigned dummy = 0;
    w_touch(c, dummy, 5, w2, c.lane < 21 ? c.w * 21 + c.lane : 162);
    poll_srv(c, ws, F_P1 + ((t.mt * 4 + (t.own >> 3)) * 32 + t.wq * 8) * 8, 8);
    red_load(c, rd, WS_P1 + (t.mt * 256 + t.wq * 64 + (t.own >> 2)) * 4608 + (t.own & 3) * 1152);
    STAMPG(17);
    asm volatile("" ::"v"(dummy));
    PIN(xres[0]); PIN(xres[1]); PIN(xres[2]); PIN(xres[3]);
    RED_PIN(rd);
    STAMPG(18);
    ln2_job(c, rd, xres, hw, t.mt * 128 + t.own * 4);
#pragma unroll
    for (int e = 0; e < 8; ++e) PIN(hw[e]);
    STAMPG(19);
    flag_w(c, F_HN + ((t.mt * 32 + t.own) * 4 + t.wq) * 8);
    STAMPG(20);
    w_signal(c, ws, ws.nx);
    poll_srv(c, ws, F_HN + t.mb * 256, 32);
    STAMPG(21);
    const unsigned xh = WS_HN + t.mb * 36 * 1024;
    x_pro<18>(c, xr, x_voff(c), xh, xh + 32768);
    w_service(c, ws);
    wsrc_setup(c, wt, mk_rsrc(w2, FF * DM * 2), DM * 2, t.nt * 256, 1024 + t.nt * 32, 512 * t.sp, 4096 + 64 * t.sp, 2);
#pragma unroll
    for (int a = 0; a < 9; ++a) {
      const u32x2 bias = *(LAS u32x2*)(size_t)(c.ldsb + LB1 + a * 32 + (c.lane >> 4) * 8);
      acc[0][a] = acc[1][a] = f32x4{bflo(bias[0]), bfhi(bias[0]), bflo(bias[1]), bfhi(bias[1])};
    }
    const unsigned vo = x_voff(c), ho = WS_H1 + (t.mb * 288 + 8 * t.nt32) * 1024, hs = WS_H1 + (t.mb * 288 + 256 + t.nt32) * 1024;
    gemm_g<18, 0, 1, 22>(c, acc, xr, tl, tl, ws, wt, xh, xh + 32768, F_H1 + (t.mb * 32 + t.nt32) * 8, [&](auto I_) AI {
      constexpr int I = decltype(I_)::v;
      if constexpr (I == 0) {
        const unsigned g = c.lane >> 4;
        gst16c(pack_single(relu2(acc[0][8]), relu2(acc[1][8])), c.rws, ((g & 1) * 32 + (g >> 1) * 16 + (c.lane & 15)) * 16, hs);
      } else {
        constexpr int i = (I - 1) >> 1, u = (I - 1) & 1;
        gst16c(pack_pair(relu2(acc[u][2 * i]), relu2(acc[u][2 * i + 1])), c.rws, vo, ho + i * 2048 + u * 256);
      }
    });
    STAMPG(25);
  }
  {
    const TId t = tids(c);
    poll_srv(c, wt, F_H1 + (t.mb * 32 + t.sp * 4) * 8, 4);
    STAMPG(26);
    const unsigned x1 = WS_H1 + (t.mb * 288 + t.sp * 32) * 1024, x1t = WS_H1 + (t.mb * 288 + 256 + t.sp * 4) * 1024;
    x_pro<18>(c, xr, x_voff(c), x1, x1t);
    w_service(c, wt);
    acc_zero(acc);
    const unsigned pb = part_base(c), po = WS_P2 + (((t.mt * 4 + t.nt) * 8 + t.sp) * 8 + 2 * t.wq) * 4608;
    gemm_g<18, 0, 2, 27>(c, acc, xr, tl2, tl2, wt, wt, x1, x1t, F_P2 + (t.mb * 32 + t.own) * 8, [&](auto I) AI { part_store<decltype(I)::v>(c, acc, pb, po); });
    STAMPG(30);
  }
  {
    const TId t = tids(c);
    u32x2 bb[4]; Red rd;
    {
      unsigned o0, o2; job_offs(c, 0, o0, o2); o2 = c.lane < 8 ? o2 : 0;
      bb[0] = *(LAS u32x2*)(size_t)(c.ldsb + LB2 + o0); bb[1] = *(LAS u32x2*)(size_t)(c.ldsb + LB2 + o0 + 32);
      bb[2] = *(LAS u32x2*)(size_t)(c.ldsb + LB2 + o2); bb[3] = *(LAS u32x2*)(size_t)(c.ldsb + LB2 + o2 + 16);
    }
    poll_w(c, F_P2 + ((t.mt * 4 + (t.own >> 3)) * 32 + t.wq * 8) * 8, 8);
    STAMPG(31);
    red_load(c, rd, WS_P2 + (t.mt * 256 + t.wq * 64 + (t.own >> 2)) * 4608 + (t.own & 3) * 1152);
    RED_PIN(rd);
    const rsrc_t ro = mk_rsrc(out, SEQ * DM * 2);
    float a[16]; unsigned ow[8];
    red_sum(rd, a);
#pragma unroll
    for (int e = 0; e < 8; ++e) {
      const unsigned bv = bb[e >> 1][e & 1];
      ow[e] = cvtpk(a[2 * e] + bflo(bv) + bflo(hw[e]), a[2 * e + 1] + bfhi(bv) + bfhi(hw[e]));
    }
    unsigned o0, o2; job_offs(c, t.mt * 128 + t.own * 4 + (c.lane & 3), o0, o2);
    __builtin_amdgcn_raw_buffer_store_b64(u32x2{ow[0], ow[1]}, ro, o0, 0, 0);
    __builtin_amdgcn_raw_buffer_store_b64(u32x2{ow[2], ow[3]}, ro, o0, 32, 0);
    __builtin_amdgcn_raw_buffer_store_b64(u32x2{ow[4], ow[5]}, ro, o2, 0, 0);
    __builtin_amdgcn_raw_buffer_store_b64(u32x2{ow[6], ow[7]}, ro, o2, 16, 0);
    STAMPG(32);
    if (c.b == 0 && c.w == 0) gst8c(u32x2{c.ep0, c.ep1}, c.rws, c.lane == 0 ? F_EPOCH : OOB, WS_FLAGS);
  }
}

__global__ __launch_bounds__(512, 2) void fused_block(const u16* __restrict__ x, const u16* __restrict__ ckv, const u16* __restrict__ wq,
                                                      const u16* __restrict__ wuk, const u16* __restrict__ wuv, const u16* __restrict__ wo,
                                                      const u16* __restrict__ w1, const u16* __restrict__ b1, const u16* __restrict__ w2,
                                                      const u16* __restrict__ b2, u16* __restrict__ out, char* __restrict__ ws) {
  extern __shared__ __attribute__((aligned(16))) char lds[];
  Ctx c;
  asm volatile("s_getpc_b64 %0\n.Lfb_begin:" : "=s"(c.pc0));
  c.ldsb = (unsigned)(size_t)lds; c.lane = threadIdx.x & 63; c.w = rfl(threadIdx.x >> 6); c.b = blockIdx.x;
  c.rws = mk_rsrc(ws, WS_END);
  STAMP(0);
  QOut qo;
  WSrc wso; u32x4 xr[XM][2];
  phase_qkv(c, qo, x, ckv, wq, wuk, wuv);
  fresh(c);
  phase_attn(c, qo, wso, xr, wo, w1, b1, w2, b2);
  fresh(c);
  phase_tail(c, wso, xr, x, w1, b1, w2, b2, out);
  asm volatile(".Lfb_end:");
}

extern "C" void kernel_launch(void* const* d_in, const int* in_sizes, int n_in, void* d_out, int out_size, void* d_ws, size_t ws_size,
                              hipStream_t stream) {
  hipFuncSetAttribute((const void*)fused_block, hipFuncAttributeMaxDynamicSharedMemorySize, 163840);
  fused_block<<<256, 512, 163840, stream>>>((const u16*)d_in[0], (const u16*)d_in[1], (const u16*)d_in[2], (const u16*)d_in[3], (const u16*)d_in[4],
                                            (const u16*)d_in[5], (const u16*)d_in[7], (const u16*)d_in[8], (const u16*)d_in[9], (const u16*)d_in[10],
                                            (u16*)d_out, (char*)d_ws);
}
```

```cpp
#include <hip/hip_runtime.h>
#include <stdint.h>
#include <utility>

typedef __bf16 bf16x8 __attribute__((ext_vector_type(8)));
typedef __bf16 bf16x4 __attribute__((ext_vector_type(4)));
typedef __bf16 bf16x2 __attribute__((ext_vector_type(2)));
typedef float f32x16 __attribute__((ext_vector_type(16)));
typedef float f32x4 __attribute__((ext_vector_type(4)));
typedef float f32x2 __attribute__((ext_vector_type(2)));
typedef unsigned u32x4 __attribute__((ext_vector_type(4)));
typedef unsigned u32x2 __attribute__((ext_vector_type(2)));
typedef unsigned short u16;
typedef unsigned long long u64;
#define LAS __attribute__((address_space(3)))
#define DI __device__ __forceinline__
#define AI __attribute__((always_inline))
typedef __amdgpu_buffer_rsrc_t rsrc_t;

constexpr int SEQ = 2048, DM = 576, NH = 32, HD = 128, DC = 512, FF = 4608, NQ = 4096;

constexpr unsigned WS_FLAGS = 0;
constexpr unsigned WS_XN = 131072;
constexpr unsigned WS_K = WS_XN + SEQ * DM * 2;
constexpr unsigned WS_V = WS_K + SEQ * NQ * 2;
constexpr unsigned WS_CTX = WS_V + SEQ * NQ * 2;
constexpr unsigned WS_P1 = WS_CTX + SEQ * NQ * 2;
constexpr unsigned WS_H = WS_P1 + 8 * SEQ * DM * 2;
constexpr unsigned WS_HN = WS_H + SEQ * DM * 2;
constexpr unsigned WS_H1 = WS_HN + SEQ * DM * 2;
constexpr unsigned WS_P2 = WS_H1 + SEQ * FF * 2;
constexpr unsigned WS_END = WS_P2 + 8 * SEQ * DM * 2;
constexpr unsigned F_EPOCH = 0, F_XN = 4096, F_KV = 8192, F_CTX = 12288, F_P1 = 16384, F_HN = 32768, F_H1 = 49152, F_P2 = 65536, F_K = 81920, F_V = 90112;

template <int... Is> using iseq = std::integer_sequence<int, Is...>;
template <int N> using make_iseq = std::make_integer_sequence<int, N>;
template <int I> struct IC { static constexpr int v = I; };
template <class F, int... Is> DI void static_for_(F&& f, iseq<Is...>) { (f(IC<Is>{}), ...); }
template <int N, class F> DI void static_for(F&& f) { static_for_(f, make_iseq<N>{}); }

#define SB() __builtin_amdgcn_sched_barrier(0)
#define PIN(x) asm volatile("" : "+v"(x))
DI unsigned rfl(unsigned x) { return __builtin_amdgcn_readfirstlane(x); }
DI rsrc_t mk_rsrc(const void* p, unsigned bytes) { return __builtin_amdgcn_make_buffer_rsrc((void*)p, (short)0, (int)bytes, 0x00020000); }
constexpr unsigned OOB = 0xFFFFFF00u;
DI void dma16(unsigned lds_addr, unsigned voff, rsrc_t r, unsigned soff) {
  asm volatile("s_mov_b32 m0, %0\n\ts_nop 3\n\tbuffer_load_dwordx4 %1, %2, %3 offen lds" ::"s"(lds_addr), "v"(voff), "s"(r), "s"(soff) : "memory", "m0");
}
DI void dma16c(unsigned lds_addr, unsigned voff, rsrc_t r, unsigned soff) {
  asm volatile("s_mov_b32 m0, %0\n\ts_nop 3\n\tbuffer_load_dwordx4 %1, %2, %3 offen sc1 lds" ::"s"(lds_addr), "v"(voff), "s"(r), "s"(soff) : "memory", "m0");
}
template <int IMM> DI void gld16c(u32x4& d, unsigned voff, rsrc_t r, unsigned soff) {
  asm volatile("s_nop 4\n\tbuffer_load_dwordx4 %0, %1, %2, %3 offen offset:%4 sc1" : "=v"(d) : "v"(voff), "s"(r), "s"(soff), "i"(IMM) : "memory");
}
template <int IMM> DI void gld16(u32x4& d, unsigned voff, rsrc_t r, unsigned soff) {
  asm volatile("s_nop 4\n\tbuffer_load_dwordx4 %0, %1, %2, %3 offen offset:%4" : "=v"(d) : "v"(voff), "s"(r), "s"(soff), "i"(IMM) : "memory");
}
DI void gld8c(u32x2& d, unsigned voff, rsrc_t r, unsigned soff) {
  asm volatile("s_nop 4\n\tbuffer_load_dwordx2 %0, %1, %2, %3 offen sc1" : "=v"(d) : "v"(voff), "s"(r), "s"(soff) : "memory");
}
DI void gst16c(u32x4 v, rsrc_t r, unsigned voff, unsigned soff) {
  asm volatile("s_nop 4\n\tbuffer_store_dwordx4 %0, %1, %2, %3 offen sc1\n\ts_nop 1" ::"v"(v), "v"(voff), "s"(r), "s"(soff) : "memory");
}
DI void gst8c(u32x2 v, rsrc_t r, unsigned voff, unsigned soff) { __builtin_amdgcn_raw_buffer_store_b64(v, r, voff, soff, 16); }
DI void fput(u32x2 v, rsrc_t r, unsigned voff) { asm volatile("s_nop 4\n\tbuffer_atomic_swap_x2 %0, %1, %2, 0 offen sc1\n\ts_nop 1" ::"v"(v), "v"(voff), "s"(r) : "memory"); }
DI void fget(u32x2& d, rsrc_t r, unsigned voff) {
  d = u32x2{0u, 0u};
  asm volatile("s_nop 4\n\tbuffer_atomic_add_x2 %0, %1, %2, 0 offen sc0 sc1" : "+v"(d) : "v"(voff), "s"(r) : "memory");
}
template <int N> DI void wait_vm() { asm volatile("s_waitcnt vmcnt(%0)" ::"i"(N) : "memory"); }
DI void bar() { asm volatile("s_waitcnt lgkmcnt(0)" ::: "memory"); __builtin_amdgcn_s_barrier(); asm volatile("" ::: "memory"); }
DI unsigned cvtpk(float lo, float hi) { f32x2 v = {lo, hi}; bf16x2 b = __builtin_convertvector(v, bf16x2); return __builtin_bit_cast(unsigned, b); }
DI float bflo(unsigned x) { return __uint_as_float(x << 16); }
DI float bfhi(unsigned x) { return __uint_as_float(x & 0xffff0000u); }
DI bf16x8 lds_b128(unsigned a) { return *(LAS bf16x8*)(size_t)a; }
DI bf16x4 lds_tr(unsigned a) { return __builtin_amdgcn_ds_read_tr16_b64_v4bf16((LAS bf16x4*)(size_t)a); }
DI bf16x8 cat(bf16x4 a, bf16x4 b) { return __builtin_shufflevector(a, b, 0, 1, 2, 3, 4, 5, 6, 7); }
DI bf16x8 asbf(u32x4 x) { return __builtin_bit_cast(bf16x8, x); }
#define MFMA32(a, b, c) __builtin_amdgcn_mfma_f32_32x32x16_bf16((a), (b), (c), 0, 0, 0)
#define MFMA16(a, b, c) __builtin_amdgcn_mfma_f32_16x16x32_bf16((a), (b), (c), 0, 0, 0)
template <int S> DI u32x4 pack8(const f32x16& x, float sc) {
  u32x4 w;
  w[0] = cvtpk(x[8 * S + 0] * sc, x[8 * S + 1] * sc); w[1] = cvtpk(x[8 * S + 2] * sc, x[8 * S + 3] * sc);
  w[2] = cvtpk(x[8 * S + 4] * sc, x[8 * S + 5] * sc); w[3] = cvtpk(x[8 * S + 6] * sc, x[8 * S + 7] * sc);
  return w;
}
template <int S> DI u32x4 pack8(const f32x16& x) {
  u32x4 w;
  w[0] = cvtpk(x[8 * S + 0], x[8 * S + 1]); w[1] = cvtpk(x[8 * S + 2], x[8 * S + 3]);
  w[2] = cvtpk(x[8 * S + 4], x[8 * S + 5]); w[3] = cvtpk(x[8 * S + 6], x[8 * S + 7]);
  return w;
}
DI float max3f(float a, float b, float c) { float r; asm("v_max3_f32 %0, %1, %2, %3" : "=v"(r) : "v"(a), "v"(b), "v"(c)); return r; }
DI float max2f(float a, float b) { float r; asm("v_max_f32_e32 %0, %1, %2" : "=v"(r) : "v"(a), "v"(b)); return r; }
DI float fadd_s(float a, float b) { float r; asm("v_add_f32_e32 %0, %1, %2" : "=v"(r) : "v"(a), "v"(b)); return r; }
DI float xhalf_sum(float x) {
  auto r = __builtin_amdgcn_permlane32_swap(__float_as_uint(x), __float_as_uint(x), false, false);
  return __uint_as_float(r[0]) + __uint_as_float(r[1]);
}
DI float wave_sum(float x) {
#pragma unroll
  for (int o = 1; o < 64; o <<= 1) x += __shfl_xor(x, o);
  return x;
}

#define STAMP(i)

struct Ctx {
  unsigned ldsb, w, lane, b;
  unsigned ep0, ep1;
  rsrc_t rws;
  u64 pc0;
};

DI void fresh(Ctx& c) {
  asm volatile("v_mbcnt_lo_u32_b32 %0, -1, 0\n\tv_mbcnt_hi_u32_b32 %0, -1, %0" : "=v"(c.lane));
  asm volatile("" : "+s"(c.w), "+s"(c.b), "+s"(c.ldsb));
}
DI void code_touch(Ctx& c, unsigned& dummy) {
  unsigned bytes;
  asm volatile("s_mov_b32 %0, .Lfb_end-.Lfb_begin" : "=s"(bytes));
  const rsrc_t r = mk_rsrc((const void*)c.pc0, bytes);
  asm volatile("s_nop 4\n\tbuffer_load_dword %0, %1, %2, 0 offen" : "+v"(dummy) : "v"(c.w == 0 ? (c.lane * 32 + (c.b >> 3)) * 128 : OOB), "s"(r) : "memory");
}
DI void publish(Ctx& c, unsigned flag_off) {
  wait_vm<0>();
  bar();
  fput(u32x2{c.ep0, c.ep1}, c.rws, (c.w == 0 && c.lane == 0) ? flag_off : OOB);
}
DI void poll_flags(Ctx& c, unsigned flag_off, unsigned n, unsigned stride) {
  const unsigned voff = c.lane < n ? flag_off + c.lane * 8 * stride : OOB;
  const u64 t0 = __builtin_amdgcn_s_memrealtime();
  for (;;) {
    u32x2 f;
    fget(f, c.rws, voff);
    asm volatile("s_waitcnt vmcnt(0)" : "+v"(f)::"memory");
    if (__all((f[0] == c.ep0 && f[1] == c.ep1) || c.lane >= n)) break;
    if (__builtin_amdgcn_s_memrealtime() - t0 > 5000000ull) break;
    __builtin_amdgcn_s_sleep(2);
  }
}
DI void wait_flags(Ctx& c, unsigned flag_off, unsigned n, unsigned stride) {
  if (c.w == 0) poll_flags(c, flag_off, n, stride);
  bar();
}

#define DBG_DRAIN()
#define DBG_NX(x)
#define STAMPG(i)
DI void flag_store(Ctx& c, unsigned flag_off) {
  fput(u32x2{c.ep0, c.ep1}, c.rws, (c.w == 0 && c.lane == 0) ? flag_off : OOB);
}
DI void flag_load(Ctx& c, u32x2& f, unsigned flag_off, unsigned n) { fget(f, c.rws, c.lane < n ? flag_off + c.lane * 8 : OOB); }
DI void flag_check(Ctx& c, u32x2& f, unsigned flag_off, unsigned n) {
  PIN(f);
  if (__builtin_expect(!__all((f[0] == c.ep0 && f[1] == c.ep1) || c.lane >= n), 0)) poll_flags(c, flag_off, n, 1);
}

constexpr float QSCALE = 0.08838834764831845f * 1.4426950408889634f;
constexpr unsigned LE = 131072;
constexpr unsigned LX = 147456;
constexpr unsigned KW = LX + 12288;
#define ROT 0
constexpr unsigned SLOT = 49152;
constexpr int slot_of(int S) { return (S + 2) % 3; }
constexpr int XQ = 6;
constexpr int LAQ = 6;
constexpr int QB1 = 40, QB2 = 88, QB3 = 116;
constexpr int Q_KPOLL = 70, Q_VPOLL = 134, Q_VST = 12;
constexpr int vm_wait(int pos, int tag) {
  int seq[512] = {}; int n = 0;
  auto push = [&](int t, int cnt) { for (int i = 0; i < cnt; ++i) seq[n++] = t; };
  push(0, 6); push(-1, 3); push(1, 6); push(2, 6);
  if (ROT == 0) push(-12, 1);
  bool done = false;
  for (int T = 0; T < 32 && !done; ++T) {
    if (pos == T) { done = true; break; }
    if (T == 1) push(-6, 2);
    if (T == 3) push(-2, 2);
    if (T == 7) push(-3, 1);
    if (T == 19) push(-5, 1);
    if (T == 27) push(-8, 1);
    if (T >= 16 && T < 24) push(-7, 1);
    if (T >= 1) push((T - 1) / 2 + 3, 3);
    if (T >= 29) push(200 + 2 * (T - 29), 2), push(201 + 2 * (T - 29), 2);
  }
  for (int q = 0; q < 144 && !done; ++q) {
    if (pos == 100 + q) break;
    if (q == QB3) push(-11, 1);
    if (q == 1 || q == 4 || q == 6) push(18, 1);
    if (tag == -9 && q == Q_KPOLL) push(-9, 1);
    if (q % Q_VST == 2 && q / Q_VST < 8) push(-10, 1);
    if ((q & 7) == 7 && (q >> 3) + XQ < 18) push(200 + (q >> 3) + XQ, 2);
  }
  int last = -1;
  for (int i = 0; i < n; ++i) if (seq[i] == tag) last = i;
  return n - 1 - last;
}
constexpr int imin(int a, int b) { return a < b ? a : b; }
DI unsigned lds_add_rtn(unsigned addr) {
  unsigned r;
  asm volatile("s_mov_b64 exec, 1\n\tds_add_rtn_u32 %0, %1, %2\n\ts_mov_b64 exec, -1\n\ts_waitcnt lgkmcnt(0)" : "=&v"(r) : "v"(addr), "v"(1u) : "memory");
  return rfl(r);
}
DI unsigned lds_word(unsigned addr) { return __hip_atomic_load((LAS unsigned*)(size_t)addr, __ATOMIC_RELAXED, __HIP_MEMORY_SCOPE_WORKGROUP); }
DI void lds_set(unsigned addr, unsigned v) { __hip_atomic_store((LAS unsigned*)(size_t)addr, v, __ATOMIC_RELAXED, __HIP_MEMORY_SCOPE_WORKGROUP); }
DI float wave_sum64(float x) {
  x += __uint_as_float(__builtin_amdgcn_update_dpp(0, __float_as_uint(x), 0xB1, 0xf, 0xf, false));
  x += __uint_as_float(__builtin_amdgcn_update_dpp(0, __float_as_uint(x), 0x4E, 0xf, 0xf, false));
  x += __uint_as_float(__builtin_amdgcn_update_dpp(0, __float_as_uint(x), 0x124, 0xf, 0xf, false));
  x += __uint_as_float(__builtin_amdgcn_update_dpp(0, __float_as_uint(x), 0x128, 0xf, 0xf, false));
  { auto r = __builtin_amdgcn_permlane16_swap(__float_as_uint(x), __float_as_uint(x), false, false); x = __uint_as_float(r[0]) + __uint_as_float(r[1]); }
  return xhalf_sum(x);
}
DI void ln1_row(Ctx& c, const u32x4& xr0, const u32x4& xr1) {
  const unsigned lane = c.lane;
  float v[16];
#pragma unroll
  for (int e = 0; e < 4; ++e) { v[2 * e] = bflo(xr0[e]); v[2 * e + 1] = bfhi(xr0[e]); v[8 + 2 * e] = bflo(xr1[e]); v[9 + 2 * e] = bfhi(xr1[e]); }
  float s = 0.f;
#pragma unroll
  for (int e = 0; e < 16; ++e) s += v[e];
  const float mean = wave_sum64(s) * (1.f / DM);
  float qq = 0.f;
#pragma unroll
  for (int e = 0; e < 16; ++e) { v[e] -= mean; qq += v[e] * v[e]; }
  qq = lane < 36 ? qq : 0.f;
  const float rs = rsqrtf(wave_sum64(qq) * (1.f / DM) + 1e-5f);
  u32x4 o0, o1;
#pragma unroll
  for (int e = 0; e < 4; ++e) { o0[e] = cvtpk(v[2 * e] * rs, v[2 * e + 1] * rs); o1[e] = cvtpk(v[8 + 2 * e] * rs, v[9 + 2 * e] * rs); }
  const unsigned ad = c.ldsb + LX + (lane < 36 ? (2 * lane * 8 + c.w) * 16 : 9216 + (lane - 36) * 16);
  *(LAS u32x4*)(size_t)ad = o0;
  *(LAS u32x4*)(size_t)(ad + 128) = o1;
}
DI void xn_store(Ctx& c) {
  const unsigned w = c.w, lane = c.lane, mb = c.b >> 2, r0 = (c.b & 3) * 8;
  const unsigned c0 = 9 * w + (lane >> 3), c1 = 9 * w + 8;
  const u32x4 a0 = *(LAS u32x4*)(size_t)(c.ldsb + LX + (9 * w * 8 + lane) * 16);
  const u32x4 a1 = *(LAS u32x4*)(size_t)(c.ldsb + LX + (c1 * 8 + (lane & 7)) * 16);
  gst16c(a0, c.rws, ((mb * 36 + (c0 >> 1)) * 64 + (c0 & 1) * 32 + r0 + (lane & 7)) * 16, WS_XN);
  gst16c(a1, c.rws, lane < 8 ? ((mb * 36 + (c1 >> 1)) * 64 + (c1 & 1) * 32 + r0 + lane) * 16 : OOB, WS_XN);
}

template <int WH> DI void w_touch(Ctx& c, unsigned& dummy, const void* wo, const void* w1, const void* w2, unsigned wv, bool two) {
  unsigned lane;
  asm volatile("v_mbcnt_lo_u32_b32 %0, -1, 0\n\tv_mbcnt_hi_u32_b32 %0, -1, %0" : "=v"(lane));
  const unsigned xcd = c.b & 7, jj = (c.b >> 3) - ((lane >> 5) << 2), l = two ? lane & 31 : lane, i = 3 * wv + l - 18;
  const bool mn = l < 18, ex = l < 21 && i < 18;
  auto ld = [&](const void* p, unsigned bytes, unsigned off) AI {
    asm volatile("s_nop 4\n\tbuffer_load_dword %0, %1, %2, 0 offen" : "+v"(dummy) : "v"(off), "s"(mk_rsrc(p, bytes)) : "memory");
  };
  const unsigned o1 = xcd * 589824 + (jj * 144 + wv * 18 + l) * 128;
  if constexpr (WH & 1) ld(wo, NQ * DM * 2, mn ? o1 : OOB);
  if constexpr (WH & 2) ld(w1, FF * DM * 2, mn ? (jj * 18 + l) * (FF * 2) + xcd * 1024 + wv * 128 : ex ? (jj * 18 + i) * (FF * 2) + 8192 + xcd * 128 : OOB);
  if constexpr (WH & 4) ld(w2, FF * DM * 2, mn ? o1 : ex ? (4096 + 64 * xcd) * (DM * 2) + (jj * 18 + i) * 128 : OOB);
}
struct QOut { u32x4 q[8]; bool kdone; };
DI u32x4 pack_pair(const f32x4& r0, const f32x4& r1) { return u32x4{cvtpk(r0[0], r0[1]), cvtpk(r0[2], r0[3]), cvtpk(r1[0], r1[1]), cvtpk(r1[2], r1[3])}; }
template <int IMM> DI void gld16c2(u32x4& d0, u32x4& d1, unsigned voff, rsrc_t r, unsigned soff) {
  asm volatile("s_nop 4\n\tbuffer_load_dwordx4 %0, %2, %3, %4 offen offset:%5 sc1\n\tbuffer_load_dwordx4 %1, %2, %3, %4 offen offset:%6 sc1"
               : "=&v"(d0), "=&v"(d1) : "v"(voff), "s"(r), "s"(soff), "i"(IMM), "i"(IMM + 256) : "memory");
}
DI unsigned x_voff(Ctx& c) { const unsigned g = c.lane >> 4; return (g >> 1) * 1024 + ((g & 1) * 32 + (c.lane & 15)) * 16; }

DI void phase_qkv(Ctx& c, QOut& qo, const u16* x, const u16* ckv, const u16* wq, const u16* wuk, const u16* wuv) {
  const unsigned w = c.w, lane = c.lane, ldsb = c.ldsb, b = c.b;
  const unsigned xcd = b & 7, jj = b >> 3, head = xcd * 4 + (jj >> 3), cb = jj & 7;
  const rsrc_t rxx = mk_rsrc(x, SEQ * DM * 2), rx = mk_rsrc(ckv, SEQ * DC * 2);
  const rsrc_t rwk = mk_rsrc(wuk, DC * NQ * 2), rwv = mk_rsrc(wuv, DC * NQ * 2), rwq = mk_rsrc(wq, DM * NQ * 2);
  unsigned dummy = 0;
  code_touch(c, dummy);
  const unsigned voffX0 = (lane >> 3) * 1024 + (((lane & 7) ^ (lane >> 4)) << 4), voffX1 = voffX0 ^ 64;
  const unsigned voffW = (lane >> 3) * 8192 + (((((lane >> 1) & 3) ^ (((lane >> 4) & 1) | ((w & 1) << 1))) << 5) | ((lane & 1) << 4));
  const unsigned sX = (cb * 256 + 32 * w) * 1024, sW = (8 * (w & 3)) * 8192 + (head * 128 + (w >> 2) * 64) * 2;
  const unsigned rot = ROT == 0 ? 2 * (cb & 3) : ROT == 1 ? 2 * ((cb + head) & 3) : ROT == 2 ? 2 * (cb & 3) + 4 * (head & 1) : (cb + 2 * head) & 7;
  const unsigned dX = ldsb + 4 * w * 1024, dW = ldsb + w * 1024;
  auto issue = [&](auto U_, auto P_) AI {
    constexpr int U = decltype(U_)::v, P = decltype(P_)::v;
    constexpr unsigned so = slot_of(U) * SLOT;
    if constexpr (U < 16) {
      const unsigned tr = (U + rot) & 7;
      constexpr int p = P < 2 ? P : P - 1;
      if constexpr (P % 3 != 2) dma16(dX + so + p * 1024, (p & 1) ? voffX1 : voffX0, rx, sX + 128 * tr + (U < 8 ? 8 * p : 16 * (p & 1) + 8 * (p >> 1)) * 1024);
      else dma16(dW + so + 32768 + (P / 3) * 8192, voffW, U < 8 ? rwk : rwv, sW + (64 * tr + 32 * (P / 3)) * 8192);
    } else dma16(dW + so + (U == 18 ? 5 - P : P) * 8192, voffW, rwq, sW + 32 * (6 * (U - 16) + P) * 8192);
  };
  u32x2 ep; u32x4 xr0, xr1;
  static_for<3>([&](auto U_) AI {
    static_for<6>([&](auto P_) AI { issue(U_, P_); });
    if constexpr (decltype(U_)::v == 0) {
      const unsigned vo = lane < 36 ? lane * 32 : OOB;
      gld8c(ep, F_EPOCH, c.rws, WS_FLAGS);
      gld16<0>(xr0, vo, rxx, (b * 8 + w) * (DM * 2));
      gld16<16>(xr1, vo, rxx, (b * 8 + w) * (DM * 2));
    }
  });
  unsigned td2 = 0;
  if constexpr (ROT == 0) {
    const unsigned row = cb * 256 + (head & 3) * 64 + w * 8 + lane / 5, ch = (rot + 3 + lane % 5) & 7;
    asm volatile("s_nop 4\n\tbuffer_load_dword %0, %1, %2, 0 offen" : "+v"(td2) : "v"(lane < 40 ? row * 1024 + ch * 128 : OOB), "s"(rx) : "memory");
  }
  const unsigned kh = w >> 2, rg = kh ? 7 - w : w;
  const unsigned qmb = (kh ? cb : 15 - cb) * 4 + rg;
  const unsigned wm = w >> 1, wn = w & 1, g = lane >> 4, l15 = lane & 15, q = l15 >> 2, pp = lane & 3;
  unsigned xb[3][2], wb[3][4], wqb[3][4];
  {
    const unsigned sg = ((q >> 1) & 1) | ((g & 1) << 1);
#pragma unroll
    for (int i = 0; i < 3; ++i) {
      xb[i][0] = ldsb + i * SLOT + (wm * 64 + l15) * 128 + ((g ^ (l15 >> 1)) << 4); xb[i][1] = xb[i][0] ^ 64; PIN(xb[i][0]); PIN(xb[i][1]);
#pragma unroll
      for (int j = 0; j < 4; ++j) { wb[i][j] = ldsb + i * SLOT + wn * 4096 + (8 * g + q) * 128 + ((j ^ sg) << 5) + pp * 8; PIN(wb[i][j]); }
    }
  }
  wait_vm<vm_wait(0, 0)>();
  asm volatile("" ::"v"(dummy));
  bar();
  STAMP(2);
  f32x4 acc[4][4];
  bf16x8 xf[2][4]; bf16x4 wl[2][4][2];
  u32x4 pk[8], pv[8], xq[XQ][2]; u32x2 xnf; unsigned td0, td1;
  auto rd = [&](auto T_, auto R_) AI {
    constexpr int T = decltype(T_)::v, R = decltype(R_)::v, B = T & 1, sl = slot_of(T >> 1);
    constexpr int K = R % 3, I = R / 3;
    if constexpr (K == 0) xf[B][I] = lds_b128(xb[sl][B] + I * 2048);
    else wl[B][I][K - 1] = lds_tr(wb[sl][I] + 32768 + B * 8192 + (K - 1) * 512);
  };
  bf16x4 wq8[8][2];
  auto rdq = [&](auto N_, auto H_) AI {
    constexpr int N = decltype(N_)::v, H = decltype(H_)::v, v = N >> 3, a = N & 7, sl = slot_of(16 + v / 6);
    wq8[N & 7][H] = lds_tr(wqb[sl][a & 3] + (v >= 12 ? 17 - v : v % 6) * 8192 + (a >> 2) * 4096 + H * 512);
  };
  const unsigned xvo = x_voff(c), xso = WS_XN + qmb * 36 * 1024;
  auto ldx = [&](auto V_) AI { constexpr int v = decltype(V_)::v; gld16c2<(v & 1) * 2048>(xq[v % XQ][0], xq[v % XQ][1], xvo, c.rws, xso + (v >> 1) * 4096); };
  const unsigned kst = WS_K + ((head * 64 + cb * 8 + wm * 2) * 8 + 4 * wn) * 1024, vst = WS_V + (((head * 32 + cb * 4 + wm) * 4 + 2 * wn) * 4) * 1024;
  static_for<12>([&](auto R) AI { rd(IC<0>{}, R); });
  SB();
  static_for<32>([&](auto T_) AI {
    constexpr int T = decltype(T_)::v, B = T & 1;
    if constexpr (B) { wait_vm<vm_wait(T, (T >> 1) + 1)>(); bar(); }
    if constexpr (T == 1) {
      PIN(ep); PIN(xr0); PIN(xr1);
      u64 z = (((u64)rfl(ep[1]) << 32) | rfl(ep[0])) + 0x9E3779B97F4A7C15ull;
      z = (z ^ (z >> 30)) * 0xBF58476D1CE4E5B9ull; z = (z ^ (z >> 27)) * 0x94D049BB133111EBull; z ^= z >> 31;
      c.ep0 = (unsigned)z; c.ep1 = (unsigned)(z >> 32);
      asm volatile("s_nop 4\n\tbuffer_load_dword %0, %1, %2, 0 offen" : "=v"(td0) : "v"(lane < 16 ? (b * 128 + w * 16 + lane) * 128 : OOB), "s"(rwv) : "memory");
      asm volatile("s_nop 4\n\tbuffer_load_dword %0, %1, %2, 0 offen" : "=v"(td1) : "v"(lane < 18 ? (b * 144 + w * 18 + lane) * 128 : OOB), "s"(rwq) : "memory");
      if (w == 0) lds_set(ldsb + KW + (lane & 3) * 4, 0);
    }
    if constexpr (T == 1 || T == 2) { if (kh == T - 1) ln1_row(c, xr0, xr1); }
    if constexpr (T == 3) xn_store(c);
    if constexpr (T == 7) { asm volatile("" ::"v"(td0), "v"(td1), "v"(td2)); flag_store(c, F_XN + b * 8); }
    if constexpr (T == 19) flag_load(c, xnf, F_XN + qmb * 32, 4);
    if constexpr (T == 27) { flag_store(c, F_K + (head * 8 + cb) * 8); flag_check(c, xnf, F_XN + qmb * 32, 4); }
    if constexpr (T == 31) {
#pragma unroll
      for (int i = 0; i < 3; ++i)
#pragma unroll
        for (int j = 0; j < 4; ++j) { wqb[i][j] = wb[i][j] - wn * 4096; PIN(wqb[i][j]); }
    }
    if constexpr (T == 8 || T == 16 || T == 24) STAMP(100 + T / 2);
    SB();
    static_for<16>([&](auto M_) AI {
      constexpr int M = decltype(M_)::v, i = M >> 2, j = M & 3;
      const bf16x8 wf = cat(wl[B][j][0], wl[B][j][1]);
      const f32x4 cin = (T & 15) == 0 ? f32x4{} : acc[i][j];
      acc[i][j] = T < 16 ? MFMA16(wf, xf[B][i], cin) : MFMA16(xf[B][i], wf, cin);
      SB();
      if constexpr (M < 12) {
        if constexpr (T < 31) rd(IC<T + 1>{}, IC<M>{});
        else rdq(IC<(M >> 1)>{}, IC<(M & 1)>{});
      }
      if constexpr (M == 1 && T >= 16 && T < 24) {
        constexpr int e = T - 16;
        gst16c(pk[e], c.rws, xvo, kst + (e >> 2) * 8192 + (e & 1) * 2048 + ((e >> 1) & 1) * 256);
      }
      if constexpr (T >= 1 && (M == 4 || M == 8 || M == 12)) issue(IC<(T - 1) / 2 + 3>{}, IC<(B ? 0 : 3) + (M >> 2) - 1>{});
      if constexpr (T >= 29 && (M == 13 || M == 15)) ldx(IC<2 * (T - 29) + (M == 15)>{});
      SB();
    });
    if constexpr (T == 15) {
#pragma unroll
      for (int e = 0; e < 8; ++e) { pk[e] = pack_pair(acc[e >> 1][2 * (e & 1)], acc[e >> 1][2 * (e & 1) + 1]); PIN(pk[e]); }
    }
    if constexpr (T == 31) {
#pragma unroll
      for (int e = 0; e < 8; ++e) { pv[e] = pack_pair(acc[2 * (e >> 2)][e & 3], acc[2 * (e >> 2) + 1][e & 3]); PIN(pv[e]); }
    }
  });
  STAMP(3);
  f32x4 qa[2][8];
  u32x2 kf; bool kdone = false;
  const unsigned kso = WS_K + head * (SEQ * HD * 2) + w * 4096;
  static_for<144>([&](auto N_) AI {
    constexpr int N = decltype(N_)::v, v = N >> 3, a = N & 7;
    if constexpr (a == 0) { wait_vm<vm_wait(100 + N, 200 + v)>(); PIN(xq[v % XQ][0]); PIN(xq[v % XQ][1]); }
    if constexpr (N == QB1) { wait_vm<vm_wait(100 + N, 17)>(); bar(); SB(); }
    if constexpr (N == QB2) {
      wait_vm<vm_wait(100 + N, 18)>();
      if (w == 0) wait_vm<vm_wait(100 + N, -9)>();
      PIN(kf);
      if (w == 0 && __all((kf[0] == c.ep0 && kf[1] == c.ep1) || lane >= 8)) lds_set(ldsb + KW + 8, 1);
      bar();
      kdone = rfl(lds_word(ldsb + KW + 8)) != 0;
      SB();
    }
    if constexpr (N == QB3) { wait_vm<vm_wait(100 + N, -10)>(); bar(); flag_store(c, F_V + (head * 8 + cb) * 8); SB(); }
    const bf16x8 wf = cat(wq8[N & 7][0], wq8[N & 7][1]);
    qa[0][a] = MFMA16(wf, asbf(xq[v % XQ][0]), v == 0 ? f32x4{} : qa[0][a]);
    SB();
    if constexpr (N + LAQ < 144) rdq(IC<N + LAQ>{}, IC<0>{});
    SB();
    qa[1][a] = MFMA16(wf, asbf(xq[v % XQ][1]), v == 0 ? f32x4{} : qa[1][a]);
    SB();
    if constexpr (N + LAQ < 144) rdq(IC<N + LAQ>{}, IC<1>{});
    if constexpr (N == 1 || N == 4 || N == 6) issue(IC<18>{}, IC<(N == 1 ? 3 : N == 4 ? 4 : 5)>{});
    if constexpr (N == Q_KPOLL || N == Q_VPOLL) {
      kf = u32x2{0u, 0u};
      asm volatile("s_mov_b32 exec_lo, %3\n\ts_mov_b32 exec_hi, 0\n\ts_nop 4\n\tbuffer_atomic_add_x2 %0, %1, %2, 0 offen sc0 sc1\n\ts_mov_b64 exec, -1"
                   : "+v"(kf) : "v"((N == Q_KPOLL ? F_K : F_V) + (head * 8 + lane) * 8), "s"(c.rws), "s"(w == 0 ? 0xffu : 0u) : "memory");
    }
    if constexpr (N % Q_VST == 2 && N / Q_VST < 8) {
      constexpr int e = N / Q_VST;
      gst16c(pv[e], c.rws, xvo, vst + ((e >> 1) & 1) * 4096 + (e >> 2) * 2048 + (e & 1) * 256);
    }
    if constexpr (a == 7 && v + XQ < 18) ldx(IC<v + XQ>{});
    if constexpr (N > QB3 && N <= QB3 + 16 && (N - QB3) % 4 == 0) {
      constexpr int p = (N - QB3) / 4 - 1;
      if (kdone) dma16c(ldsb + kh * 16384 + w * 4096 + p * 1024, lane * 16, c.rws, kso + p * 1024);
    }
    SB();
  });
#pragma unroll
  for (int A = 0; A < 4; ++A) {
    u32x4 p0, p1;
#pragma unroll
    for (int e = 0; e < 4; ++e) {
      const int t = 2 * A + (e >> 1), k = 2 * (e & 1);
      const unsigned u0 = cvtpk(qa[0][t][k] * QSCALE, qa[0][t][k + 1] * QSCALE), u1 = cvtpk(qa[1][t][k] * QSCALE, qa[1][t][k + 1] * QSCALE);
      const auto s = __builtin_amdgcn_permlane16_swap(u0, u1, false, false);
      const auto r = __builtin_amdgcn_permlane32_swap(s[0], s[1], false, false);
      p0[e] = r[0]; p1[e] = r[1];
    }
    qo.q[2 * A] = p0; qo.q[2 * A + 1] = p1;
  }
  qo.kdone = kdone;
  STAMP(6);
  if (w == 0) wait_vm<0>();
  flag_check(c, kf, F_V + head * 64, w == 0 ? 8 : 0);
}

constexpr float THR = 32.f;

struct AttnState {
  bf16x8 qf[8];
  f32x16 o[4];
  f32x16 S[2][2];
  f32x16 negm;
  u32x4 pk[4];
  bf16x8 fr[8];
  float mhat, l0, l1, fct, mx0, mx1, t0, t1;
  bool pend;
  unsigned kb[2], vb[2];
  unsigned voff, ldsb, w, kbase, vbase, n1, n1k;
  u64 ta; unsigned dummy;
};

constexpr int RD0[16] = {4, 5, 6, 7, 9, 10, 12, 13, 15, -1, -1, -1, -1, -1, -1, -1};
constexpr int RD1[16] = {-1, -1, -1, 8, -1, 11, -1, 14, -1, -1, -1, -1, -1, -1, -1, -1};

template <int CR> DI float mask1(float x, int dq, float ninf) {
  asm volatile("v_cmp_le_i32_e32 vcc, %2, %1\n\tv_cndmask_b32_e32 %0, %3, %0, vcc" : "+v"(x) : "v"(dq), "i"(CR), "v"(ninf) : "vcc");
  return x;
}
template <int B, int... R> DI void mask_rows(f32x16& s, int dq, float ninf, iseq<R...>) { ((s[R] = mask1<B + (R & 3) + 8 * (R >> 2)>(s[R], dq, ninf)), ...); }
struct NoHook { template <class T> DI void operator()(T) const {} };
template <int X, bool QK, bool PV, bool VDMA = true, class Hook = NoHook>
DI void attn_iter(Ctx& c, AttnState& a, const unsigned s, const bool first, const bool diag, const int dq, Hook&& hook = Hook{}) {
  constexpr int Y = X ^ 1;
  auto kaddr = [&](int slot, int n) AI { return a.kb[slot] + ((n & 1) * 8 + (n >> 1)) * 1024; };
  auto vaddr = [&](int slot, int n) AI { return a.vb[slot] + ((n & 3) * 4 + (n >> 2)) * 1024; };
  const unsigned sk = s + 2;
  const unsigned sok = a.kbase + (sk + (sk > a.n1 ? sk - a.n1 : 0)) * 16384, sov = a.vbase + (s + (s > a.n1 ? s - a.n1 : 0)) * 16384;
  const unsigned mk = (sk < a.n1k || sk > 16) ? 0xFFFFFC00u : 0u, mv = s < a.n1k ? 0xFFFFFC00u : 0u;
  unsigned vo = 0;
  const unsigned dk = a.ldsb + X * 32768 + a.w * 4096, dv = dk + 65536;
  static_for<16>([&](auto M_) AI {
    constexpr int M = decltype(M_)::v, d = M >> 1, kb = M & 1;
    if constexpr (QK) {
      if constexpr (d == 0) a.S[X][kb] = MFMA32(a.fr[M & 7], a.qf[d], a.negm);
      else a.S[X][kb] = MFMA32(a.fr[M & 7], a.qf[d], a.S[X][kb]);
      if constexpr (RD0[M] >= 0) a.fr[RD0[M] & 7] = lds_b128(kaddr(X, RD0[M]));
      if constexpr (RD1[M] >= 0) a.fr[RD1[M] & 7] = lds_b128(kaddr(X, RD1[M]));
      if constexpr (M == 0) vo = a.voff | mv;
      if constexpr (VDMA && (M == 0 || M == 2 || M == 6 || M == 9)) { constexpr int P = M == 0 ? 0 : M == 2 ? 1 : M == 6 ? 2 : 3; dma16c(dv + P * 1024, vo, c.rws, sov + P * 1024); }
    }
    if constexpr (PV) {
      if constexpr (M < 8) {
        a.S[Y][1][2 * M] = __builtin_amdgcn_exp2f(a.S[Y][1][2 * M]);
        a.S[Y][1][2 * M + 1] = __builtin_amdgcn_exp2f(a.S[Y][1][2 * M + 1]);
        a.pk[M >> 2][M & 3] = cvtpk(a.S[Y][0][2 * M], a.S[Y][0][2 * M + 1]);
        PIN(a.pk[M >> 2][M & 3]);
        if constexpr (M == 0) { a.l0 = fadd_s(a.l0, a.S[Y][0][14]); a.l1 = fadd_s(a.l1, a.S[Y][0][15]); }
      } else {
        constexpr int e = M - 8;
        a.pk[2 + (e >> 2)][e & 3] = cvtpk(a.S[Y][1][2 * e], a.S[Y][1][2 * e + 1]);
        PIN(a.pk[2 + (e >> 2)][e & 3]);
        a.l0 = fadd_s(a.l0, a.S[Y][1][2 * e]); a.l1 = fadd_s(a.l1, a.S[Y][1][2 * e + 1]);
      }
    }
    if constexpr (M == 11) { SB(); if constexpr (QK) wait_vm<9>(); else wait_vm<0>(); hook(IC<0>{}); bar(); hook(IC<1>{}); SB(); }
    if constexpr (PV && M >= 12) a.fr[(M - 12) & 7] = lds_b128(vaddr(Y, M - 12));
    SB();
  });
  static_for<16>([&](auto M_) AI {
    constexpr int M = decltype(M_)::v, ks = M >> 2, db = M & 3;
    if constexpr (PV) {
      a.o[db] = MFMA32(a.fr[M & 7], asbf(a.pk[ks]), a.o[db]);
      if constexpr (RD0[M] >= 0) a.fr[RD0[M] & 7] = lds_b128(vaddr(Y, RD0[M]));
      if constexpr (RD1[M] >= 0) a.fr[RD1[M] & 7] = lds_b128(vaddr(Y, RD1[M]));
    }
    if constexpr (QK) {
      if constexpr (M == 0) vo = a.voff | mk;
      if constexpr (M == 0 || M == 1 || M == 6 || M == 9) { constexpr int P = M == 0 ? 0 : M == 1 ? 1 : M == 6 ? 2 : 3; dma16c(dk + P * 1024, vo, c.rws, sok + P * 1024); }
      if constexpr (M == 2) {
        asm volatile("s_nop 7\n\ts_nop 3" : "+v"(a.S[X][0]), "+v"(a.S[X][1]));
        if (__builtin_expect(diag, 0)) {
          float ninf;
          asm volatile("v_mov_b32 %0, 0xff800000" : "=v"(ninf));
          const int dq2 = dq + (s + 2 == a.n1k ? 64 : 0);
          mask_rows<0>(a.S[X][0], dq2, ninf, make_iseq<16>{}); mask_rows<32>(a.S[X][1], dq2, ninf, make_iseq<16>{});
        }
      }
      if constexpr (M >= 2 && M < 6) {
        constexpr int r = 4 * (M - 2);
        if constexpr (M == 2) {
          a.mx0 = max3f(a.S[X][0][0], a.S[X][0][1], a.S[X][1][0]); a.mx1 = max3f(a.S[X][0][2], a.S[X][0][3], a.S[X][1][1]);
          a.mx0 = max3f(a.mx0, a.S[X][1][2], a.S[X][1][3]);
        } else {
          a.mx0 = max3f(a.mx0, a.S[X][0][r], a.S[X][0][r + 1]); a.mx1 = max3f(a.mx1, a.S[X][0][r + 2], a.S[X][0][r + 3]);
          a.mx0 = max3f(a.mx0, a.S[X][1][r], a.S[X][1][r + 1]); a.mx1 = max3f(a.mx1, a.S[X][1][r + 2], a.S[X][1][r + 3]);
        }
      }
      if constexpr (M == 6) {
        const float mm = max2f(a.mx0, a.mx1);
        auto pr = __builtin_amdgcn_permlane32_swap(__float_as_uint(mm), __float_as_uint(mm), false, false);
        a.mx0 = max2f(__uint_as_float(pr[0]), __uint_as_float(pr[1]));
      }
      if constexpr (M == 7) {
        if (__builtin_expect(first || __any(a.mx0 > THR), 0)) {
          const float dl = first ? fmaxf(a.mx0, -1e30f) : fmaxf(a.mx0, 0.f);
          a.mhat = first ? dl : a.mhat + dl;
          a.fct = __builtin_amdgcn_exp2f(-dl);
#pragma unroll
          for (int r = 0; r < 16; ++r) { a.S[X][0][r] -= dl; a.S[X][1][r] -= dl; a.negm[r] = -a.mhat; }
          a.pend = !first;
        }
      }
      if constexpr (M >= 8) {
        constexpr int e = M - 8;
        a.S[X][0][2 * e] = __builtin_amdgcn_exp2f(a.S[X][0][2 * e]);
        a.S[X][0][2 * e + 1] = __builtin_amdgcn_exp2f(a.S[X][0][2 * e + 1]);
        PIN(a.S[X][0][2 * e]); PIN(a.S[X][0][2 * e + 1]);
        if constexpr (e > 0) {
          a.t0 = e == 1 ? a.S[X][0][0] : fadd_s(a.t0, a.S[X][0][2 * e - 2]); a.t1 = e == 1 ? a.S[X][0][1] : fadd_s(a.t1, a.S[X][0][2 * e - 1]);
          PIN(a.t0); PIN(a.t1);
        }
      }
      if constexpr (M == 11) { SB(); wait_vm<PV ? 9 : 8>(); bar(); }
      if constexpr (M == 13) asm volatile("s_lshl_b64 exec, 15, %2\n\tglobal_load_dword %0, %1, off\n\ts_mov_b64 exec, -1" : "+v"(a.dummy) : "v"(a.ta), "s"(4 * s) : "memory", "scc");
      if constexpr (M >= 12) a.fr[(M - 12) & 7] = lds_b128(kaddr(Y, M - 12));
    }
    SB();
  });
}

constexpr unsigned WP = 288;
constexpr unsigned LSYNC = 161280;
constexpr unsigned C_LAND = 0, C_FREE = 64, C_GB = 128, C_CTXD = 136, C_MRG = 140, C_TICK = 144, LSTAT = LSYNC + 256;
#define ARRV 0
constexpr int XM = 12;
constexpr int XDEP(int NK) { return NK == 16 ? XM : 8; }
DI unsigned land(unsigned ph, unsigned q) { return LSYNC + C_LAND + (ph * 5 + q) * 4; }
DI unsigned fre(unsigned ph, unsigned q) { return LSYNC + C_FREE + (ph * 5 + q) * 4; }
DI void cnt_add(Ctx& c, unsigned off) {
  asm volatile("s_mov_b64 exec, 1\n\tds_add_u32 %0, %1\n\ts_mov_b64 exec, -1" ::"v"(c.ldsb + off), "v"(1u) : "memory");
}
#define wlog(...)
DI unsigned cnt_add_rtn(Ctx& c, unsigned off) {
  unsigned r;
  asm volatile("s_mov_b64 exec, 1\n\tds_add_rtn_u32 %0, %1, %2\n\ts_mov_b64 exec, -1\n\ts_waitcnt lgkmcnt(0)" : "=&v"(r) : "v"(c.ldsb + off), "v"(1u) : "memory");
  return rfl(r);
}
DI unsigned cnt_read(Ctx& c, unsigned off) { return __hip_atomic_load((LAS unsigned*)(size_t)(c.ldsb + off), __ATOMIC_RELAXED, __HIP_MEMORY_SCOPE_WORKGROUP); }
DI void prio_lo() { __builtin_amdgcn_s_setprio(0); }
DI void prio_hi(Ctx& c) { __builtin_amdgcn_s_setprio(3); }
DI void prio_gemm(Ctx& c, bool behind) {
  if (c.w >= 4) __builtin_amdgcn_s_setprio(1); else if (behind) __builtin_amdgcn_s_setprio(0); else __builtin_amdgcn_s_setprio(2);
}
DI void cnt_wait(Ctx& c, unsigned off, unsigned target) {
  asm volatile("" ::: "memory");
  unsigned spins = 0;
#pragma unroll 1
  while (rfl(cnt_read(c, off)) < target) { __builtin_amdgcn_s_sleep(1); if (++spins > (1u << 20)) break; }
  asm volatile("" ::: "memory");
}
DI void flag_w(Ctx& c, unsigned flag_off) {
  wait_vm<0>();
  fput(u32x2{c.ep0, c.ep1}, c.rws, c.lane == 0 ? flag_off : OOB);
}
DI void ctx_arrive(Ctx& c) {
  if (cnt_add_rtn(c, LSYNC + C_CTXD) == 7) {
    const unsigned jj = c.b >> 3;
    fput(u32x2{c.ep0, c.ep1}, c.rws, c.lane == 0 ? F_CTX + ((15 - (jj & 7)) * 32 + (c.b & 7) * 4 + (jj >> 3)) * 8 : OOB);
  }
}

DI void dma16m(unsigned lds_addr, unsigned voff, rsrc_t r, unsigned soff, u64 mask) {
  asm volatile("s_mov_b32 m0, %0\n\ts_mov_b64 exec, %4\n\ts_nop 3\n\tbuffer_load_dwordx4 %1, %2, %3 offen lds\n\ts_mov_b64 exec, -1" ::"s"(lds_addr), "v"(voff), "s"(r), "s"(soff), "s"(mask)
               : "memory", "m0");
}
struct WSrc { unsigned voff[9]; rsrc_t r; unsigned main, tail, pitch, colA, colB; unsigned idx, pend, ph; };
template <int NV = 5> DI void wsrc_voff(Ctx& c, WSrc& s) {
  unsigned lane;
  asm volatile("v_mbcnt_lo_u32_b32 %0, -1, 0\n\tv_mbcnt_hi_u32_b32 %0, -1, %0" : "=v"(lane));
#pragma unroll
  for (int p = 0; p < NV; ++p) {
    const unsigned ci = lane + 64 * p, rr = ci / 18, cc = ci - rr * 18;
    s.voff[p] = rr * s.pitch + (cc < 16 ? s.colA + cc * 16 : s.colB + (cc - 16) * 16);
  }
  if constexpr (NV == 5) {
    s.voff[5] = lane >= 16 ? ~0u : 0u;
    const unsigned l7 = (lane & 7) < 5 ? lane & 7 : 0;
    s.voff[6] = c.ldsb + (lane < 8 ? LSYNC + C_TICK + (s.ph * 5 + l7) * 4 : fre(s.ph - 1, 4) - 4 * l7);
    s.voff[7] = lane == 0 ? 3 : 8;
  }
}
template <int NV = 0> DI void wsrc_setup(Ctx& c, WSrc& s, rsrc_t r, unsigned pitch, unsigned colA, unsigned colB, unsigned rowMain, unsigned rowTail, unsigned ph) {
  s.r = r; s.pitch = pitch; s.main = rowMain * pitch; s.tail = rowTail * pitch;
  s.colA = colA; s.colB = colB; s.ph = ph; s.idx = s.pend = 0;
  if constexpr (NV > 0) wsrc_voff<NV>(c, s);
}
DI void w_group(Ctx& c, const WSrc& s, unsigned q) {
  const unsigned r0 = 128 * q + 32 * (c.w & 3);
#pragma unroll
  for (int p = 0; p < 9; ++p) dma16(c.ldsb + r0 * WP + p * 1024, s.voff[p], s.r, s.main + r0 * s.pitch);
}
DI void w_group8(Ctx& c, const WSrc& s, unsigned q) {
  const unsigned r0 = 128 * q + 16 * c.w;
#pragma unroll
  for (int p = 0; p < 4; ++p) dma16(c.ldsb + r0 * WP + p * 1024, s.voff[p], s.r, s.main + r0 * s.pitch);
  dma16m(c.ldsb + r0 * WP + 4096, s.voff[4], s.r, s.main + r0 * s.pitch, 0xffffffffull);
}
DI void poll_w(Ctx& c, unsigned flag_off, unsigned n) {
  prio_lo();
  poll_flags(c, flag_off, n, 1);
  prio_hi(c);
}
constexpr unsigned nitems(unsigned q) { return q == 4 ? 3 : 8; }
DI bool w_try(Ctx& c, WSrc& s) {
  if (s.idx) return false;
  unsigned idx, t;
#pragma unroll 1
  for (;;) {
    const unsigned v = __hip_atomic_load((LAS unsigned*)(size_t)s.voff[6], __ATOMIC_RELAXED, __HIP_MEMORY_SCOPE_WORKGROUP);
    const unsigned m = (unsigned)__builtin_amdgcn_ballot_w64(v < s.voff[7]);
    if ((m & 31) == 0) s.idx = 1;
    const unsigned av = m & ~(m >> 8) & 31;
    if (av == 0) return false;
    idx = __builtin_ctz(av);
    t = cnt_add_rtn(c, LSYNC + C_TICK + (s.ph * 5 + idx) * 4);
    if (t < (idx ? 8u : 3u)) break;
  }
  s.pend += 1u << (4 * idx);
  wlog(c, s.ph, 1, idx, true); wlog(c, s.ph, 2, idx);
  const unsigned lrow = 128 * (4 - idx) + 16 * t, so = (idx ? s.main + lrow * s.pitch : s.tail + 16 * t * s.pitch), sk = lrow == 544 ? 8 * s.pitch : 0u;
  const unsigned la = c.ldsb + lrow * WP;
  dma16(la, s.voff[0], s.r, so);
  dma16(la + 1024, s.voff[1], s.r, so);
  dma16(la + 2048, s.voff[2] + (s.voff[5] & sk), s.r, so);
  dma16(la + 3072, s.voff[3], s.r, so + sk);
  dma16m(la + 4096, s.voff[4], s.r, so + sk, 0xffffffffull);
  return true;
}
DI void w_signal(Ctx& c, const WSrc& s, unsigned items) {
  if (items) {
    unsigned lane;
    asm volatile("v_mbcnt_lo_u32_b32 %0, -1, 0" : "=v"(lane));
    const unsigned n = (items >> (4 * lane)) & 15, ad = c.ldsb + land(s.ph, 4) - 4 * lane;
    asm volatile("s_mov_b64 exec, 31\n\tds_add_u32 %0, %1\n\ts_mov_b64 exec, -1" ::"v"(ad), "v"(n) : "memory");
  }
}
DI void w_flush(Ctx& c, WSrc& s) { w_signal(c, s, s.pend); s.pend = 0; }
#define WMAX 2
#define FMAX 0
#define NMAX WMAX
#define LSRV 7
template <unsigned MX = WMAX> DI void w_try_wait(Ctx& c, WSrc& s, bool en = true) {
  const unsigned old = s.pend;
  unsigned k = 0;
  s.pend = 0;
#pragma unroll 1
  while (en && k < MX && w_try(c, s)) ++k;
  if (k == 0) wait_vm<0>(); else if (k == 1) wait_vm<5>(); else if (k == 2) wait_vm<10>(); else if (k == 3) wait_vm<15>(); else wait_vm<20>();
  w_signal(c, s, old);
}
DI void w_need(Ctx& c, WSrc& s, unsigned q, unsigned n) {
  unsigned spins = 0;
  wsrc_voff(c, s);
#pragma unroll 1
  for (;;) {
    w_try_wait<NMAX>(c, s);
    if (s.pend) { wait_vm<0>(); w_flush(c, s); }
    if (rfl(cnt_read(c, land(s.ph, q))) >= n || ++spins > (1u << 20)) break;
    __builtin_amdgcn_s_sleep(1);
  }
  asm volatile("" ::: "memory");
}
DI void flag_srv(Ctx& c, WSrc& s, unsigned flag_off, bool lsrv) {
  w_try_wait<FMAX>(c, s, lsrv || c.w < 4);
  fput(u32x2{c.ep0, c.ep1}, c.rws, c.lane == 0 ? flag_off : OOB);
}
DI void poll_srv(Ctx& c, WSrc& s, unsigned flag_off, unsigned n, bool lsrv) {
  const unsigned voff = c.lane < n ? flag_off + c.lane * 8 : OOB;
  const u64 t0 = __builtin_amdgcn_s_memrealtime();
  prio_lo();
  for (;;) {
    u32x2 f;
    fget(f, c.rws, voff);
    if (s.pend) { wait_vm<1>(); w_flush(c, s); }
    w_try_wait(c, s, lsrv || c.w < 4);
    PIN(f);
    if (__all((f[0] == c.ep0 && f[1] == c.ep1) || c.lane >= n)) break;
    if (__builtin_amdgcn_s_memrealtime() - t0 > 5000000ull) break;
    if (s.pend == 0) __builtin_amdgcn_s_sleep(1);
  }
  prio_hi(c);
}
constexpr unsigned LB1 = LSTAT + 256, LB2 = LB1 + 320;
DI void park_loads(Ctx& c, const u16* w1, const u16* b1, const u16* w2, const u16* b2) {
  const unsigned xcd = c.b & 7, nt = (c.b >> 3) & 3, nt32 = xcd * 4 + nt, w = c.w;
  const unsigned rr = c.lane >= 18 ? 1 : 0, cc = c.lane - 18 * rr, r0 = (w < 4 ? 40 : 48) + 2 * w;
  const unsigned c1 = cc < 16 ? nt32 * 256 + cc * 16 : 8192 + nt32 * 32 + (cc - 16) * 16, c2 = cc < 16 ? nt * 256 + cc * 16 : 1024 + nt * 32 + (cc - 16) * 16;
  dma16m(c.ldsb + (512 + 2 * w) * WP, rr * (FF * 2) + c1, mk_rsrc(w1, DM * FF * 2), (512 + r0) * (FF * 2), 0xfffffffffull);
  dma16m(c.ldsb + (528 + 2 * w) * WP, rr * (DM * 2) + c2, mk_rsrc(w2, FF * DM * 2), (4096 + 64 * xcd + r0) * (DM * 2), 0xfffffffffull);
  if (w == 0) dma16m(c.ldsb + LB1, c1, mk_rsrc(b1, FF * 2), 0, 0x3ffffull);
  if (w == 1) dma16(c.ldsb + LB2, c.lane * 16, mk_rsrc(b2, DM * 2), 0);
  if (w == 2) dma16m(c.ldsb + LB2 + 1024, c.lane * 16, mk_rsrc(b2, DM * 2), 1024, 0xffull);
}
DI void park_read(Ctx& c, u32x2 (&tl)[9], u32x2 (&tl2)[9]) {
  const unsigned ad = c.ldsb + (512 + (c.lane >> 2)) * WP + (c.lane & 3) * 8;
#pragma unroll
  for (int a = 0; a < 9; ++a) { tl[a] = __builtin_bit_cast(u32x2, lds_tr(ad + a * 32)); tl2[a] = __builtin_bit_cast(u32x2, lds_tr(ad + 16 * WP + a * 32)); }
  SB();
  cnt_add(c, fre(0, 4)); wlog(c, 1, 0, 0);
#pragma unroll
  for (int a = 0; a < 9; ++a) { PIN(tl[a]); PIN(tl2[a]); }
}
constexpr int KK(int NK, int v) { return NK == 18 ? (v < 2 ? 16 + v : 4 * (3 - (v - 2) / 4) + (v - 2) % 4) : 4 * (3 - v / 4) + v % 4; }
constexpr bool QSTART(int NK, int v) { return v < NK && v > 0 && (KK(NK, v) & 3) == 0; }
constexpr bool QEND(int NK, int v) { return (KK(NK, v) & 3) == 3 || KK(NK, v) == 17; }
template <int NK, int V> DI void x_ld(Ctx& c, u32x4 (&x)[2], unsigned voff, unsigned so, unsigned so2) {
  constexpr int k = KK(NK, V);
  gld16c2<(k & 1) * 2048>(x[0], x[1], voff, c.rws, k < 16 ? so + (k >> 1) * 4096 : so2);
}
template <int NK, int V0 = 0, int NV = 8> DI void x_pro(Ctx& c, u32x4 (&x)[XM][2], unsigned voff, unsigned so, unsigned so2) {
  static_for<NV>([&](auto V_) AI { x_ld<NK, V0 + decltype(V_)::v>(c, x[V0 + decltype(V_)::v], voff, so, so2); });
}
DI void x_ld4_masked(u32x4& a, u32x4& b, u32x4& d, u32x4& e, unsigned voff, rsrc_t r, unsigned so, unsigned mask) {
  asm volatile("s_mov_b32 exec_lo, %7\n\ts_mov_b32 exec_hi, %7\n\ts_nop 4\n\tbuffer_load_dwordx4 %0, %4, %5, %6 offen sc1\n\tbuffer_load_dwordx4 %1, %4, %5, %6 offen offset:256 sc1\n\t"
               "buffer_load_dwordx4 %2, %4, %5, %6 offen offset:2048 sc1\n\tbuffer_load_dwordx4 %3, %4, %5, %6 offen offset:2304 sc1\n\ts_mov_b64 exec, -1"
               : "+v"(a), "+v"(b), "+v"(d), "+v"(e) : "v"(voff), "s"(r), "s"(so), "s"(mask) : "memory");
}
DI void x_pro_masked16(Ctx& c, u32x4 (&x)[XM][2], unsigned so, unsigned mask) {
  const unsigned voff = x_voff(c);
#pragma unroll
  for (int p = 0; p < XM / 2; ++p) x_ld4_masked(x[2 * p][0], x[2 * p][1], x[2 * p + 1][0], x[2 * p + 1][1], voff, c.rws, so + (KK(16, 2 * p) >> 1) * 4096, mask);
}
template <int L> DI void wlane(unsigned& v, unsigned s) { asm volatile("v_writelane_b32 %0, %1, %2" : "+v"(v) : "s"(s), "i"(L)); }
struct Sched {
  int NK, XD;
  constexpr int T0() const { return 9 * (NK - 4); }
  constexpr int sv(int n) const { return n < T0() ? n / 9 : NK - 4 + (n - T0()) % 4; }
  constexpr int sa(int n) const { return n < T0() ? n % 9 : ((n - T0()) / 4 == 0 ? 8 : (n - T0()) / 4 - 1); }
  constexpr int g0(int G) const { return G == 3 ? T0() + 8 : 9 * (NK - 16 + 1 + 4 * G) + 1; }
  constexpr int gn(int G) const { return G == 0 ? 4 : 9; }
  constexpr int wg(int n) const { for (int G = 0; G < 4; ++G) if (n >= g0(G) && (n - g0(G)) % 3 == 0 && (n - g0(G)) / 3 < gn(G)) return G; return -1; }
  constexpr int wp(int n) const { return (n - g0(wg(n))) / 3; }
  constexpr int gsig(int n) const { for (int G = 0; G < 3; ++G) if (n == g0(G) + 3 * (gn(G) - 1) + 27) return G; return -1; }
  constexpr int st(int n) const {
    const int m = n - T0();
    if (m == 6) return 0;
    if (m >= 13 && (m - 13) % 8 == 0) return 1 + 2 * ((m - 13) / 8);
    if (m >= 15 && (m - 15) % 8 == 0) return 2 + 2 * ((m - 15) / 8);
    return -1;
  }
  constexpr bool sp(int n) const { return n < T0() ? (n % 18 == 0 && n >= 18) : ((n - T0()) % 18 == 0); }
  constexpr int between(int n0, int n1) const {
    int cnt = 0;
    for (int i = n0; i < n1; ++i) { if (i < T0() && sa(i) == 8 && sv(i) + XD < NK) cnt += 2; if (st(i) >= 0) cnt += 1; }
    return cnt;
  }
  constexpr int younger(int tag, int n, bool ldr) const {
    int cnt = 0, last = -1;
    for (int v = 0; v < XD; ++v) { if (100 + v == tag) last = cnt + 1; cnt += 2; }
    for (int i = 0; i < n; ++i) {
      if (i < T0() && sa(i) == 8 && sv(i) + XD < NK) { if (100 + sv(i) + XD == tag) last = cnt + 1; cnt += 2; }
      if (ldr && wg(i) >= 0) { if (200 + wg(i) == tag) last = cnt; cnt += 1; }
      if (st(i) >= 0) cnt += 1;
    }
    return cnt - 1 - last;
  }
};
template <int NK, int KORD, int PH, int GST, class St>
DI void gemm_g(Ctx& c, f32x4 (&acc)[2][9], u32x4 (&x)[XM][2], u32x2 (&tl)[9], u32x2 (&tl2)[9], WSrc& ws, WSrc& wn, unsigned so, unsigned so2, unsigned flag_off, St&& st) {
  constexpr unsigned RT = KORD == 0 ? 16 : 8;
  constexpr int LA = 6, NS = NK * 9;
  constexpr int XD = XDEP(NK);
  constexpr Sched S{NK, XD};
  constexpr bool LD = PH < 2;
  const unsigned g = c.lane >> 4, q = (c.lane & 15) >> 2, pp = c.lane & 3;
  const unsigned wrow1 = 16 * (g >> 1) + 4 * (g & 1) + q, wrow = KORD == 0 ? 4 * g + q : wrow1;
  unsigned wb[3], wt[2];
#pragma unroll
  for (int i = 0; i < 3; ++i) { wb[i] = c.ldsb + wrow * WP + pp * 8 + i * 7 * 32 * WP; PIN(wb[i]); }
  if constexpr (NK == 18) {
    wt[0] = c.ldsb + (512 + wrow1) * WP + pp * 8; wt[1] = c.ldsb + (544 + wrow1 - 8 * (g >> 1)) * WP + pp * 8;
    PIN(wt[0]); PIN(wt[1]);
  }
  unsigned voff = x_voff(c); PIN(voff);
  bf16x4 wl[8][2];
  auto rdw = [&](auto N_, auto H_) AI {
    constexpr int N = decltype(N_)::v, H = decltype(H_)::v, v = S.sv(N), a = S.sa(N), k = KK(NK, v);
    if constexpr (k < 16) wl[N & 7][H] = lds_tr(wb[k / 7] + ((k % 7) * 32 + H * RT) * WP + a * 32);
    else if constexpr (k == 16) wl[N & 7][H] = lds_tr(wt[0] + H * 8 * WP + a * 32);
    else if constexpr (H == 0) wl[N & 7][H] = lds_tr(wt[1] + a * 32);
    else {
      u32x2 t;
      asm volatile("v_mov_b64 %0, %1" : "=v"(t) : "v"(tl[a]));
      wl[N & 7][1] = __builtin_bit_cast(bf16x4, t);
    }
    if constexpr (LD && H == 1 && ((N < S.T0() && a == 8 && QEND(NK, v)) || N == NS - 1)) { SB(); cnt_add(c, fre(PH, k >> 2)); wlog(c, PH + 1, 0, 4 - (k >> 2)); }
  };
  bool behind = false;
  if constexpr (PH > 0) {
    if (ws.pend) { wait_vm<2 * XD>(); w_flush(c, ws); }
    const unsigned l4 = cnt_read(c, land(PH, 4)), f0 = cnt_read(c, fre(PH - 1, 0));
    behind = (c.w < 4) & (rfl(f0) < 8);
    if (__builtin_expect(rfl(l4) < 3, 0)) w_need(c, ws, 4, 3);
  }
  prio_gemm(c, behind);
  static_for<LA>([&](auto N) AI { rdw(N, IC<0>{}); rdw(N, IC<1>{}); });
  unsigned lc = 0;
  SB();
  static_for<NS>([&](auto N_) AI {
    constexpr int N = decltype(N_)::v, v = S.sv(N), a = S.sa(N);
    if constexpr (N < S.T0() ? a == 0 : N < S.T0() + 4) {
      wait_vm<S.younger(100 + v, N, false)>();
      PIN(x[v % XD][0]); PIN(x[v % XD][1]);
    }
    if constexpr (N < S.T0() && a == 0) {
      if constexpr (v == 0 || v == 8) STAMPG(GST + (v >> 3));
      if constexpr (PH == 0 && v == ARRV) { if (c.w < 4) { wait_vm<S.between(0, N)>(); ctx_arrive(c); } }
      if constexpr (PH == 0 && v == 7) park_read(c, tl, tl2);
      constexpr bool chk2 = QSTART(NK, v + 2), chk1 = QSTART(NK, v + 1);
      if constexpr (chk1) {
        if (__builtin_expect(rfl(lc) < (PH > 0 ? 8 : 4), 0)) { if constexpr (PH > 0) { prio_hi(c); w_need(c, ws, KK(NK, v + 1) >> 2, 8); prio_gemm(c, behind); } else cnt_wait(c, land(0, KK(NK, v + 1) >> 2), 4); }
      }
      if constexpr (chk2) lc = cnt_read(c, land(PH, KK(NK, v + 2) >> 2));
    }
    if constexpr (PH > 0 && S.sp(N) && N < S.T0()) {
      if (__builtin_expect(behind, 0)) { behind = rfl(cnt_read(c, fre(PH - 1, 0))) < 8; prio_gemm(c, behind); }
    }
    SB();
    const bf16x8 wf = cat(wl[N & 7][0], wl[N & 7][1]);
    acc[0][a] = MFMA16(wf, asbf(x[v % XD][0]), acc[0][a]);
    SB();
    if constexpr (N + LA < NS) rdw(IC<N + LA>{}, IC<0>{});
    SB();
    acc[1][a] = MFMA16(wf, asbf(x[v % XD][1]), acc[1][a]);
    SB();
    if constexpr (N + LA < NS) rdw(IC<N + LA>{}, IC<1>{});
    if constexpr (N < S.T0() && a == 8 && v + XD < NK) x_ld<NK, v + XD>(c, x[v % XD], voff, so, so2);
    if constexpr (S.st(N) >= 0) st(IC<S.st(N)>{});
    SB();
  });
  prio_hi(c);
  st(IC<7>{}); st(IC<8>{});
  STAMPG(GST + 2);
  if constexpr (LD) { wsrc_voff(c, wn); flag_srv(c, wn, flag_off, LSRV & (PH ? 4 : 1)); } else flag_w(c, flag_off);
}
DI f32x4 relu2(const f32x4& a) {
  const float inf = 3.4028234664e38f;
  const float r0 = __builtin_amdgcn_fmed3f(a[0], 0.f, inf), r1 = __builtin_amdgcn_fmed3f(a[1], 0.f, inf), r2 = __builtin_amdgcn_fmed3f(a[2], 0.f, inf), r3 = __builtin_amdgcn_fmed3f(a[3], 0.f, inf);
  return f32x4{r0 * r0, r1 * r1, r2 * r2, r3 * r3};
}
DI u32x4 pack_single(const f32x4& r0, const f32x4& r1) {
  const auto a = __builtin_amdgcn_permlane32_swap(cvtpk(r0[0], r0[1]), cvtpk(r1[0], r1[1]), false, false);
  const auto b = __builtin_amdgcn_permlane32_swap(cvtpk(r0[2], r0[3]), cvtpk(r1[2], r1[3]), false, false);
  return u32x4{a[0], b[0], a[1], b[1]};
}
template <int I> DI void part_store(Ctx& c, const f32x4 (&acc)[2][9], unsigned base, unsigned so) {
  const unsigned g = c.lane >> 4;
  if constexpr (I == 0) gst16c(pack_single(acc[0][8], acc[1][8]), c.rws, (g >> 1) * (4608 - 128) + base + 1024, so);
  else { constexpr int i = (I - 1) >> 1, u = (I - 1) & 1; gst16c(pack_pair(acc[u][2 * i], acc[u][2 * i + 1]), c.rws, base, so + u * 4608 + i * 256); }
}
DI unsigned part_base(Ctx& c) { const unsigned l15 = c.lane & 15; return (l15 >> 2) * 1152 + (l15 & 3) * 16 + (c.lane >> 4) * 64; }
struct Red { u32x4 p[8]; u32x4 s; };
DI void red_load(Ctx& c, Red& d, unsigned so) {
  gld16c<1024>(d.s, (c.lane >> 3) * 36864 + (c.lane & 7) * 16, c.rws, so);
#pragma unroll
  for (int s = 0; s < 8; ++s) gld16c<0>(d.p[s], c.lane * 16, c.rws, so + s * 36864);
}
#define RED_PIN(d) asm volatile("s_waitcnt vmcnt(0)" : "+v"(d.p[0]), "+v"(d.p[1]), "+v"(d.p[2]), "+v"(d.p[3]), "+v"(d.p[4]), "+v"(d.p[5]), "+v"(d.p[6]), "+v"(d.p[7]), "+v"(d.s)::"memory")
DI void gld8(u32x2& d, unsigned voff, rsrc_t r, unsigned soff) { asm volatile("s_nop 4\n\tbuffer_load_dwordx2 %0, %1, %2, %3 offen" : "=v"(d) : "v"(voff), "s"(r), "s"(soff) : "memory"); }
DI void job_offs(Ctx& c, unsigned row, unsigned& o0, unsigned& o2) {
  const unsigned pr = c.lane >> 4, g = (c.lane >> 2) & 3, nt = c.w & 3;
  o0 = row * (DM * 2) + (nt * 128 + pr * 32 + 4 * g) * 2;
  o2 = c.lane < 8 ? row * (DM * 2) + (512 + nt * 16 + 4 * g) * 2 : OOB;
}
DI void row_load(Ctx& c, u32x2 (&r)[4], rsrc_t rs, unsigned row) {
  unsigned o0, o2; job_offs(c, row, o0, o2);
  gld8(r[0], o0, rs, 0); gld8(r[1], o0, rs, 32); gld8(r[2], o2, rs, 0); gld8(r[3], o2, rs, 16);
}
DI float sum_hi3(float x) {
  x += __uint_as_float(__builtin_amdgcn_update_dpp(0, __float_as_uint(x), 0x128, 0xf, 0xf, false));
  { auto r = __builtin_amdgcn_permlane16_swap(__float_as_uint(x), __float_as_uint(x), false, false); x = __uint_as_float(r[0]) + __uint_as_float(r[1]); }
  return xhalf_sum(x);
}
DI void red_sum(const Red& d, float (&a)[16]) {
#pragma unroll
  for (int e = 0; e < 8; ++e) a[e] = 0.f;
#pragma unroll
  for (int s = 0; s < 8; ++s) {
#pragma unroll
    for (int e = 0; e < 4; ++e) { a[2 * e] += bflo(d.p[s][e]); a[2 * e + 1] += bfhi(d.p[s][e]); }
  }
#pragma unroll
  for (int e = 0; e < 4; ++e) { a[8 + 2 * e] = sum_hi3(bflo(d.s[e])); a[9 + 2 * e] = sum_hi3(bfhi(d.s[e])); }
}
DI float sum_tok4(float x) {
  x += __uint_as_float(__builtin_amdgcn_update_dpp(0, __float_as_uint(x), 0x124, 0xf, 0xf, false));
  x += __uint_as_float(__builtin_amdgcn_update_dpp(0, __float_as_uint(x), 0x128, 0xf, 0xf, false));
  { auto r = __builtin_amdgcn_permlane16_swap(__float_as_uint(x), __float_as_uint(x), false, false); x = __uint_as_float(r[0]) + __uint_as_float(r[1]); }
  return xhalf_sum(x);
}
DI void ln2_job(Ctx& c, const Red& d, const u32x2 (&xr)[4], unsigned (&hw)[8], unsigned row0) {
  const unsigned lane = c.lane, tok = lane & 3, g = (lane >> 2) & 3, pr = lane >> 4, nt = c.w & 3;
  const unsigned stat = c.ldsb + LSTAT + (c.w >> 2) * 128;
  const bool sing = lane < 8;
  float a[16], v[16];
  red_sum(d, a);
#pragma unroll
  for (int e = 0; e < 8; ++e) {
    const unsigned xx = xr[e >> 1][e & 1];
    hw[e] = cvtpk(bflo(xx) + a[2 * e], bfhi(xx) + a[2 * e + 1]); v[2 * e] = bflo(hw[e]); v[2 * e + 1] = bfhi(hw[e]);
  }
  float s1 = 0.f, s0 = 0.f;
#pragma unroll
  for (int e = 0; e < 8; ++e) { s1 += v[e]; s0 += v[8 + e]; }
  const float mw = sum_tok4(sing ? s1 + s0 : s1) * (1.f / 144);
  float s2 = 0.f, s3 = 0.f;
#pragma unroll
  for (int e = 0; e < 8; ++e) { const float t = v[e] - mw, t2 = v[8 + e] - mw; s2 += t * t; s3 += t2 * t2; }
  s2 = sum_tok4(sing ? s2 + s3 : s2);
  if (lane < 4) *(LAS f32x2*)(size_t)(stat + (nt * 4 + lane) * 8) = f32x2{mw, s2};
  SB();
  cnt_add(c, LSYNC + C_GB + (c.w >> 2) * 4);
  cnt_wait(c, LSYNC + C_GB + (c.w >> 2) * 4, 4);
  f32x2 st[4];
#pragma unroll
  for (int i = 0; i < 4; ++i) st[i] = *(LAS f32x2*)(size_t)(stat + (i * 4 + tok) * 8);
  const float mean = (st[0][0] + st[1][0] + st[2][0] + st[3][0]) * 0.25f;
  float m2 = st[0][1] + st[1][1] + st[2][1] + st[3][1];
#pragma unroll
  for (int i = 0; i < 4; ++i) { const float t = st[i][0] - mean; m2 += 144.f * t * t; }
  const float rs = rsqrtf(m2 * (1.f / DM) + 1e-5f);
#pragma unroll
  for (int e = 0; e < 16; ++e) v[e] = (v[e] - mean) * rs;
  const unsigned row = row0 + tok, mb = row >> 5, r32 = row & 31;
  const unsigned o0 = ((mb * 36 + 2 * (nt * 4 + pr) + (g >> 1)) * 64 + (g & 1) * 32 + r32) * 16;
  const unsigned o1 = sing ? ((mb * 36 + 32 + nt) * 64 + g * 32 + r32) * 16 : OOB;
  gst16c(u32x4{cvtpk(v[0], v[1]), cvtpk(v[2], v[3]), cvtpk(v[4], v[5]), cvtpk(v[6], v[7])}, c.rws, o0, WS_HN);
  gst16c(u32x4{cvtpk(v[8], v[9]), cvtpk(v[10], v[11]), cvtpk(v[12], v[13]), cvtpk(v[14], v[15])}, c.rws, o1, WS_HN);
}

DI void phase_attn(Ctx& c, QOut& qo, WSrc& wso, u32x4 (&xr)[XM][2], const u16* wo, const u16* w1, const u16* b1, const u16* w2, const u16* b2) {
  AttnState a;
  const unsigned ldsb = c.ldsb, lane = c.lane, w = c.w, b = c.b;
  const unsigned xcd = b & 7, jj = b >> 3, head = xcd * 4 + (jj >> 3), pi = jj & 7;
  const unsigned l31 = lane & 31, h = lane >> 5;
  const unsigned kh = w >> 2, rg = kh ? 7 - w : w, partner = 7 - w;
  const unsigned n1 = 2 * pi + 2, n1k = kh ? n1 : 0, n1t = kh ? n1 : 99;
  a.voff = lane * 16; a.ldsb = ldsb; a.w = w; a.n1 = n1; a.n1k = n1k;
  a.kbase = WS_K + head * (SEQ * HD * 2) + w * 4096; a.vbase = WS_V + head * (SEQ * HD * 2) + w * 4096;
  a.kb[0] = ldsb + lane * 16; a.kb[1] = a.kb[0] + 32768; a.vb[0] = a.kb[0] + 65536; a.vb[1] = a.kb[0] + 98304;
  PIN(a.kb[0]); PIN(a.kb[1]); PIN(a.vb[0]); PIN(a.vb[1]);
  if (kh == 0) {
#pragma unroll
    for (int d = 0; d < 8; ++d) *(LAS u32x4*)(size_t)(ldsb + LE + rg * 8192 + d * 1024 + lane * 16) = qo.q[d];
  }
  if (__builtin_expect(!qo.kdone, 0)) {
    wait_flags(c, F_K + head * 64, 8, 1);
#pragma unroll
    for (int p = 0; p < 4; ++p) dma16c(ldsb + kh * 16384 + w * 4096 + p * 1024, a.voff, c.rws, a.kbase + p * 1024);
  }
  STAMP(8);
#pragma unroll
  for (int d = 0; d < 4; ++d) a.o[d] = f32x16{};
#pragma unroll
  for (int d = 0; d < 8; ++d) a.qf[d] = asbf(qo.q[d]);
  a.negm = f32x16{}; a.mhat = 0.f; a.l0 = a.l1 = 0.f; a.fct = 1.f; a.pend = false;
  {
    unsigned bytes;
    asm volatile("s_mov_b32 %0, .Lfb_end-.Lfb_begin" : "=s"(bytes));
    const unsigned g = lane * 8 + w, sel = g < 162 ? 0 : g < 324 ? 1 : 2, li = g - 162 * sel, r = (li * 57) >> 9, cl = li - 9 * r;
    const unsigned o2 = (r < 16 ? 512 * xcd + jj * 16 + r : 4096 + 64 * xcd + jj * 2 + r - 16) * (DM * 2) + cl * 128;
    const unsigned o1 = (jj * 18 + r) * (FF * 2) + (cl < 8 ? xcd * 1024 + cl * 128 : 8192 + xcd * 128);
    const unsigned oc = ((g - 468) * 32 + jj) * 128;
    const bool code = g >= 468;
    const u64 base = code ? c.pc0 : sel == 0 ? (u64)w2 : sel == 1 ? (u64)w1 : (u64)wo;
    a.ta = base + (code ? (oc < bytes ? oc : 0) : sel == 1 ? o1 : o2);
    a.dummy = 0;
    PIN(a.ta);
  }
  if (kh == 0) wait_vm<0>();
  bar();
  STAMP(9);
#pragma unroll
  for (int n = 0; n < 4; ++n) a.fr[n] = lds_b128(a.kb[0] + ((n & 1) * 8 + (n >> 1)) * 1024);
  const int dq = (int)(rg * 32 + l31) - (int)(kh * 64) - 4 * (int)h;
  const unsigned dA = rg < 2 ? n1t - 2 : 99, dB = n1t - 1, dC = (kh || rg < 2) ? 16 : 99;

  auto post = [&](auto X_, unsigned s) AI {
    constexpr int X = decltype(X_)::v;
    if constexpr (X == 0) {
      if (__builtin_expect(s == n1t, 0)) {
        const float inv = 1.f / xhalf_sum(a.l0 + a.l1);
        const unsigned so = WS_CTX + ((pi * 4 + rg) * 256 + head * 8) * 1024;
#pragma unroll
        for (int d = 0; d < 4; ++d) {
          gst16c(pack8<0>(a.o[d], inv), c.rws, a.voff, so + d * 2048);
          gst16c(pack8<1>(a.o[d], inv), c.rws, a.voff, so + d * 2048 + 1024);
          a.o[d] = f32x16{};
        }
        a.l0 = a.l1 = 0.f;
        a.vb[1] += 16384;
      }
      if (__builtin_expect(s + 2 == n1t, 0)) a.kb[0] += 16384;
    } else {
      if (__builtin_expect(s == n1 + 1, 0)) flag_store(c, F_CTX + (pi * 32 + head) * 8);
    }
    if (__builtin_expect(a.pend, 0)) {
#pragma unroll
      for (int d = 0; d < 4; ++d)
#pragma unroll
        for (int r = 0; r < 16; ++r) a.o[d][r] *= a.fct;
      a.l0 *= a.fct; a.l1 *= a.fct; a.pend = false;
    }
    a.l0 = fadd_s(a.l0, a.t0); a.l1 = fadd_s(a.l1, a.t1);
    if constexpr (X == 1) {
      if (__builtin_expect(s + 1 == n1t, 0)) {
#pragma unroll
        for (int d = 0; d < 8; ++d) a.qf[d] = lds_b128(ldsb + LE + rg * 8192 + d * 1024 + lane * 16);
        a.negm = f32x16{};
        a.kb[1] += 16384; a.vb[0] += 16384;
      }
    }
    SB();
  };
  attn_iter<0, true, false>(c, a, 0, true, dA == 0, dq);
  post(IC<0>{}, 0);
  for (unsigned s = 1; s < 17; s += 2) {
    attn_iter<1, true, true>(c, a, s, false, s == dB, dq); post(IC<1>{}, s);
    attn_iter<0, true, true>(c, a, s + 1, s + 1 == n1t, s + 1 == dA || s + 1 == dC, dq); post(IC<0>{}, s + 1);
  }
  const unsigned mtH = jj >> 2;
  u32x2 fl;
  flag_load(c, fl, F_CTX + (mtH * 32 + xcd * 4) * 8, 4);
  if (w == 0) *(LAS unsigned*)(size_t)(ldsb + LSYNC + lane * 4) = 0;
  attn_iter<1, false, true, true>(c, a, 17, false, false, dq, [&](auto P_) AI {
    if constexpr (decltype(P_)::v == 0) return;
    asm volatile("" ::"v"(a.dummy));
    if (n1 == 16) flag_store(c, F_CTX + (7 * 32 + head) * 8);
    wsrc_setup<9>(c, wso, mk_rsrc(wo, NQ * DM * 2), DM * 2, (jj & 3) * 256, 1024 + (jj & 3) * 32, 512 * xcd, 0, 0);
    w_group8(c, wso, 3);
    park_loads(c, w1, b1, w2, b2);
    flag_check(c, fl, F_CTX + (mtH * 32 + xcd * 4) * 8, 4);
    x_pro<16>(c, xr, kh == 0 ? x_voff(c) : OOB, WS_CTX + ((mtH * 4 + (w & 3)) * 256 + xcd * 32) * 1024, 0);
  });
  STAMP(10);
  {
    const unsigned stat = ldsb + 544 * WP;
    const float mo = a.mhat, lt = xhalf_sum(a.l0 + a.l1);
    *(LAS f32x2*)(size_t)(stat + w * 512 + lane * 8) = f32x2{mo, lt};
    const unsigned mine = ldsb + w * 8192 + lane * 16, theirs = ldsb + partner * 8192 + lane * 16;
    auto put = [&](const f32x16 x, int r) AI {
#pragma unroll
      for (int e = 0; e < 4; ++e) *(LAS f32x4*)(size_t)(mine + (r * 4 + e) * 1024) = f32x4{x[4 * e], x[4 * e + 1], x[4 * e + 2], x[4 * e + 3]};
    };
    if (kh) { put(a.o[0], 0); put(a.o[1], 1); SB(); a.o[0] = a.o[2]; a.o[1] = a.o[3]; } else { put(a.o[2], 0); put(a.o[3], 1); SB(); }
    x_pro<16, 8, XM - 8>(c, xr, kh == 0 ? x_voff(c) : OOB, WS_CTX + ((mtH * 4 + (w & 3)) * 256 + xcd * 32) * 1024, 0);
    wait_vm<2 * (XM - 8)>();
    bar();
    if (kh) w_group(c, wso, 2);
    const f32x2 ps = *(LAS f32x2*)(size_t)(stat + partner * 512 + lane * 8);
    const float mm = fmaxf(mo, ps[0]);
    const float fo = __builtin_amdgcn_exp2f(mo - mm), fp = __builtin_amdgcn_exp2f(ps[0] - mm);
    const float inv = 1.f / (lt * fo + ps[1] * fp), f = fo * inv, f2 = fp * inv;
    const unsigned mb = (15 - pi) * 4 + rg;
#pragma unroll
    for (int r = 0; r < 2; ++r) {
      f32x16 x = a.o[r];
#pragma unroll
      for (int e = 0; e < 4; ++e) {
        const f32x4 y = *(LAS f32x4*)(size_t)(theirs + (r * 4 + e) * 1024);
#pragma unroll
        for (int k = 0; k < 4; ++k) x[4 * e + k] = x[4 * e + k] * f + y[k] * f2;
      }
      if (r == 1) { SB(); cnt_add(c, LSYNC + C_MRG); }
      const unsigned so = WS_CTX + (mb * 256 + head * 8 + 2 * (kh * 2 + r)) * 1024;
      gst16c(pack8<0>(x), c.rws, lane * 16, so);
      gst16c(pack8<1>(x), c.rws, lane * 16, so + 1024);
    }
  }
  STAMP(11);
}

struct TId { unsigned sp, nt, mt, nt32, mb, own, wq; bool isH; };
DI TId tids(Ctx& c) {
  fresh(c);
  TId t;
  const unsigned xcd = c.b & 7, jj = c.b >> 3;
  t.isH = c.w < 4; t.wq = c.w & 3; t.sp = xcd; t.nt = jj & 3; t.mt = (t.isH ? 0 : 8) + (jj >> 2);
  t.nt32 = xcd * 4 + t.nt; t.mb = t.mt * 4 + t.wq; t.own = t.nt * 8 + t.sp;
  return t;
}
DI void acc_zero(f32x4 (&acc)[2][9]) {
#pragma unroll
  for (int u = 0; u < 2; ++u)
#pragma unroll
    for (int a = 0; a < 9; ++a) acc[u][a] = f32x4{};
}
DI void phase_tail(Ctx& c, WSrc& wso, u32x4 (&xr)[XM][2], const u16* x, const u16* w1, const u16* b1, const u16* w2, const u16* b2, u16* out) {
  f32x4 acc[2][9]; u32x2 tl[9], tl2[9]; unsigned hw[8]; WSrc ws, wt;
  {
    const TId t = tids(c);
    const unsigned xo = WS_CTX + (t.mb * 256 + t.sp * 32) * 1024;
    if (!t.isH) { cnt_wait(c, LSYNC + C_MRG, 8); w_group(c, wso, 1); wait_vm<13>(); cnt_add(c, land(0, 2)); w_group(c, wso, 0); }
    if (!t.isH) {
      wait_vm<18>(); ctx_arrive(c);
      wait_vm<9>(); cnt_add(c, land(0, 1));
      wait_vm<0>(); cnt_add(c, land(0, 0));
      unsigned dm = 0;
      w_touch<2>(c, dm, w1, w1, w1, c.w, false); w_touch<2>(c, dm, w1, w1, w1, c.w - 4, false);
      poll_w(c, F_CTX + (t.mt * 32 + t.sp * 4) * 8, 4);
      asm volatile("" ::"v"(dm));
    }
    prio_hi(c);
    STAMPG(12);
    x_pro_masked16(c, xr, xo, 0u - (c.w >> 2));
    wsrc_setup(c, ws, mk_rsrc(w1, DM * FF * 2), FF * 2, t.nt32 * 256, 8192 + t.nt32 * 32, 0, 512, 1);
    acc_zero(acc);
    const unsigned pb = part_base(c), po = WS_P1 + (((t.mt * 4 + t.nt) * 8 + t.sp) * 8 + 2 * t.wq) * 4608;
    gemm_g<16, 1, 0, 13>(c, acc, xr, tl, tl2, wso, ws, xo, 0, F_P1 + (t.mb * 32 + t.own) * 8, [&](auto I) AI { part_store<decltype(I)::v>(c, acc, pb, po); });
    STAMPG(16);
  }
  {
    const TId t = tids(c);
    u32x2 xres[4]; Red rd;
    row_load(c, xres, mk_rsrc(x, SEQ * DM * 2), t.mt * 128 + t.own * 4 + (c.lane & 3));
    unsigned dummy = 0;
    w_touch<4>(c, dummy, w2, w2, w2, c.w, false);
    poll_srv(c, ws, F_P1 + ((t.mt * 4 + (t.own >> 3)) * 32 + t.wq * 8) * 8, 8, LSRV & 1);
    red_load(c, rd, WS_P1 + (t.mt * 256 + t.wq * 64 + (t.own >> 2)) * 4608 + (t.own & 3) * 1152);
    STAMPG(17);
    asm volatile("" ::"v"(dummy));
    PIN(xres[0]); PIN(xres[1]); PIN(xres[2]); PIN(xres[3]);
    RED_PIN(rd);
    w_flush(c, ws);
    STAMPG(18);
    ln2_job(c, rd, xres, hw, t.mt * 128 + t.own * 4);
#pragma unroll
    for (int e = 0; e < 8; ++e) PIN(hw[e]);
    STAMPG(19);
    flag_srv(c, ws, F_HN + ((t.mt * 32 + t.own) * 4 + t.wq) * 8, LSRV & 2);
    STAMPG(20);
    poll_srv(c, ws, F_HN + t.mb * 256, 32, LSRV & 2);
    STAMPG(21);
    asm volatile("" ::"v"(dummy));
    const unsigned xh = WS_HN + t.mb * 36 * 1024;
    x_pro<18>(c, xr, x_voff(c), xh, xh + 32768);
    wsrc_setup(c, wt, mk_rsrc(w2, FF * DM * 2), DM * 2, t.nt * 256, 1024 + t.nt * 32, 512 * t.sp, 4096 + 64 * t.sp, 2);
#pragma unroll
    for (int a = 0; a < 9; ++a) {
      const u32x2 bias = *(LAS u32x2*)(size_t)(c.ldsb + LB1 + a * 32 + (c.lane >> 4) * 8);
      acc[0][a] = acc[1][a] = f32x4{bflo(bias[0]), bfhi(bias[0]), bflo(bias[1]), bfhi(bias[1])};
    }
    const unsigned vo = x_voff(c), ho = WS_H1 + (t.mb * 288 + 8 * t.nt32) * 1024, hs = WS_H1 + (t.mb * 288 + 256 + t.nt32) * 1024;
    gemm_g<18, 0, 1, 22>(c, acc, xr, tl, tl, ws, wt, xh, xh + 32768, F_H1 + (t.mb * 32 + t.nt32) * 8, [&](auto I_) AI {
      constexpr int I = decltype(I_)::v;
      if constexpr (I == 0) {
        const unsigned g = c.lane >> 4;
        gst16c(pack_single(relu2(acc[0][8]), relu2(acc[1][8])), c.rws, ((g & 1) * 32 + (g >> 1) * 16 + (c.lane & 15)) * 16, hs);
      } else {
        constexpr int i = (I - 1) >> 1, u = (I - 1) & 1;
        gst16c(pack_pair(relu2(acc[u][2 * i]), relu2(acc[u][2 * i + 1])), c.rws, vo, ho + i * 2048 + u * 256);
      }
    });
    STAMPG(25);
  }
  {
    const TId t = tids(c);
    poll_srv(c, wt, F_H1 + (t.mb * 32 + t.sp * 4) * 8, 4, LSRV & 4);
    STAMPG(26);
    const unsigned x1 = WS_H1 + (t.mb * 288 + t.sp * 32) * 1024, x1t = WS_H1 + (t.mb * 288 + 256 + t.sp * 4) * 1024;
    x_pro<18>(c, xr, x_voff(c), x1, x1t);
    acc_zero(acc);
    const unsigned pb = part_base(c), po = WS_P2 + (((t.mt * 4 + t.nt) * 8 + t.sp) * 8 + 2 * t.wq) * 4608;
    gemm_g<18, 0, 2, 27>(c, acc, xr, tl2, tl2, wt, wt, x1, x1t, F_P2 + (t.mb * 32 + t.own) * 8, [&](auto I) AI { part_store<decltype(I)::v>(c, acc, pb, po); });
    STAMPG(30);
  }
  {
    const TId t = tids(c);
    u32x2 bb[4]; Red rd;
    {
      unsigned o0, o2; job_offs(c, 0, o0, o2); o2 = c.lane < 8 ? o2 : 0;
      bb[0] = *(LAS u32x2*)(size_t)(c.ldsb + LB2 + o0); bb[1] = *(LAS u32x2*)(size_t)(c.ldsb + LB2 + o0 + 32);
      bb[2] = *(LAS u32x2*)(size_t)(c.ldsb + LB2 + o2); bb[3] = *(LAS u32x2*)(size_t)(c.ldsb + LB2 + o2 + 16);
    }
    poll_w(c, F_P2 + ((t.mt * 4 + (t.own >> 3)) * 32 + t.wq * 8) * 8, 8);
    STAMPG(31);
    red_load(c, rd, WS_P2 + (t.mt * 256 + t.wq * 64 + (t.own >> 2)) * 4608 + (t.own & 3) * 1152);
    RED_PIN(rd);
    const rsrc_t ro = mk_rsrc(out, SEQ * DM * 2);
    float a[16]; unsigned ow[8];
    red_sum(rd, a);
#pragma unroll
    for (int e = 0; e < 8; ++e) {
      const unsigned bv = bb[e >> 1][e & 1];
      ow[e] = cvtpk(a[2 * e] + bflo(bv) + bflo(hw[e]), a[2 * e + 1] + bfhi(bv) + bfhi(hw[e]));
    }
    unsigned o0, o2; job_offs(c, t.mt * 128 + t.own * 4 + (c.lane & 3), o0, o2);
    __builtin_amdgcn_raw_buffer_store_b64(u32x2{ow[0], ow[1]}, ro, o0, 0, 0);
    __builtin_amdgcn_raw_buffer_store_b64(u32x2{ow[2], ow[3]}, ro, o0, 32, 0);
    __builtin_amdgcn_raw_buffer_store_b64(u32x2{ow[4], ow[5]}, ro, o2, 0, 0);
    __builtin_amdgcn_raw_buffer_store_b64(u32x2{ow[6], ow[7]}, ro, o2, 16, 0);
    STAMPG(32);
    if (c.b == 0 && c.w == 0) gst8c(u32x2{c.ep0, c.ep1}, c.rws, c.lane == 0 ? F_EPOCH : OOB, WS_FLAGS);
  }
}

__global__ __launch_bounds__(512, 2) void fused_block(const u16* __restrict__ x, const u16* __restrict__ ckv, const u16* __restrict__ wq,
                                                      const u16* __restrict__ wuk, const u16* __restrict__ wuv, const u16* __restrict__ wo,
                                                      const u16* __restrict__ w1, const u16* __restrict__ b1, const u16* __restrict__ w2,
                                                      const u16* __restrict__ b2, u16* __restrict__ out, char* __restrict__ ws) {
  extern __shared__ __attribute__((aligned(16))) char lds[];
  Ctx c;
  asm volatile("s_getpc_b64 %0\n.Lfb_begin:" : "=s"(c.pc0));
  c.ldsb = (unsigned)(size_t)lds; c.lane = threadIdx.x & 63; c.w = rfl(threadIdx.x >> 6); c.b = blockIdx.x;
  c.rws = mk_rsrc(ws, WS_END);
  STAMP(0);
  QOut qo;
  WSrc wso; u32x4 xr[XM][2];
  phase_qkv(c, qo, x, ckv, wq, wuk, wuv);
  fresh(c);
  phase_attn(c, qo, wso, xr, wo, w1, b1, w2, b2);
  fresh(c);
  phase_tail(c, wso, xr, x, w1, b1, w2, b2, out);
  asm volatile(".Lfb_end:");
}

extern "C" void kernel_launch(void* const* d_in, const int* in_sizes, int n_in, void* d_out, int out_size, void* d_ws, size_t ws_size,
                              hipStream_t stream) {
  hipFuncSetAttribute((const void*)fused_block, hipFuncAttributeMaxDynamicSharedMemorySize, 163840);
  fused_block<<<256, 512, 163840, stream>>>((const u16*)d_in[0], (const u16*)d_in[1], (const u16*)d_in[2], (const u16*)d_in[3], (const u16*)d_in[4],
                                            (const u16*)d_in[5], (const u16*)d_in[7], (const u16*)d_in[8], (const u16*)d_in[9], (const u16*)d_in[10],
                                            (u16*)d_out, (char*)d_ws);
}
```

```cpp
#include <hip/hip_runtime.h>
#include <stdint.h>
#include <utility>

typedef __bf16 bf16x8 __attribute__((ext_vector_type(8)));
typedef __bf16 bf16x4 __attribute__((ext_vector_type(4)));
typedef __bf16 bf16x2 __attribute__((ext_vector_type(2)));
typedef float f32x16 __attribute__((ext_vector_type(16)));
typedef float f32x4 __attribute__((ext_vector_type(4)));
typedef float f32x2 __attribute__((ext_vector_type(2)));
typedef unsigned u32x4 __attribute__((ext_vector_type(4)));
typedef unsigned u32x2 __attribute__((ext_vector_type(2)));
typedef unsigned short u16;
typedef unsigned long long u64;
#define LAS __attribute__((address_space(3)))
#define DI __device__ __forceinline__
#define AI __attribute__((always_inline))
typedef __amdgpu_buffer_rsrc_t rsrc_t;

constexpr int SEQ = 2048, DM = 576, NH = 32, HD = 128, DC = 512, FF = 4608, NQ = 4096;

constexpr unsigned WS_FLAGS = 0;
constexpr unsigned WS_XN = 131072;
constexpr unsigned WS_K = WS_XN + SEQ * DM * 2;
constexpr unsigned WS_V = WS_K + SEQ * NQ * 2;
constexpr unsigned WS_CTX = WS_V + SEQ * NQ * 2;
constexpr unsigned WS_P1 = WS_CTX + SEQ * NQ * 2;
constexpr unsigned WS_H = WS_P1 + 8 * SEQ * DM * 2;
constexpr unsigned WS_HN = WS_H + SEQ * DM * 2;
constexpr unsigned WS_H1 = WS_HN + SEQ * DM * 2;
constexpr unsigned WS_P2 = WS_H1 + SEQ * FF * 2;
constexpr unsigned WS_END = WS_P2 + 8 * SEQ * DM * 2;
constexpr unsigned F_EPOCH = 0, F_XN = 4096, F_KV = 8192, F_CTX = 12288, F_P1 = 16384, F_HN = 32768, F_H1 = 49152, F_P2 = 65536, F_K = 81920, F_V = 90112, F_CX2 = 98304;

template <int... Is> using iseq = std::integer_sequence<int, Is...>;
template <int N> using make_iseq = std::make_integer_sequence<int, N>;
template <int I> struct IC { static constexpr int v = I; };
template <class F, int... Is> DI void static_for_(F&& f, iseq<Is...>) { (f(IC<Is>{}), ...); }
template <int N, class F> DI void static_for(F&& f) { static_for_(f, make_iseq<N>{}); }

#define SB() __builtin_amdgcn_sched_barrier(0)
#define PIN(x) asm volatile("" : "+v"(x))
DI unsigned rfl(unsigned x) { return __builtin_amdgcn_readfirstlane(x); }
DI rsrc_t mk_rsrc(const void* p, unsigned bytes) { return __builtin_amdgcn_make_buffer_rsrc((void*)p, (short)0, (int)bytes, 0x00020000); }
constexpr unsigned OOB = 0xFFFFFF00u;
DI void dma16(unsigned lds_addr, unsigned voff, rsrc_t r, unsigned soff) {
  asm volatile("s_mov_b32 m0, %0\n\ts_nop 3\n\tbuffer_load_dwordx4 %1, %2, %3 offen lds" ::"s"(lds_addr), "v"(voff), "s"(r), "s"(soff) : "memory", "m0");
}
DI void dma16c(unsigned lds_addr, unsigned voff, rsrc_t r, unsigned soff) {
  asm volatile("s_mov_b32 m0, %0\n\ts_nop 3\n\tbuffer_load_dwordx4 %1, %2, %3 offen sc1 lds" ::"s"(lds_addr), "v"(voff), "s"(r), "s"(soff) : "memory", "m0");
}
template <int IMM> DI void gld16c(u32x4& d, unsigned voff, rsrc_t r, unsigned soff) {
  asm volatile("s_nop 4\n\tbuffer_load_dwordx4 %0, %1, %2, %3 offen offset:%4 sc1" : "=v"(d) : "v"(voff), "s"(r), "s"(soff), "i"(IMM) : "memory");
}
template <int IMM> DI void gld16(u32x4& d, unsigned voff, rsrc_t r, unsigned soff) {
  asm volatile("s_nop 4\n\tbuffer_load_dwordx4 %0, %1, %2, %3 offen offset:%4" : "=v"(d) : "v"(voff), "s"(r), "s"(soff), "i"(IMM) : "memory");
}
DI void gld8c(u32x2& d, unsigned voff, rsrc_t r, unsigned soff) {
  asm volatile("s_nop 4\n\tbuffer_load_dwordx2 %0, %1, %2, %3 offen sc1" : "=v"(d) : "v"(voff), "s"(r), "s"(soff) : "memory");
}
DI void gst16c(u32x4 v, rsrc_t r, unsigned voff, unsigned soff) {
  asm volatile("s_nop 4\n\tbuffer_store_dwordx4 %0, %1, %2, %3 offen sc1\n\ts_nop 1" ::"v"(v), "v"(voff), "s"(r), "s"(soff) : "memory");
}
DI void gst8c(u32x2 v, rsrc_t r, unsigned voff, unsigned soff) { __builtin_amdgcn_raw_buffer_store_b64(v, r, voff, soff, 16); }
DI void fput(u32x2 v, rsrc_t r, unsigned voff) { asm volatile("s_nop 4\n\tbuffer_atomic_swap_x2 %0, %1, %2, 0 offen sc1\n\ts_nop 1" ::"v"(v), "v"(voff), "s"(r) : "memory"); }
DI void fget(u32x2& d, rsrc_t r, unsigned voff) {
  d = u32x2{0u, 0u};
  asm volatile("s_nop 4\n\tbuffer_atomic_add_x2 %0, %1, %2, 0 offen sc0 sc1" : "+v"(d) : "v"(voff), "s"(r) : "memory");
}
template <int N> DI void wait_vm() { asm volatile("s_waitcnt vmcnt(%0)" ::"i"(N) : "memory"); }
DI void bar() { asm volatile("s_waitcnt lgkmcnt(0)" ::: "memory"); __builtin_amdgcn_s_barrier(); asm volatile("" ::: "memory"); }
DI unsigned cvtpk(float lo, float hi) { f32x2 v = {lo, hi}; bf16x2 b = __builtin_convertvector(v, bf16x2); return __builtin_bit_cast(unsigned, b); }
DI float bflo(unsigned x) { return __uint_as_float(x << 16); }
DI float bfhi(unsigned x) { return __uint_as_float(x & 0xffff0000u); }
DI bf16x8 lds_b128(unsigned a) { return *(LAS bf16x8*)(size_t)a; }
DI bf16x4 lds_tr(unsigned a) { return __builtin_amdgcn_ds_read_tr16_b64_v4bf16((LAS bf16x4*)(size_t)a); }
DI bf16x8 cat(bf16x4 a, bf16x4 b) { return __builtin_shufflevector(a, b, 0, 1, 2, 3, 4, 5, 6, 7); }
DI bf16x8 asbf(u32x4 x) { return __builtin_bit_cast(bf16x8, x); }
#define MFMA32(a, b, c) __builtin_amdgcn_mfma_f32_32x32x16_bf16((a), (b), (c), 0, 0, 0)
#define MFMA16(a, b, c) __builtin_amdgcn_mfma_f32_16x16x32_bf16((a), (b), (c), 0, 0, 0)
template <int S> DI u32x4 pack8(const f32x16& x, float sc) {
  u32x4 w;
  w[0] = cvtpk(x[8 * S + 0] * sc, x[8 * S + 1] * sc); w[1] = cvtpk(x[8 * S + 2] * sc, x[8 * S + 3] * sc);
  w[2] = cvtpk(x[8 * S + 4] * sc, x[8 * S + 5] * sc); w[3] = cvtpk(x[8 * S + 6] * sc, x[8 * S + 7] * sc);
  return w;
}
template <int S> DI u32x4 pack8(const f32x16& x) {
  u32x4 w;
  w[0] = cvtpk(x[8 * S + 0], x[8 * S + 1]); w[1] = cvtpk(x[8 * S + 2], x[8 * S + 3]);
  w[2] = cvtpk(x[8 * S + 4], x[8 * S + 5]); w[3] = cvtpk(x[8 * S + 6], x[8 * S + 7]);
  return w;
}
DI float max3f(float a, float b, float c) { float r; asm("v_max3_f32 %0, %1, %2, %3" : "=v"(r) : "v"(a), "v"(b), "v"(c)); return r; }
DI float max2f(float a, float b) { float r; asm("v_max_f32_e32 %0, %1, %2" : "=v"(r) : "v"(a), "v"(b)); return r; }
DI float fadd_s(float a, float b) { float r; asm("v_add_f32_e32 %0, %1, %2" : "=v"(r) : "v"(a), "v"(b)); return r; }
DI float xhalf_sum(float x) {
  auto r = __builtin_amdgcn_permlane32_swap(__float_as_uint(x), __float_as_uint(x), false, false);
  return __uint_as_float(r[0]) + __uint_as_float(r[1]);
}
DI float wave_sum(float x) {
#pragma unroll
  for (int o = 1; o < 64; o <<= 1) x += __shfl_xor(x, o);
  return x;
}

#define STAMP(i)

struct Ctx {
  unsigned ldsb, w, lane, b;
  unsigned ep0, ep1;
  rsrc_t rws;
  u64 pc0;
};

DI void fresh(Ctx& c) {
  asm volatile("v_mbcnt_lo_u32_b32 %0, -1, 0\n\tv_mbcnt_hi_u32_b32 %0, -1, %0" : "=v"(c.lane));
  asm volatile("" : "+s"(c.w), "+s"(c.b), "+s"(c.ldsb));
}
DI void code_touch(Ctx& c, unsigned& dummy) {
  unsigned bytes;
  asm volatile("s_mov_b32 %0, .Lfb_end-.Lfb_begin" : "=s"(bytes));
  const rsrc_t r = mk_rsrc((const void*)c.pc0, bytes);
  asm volatile("s_nop 4\n\tbuffer_load_dword %0, %1, %2, 0 offen" : "+v"(dummy) : "v"(c.w == 0 ? (c.lane * 32 + (c.b >> 3)) * 128 : OOB), "s"(r) : "memory");
}
DI void publish(Ctx& c, unsigned flag_off) {
  wait_vm<0>();
  bar();
  fput(u32x2{c.ep0, c.ep1}, c.rws, (c.w == 0 && c.lane == 0) ? flag_off : OOB);
}
DI void poll_flags(Ctx& c, unsigned flag_off, unsigned n, unsigned stride) {
  const unsigned voff = c.lane < n ? flag_off + c.lane * 8 * stride : OOB;
  const u64 t0 = __builtin_amdgcn_s_memrealtime();
  for (;;) {
    u32x2 f;
    fget(f, c.rws, voff);
    asm volatile("s_waitcnt vmcnt(0)" : "+v"(f)::"memory");
    if (__all((f[0] == c.ep0 && f[1] == c.ep1) || c.lane >= n)) break;
    if (__builtin_amdgcn_s_memrealtime() - t0 > 5000000ull) break;
    __builtin_amdgcn_s_sleep(2);
  }
}
DI void wait_flags(Ctx& c, unsigned flag_off, unsigned n, unsigned stride) {
  if (c.w == 0) poll_flags(c, flag_off, n, stride);
  bar();
}

#define DBG_DRAIN()
#define DBG_NX(x)
#define STAMPG(i)
DI void flag_store(Ctx& c, unsigned flag_off) {
  fput(u32x2{c.ep0, c.ep1}, c.rws, (c.w == 0 && c.lane == 0) ? flag_off : OOB);
}
DI void flag_load(Ctx& c, u32x2& f, unsigned flag_off, unsigned n) { fget(f, c.rws, c.lane < n ? flag_off + c.lane * 8 : OOB); }
DI void flag_check(Ctx& c, u32x2& f, unsigned flag_off, unsigned n) {
  PIN(f);
  if (__builtin_expect(!__all((f[0] == c.ep0 && f[1] == c.ep1) || c.lane >= n), 0)) poll_flags(c, flag_off, n, 1);
}

constexpr float QSCALE = 0.08838834764831845f * 1.4426950408889634f;
constexpr unsigned LE = 131072;
constexpr unsigned LX = 147456;
constexpr unsigned KW = LX + 12288;
#define ROT 0
constexpr unsigned SLOT = 49152;
constexpr int slot_of(int S) { return (S + 2) % 3; }
constexpr int XQ = 6;
constexpr int LAQ = 6;
constexpr int QB1 = 40, QB2 = 88, QB3 = 116;
constexpr int Q_KPOLL = 70, Q_VPOLL = 134, Q_VST = 12;
#define PRO 0
#define XLS 1
constexpr int XLS1 = XLS > 0 ? XLS : 1, XL0 = 31 - 5 * XLS1, XFL = XLS == 0 ? 19 : ((XL0 - 8) | 1) - ((XL0 & 1) ? 0 : 2);
#define KSP 2
#define TWV 5
#define TWQ 13
#define XT 1
#define LNE 1
#define PRO_N 12
#define PRO_B 5
constexpr int PRO_U[18] = {0, 0, 0, 0, 0, 0, 1, 1, 1, 1, 1, 1, 2, 2, 2, 2, 2, 2}, PRO_I[18] = {0, 4, 1, 2, 5, 3, 0, 4, 1, 2, 5, 3, 0, 1, 2, 3, 4, 5}, PRO_X = LNE ? LNE - 1 : 5;
#define WQL 1
#define WQ2 41
constexpr int wq_piece(int q) { return q >= 1 && q < 25 && (q - 1) % 4 == 0 ? (q - 1) / 4 : q >= WQ2 && q < WQ2 + 24 && (q - WQ2) % 4 == 0 ? 6 + (q - WQ2) / 4 : -1; }
constexpr int vm_wait(int pos, int tag) {
  int seq[512] = {}; int n = 0;
  auto push = [&](int t, int cnt) { for (int i = 0; i < cnt; ++i) seq[n++] = t; };
  for (int i = 0; i < PRO_N; ++i) { push(PRO_U[i], 1); if (i == PRO_X) push(-1, 3); }
  if (PRO_N == 18) push(-12, 1);
  bool done = false;
  for (int T = 0; T < 32 && !done; ++T) {
    if (pos == T) { done = true; break; }
    if (T == 0 && PRO_N < 18) { for (int i = PRO_N; i < 18; ++i) push(PRO_U[i], 1); push(-12, 1); }
    if (T == 3) push(-2, 2);
    if (T == TWV) push(-6, 1);
    if (T == TWQ) push(-6, 1);
    if (T == 7) push(-3, 1);
    if (T == XFL) push(-5, 1);
    if (KSP == 1 && T == 27) push(-8, 1);
    if (T >= 16 && (T - 16) % KSP == 0 && (T - 16) / KSP < 8) push(-7, 1);
    if (T >= 1 && (!WQL || T < 29)) push((T - 1) / 2 + 3, 3);
    if (XLS == 0 && T >= 29) push(200 + 2 * (T - 29), 2), push(201 + 2 * (T - 29), 2);
    if (XLS > 0 && T >= XL0 && (T - XL0) % XLS1 == 0) push(200 + (T - XL0) / XLS1, 2);
  }
  for (int q = 0; q < 144 && !done; ++q) {
    if (pos == 100 + q) break;
    if (q == QB3) push(-11, 1);
    if (KSP == 2 && q == QB1) push(-8, 1);
    if (!WQL && (q == 1 || q == 4 || q == 6)) push(18, 1);
    if (WQL && wq_piece(q) >= 0) push(17 + wq_piece(q) / 6, 1);
    if (tag == -9 && q == Q_KPOLL) push(-9, 1);
    if (q % Q_VST == 2 && q / Q_VST < 8) push(-10, 1);
    if ((q & 7) == 7 && (q >> 3) + XQ < 18) push(200 + (q >> 3) + XQ, 2);
  }
  int last = -1;
  for (int i = 0; i < n; ++i) if (seq[i] == tag) last = i;
  return n - 1 - last;
}
constexpr int imin(int a, int b) { return a < b ? a : b; }
DI unsigned lds_add_rtn(unsigned addr) {
  unsigned r;
  asm volatile("s_mov_b64 exec, 1\n\tds_add_rtn_u32 %0, %1, %2\n\ts_mov_b64 exec, -1\n\ts_waitcnt lgkmcnt(0)" : "=&v"(r) : "v"(addr), "v"(1u) : "memory");
  return rfl(r);
}
DI unsigned lds_word(unsigned addr) { return __hip_atomic_load((LAS unsigned*)(size_t)addr, __ATOMIC_RELAXED, __HIP_MEMORY_SCOPE_WORKGROUP); }
DI void lds_set(unsigned addr, unsigned v) { __hip_atomic_store((LAS unsigned*)(size_t)addr, v, __ATOMIC_RELAXED, __HIP_MEMORY_SCOPE_WORKGROUP); }
DI float wave_sum64(float x) {
  x += __uint_as_float(__builtin_amdgcn_update_dpp(0, __float_as_uint(x), 0xB1, 0xf, 0xf, false));
  x += __uint_as_float(__builtin_amdgcn_update_dpp(0, __float_as_uint(x), 0x4E, 0xf, 0xf, false));
  x += __uint_as_float(__builtin_amdgcn_update_dpp(0, __float_as_uint(x), 0x124, 0xf, 0xf, false));
  x += __uint_as_float(__builtin_amdgcn_update_dpp(0, __float_as_uint(x), 0x128, 0xf, 0xf, false));
  { auto r = __builtin_amdgcn_permlane16_swap(__float_as_uint(x), __float_as_uint(x), false, false); x = __uint_as_float(r[0]) + __uint_as_float(r[1]); }
  return xhalf_sum(x);
}
DI void ln1_row(Ctx& c, const u32x4& xr0, const u32x4& xr1) {
  const unsigned lane = c.lane;
  float v[16];
#pragma unroll
  for (int e = 0; e < 4; ++e) { v[2 * e] = bflo(xr0[e]); v[2 * e + 1] = bfhi(xr0[e]); v[8 + 2 * e] = bflo(xr1[e]); v[9 + 2 * e] = bfhi(xr1[e]); }
  float s = 0.f;
#pragma unroll
  for (int e = 0; e < 16; ++e) s += v[e];
  const float mean = wave_sum64(s) * (1.f / DM);
  float qq = 0.f;
#pragma unroll
  for (int e = 0; e < 16; ++e) { v[e] -= mean; qq += v[e] * v[e]; }
  qq = lane < 36 ? qq : 0.f;
  const float rs = rsqrtf(wave_sum64(qq) * (1.f / DM) + 1e-5f);
  u32x4 o0, o1;
#pragma unroll
  for (int e = 0; e < 4; ++e) { o0[e] = cvtpk(v[2 * e] * rs, v[2 * e + 1] * rs); o1[e] = cvtpk(v[8 + 2 * e] * rs, v[9 + 2 * e] * rs); }
  const unsigned ad = c.ldsb + LX + (lane < 36 ? (2 * lane * 8 + c.w) * 16 : 9216 + (lane - 36) * 16);
  *(LAS u32x4*)(size_t)ad = o0;
  *(LAS u32x4*)(size_t)(ad + 128) = o1;
}
DI void xn_store(Ctx& c) {
  const unsigned w = c.w, lane = c.lane, mb = c.b >> 2, r0 = (c.b & 3) * 8;
  const unsigned c0 = 9 * w + (lane >> 3), c1 = 9 * w + 8;
  const u32x4 a0 = *(LAS u32x4*)(size_t)(c.ldsb + LX + (9 * w * 8 + lane) * 16);
  const u32x4 a1 = *(LAS u32x4*)(size_t)(c.ldsb + LX + (c1 * 8 + (lane & 7)) * 16);
  gst16c(a0, c.rws, ((mb * 36 + (c0 >> 1)) * 64 + (c0 & 1) * 32 + r0 + (lane & 7)) * 16, WS_XN);
  gst16c(a1, c.rws, lane < 8 ? ((mb * 36 + (c1 >> 1)) * 64 + (c1 & 1) * 32 + r0 + lane) * 16 : OOB, WS_XN);
}

template <int WH> DI void w_touch(Ctx& c, unsigned& dummy, const void* wo, const void* w1, const void* w2, unsigned wv, bool two) {
  unsigned lane;
  asm volatile("v_mbcnt_lo_u32_b32 %0, -1, 0\n\tv_mbcnt_hi_u32_b32 %0, -1, %0" : "=v"(lane));
  const unsigned xcd = c.b & 7, jj = (c.b >> 3) - ((lane >> 5) << 2), l = two ? lane & 31 : lane, i = 3 * wv + l - 18;
  const bool mn = l < 18, ex = l < 21 && i < 18;
  auto ld = [&](const void* p, unsigned bytes, unsigned off) AI {
    asm volatile("s_nop 4\n\tbuffer_load_dword %0, %1, %2, 0 offen" : "+v"(dummy) : "v"(off), "s"(mk_rsrc(p, bytes)) : "memory");
  };
  const unsigned o1 = xcd * 589824 + (jj * 144 + wv * 18 + l) * 128;
  if constexpr (WH & 1) ld(wo, NQ * DM * 2, mn ? o1 : OOB);
  if constexpr (WH & 2) ld(w1, FF * DM * 2, mn ? (jj * 18 + l) * (FF * 2) + xcd * 1024 + wv * 128 : ex ? (jj * 18 + i) * (FF * 2) + 8192 + xcd * 128 : OOB);
  if constexpr (WH & 4) ld(w2, FF * DM * 2, mn ? o1 : ex ? (4096 + 64 * xcd) * (DM * 2) + (jj * 18 + i) * 128 : OOB);
}
struct QOut { u32x4 q[8]; bool kdone; };
DI u32x4 pack_pair(const f32x4& r0, const f32x4& r1) { return u32x4{cvtpk(r0[0], r0[1]), cvtpk(r0[2], r0[3]), cvtpk(r1[0], r1[1]), cvtpk(r1[2], r1[3])}; }
template <int IMM> DI void gld16c2(u32x4& d0, u32x4& d1, unsigned voff, rsrc_t r, unsigned soff) {
  asm volatile("s_nop 4\n\tbuffer_load_dwordx4 %0, %2, %3, %4 offen offset:%5 sc1\n\tbuffer_load_dwordx4 %1, %2, %3, %4 offen offset:%6 sc1"
               : "=&v"(d0), "=&v"(d1) : "v"(voff), "s"(r), "s"(soff), "i"(IMM), "i"(IMM + 256) : "memory");
}
DI unsigned x_voff(Ctx& c) { const unsigned g = c.lane >> 4; return (g >> 1) * 1024 + ((g & 1) * 32 + (c.lane & 15)) * 16; }

DI void phase_qkv(Ctx& c, QOut& qo, const u16* x, const u16* ckv, const u16* wq, const u16* wuk, const u16* wuv) {
  const unsigned w = c.w, lane = c.lane, ldsb = c.ldsb, b = c.b;
  const unsigned xcd = b & 7, jj = b >> 3, head = xcd * 4 + (jj >> 3), cb = jj & 7;
  const rsrc_t rxx = mk_rsrc(x, SEQ * DM * 2), rx = mk_rsrc(ckv, SEQ * DC * 2);
  const rsrc_t rwk = mk_rsrc(wuk, DC * NQ * 2), rwv = mk_rsrc(wuv, DC * NQ * 2), rwq = mk_rsrc(wq, DM * NQ * 2);
  unsigned dummy = 0;
  code_touch(c, dummy);
  const unsigned voffX0 = (lane >> 3) * 1024 + (((lane & 7) ^ (lane >> 4)) << 4), voffX1 = voffX0 ^ 64;
  const unsigned voffW = (lane >> 3) * 8192 + (((((lane >> 1) & 3) ^ (((lane >> 4) & 1) | ((w & 1) << 1))) << 5) | ((lane & 1) << 4));
  const unsigned sX = (cb * 256 + 32 * w) * 1024, sW = (8 * (w & 3)) * 8192 + (head * 128 + (w >> 2) * 64) * 2;
  const unsigned rot = ROT == 0 ? 2 * (cb & 3) : ROT == 1 ? 2 * ((cb + head) & 3) : ROT == 2 ? 2 * (cb & 3) + 4 * (head & 1) : (cb + 2 * head) & 7;
  const unsigned dX = ldsb + 4 * w * 1024, dW = ldsb + w * 1024;
  auto issue = [&](auto U_, auto P_) AI {
    constexpr int U = decltype(U_)::v, P = decltype(P_)::v;
    constexpr unsigned so = slot_of(U) * SLOT;
    if constexpr (U < 16) {
      const unsigned tr = (U + rot) & 7;
      constexpr int p = P < 2 ? P : P - 1;
      if constexpr (P % 3 != 2) dma16(dX + so + p * 1024, (p & 1) ? voffX1 : voffX0, rx, sX + 128 * tr + (U < 8 ? 8 * p : 16 * (p & 1) + 8 * (p >> 1)) * 1024);
      else dma16(dW + so + 32768 + (P / 3) * 8192, voffW, U < 8 ? rwk : rwv, sW + (64 * tr + 32 * (P / 3)) * 8192);
    } else {
      dma16(dW + so + (U == 18 ? 5 - P : P) * 8192, voffW, rwq, sW + 32 * (6 * (U - 16) + P) * 8192);
    }
  };
  u32x2 ep; u32x4 xr0, xr1;
    const unsigned hr = head & 3, vA = voffX0 ^ ((hr & 1) << 6), vB = vA ^ 64, wj = (cb >> 2) * 8192;
    auto pro = [&](auto U_, auto I_) AI {
      constexpr int U = decltype(U_)::v, I = decltype(I_)::v;
      constexpr unsigned so = slot_of(U) * SLOT;
      const unsigned tr = (U + rot) & 7;
      if constexpr (I < 4) { const unsigned po = ((I + hr) & 3) * 1024; dma16(dX + so + po, (I & 1) ? vB : vA, rx, sX + 128 * tr + 8 * po); }
      else { const unsigned j = wj ^ ((I - 4) * 8192); dma16(dW + so + 32768 + j, voffW, rwk, sW + 64 * tr * 8192 + 32 * j); }
    };
  {
    static_for<PRO_N>([&](auto N_) AI {
      constexpr int N = decltype(N_)::v;
      pro(IC<PRO_U[N]>{}, IC<PRO_I[N]>{});
      if constexpr (N == PRO_B) __builtin_amdgcn_s_barrier();
      if constexpr (N == PRO_X) {
        const unsigned vo = lane < 36 ? lane * 32 : OOB;
        gld8c(ep, F_EPOCH, c.rws, WS_FLAGS);
        gld16<0>(xr0, vo, rxx, (b * 8 + w) * (DM * 2));
        gld16<16>(xr1, vo, rxx, (b * 8 + w) * (DM * 2));
      }
    });
  }
  unsigned td2 = 0;
  auto xtouch = [&]() AI {
    const unsigned l5 = (lane * 52) >> 8, row = cb * 256 + (head & 3) * 64 + w * 8 + l5, ch = (rot + 3 + lane - 5 * l5) & 7;
    asm volatile("s_nop 4\n\tbuffer_load_dword %0, %1, %2, 0 offen" : "+v"(td2) : "v"(XT && lane < 40 ? row * 1024 + ch * 128 : OOB), "s"(rx) : "memory");
  };
  if constexpr (PRO_N == 18) xtouch();
  const unsigned kh = w >> 2, rg = kh ? 7 - w : w;
  const unsigned qmb = (kh ? cb : 15 - cb) * 4 + rg;
  const unsigned wm = w >> 1, wn = w & 1, g = lane >> 4, l15 = lane & 15, q = l15 >> 2, pp = lane & 3;
  unsigned xb[3][2], wb[3][4], wqb[3][4];
  {
    const unsigned sg = ((q >> 1) & 1) | ((g & 1) << 1);
#pragma unroll
    for (int i = 0; i < 3; ++i) {
      xb[i][0] = ldsb + i * SLOT + (wm * 64 + l15) * 128 + ((g ^ (l15 >> 1)) << 4); xb[i][1] = xb[i][0] ^ 64; PIN(xb[i][0]); PIN(xb[i][1]);
#pragma unroll
      for (int j = 0; j < 4; ++j) { wb[i][j] = ldsb + i * SLOT + wn * 4096 + (8 * g + q) * 128 + ((j ^ sg) << 5) + pp * 8; PIN(wb[i][j]); }
    }
  }
  if constexpr (LNE != 0) { wait_vm<vm_wait(0, -1)>(); PIN(xr0); PIN(xr1); ln1_row(c, xr0, xr1); }
  wait_vm<vm_wait(0, 0)>();
  asm volatile("" ::"v"(dummy));
  bar();
  STAMP(2);
  f32x4 acc[4][4];
  bf16x8 xf[2][4]; bf16x4 wl[2][4][2];
  u32x4 pk[8], pv[8], xq[XQ][2]; u32x2 xnf;
  auto rd = [&](auto T_, auto R_) AI {
    constexpr int T = decltype(T_)::v, R = decltype(R_)::v, B = T & 1, sl = slot_of(T >> 1);
    constexpr int K = R % 3, I = R / 3;
    if constexpr (K == 0) xf[B][I] = lds_b128(xb[sl][B] + I * 2048);
    else wl[B][I][K - 1] = lds_tr(wb[sl][I] + 32768 + B * 8192 + (K - 1) * 512);
  };
  bf16x4 wq8[8][2];
  auto rdq = [&](auto N_, auto H_) AI {
    constexpr int N = decltype(N_)::v, H = decltype(H_)::v, v = N >> 3, a = N & 7, sl = slot_of(16 + v / 6);
    wq8[N & 7][H] = lds_tr(wqb[sl][a & 3] + (v >= 12 ? 17 - v : v % 6) * 8192 + (a >> 2) * 4096 + H * 512);
  };
  const unsigned xvo = x_voff(c), xso = WS_XN + qmb * 36 * 1024;
  auto ldx = [&](auto V_) AI { constexpr int v = decltype(V_)::v; gld16c2<(v & 1) * 2048>(xq[v % XQ][0], xq[v % XQ][1], xvo, c.rws, xso + (v >> 1) * 4096); };
  const unsigned kst = WS_K + ((head * 64 + cb * 8 + wm * 2) * 8 + 4 * wn) * 1024, vst = WS_V + (((head * 32 + cb * 4 + wm) * 4 + 2 * wn) * 4) * 1024;
  static_for<12>([&](auto R) AI { rd(IC<0>{}, R); });
  SB();
  static_for<32>([&](auto T_) AI {
    constexpr int T = decltype(T_)::v, B = T & 1;
    if constexpr (B) { wait_vm<vm_wait(T, (T >> 1) + 1)>(); bar(); }
    if constexpr (T == 1) {
      PIN(ep);
      if constexpr (!LNE) { PIN(xr0); PIN(xr1); }
      u64 z = (((u64)rfl(ep[1]) << 32) | rfl(ep[0])) + 0x9E3779B97F4A7C15ull;
      z = (z ^ (z >> 30)) * 0xBF58476D1CE4E5B9ull; z = (z ^ (z >> 27)) * 0x94D049BB133111EBull; z ^= z >> 31;
      c.ep0 = (unsigned)z; c.ep1 = (unsigned)(z >> 32);
      if (w == 0) lds_set(ldsb + KW + (lane & 3) * 4, 0);
    }
    if constexpr (!LNE && (T == 1 || T == 2)) { if (kh == T - 1) ln1_row(c, xr0, xr1); }
    if constexpr (T == 3) xn_store(c);
    if constexpr (T == TWV) asm volatile("s_nop 4\n\tbuffer_load_dword %0, %1, %2, 0 offen" : "+v"(td2) : "v"(lane < 16 ? (cb * 64 + w * 8 + (lane >> 1)) * 8192 + head * 256 + (lane & 1) * 128 : OOB), "s"(rwv) : "memory");
    if constexpr (T == TWQ) asm volatile("s_nop 4\n\tbuffer_load_dword %0, %1, %2, 0 offen" : "+v"(td2) : "v"(lane < 18 ? (cb * 72 + w * 9 + (lane >> 1)) * 8192 + head * 256 + (lane & 1) * 128 : OOB), "s"(rwq) : "memory");
    if constexpr (T == 31) asm volatile("" ::"v"(td2));
    if constexpr (T == 7) flag_store(c, F_XN + b * 8);
    if constexpr (T == XFL) flag_load(c, xnf, F_XN + qmb * 32, 4);
    if constexpr (T == XFL + 8) flag_check(c, xnf, F_XN + qmb * 32, 4);
    if constexpr (T == 27) { if constexpr (KSP == 1) flag_store(c, F_K + (head * 8 + cb) * 8); }
    if constexpr (T == 31) {
#pragma unroll
      for (int i = 0; i < 3; ++i)
#pragma unroll
        for (int j = 0; j < 4; ++j) { wqb[i][j] = wb[i][j] - wn * 4096; PIN(wqb[i][j]); }
    }
    if constexpr (T == 8 || T == 16 || T == 24) STAMP(100 + T / 2);
    SB();
    static_for<16>([&](auto M_) AI {
      constexpr int M = decltype(M_)::v, i = M >> 2, j = M & 3;
      const bf16x8 wf = cat(wl[B][j][0], wl[B][j][1]);
      const f32x4 cin = (T & 15) == 0 ? f32x4{} : acc[i][j];
      acc[i][j] = T < 16 ? MFMA16(wf, xf[B][i], cin) : MFMA16(xf[B][i], wf, cin);
      SB();
      if constexpr (M < 12) {
        if constexpr (T < 31) rd(IC<T + 1>{}, IC<M>{});
        else rdq(IC<(M >> 1)>{}, IC<(M & 1)>{});
      }
      if constexpr (M == 1 && T >= 16 && (T - 16) % KSP == 0 && (T - 16) / KSP < 8) {
        constexpr int e = (T - 16) / KSP;
        gst16c(pk[e], c.rws, xvo, kst + (e >> 2) * 8192 + (e & 1) * 2048 + ((e >> 1) & 1) * 256);
      }
      if constexpr (T >= 1 && (!WQL || T < 29) && (M == 4 || M == 8 || M == 12)) issue(IC<(T - 1) / 2 + 3>{}, IC<(B ? 0 : 3) + (M >> 2) - 1>{});
      if constexpr (T == 0 && (M & 1) && PRO_N + (M >> 1) < 18) pro(IC<PRO_U[PRO_N + (M >> 1)]>{}, IC<PRO_I[PRO_N + (M >> 1)]>{});
      if constexpr (T == 0 && M == 15 && PRO_N < 18) xtouch();
      if constexpr (XLS == 0 && T >= 29 && (M == 13 || M == 15)) ldx(IC<2 * (T - 29) + (M == 15)>{});
      if constexpr (XLS > 0 && T >= XL0 && (T - XL0) % XLS1 == 0 && M == 14) ldx(IC<(T - XL0) / XLS1>{});
      SB();
    });
    if constexpr (T == 15) {
#pragma unroll
      for (int e = 0; e < 8; ++e) { pk[e] = pack_pair(acc[e >> 1][2 * (e & 1)], acc[e >> 1][2 * (e & 1) + 1]); PIN(pk[e]); }
    }
    if constexpr (T == 31) {
#pragma unroll
      for (int e = 0; e < 8; ++e) { pv[e] = pack_pair(acc[2 * (e >> 2)][e & 3], acc[2 * (e >> 2) + 1][e & 3]); PIN(pv[e]); }
    }
  });
  STAMP(3);
  f32x4 qa[2][8];
  u32x2 kf; bool kdone = false;
  const unsigned kso = WS_K + head * (SEQ * HD * 2) + w * 4096;
  static_for<144>([&](auto N_) AI {
    constexpr int N = decltype(N_)::v, v = N >> 3, a = N & 7;
    if constexpr (a == 0) { wait_vm<vm_wait(100 + N, 200 + v)>(); PIN(xq[v % XQ][0]); PIN(xq[v % XQ][1]); }
    if constexpr (N == QB1) { wait_vm<vm_wait(100 + N, 17)>(); bar(); if constexpr (KSP == 2) flag_store(c, F_K + (head * 8 + cb) * 8); SB(); }
    if constexpr (N == QB2) {
      wait_vm<vm_wait(100 + N, 18)>();
      if (w == 0) wait_vm<vm_wait(100 + N, -9)>();
      PIN(kf);
      if (w == 0 && __all((kf[0] == c.ep0 && kf[1] == c.ep1) || lane >= 8)) lds_set(ldsb + KW + 8, 1);
      bar();
      kdone = rfl(lds_word(ldsb + KW + 8)) != 0;
      SB();
    }
    if constexpr (N == QB3) { wait_vm<vm_wait(100 + N, -10)>(); bar(); flag_store(c, F_V + (head * 8 + cb) * 8); SB(); }
    const bf16x8 wf = cat(wq8[N & 7][0], wq8[N & 7][1]);
    qa[0][a] = MFMA16(wf, asbf(xq[v % XQ][0]), v == 0 ? f32x4{} : qa[0][a]);
    SB();
    if constexpr (N + LAQ < 144) rdq(IC<N + LAQ>{}, IC<0>{});
    SB();
    qa[1][a] = MFMA16(wf, asbf(xq[v % XQ][1]), v == 0 ? f32x4{} : qa[1][a]);
    SB();
    if constexpr (N + LAQ < 144) rdq(IC<N + LAQ>{}, IC<1>{});
    if constexpr (!WQL && (N == 1 || N == 4 || N == 6)) issue(IC<18>{}, IC<(N == 1 ? 3 : N == 4 ? 4 : 5)>{});
    if constexpr (WQL && wq_piece(N) >= 0) issue(IC<17 + wq_piece(N) / 6>{}, IC<wq_piece(N) % 6>{});
    if constexpr (N == Q_KPOLL || N == Q_VPOLL) {
      kf = u32x2{0u, 0u};
      asm volatile("s_mov_b32 exec_lo, %3\n\ts_mov_b32 exec_hi, 0\n\ts_nop 4\n\tbuffer_atomic_add_x2 %0, %1, %2, 0 offen sc0 sc1\n\ts_mov_b64 exec, -1"
                   : "+v"(kf) : "v"((N == Q_KPOLL ? F_K : F_V) + (head * 8 + lane) * 8), "s"(c.rws), "s"(w == 0 ? 0xffu : 0u) : "memory");
    }
    if constexpr (N % Q_VST == 2 && N / Q_VST < 8) {
      constexpr int e = N / Q_VST;
      gst16c(pv[e], c.rws, xvo, vst + ((e >> 1) & 1) * 4096 + (e >> 2) * 2048 + (e & 1) * 256);
    }
    if constexpr (a == 7 && v + XQ < 18) ldx(IC<v + XQ>{});
    if constexpr (N > QB3 && N <= QB3 + 16 && (N - QB3) % 4 == 0) {
      constexpr int p = (N - QB3) / 4 - 1;
      if (kdone) dma16c(ldsb + kh * 16384 + w * 4096 + p * 1024, lane * 16, c.rws, kso + p * 1024);
    }
    SB();
  });
#pragma unroll
  for (int A = 0; A < 4; ++A) {
    u32x4 p0, p1;
#pragma unroll
    for (int e = 0; e < 4; ++e) {
      const int t = 2 * A + (e >> 1), k = 2 * (e & 1);
      const unsigned u0 = cvtpk(qa[0][t][k] * QSCALE, qa[0][t][k + 1] * QSCALE), u1 = cvtpk(qa[1][t][k] * QSCALE, qa[1][t][k + 1] * QSCALE);
      const auto s = __builtin_amdgcn_permlane16_swap(u0, u1, false, false);
      const auto r = __builtin_amdgcn_permlane32_swap(s[0], s[1], false, false);
      p0[e] = r[0]; p1[e] = r[1];
    }
    qo.q[2 * A] = p0; qo.q[2 * A + 1] = p1;
  }
  qo.kdone = kdone;
  STAMP(6);
  if (w == 0) wait_vm<0>();
  flag_check(c, kf, F_V + head * 64, w == 0 ? 8 : 0);
}

constexpr float THR = 32.f;

struct AttnState {
  bf16x8 qf[8];
  f32x16 o[4];
  f32x16 S[2][2];
  f32x16 negm;
  u32x4 pk[4];
  bf16x8 fr[8];
  float mhat, l0, l1, fct, mx0, mx1, t0, t1;
  bool pend;
  unsigned kb[2], vb[2];
  unsigned voff, ldsb, w, kbase, vbase, n1, n1k;
  u64 ta, tm; unsigned dummy;
};

constexpr int RD0[16] = {4, 5, 6, 7, 9, 10, 12, 13, 15, -1, -1, -1, -1, -1, -1, -1};
constexpr int RD1[16] = {-1, -1, -1, 8, -1, 11, -1, 14, -1, -1, -1, -1, -1, -1, -1, -1};

template <int CR> DI float mask1(float x, int dq, float ninf) {
  asm volatile("v_cmp_le_i32_e32 vcc, %2, %1\n\tv_cndmask_b32_e32 %0, %3, %0, vcc" : "+v"(x) : "v"(dq), "i"(CR), "v"(ninf) : "vcc");
  return x;
}
template <int B, int... R> DI void mask_rows(f32x16& s, int dq, float ninf, iseq<R...>) { ((s[R] = mask1<B + (R & 3) + 8 * (R >> 2)>(s[R], dq, ninf)), ...); }
struct NoHook { template <class T> DI void operator()(T) const {} };
template <int X, bool QK, bool PV, bool VDMA = true, class Hook = NoHook>
DI void attn_iter(Ctx& c, AttnState& a, const unsigned s, const bool first, const bool diag, const int dq, Hook&& hook = Hook{}) {
  constexpr int Y = X ^ 1;
  auto kaddr = [&](int slot, int n) AI { return a.kb[slot] + ((n & 1) * 8 + (n >> 1)) * 1024; };
  auto vaddr = [&](int slot, int n) AI { return a.vb[slot] + ((n & 3) * 4 + (n >> 2)) * 1024; };
  const unsigned sk = s + 2;
  const unsigned sok = a.kbase + (sk + (sk > a.n1 ? sk - a.n1 : 0)) * 16384, sov = a.vbase + (s + (s > a.n1 ? s - a.n1 : 0)) * 16384;
  const unsigned mk = (sk < a.n1k || sk > 16) ? 0xFFFFFC00u : 0u, mv = s < a.n1k ? 0xFFFFFC00u : 0u;
  unsigned vo = 0;
  const unsigned dk = a.ldsb + X * 32768 + a.w * 4096, dv = dk + 65536;
  static_for<16>([&](auto M_) AI {
    constexpr int M = decltype(M_)::v, d = M >> 1, kb = M & 1;
    if constexpr (QK) {
      if constexpr (d == 0) a.S[X][kb] = MFMA32(a.fr[M & 7], a.qf[d], a.negm);
      else a.S[X][kb] = MFMA32(a.fr[M & 7], a.qf[d], a.S[X][kb]);
      if constexpr (RD0[M] >= 0) a.fr[RD0[M] & 7] = lds_b128(kaddr(X, RD0[M]));
      if constexpr (RD1[M] >= 0) a.fr[RD1[M] & 7] = lds_b128(kaddr(X, RD1[M]));
      if constexpr (M == 0) vo = a.voff | mv;
      if constexpr (VDMA && (M == 0 || M == 2 || M == 6 || M == 9)) { constexpr int P = M == 0 ? 0 : M == 2 ? 1 : M == 6 ? 2 : 3; dma16c(dv + P * 1024, vo, c.rws, sov + P * 1024); }
    }
    if constexpr (PV) {
      if constexpr (M < 8) {
        a.S[Y][1][2 * M] = __builtin_amdgcn_exp2f(a.S[Y][1][2 * M]);
        a.S[Y][1][2 * M + 1] = __builtin_amdgcn_exp2f(a.S[Y][1][2 * M + 1]);
        a.pk[M >> 2][M & 3] = cvtpk(a.S[Y][0][2 * M], a.S[Y][0][2 * M + 1]);
        PIN(a.pk[M >> 2][M & 3]);
        if constexpr (M == 0) { a.l0 = fadd_s(a.l0, a.S[Y][0][14]); a.l1 = fadd_s(a.l1, a.S[Y][0][15]); }
      } else {
        constexpr int e = M - 8;
        a.pk[2 + (e >> 2)][e & 3] = cvtpk(a.S[Y][1][2 * e], a.S[Y][1][2 * e + 1]);
        PIN(a.pk[2 + (e >> 2)][e & 3]);
        a.l0 = fadd_s(a.l0, a.S[Y][1][2 * e]); a.l1 = fadd_s(a.l1, a.S[Y][1][2 * e + 1]);
      }
    }
    if constexpr (M == 11) { SB(); if constexpr (QK) wait_vm<9>(); else wait_vm<0>(); hook(IC<0>{}); bar(); hook(IC<1>{}); SB(); }
    if constexpr (PV && M >= 12) a.fr[(M - 12) & 7] = lds_b128(vaddr(Y, M - 12));
    SB();
  });
  static_for<16>([&](auto M_) AI {
    constexpr int M = decltype(M_)::v, ks = M >> 2, db = M & 3;
    if constexpr (PV) {
      a.o[db] = MFMA32(a.fr[M & 7], asbf(a.pk[ks]), a.o[db]);
      if constexpr (RD0[M] >= 0) a.fr[RD0[M] & 7] = lds_b128(vaddr(Y, RD0[M]));
      if constexpr (RD1[M] >= 0) a.fr[RD1[M] & 7] = lds_b128(vaddr(Y, RD1[M]));
    }
    if constexpr (QK) {
      if constexpr (M == 0) vo = a.voff | mk;
      if constexpr (M == 0 || M == 1 || M == 6 || M == 9) { constexpr int P = M == 0 ? 0 : M == 1 ? 1 : M == 6 ? 2 : 3; dma16c(dk + P * 1024, vo, c.rws, sok + P * 1024); }
      if constexpr (M == 2) {
        asm volatile("s_nop 7\n\ts_nop 3" : "+v"(a.S[X][0]), "+v"(a.S[X][1]));
        if (__builtin_expect(diag, 0)) {
          float ninf;
          asm volatile("v_mov_b32 %0, 0xff800000" : "=v"(ninf));
          const int dq2 = dq + (s + 2 == a.n1k ? 64 : 0);
          mask_rows<0>(a.S[X][0], dq2, ninf, make_iseq<16>{}); mask_rows<32>(a.S[X][1], dq2, ninf, make_iseq<16>{});
        }
      }
      if constexpr (M >= 2 && M < 6) {
        constexpr int r = 4 * (M - 2);
        if constexpr (M == 2) {
          a.mx0 = max3f(a.S[X][0][0], a.S[X][0][1], a.S[X][1][0]); a.mx1 = max3f(a.S[X][0][2], a.S[X][0][3], a.S[X][1][1]);
          a.mx0 = max3f(a.mx0, a.S[X][1][2], a.S[X][1][3]);
        } else {
          a.mx0 = max3f(a.mx0, a.S[X][0][r], a.S[X][0][r + 1]); a.mx1 = max3f(a.mx1, a.S[X][0][r + 2], a.S[X][0][r + 3]);
          a.mx0 = max3f(a.mx0, a.S[X][1][r], a.S[X][1][r + 1]); a.mx1 = max3f(a.mx1, a.S[X][1][r + 2], a.S[X][1][r + 3]);
        }
      }
      if constexpr (M == 6) {
        const float mm = max2f(a.mx0, a.mx1);
        auto pr = __builtin_amdgcn_permlane32_swap(__float_as_uint(mm), __float_as_uint(mm), false, false);
        a.mx0 = max2f(__uint_as_float(pr[0]), __uint_as_float(pr[1]));
      }
      if constexpr (M == 7) {
        if (__builtin_expect(first || __any(a.mx0 > THR), 0)) {
          const float dl = first ? fmaxf(a.mx0, -1e30f) : fmaxf(a.mx0, 0.f);
          a.mhat = first ? dl : a.mhat + dl;
          a.fct = __builtin_amdgcn_exp2f(-dl);
#pragma unroll
          for (int r = 0; r < 16; ++r) { a.S[X][0][r] -= dl; a.S[X][1][r] -= dl; a.negm[r] = -a.mhat; }
          a.pend = !first;
        }
      }
      if constexpr (M >= 8) {
        constexpr int e = M - 8;
        a.S[X][0][2 * e] = __builtin_amdgcn_exp2f(a.S[X][0][2 * e]);
        a.S[X][0][2 * e + 1] = __builtin_amdgcn_exp2f(a.S[X][0][2 * e + 1]);
        PIN(a.S[X][0][2 * e]); PIN(a.S[X][0][2 * e + 1]);
        if constexpr (e > 0) {
          a.t0 = e == 1 ? a.S[X][0][0] : fadd_s(a.t0, a.S[X][0][2 * e - 2]); a.t1 = e == 1 ? a.S[X][0][1] : fadd_s(a.t1, a.S[X][0][2 * e - 1]);
          PIN(a.t0); PIN(a.t1);
        }
      }
      if constexpr (M == 11) { SB(); wait_vm<PV ? 9 : 8>(); bar(); }
      if constexpr (M == 13) { asm volatile("s_mov_b64 exec, %2\n\tglobal_load_dword %0, %1, off\n\ts_mov_b64 exec, -1" : "+v"(a.dummy) : "v"(a.ta), "s"(a.tm) : "memory"); a.tm <<= 4; }
      if constexpr (M >= 12) a.fr[(M - 12) & 7] = lds_b128(kaddr(Y, M - 12));
    }
    SB();
  });
}

constexpr unsigned WP = 288;
constexpr unsigned LSYNC = 161280;
constexpr unsigned C_LAND = 0, C_FREE = 64, C_GB = 128, C_CTXD = 136, C_MRG = 140, C_TICK = 144, LSTAT = LSYNC + 256;
#define ARRV 2
constexpr int XM = 12;
constexpr int XDEP(int NK) { return NK == 16 ? XM : 8; }
DI unsigned land(unsigned ph, unsigned q) { return LSYNC + C_LAND + (ph * 5 + q) * 4; }
DI unsigned fre(unsigned ph, unsigned q) { return LSYNC + C_FREE + (ph * 5 + q) * 4; }
DI void cnt_add(Ctx& c, unsigned off) {
  asm volatile("s_mov_b64 exec, 1\n\tds_add_u32 %0, %1\n\ts_mov_b64 exec, -1" ::"v"(c.ldsb + off), "v"(1u) : "memory");
}
#define wlog(...)
DI unsigned cnt_add_rtn(Ctx& c, unsigned off) {
  unsigned r;
  asm volatile("s_mov_b64 exec, 1\n\tds_add_rtn_u32 %0, %1, %2\n\ts_mov_b64 exec, -1\n\ts_waitcnt lgkmcnt(0)" : "=&v"(r) : "v"(c.ldsb + off), "v"(1u) : "memory");
  return rfl(r);
}
DI unsigned cnt_read(Ctx& c, unsigned off) { return __hip_atomic_load((LAS unsigned*)(size_t)(c.ldsb + off), __ATOMIC_RELAXED, __HIP_MEMORY_SCOPE_WORKGROUP); }
DI void prio_lo() { __builtin_amdgcn_s_setprio(0); }
DI void prio_hi(Ctx& c) { __builtin_amdgcn_s_setprio(3); }
DI void prio_gemm(Ctx& c, bool behind) {
  if (c.w >= 4) __builtin_amdgcn_s_setprio(1); else if (behind) __builtin_amdgcn_s_setprio(0); else __builtin_amdgcn_s_setprio(2);
}
DI void cnt_wait(Ctx& c, unsigned off, unsigned target) {
  asm volatile("" ::: "memory");
  unsigned spins = 0;
#pragma unroll 1
  while (rfl(cnt_read(c, off)) < target) { __builtin_amdgcn_s_sleep(1); if (++spins > (1u << 20)) break; }
  asm volatile("" ::: "memory");
}
DI void flag_w(Ctx& c, unsigned flag_off) {
  wait_vm<0>();
  fput(u32x2{c.ep0, c.ep1}, c.rws, c.lane == 0 ? flag_off : OOB);
}
DI void dma16m(unsigned lds_addr, unsigned voff, rsrc_t r, unsigned soff, u64 mask) {
  asm volatile("s_mov_b32 m0, %0\n\ts_mov_b64 exec, %4\n\ts_nop 3\n\tbuffer_load_dwordx4 %1, %2, %3 offen lds\n\ts_mov_b64 exec, -1" ::"s"(lds_addr), "v"(voff), "s"(r), "s"(soff), "s"(mask)
               : "memory", "m0");
}
struct WSrc { unsigned voff[9]; rsrc_t r; unsigned main, tail, pitch, colA, colB; unsigned idx, pend, ph; };
DI unsigned w_voff(const WSrc& s, unsigned lane, int p) {
  const unsigned ci = lane + 64 * p, rr = __umul24(ci, 3641) >> 16, cc = ci - __umul24(rr, 18);
  return __umul24(rr, s.pitch - WP) + ci * 16 + (cc < 16 ? s.colA : s.colB - 256);
}
DI unsigned opaque_lane() {
  unsigned lane;
  asm volatile("v_mbcnt_lo_u32_b32 %0, -1, 0\n\tv_mbcnt_hi_u32_b32 %0, -1, %0" : "=v"(lane));
  return lane;
}
template <int NV = 5> DI void wsrc_voff(Ctx& c, WSrc& s) {
  const unsigned lane = opaque_lane();
#pragma unroll
  for (int p = 0; p < NV; ++p) s.voff[p] = w_voff(s, lane, p);
  if constexpr (NV == 5) {
    s.voff[5] = lane >= 16 ? ~0u : 0u;
    const unsigned l7 = (lane & 7) < 5 ? lane & 7 : 0;
    s.voff[6] = c.ldsb + (lane < 8 ? LSYNC + C_TICK + (s.ph * 5 + l7) * 4 : fre(s.ph - 1, 4) - 4 * l7);
    s.voff[7] = lane == 0 ? 3 : 8;
  }
}
template <int NV = 0> DI void wsrc_setup(Ctx& c, WSrc& s, rsrc_t r, unsigned pitch, unsigned colA, unsigned colB, unsigned rowMain, unsigned rowTail, unsigned ph) {
  s.r = r; s.pitch = pitch; s.main = rowMain * pitch; s.tail = rowTail * pitch;
  s.colA = colA; s.colB = colB; s.ph = ph; s.idx = s.pend = 0;
  if constexpr (NV > 0) wsrc_voff<NV>(c, s);
}
DI void w_group(Ctx& c, const WSrc& s, unsigned q) {
  const unsigned r0 = 128 * q + 32 * (c.w & 3);
#pragma unroll
  for (int p = 0; p < 9; ++p) dma16(c.ldsb + r0 * WP + p * 1024, s.voff[p], s.r, s.main + r0 * s.pitch);
}
DI void poll_w(Ctx& c, unsigned flag_off, unsigned n) {
  prio_lo();
  poll_flags(c, flag_off, n, 1);
  prio_hi(c);
}
constexpr unsigned nitems(unsigned q) { return q == 4 ? 3 : 8; }
DI bool w_try(Ctx& c, WSrc& s) {
  if (s.idx) return false;
  unsigned idx, t;
#pragma unroll 1
  for (;;) {
    const unsigned v = __hip_atomic_load((LAS unsigned*)(size_t)s.voff[6], __ATOMIC_RELAXED, __HIP_MEMORY_SCOPE_WORKGROUP);
    const unsigned m = (unsigned)__builtin_amdgcn_ballot_w64(v < s.voff[7]);
    if ((m & 31) == 0) s.idx = 1;
    const unsigned av = m & ~(m >> 8) & 31;
    if (av == 0) return false;
    idx = __builtin_ctz(av);
    t = cnt_add_rtn(c, LSYNC + C_TICK + (s.ph * 5 + idx) * 4);
    if (t < (idx ? 8u : 3u)) break;
  }
  s.pend += 1u << (4 * idx);
  wlog(c, s.ph, 1, idx, true); wlog(c, s.ph, 2, idx);
  const unsigned lrow = 128 * (4 - idx) + 16 * t, so = (idx ? s.main + lrow * s.pitch : s.tail + 16 * t * s.pitch), sk = lrow == 544 ? 8 * s.pitch : 0u;
  const unsigned la = c.ldsb + lrow * WP;
  dma16(la, s.voff[0], s.r, so);
  dma16(la + 1024, s.voff[1], s.r, so);
  dma16(la + 2048, s.voff[2] + (s.voff[5] & sk), s.r, so);
  dma16(la + 3072, s.voff[3], s.r, so + sk);
  dma16m(la + 4096, s.voff[4], s.r, so + sk, 0xffffffffull);
  return true;
}
DI void w_signal(Ctx& c, const WSrc& s, unsigned items) {
  if (items) {
    unsigned lane;
    asm volatile("v_mbcnt_lo_u32_b32 %0, -1, 0" : "=v"(lane));
    const unsigned n = (items >> (4 * lane)) & 15, ad = c.ldsb + land(s.ph, 4) - 4 * lane;
    asm volatile("s_mov_b64 exec, 31\n\tds_add_u32 %0, %1\n\ts_mov_b64 exec, -1" ::"v"(ad), "v"(n) : "memory");
  }
}
DI void w_flush(Ctx& c, WSrc& s) { w_signal(c, s, s.pend); s.pend = 0; }
#define WMAX 2
#define FMAX 0
#define NMAX WMAX
#define LSRV 7
template <unsigned MX = WMAX> DI void w_try_wait(Ctx& c, WSrc& s, bool en = true) {
  const unsigned old = s.pend;
  unsigned k = 0;
  s.pend = 0;
#pragma unroll 1
  while (en && k < MX && w_try(c, s)) ++k;
  if (k == 0) wait_vm<0>(); else if (k == 1) wait_vm<5>(); else if (k == 2) wait_vm<10>(); else if (k == 3) wait_vm<15>(); else wait_vm<20>();
  w_signal(c, s, old);
}
DI void w_need(Ctx& c, WSrc& s, unsigned q, unsigned n) {
  unsigned spins = 0;
  wsrc_voff(c, s);
#pragma unroll 1
  for (;;) {
    w_try_wait<NMAX>(c, s);
    if (s.pend) { wait_vm<0>(); w_flush(c, s); }
    if (rfl(cnt_read(c, land(s.ph, q))) >= n || ++spins > (1u << 20)) break;
    __builtin_amdgcn_s_sleep(1);
  }
  asm volatile("" ::: "memory");
}
DI void flag_srv(Ctx& c, WSrc& s, unsigned flag_off, bool lsrv) {
  w_try_wait<FMAX>(c, s, lsrv || c.w < 4);
  fput(u32x2{c.ep0, c.ep1}, c.rws, c.lane == 0 ? flag_off : OOB);
}
DI void poll_srv(Ctx& c, WSrc& s, unsigned flag_off, unsigned n, bool lsrv) {
  const unsigned voff = c.lane < n ? flag_off + c.lane * 8 : OOB;
  const u64 t0 = __builtin_amdgcn_s_memrealtime();
  prio_lo();
  for (;;) {
    u32x2 f;
    fget(f, c.rws, voff);
    if (s.pend) { wait_vm<1>(); w_flush(c, s); }
    w_try_wait(c, s, lsrv || c.w < 4);
    PIN(f);
    if (__all((f[0] == c.ep0 && f[1] == c.ep1) || c.lane >= n)) break;
    if (__builtin_amdgcn_s_memrealtime() - t0 > 5000000ull) break;
    if (s.pend == 0) __builtin_amdgcn_s_sleep(1);
  }
  prio_hi(c);
}
constexpr unsigned LB1 = LSTAT + 256, LB2 = LB1 + 320;
template <int P> DI void park_load(Ctx& c, const u16* w1, const u16* b1, const u16* w2, const u16* b2) {
  const unsigned xcd = c.b & 7, nt = (c.b >> 3) & 3, nt32 = xcd * 4 + nt, w = (c.w & 3) * 2 + (P & 1);
  const unsigned rr = c.lane >= 18 ? 1 : 0, cc = c.lane - 18 * rr, r0 = (w < 4 ? 40 : 48) + 2 * w;
  const unsigned c1 = cc < 16 ? nt32 * 256 + cc * 16 : 8192 + nt32 * 32 + (cc - 16) * 16, c2 = cc < 16 ? nt * 256 + cc * 16 : 1024 + nt * 32 + (cc - 16) * 16;
  if constexpr (P < 2) dma16m(c.ldsb + (512 + 2 * w) * WP, rr * (FF * 2) + c1, mk_rsrc(w1, DM * FF * 2), (512 + r0) * (FF * 2), 0xfffffffffull);
  else if constexpr (P < 4) dma16m(c.ldsb + (528 + 2 * w) * WP, rr * (DM * 2) + c2, mk_rsrc(w2, FF * DM * 2), (4096 + 64 * xcd + r0) * (DM * 2), 0xfffffffffull);
  else {
    const bool f = c.w == 4;
    dma16m(c.ldsb + (f ? LB1 : LB2 + (c.w - 5) * 384), f ? c1 : c.lane * 16, mk_rsrc(f ? b1 : b2, f ? FF * 2 : DM * 2), f ? 0 : (c.w - 5) * 384, f ? 0x3ffffull : 0xffffffull);
  }
}
DI void park_read(Ctx& c, u32x2 (&tl)[9], u32x2 (&tl2)[9]) {
  const unsigned ad = c.ldsb + (512 + (c.lane >> 2)) * WP + (c.lane & 3) * 8;
#pragma unroll
  for (int a = 0; a < 9; ++a) { tl[a] = __builtin_bit_cast(u32x2, lds_tr(ad + a * 32)); tl2[a] = __builtin_bit_cast(u32x2, lds_tr(ad + 16 * WP + a * 32)); }
  SB();
  cnt_add(c, fre(0, 4)); wlog(c, 1, 0, 0);
#pragma unroll
  for (int a = 0; a < 9; ++a) { PIN(tl[a]); PIN(tl2[a]); }
}
constexpr int KK(int NK, int v) { return NK == 18 ? (v < 2 ? 16 + v : 4 * (3 - (v - 2) / 4) + (v - 2) % 4) : 4 * (3 - v / 4) + v % 4; }
constexpr bool QSTART(int NK, int v) { return v < NK && v > 0 && (KK(NK, v) & 3) == 0; }
constexpr bool QEND(int NK, int v) { return (KK(NK, v) & 3) == 3 || KK(NK, v) == 17; }
template <int NK, int V> DI void x_ld(Ctx& c, u32x4 (&x)[2], unsigned voff, unsigned so, unsigned so2) {
  constexpr int k = KK(NK, V);
  gld16c2<(k & 1) * 2048>(x[0], x[1], voff, c.rws, k < 16 ? so + (k >> 1) * 4096 : so2);
}
template <int NK, int V0 = 0, int NV = 8> DI void x_pro(Ctx& c, u32x4 (&x)[XM][2], unsigned voff, unsigned so, unsigned so2) {
  static_for<NV>([&](auto V_) AI { x_ld<NK, V0 + decltype(V_)::v>(c, x[V0 + decltype(V_)::v], voff, so, so2); });
}
DI void x_ld4_masked(u32x4& a, u32x4& b, u32x4& d, u32x4& e, unsigned voff, rsrc_t r, unsigned so, unsigned mask) {
  asm volatile("s_cmp_eq_u32 %7, 0\n\ts_cbranch_scc1 .Lxs%=\n\ts_nop 4\n\tbuffer_load_dwordx4 %0, %4, %5, %6 offen sc1\n\tbuffer_load_dwordx4 %1, %4, %5, %6 offen offset:256 sc1\n\t"
               "buffer_load_dwordx4 %2, %4, %5, %6 offen offset:2048 sc1\n\tbuffer_load_dwordx4 %3, %4, %5, %6 offen offset:2304 sc1\n.Lxs%=:"
               : "+v"(a), "+v"(b), "+v"(d), "+v"(e) : "v"(voff), "s"(r), "s"(so), "s"(mask) : "memory", "scc");
}
template <int IMM> DI void x_ld2_if(u32x4& a, u32x4& b, unsigned voff, rsrc_t r, unsigned so, unsigned skip) {
  asm volatile("s_cmp_lg_u32 %5, 0\n\ts_cbranch_scc1 .Lxi%=\n\ts_nop 4\n\tbuffer_load_dwordx4 %0, %2, %3, %4 offen offset:%6 sc1\n\tbuffer_load_dwordx4 %1, %2, %3, %4 offen offset:%7 sc1\n.Lxi%=:"
               : "=&v"(a), "=&v"(b) : "v"(voff), "s"(r), "s"(so), "s"(skip), "i"(IMM), "i"(IMM + 256) : "memory", "scc");
}
template <int V> DI void x_ld16_if(Ctx& c, u32x4 (&x)[2], unsigned voff, unsigned so, unsigned skip) {
  constexpr int k = KK(16, V);
  x_ld2_if<(k & 1) * 2048>(x[0], x[1], voff, c.rws, so + (k >> 1) * 4096, skip);
}
DI void dma16_if(unsigned lds_addr, unsigned voff, rsrc_t r, unsigned soff, unsigned go) {
  asm volatile("s_cmp_eq_u32 %4, 0\n\ts_cbranch_scc1 .Ldi%=\n\ts_mov_b32 m0, %0\n\ts_nop 3\n\tbuffer_load_dwordx4 %1, %2, %3 offen lds\n.Ldi%=:" ::"s"(lds_addr), "v"(voff), "s"(r), "s"(soff), "s"(go)
               : "memory", "m0", "scc");
}
DI void x_pro_masked16(Ctx& c, u32x4 (&x)[XM][2], unsigned so, unsigned mask) {
  const unsigned voff = x_voff(c);
#pragma unroll
  for (int p = 0; p < XM / 2; ++p) x_ld4_masked(x[2 * p][0], x[2 * p][1], x[2 * p + 1][0], x[2 * p + 1][1], voff, c.rws, so + (KK(16, 2 * p) >> 1) * 4096, mask);
}
template <int L> DI void wlane(unsigned& v, unsigned s) { asm volatile("v_writelane_b32 %0, %1, %2" : "+v"(v) : "s"(s), "i"(L)); }
struct Sched {
  int NK, XD;
  constexpr int T0() const { return 9 * (NK - 4); }
  constexpr int sv(int n) const { return n < T0() ? n / 9 : NK - 4 + (n - T0()) % 4; }
  constexpr int sa(int n) const { return n < T0() ? n % 9 : ((n - T0()) / 4 == 0 ? 8 : (n - T0()) / 4 - 1); }
  constexpr int g0(int G) const { return G == 3 ? T0() + 8 : 9 * (NK - 16 + 1 + 4 * G) + 1; }
  constexpr int gn(int G) const { return G == 0 ? 4 : 9; }
  constexpr int wg(int n) const { for (int G = 0; G < 4; ++G) if (n >= g0(G) && (n - g0(G)) % 3 == 0 && (n - g0(G)) / 3 < gn(G)) return G; return -1; }
  constexpr int wp(int n) const { return (n - g0(wg(n))) / 3; }
  constexpr int gsig(int n) const { for (int G = 0; G < 3; ++G) if (n == g0(G) + 3 * (gn(G) - 1) + 27) return G; return -1; }
  constexpr int st(int n) const {
    const int m = n - T0();
    if (m == 6) return 0;
    if (m >= 13 && (m - 13) % 8 == 0) return 1 + 2 * ((m - 13) / 8);
    if (m >= 15 && (m - 15) % 8 == 0) return 2 + 2 * ((m - 15) / 8);
    return -1;
  }
  constexpr bool sp(int n) const { return n < T0() ? (n % 18 == 0 && n >= 18) : ((n - T0()) % 18 == 0); }
  constexpr int between(int n0, int n1) const {
    int cnt = 0;
    for (int i = n0; i < n1; ++i) { if (i < T0() && sa(i) == 8 && sv(i) + XD < NK) cnt += 2; if (st(i) >= 0) cnt += 1; }
    return cnt;
  }
  constexpr int younger(int tag, int n, bool ldr) const {
    int cnt = 0, last = -1;
    for (int v = 0; v < XD; ++v) { if (100 + v == tag) last = cnt + 1; cnt += 2; }
    for (int i = 0; i < n; ++i) {
      if (i < T0() && sa(i) == 8 && sv(i) + XD < NK) { if (100 + sv(i) + XD == tag) last = cnt + 1; cnt += 2; }
      if (ldr && wg(i) >= 0) { if (200 + wg(i) == tag) last = cnt; cnt += 1; }
      if (st(i) >= 0) cnt += 1;
    }
    return cnt - 1 - last;
  }
};
template <int NK, int KORD, int PH, int GST, class St>
DI void gemm_g(Ctx& c, f32x4 (&acc)[2][9], u32x4 (&x)[XM][2], u32x2 (&tl)[9], u32x2 (&tl2)[9], WSrc& ws, WSrc& wn, unsigned so, unsigned so2, unsigned flag_off, St&& st) {
  constexpr unsigned RT = KORD == 0 ? 16 : 8;
  constexpr int LA = 6, NS = NK * 9;
  constexpr int XD = XDEP(NK);
  constexpr Sched S{NK, XD};
  constexpr bool LD = PH < 2;
  const unsigned g = c.lane >> 4, q = (c.lane & 15) >> 2, pp = c.lane & 3;
  const unsigned wrow1 = 16 * (g >> 1) + 4 * (g & 1) + q, wrow = KORD == 0 ? 4 * g + q : wrow1;
  unsigned wb[3], wt[2];
#pragma unroll
  for (int i = 0; i < 3; ++i) { wb[i] = c.ldsb + wrow * WP + pp * 8 + i * 7 * 32 * WP; PIN(wb[i]); }
  if constexpr (NK == 18) {
    wt[0] = c.ldsb + (512 + wrow1) * WP + pp * 8; wt[1] = c.ldsb + (544 + wrow1 - 8 * (g >> 1)) * WP + pp * 8;
    PIN(wt[0]); PIN(wt[1]);
  }
  unsigned voff = x_voff(c); PIN(voff);
  bf16x4 wl[8][2];
  auto rdw = [&](auto N_, auto H_) AI {
    constexpr int N = decltype(N_)::v, H = decltype(H_)::v, v = S.sv(N), a = S.sa(N), k = KK(NK, v);
    if constexpr (k < 16) wl[N & 7][H] = lds_tr(wb[k / 7] + ((k % 7) * 32 + H * RT) * WP + a * 32);
    else if constexpr (k == 16) wl[N & 7][H] = lds_tr(wt[0] + H * 8 * WP + a * 32);
    else if constexpr (H == 0) wl[N & 7][H] = lds_tr(wt[1] + a * 32);
    else {
      u32x2 t;
      asm volatile("v_mov_b64 %0, %1" : "=v"(t) : "v"(tl[a]));
      wl[N & 7][1] = __builtin_bit_cast(bf16x4, t);
    }
    if constexpr (LD && H == 1 && ((N < S.T0() && a == 8 && QEND(NK, v)) || N == NS - 1)) { SB(); cnt_add(c, fre(PH, k >> 2)); wlog(c, PH + 1, 0, 4 - (k >> 2)); }
  };
  bool behind = false;
  if constexpr (PH > 0) {
    if (ws.pend) { wait_vm<2 * XD>(); w_flush(c, ws); }
    const unsigned l4 = cnt_read(c, land(PH, 4)), f0 = cnt_read(c, fre(PH - 1, 0));
    behind = (c.w < 4) & (rfl(f0) < 8);
    if (__builtin_expect(rfl(l4) < 3, 0)) w_need(c, ws, 4, 3);
  }
  prio_gemm(c, behind);
  static_for<LA>([&](auto N) AI { rdw(N, IC<0>{}); rdw(N, IC<1>{}); });
  unsigned lc = 0;
  SB();
  static_for<NS>([&](auto N_) AI {
    constexpr int N = decltype(N_)::v, v = S.sv(N), a = S.sa(N);
    if constexpr (N < S.T0() ? a == 0 : N < S.T0() + 4) {
      wait_vm<S.younger(100 + v, N, false)>();
      PIN(x[v % XD][0]); PIN(x[v % XD][1]);
    }
    if constexpr (N < S.T0() && a == 0) {
      if constexpr (v == 0 || v == 8) STAMPG(GST + (v >> 3));
      if constexpr (PH == 0 && v == ARRV) { if (c.w < 4) { wait_vm<S.between(0, N)>(); cnt_add(c, land(0, 2)); } }
      constexpr bool chk2 = QSTART(NK, v + 2), chk1 = QSTART(NK, v + 1);
      if constexpr (chk1) {
        if (__builtin_expect(rfl(lc) < (PH > 0 ? 8 : 4), 0)) { if constexpr (PH > 0) { prio_hi(c); w_need(c, ws, KK(NK, v + 1) >> 2, 8); prio_gemm(c, behind); } else cnt_wait(c, land(0, KK(NK, v + 1) >> 2), 4); }
      }
      if constexpr (PH == 0 && v == 7) park_read(c, tl, tl2);
      if constexpr (chk2) lc = cnt_read(c, land(PH, KK(NK, v + 2) >> 2));
    }
    if constexpr (PH > 0 && S.sp(N) && N < S.T0()) {
      if (__builtin_expect(behind, 0)) { behind = rfl(cnt_read(c, fre(PH - 1, 0))) < 8; prio_gemm(c, behind); }
    }
    SB();
    const bf16x8 wf = cat(wl[N & 7][0], wl[N & 7][1]);
    acc[0][a] = MFMA16(wf, asbf(x[v % XD][0]), acc[0][a]);
    SB();
    if constexpr (N + LA < NS) rdw(IC<N + LA>{}, IC<0>{});
    SB();
    acc[1][a] = MFMA16(wf, asbf(x[v % XD][1]), acc[1][a]);
    SB();
    if constexpr (N + LA < NS) rdw(IC<N + LA>{}, IC<1>{});
    if constexpr (N < S.T0() && a == 8 && v + XD < NK) x_ld<NK, v + XD>(c, x[v % XD], voff, so, so2);
    if constexpr (S.st(N) >= 0) st(IC<S.st(N)>{});
    SB();
  });
  prio_hi(c);
  st(IC<7>{}); st(IC<8>{});
  STAMPG(GST + 2);
  if constexpr (LD) { wsrc_voff(c, wn); flag_srv(c, wn, flag_off, LSRV & (PH ? 4 : 1)); } else flag_w(c, flag_off);
}
DI f32x4 relu2(const f32x4& a) {
  const float inf = 3.4028234664e38f;
  const float r0 = __builtin_amdgcn_fmed3f(a[0], 0.f, inf), r1 = __builtin_amdgcn_fmed3f(a[1], 0.f, inf), r2 = __builtin_amdgcn_fmed3f(a[2], 0.f, inf), r3 = __builtin_amdgcn_fmed3f(a[3], 0.f, inf);
  return f32x4{r0 * r0, r1 * r1, r2 * r2, r3 * r3};
}
DI u32x4 pack_single(const f32x4& r0, const f32x4& r1) {
  const auto a = __builtin_amdgcn_permlane32_swap(cvtpk(r0[0], r0[1]), cvtpk(r1[0], r1[1]), false, false);
  const auto b = __builtin_amdgcn_permlane32_swap(cvtpk(r0[2], r0[3]), cvtpk(r1[2], r1[3]), false, false);
  return u32x4{a[0], b[0], a[1], b[1]};
}
template <int I> DI void part_store(Ctx& c, const f32x4 (&acc)[2][9], unsigned base, unsigned so) {
  const unsigned g = c.lane >> 4;
  if constexpr (I == 0) gst16c(pack_single(acc[0][8], acc[1][8]), c.rws, (g >> 1) * (4608 - 128) + base + 1024, so);
  else { constexpr int i = (I - 1) >> 1, u = (I - 1) & 1; gst16c(pack_pair(acc[u][2 * i], acc[u][2 * i + 1]), c.rws, base, so + u * 4608 + i * 256); }
}
DI unsigned part_base(Ctx& c) { const unsigned l15 = c.lane & 15; return (l15 >> 2) * 1152 + (l15 & 3) * 16 + (c.lane >> 4) * 64; }
struct Red { u32x4 p[8]; u32x4 s; };
DI void red_load(Ctx& c, Red& d, unsigned so) {
  gld16c<1024>(d.s, (c.lane >> 3) * 36864 + (c.lane & 7) * 16, c.rws, so);
#pragma unroll
  for (int s = 0; s < 8; ++s) gld16c<0>(d.p[s], c.lane * 16, c.rws, so + s * 36864);
}
#define RED_PIN(d) asm volatile("s_waitcnt vmcnt(0)" : "+v"(d.p[0]), "+v"(d.p[1]), "+v"(d.p[2]), "+v"(d.p[3]), "+v"(d.p[4]), "+v"(d.p[5]), "+v"(d.p[6]), "+v"(d.p[7]), "+v"(d.s)::"memory")
DI void gld8(u32x2& d, unsigned voff, rsrc_t r, unsigned soff) { asm volatile("s_nop 4\n\tbuffer_load_dwordx2 %0, %1, %2, %3 offen" : "=v"(d) : "v"(voff), "s"(r), "s"(soff) : "memory"); }
DI void job_offs(Ctx& c, unsigned row, unsigned& o0, unsigned& o2) {
  const unsigned pr = c.lane >> 4, g = (c.lane >> 2) & 3, nt = c.w & 3;
  o0 = row * (DM * 2) + (nt * 128 + pr * 32 + 4 * g) * 2;
  o2 = c.lane < 8 ? row * (DM * 2) + (512 + nt * 16 + 4 * g) * 2 : OOB;
}
DI void row_load(Ctx& c, u32x2 (&r)[4], rsrc_t rs, unsigned row) {
  unsigned o0, o2; job_offs(c, row, o0, o2);
  gld8(r[0], o0, rs, 0); gld8(r[1], o0, rs, 32); gld8(r[2], o2, rs, 0); gld8(r[3], o2, rs, 16);
}
DI float sum_hi3(float x) {
  x += __uint_as_float(__builtin_amdgcn_update_dpp(0, __float_as_uint(x), 0x128, 0xf, 0xf, false));
  { auto r = __builtin_amdgcn_permlane16_swap(__float_as_uint(x), __float_as_uint(x), false, false); x = __uint_as_float(r[0]) + __uint_as_float(r[1]); }
  return xhalf_sum(x);
}
DI void red_sum(const Red& d, float (&a)[16]) {
#pragma unroll
  for (int e = 0; e < 8; ++e) a[e] = 0.f;
#pragma unroll
  for (int s = 0; s < 8; ++s) {
#pragma unroll
    for (int e = 0; e < 4; ++e) { a[2 * e] += bflo(d.p[s][e]); a[2 * e + 1] += bfhi(d.p[s][e]); }
  }
#pragma unroll
  for (int e = 0; e < 4; ++e) { a[8 + 2 * e] = sum_hi3(bflo(d.s[e])); a[9 + 2 * e] = sum_hi3(bfhi(d.s[e])); }
}
DI float sum_tok4(float x) {
  x += __uint_as_float(__builtin_amdgcn_update_dpp(0, __float_as_uint(x), 0x124, 0xf, 0xf, false));
  x += __uint_as_float(__builtin_amdgcn_update_dpp(0, __float_as_uint(x), 0x128, 0xf, 0xf, false));
  { auto r = __builtin_amdgcn_permlane16_swap(__float_as_uint(x), __float_as_uint(x), false, false); x = __uint_as_float(r[0]) + __uint_as_float(r[1]); }
  return xhalf_sum(x);
}
DI void ln2_job(Ctx& c, const Red& d, const u32x2 (&xr)[4], unsigned (&hw)[8], unsigned row0) {
  const unsigned lane = c.lane, tok = lane & 3, g = (lane >> 2) & 3, pr = lane >> 4, nt = c.w & 3;
  const unsigned stat = c.ldsb + LSTAT + (c.w >> 2) * 128;
  const bool sing = lane < 8;
  float a[16], v[16];
  red_sum(d, a);
#pragma unroll
  for (int e = 0; e < 8; ++e) {
    const unsigned xx = xr[e >> 1][e & 1];
    hw[e] = cvtpk(bflo(xx) + a[2 * e], bfhi(xx) + a[2 * e + 1]); v[2 * e] = bflo(hw[e]); v[2 * e + 1] = bfhi(hw[e]);
  }
  float s1 = 0.f, s0 = 0.f;
#pragma unroll
  for (int e = 0; e < 8; ++e) { s1 += v[e]; s0 += v[8 + e]; }
  const float mw = sum_tok4(sing ? s1 + s0 : s1) * (1.f / 144);
  float s2 = 0.f, s3 = 0.f;
#pragma unroll
  for (int e = 0; e < 8; ++e) { const float t = v[e] - mw, t2 = v[8 + e] - mw; s2 += t * t; s3 += t2 * t2; }
  s2 = sum_tok4(sing ? s2 + s3 : s2);
  if (lane < 4) *(LAS f32x2*)(size_t)(stat + (nt * 4 + lane) * 8) = f32x2{mw, s2};
  SB();
  cnt_add(c, LSYNC + C_GB + (c.w >> 2) * 4);
  cnt_wait(c, LSYNC + C_GB + (c.w >> 2) * 4, 4);
  f32x2 st[4];
#pragma unroll
  for (int i = 0; i < 4; ++i) st[i] = *(LAS f32x2*)(size_t)(stat + (i * 4 + tok) * 8);
  const float mean = (st[0][0] + st[1][0] + st[2][0] + st[3][0]) * 0.25f;
  float m2 = st[0][1] + st[1][1] + st[2][1] + st[3][1];
#pragma unroll
  for (int i = 0; i < 4; ++i) { const float t = st[i][0] - mean; m2 += 144.f * t * t; }
  const float rs = rsqrtf(m2 * (1.f / DM) + 1e-5f);
#pragma unroll
  for (int e = 0; e < 16; ++e) v[e] = (v[e] - mean) * rs;
  const unsigned row = row0 + tok, mb = row >> 5, r32 = row & 31;
  const unsigned o0 = ((mb * 36 + 2 * (nt * 4 + pr) + (g >> 1)) * 64 + (g & 1) * 32 + r32) * 16;
  const unsigned o1 = sing ? ((mb * 36 + 32 + nt) * 64 + g * 32 + r32) * 16 : OOB;
  gst16c(u32x4{cvtpk(v[0], v[1]), cvtpk(v[2], v[3]), cvtpk(v[4], v[5]), cvtpk(v[6], v[7])}, c.rws, o0, WS_HN);
  gst16c(u32x4{cvtpk(v[8], v[9]), cvtpk(v[10], v[11]), cvtpk(v[12], v[13]), cvtpk(v[14], v[15])}, c.rws, o1, WS_HN);
}

#define XS0 4
#define XS1 12
DI unsigned phase_attn(Ctx& c, QOut& qo, WSrc& wso, u32x4 (&xr)[XM][2], const u16* wo, const u16* w1, const u16* b1, const u16* w2, const u16* b2) {
  AttnState a;
  const unsigned ldsb = c.ldsb, lane = c.lane, w = c.w, b = c.b;
  const unsigned xcd = b & 7, jj = b >> 3, head = xcd * 4 + (jj >> 3), pi = jj & 7;
  const unsigned l31 = lane & 31, h = lane >> 5;
  const unsigned kh = w >> 2, rg = kh ? 7 - w : w, partner = 7 - w;
  const unsigned n1 = 2 * pi + 2, n1k = kh ? n1 : 0, n1t = kh ? n1 : 99;
  a.voff = lane * 16; a.ldsb = ldsb; a.w = w; a.n1 = n1; a.n1k = n1k;
  a.kbase = WS_K + head * (SEQ * HD * 2) + w * 4096; a.vbase = WS_V + head * (SEQ * HD * 2) + w * 4096;
  a.kb[0] = ldsb + lane * 16; a.kb[1] = a.kb[0] + 32768; a.vb[0] = a.kb[0] + 65536; a.vb[1] = a.kb[0] + 98304;
  PIN(a.kb[0]); PIN(a.kb[1]); PIN(a.vb[0]); PIN(a.vb[1]);
  if (kh == 0) {
#pragma unroll
    for (int d = 0; d < 8; ++d) *(LAS u32x4*)(size_t)(ldsb + LE + rg * 8192 + d * 1024 + lane * 16) = qo.q[d];
  }
  if (__builtin_expect(!qo.kdone, 0)) {
    wait_flags(c, F_K + head * 64, 8, 1);
#pragma unroll
    for (int p = 0; p < 4; ++p) dma16c(ldsb + kh * 16384 + w * 4096 + p * 1024, a.voff, c.rws, a.kbase + p * 1024);
  }
  STAMP(8);
#pragma unroll
  for (int d = 0; d < 4; ++d) a.o[d] = f32x16{};
#pragma unroll
  for (int d = 0; d < 8; ++d) a.qf[d] = asbf(qo.q[d]);
  a.negm = f32x16{}; a.mhat = 0.f; a.l0 = a.l1 = 0.f; a.fct = 1.f; a.pend = false;
  {
    unsigned bytes;
    asm volatile("s_mov_b32 %0, .Lfb_end-.Lfb_begin" : "=s"(bytes));
    const unsigned g = lane * 8 + w, sel = g < 162 ? 0 : g < 324 ? 1 : 2, li = g - 162 * sel, r = (li * 57) >> 9, cl = li - 9 * r;
    const unsigned o2 = (r < 16 ? 512 * xcd + jj * 16 + r : 4096 + 64 * xcd + jj * 2 + r - 16) * (DM * 2) + cl * 128;
    const unsigned o1 = (jj * 18 + r) * (FF * 2) + (cl < 8 ? xcd * 1024 + cl * 128 : 8192 + xcd * 128);
    const unsigned oc = ((g - 468) * 32 + jj) * 128;
    const bool code = g >= 468;
    const u64 base = code ? c.pc0 : sel == 0 ? (u64)w2 : sel == 1 ? (u64)w1 : (u64)wo;
    a.ta = base + (code ? (oc < bytes ? oc : 0) : sel == 1 ? o1 : o2);
    a.dummy = 0; a.tm = 15;
    PIN(a.ta);
  }
  if (kh == 0) wait_vm<0>();
  bar();
  STAMP(9);
#pragma unroll
  for (int n = 0; n < 4; ++n) a.fr[n] = lds_b128(a.kb[0] + ((n & 1) * 8 + (n >> 1)) * 1024);
  const int dq = (int)(rg * 32 + l31) - (int)(kh * 64) - 4 * (int)h;
  const unsigned dA = rg < 2 ? n1t - 2 : 99, dB = n1t - 1, dC = (kh || rg < 2) ? 16 : 99;

  const unsigned mtH = jj >> 2, fH = F_CTX + (mtH * 32 + xcd * 4) * 8;
  u32x2 fl = {0u, 0u};
  auto post = [&](auto X_, unsigned s) AI {
    constexpr int X = decltype(X_)::v;
    if constexpr (X == 0) {
      if (__builtin_expect(s == n1t, 0)) {
        const float inv = __builtin_amdgcn_rcpf(xhalf_sum(a.l0 + a.l1));
        const unsigned so = WS_CTX + ((pi * 4 + rg) * 256 + head * 8) * 1024;
#pragma unroll
        for (int d = 0; d < 4; ++d) {
          gst16c(pack8<0>(a.o[d], inv), c.rws, a.voff, so + d * 2048);
          gst16c(pack8<1>(a.o[d], inv), c.rws, a.voff, so + d * 2048 + 1024);
          a.o[d] = f32x16{};
        }
        a.l0 = a.l1 = 0.f;
        a.vb[1] += 16384;
      }
      if (__builtin_expect(s + 2 == n1t, 0)) a.kb[0] += 16384;
    } else {
      if (__builtin_expect(s == n1 + 1, 0)) flag_store(c, F_CTX + (pi * 32 + head) * 8);
      if (__builtin_expect(s == 15, 0)) flag_load(c, fl, fH, kh ? 0 : 4);
    }
    if (__builtin_expect(a.pend, 0)) {
#pragma unroll
      for (int d = 0; d < 4; ++d)
#pragma unroll
        for (int r = 0; r < 16; ++r) a.o[d][r] *= a.fct;
      a.l0 *= a.fct; a.l1 *= a.fct; a.pend = false;
    }
    a.l0 = fadd_s(a.l0, a.t0); a.l1 = fadd_s(a.l1, a.t1);
    if constexpr (X == 1) {
      if (__builtin_expect(s + 1 == n1t, 0)) {
#pragma unroll
        for (int d = 0; d < 8; ++d) a.qf[d] = lds_b128(ldsb + LE + rg * 8192 + d * 1024 + lane * 16);
        a.negm = f32x16{};
        a.kb[1] += 16384; a.vb[0] += 16384;
      }
    }
    SB();
  };
  attn_iter<0, true, false>(c, a, 0, true, dA == 0, dq);
  post(IC<0>{}, 0);
  for (unsigned s = 1; s < 17; s += 2) {
    attn_iter<1, true, true>(c, a, s, false, s == dB, dq); post(IC<1>{}, s);
    attn_iter<0, true, true>(c, a, s + 1, s + 1 == n1t, s + 1 == dA || s + 1 == dC, dq); post(IC<0>{}, s + 1);
  }
  if (w == 0) *(LAS unsigned*)(size_t)(ldsb + LSYNC + lane * 4) = 0;
  wsrc_setup<0>(c, wso, mk_rsrc(wo, NQ * DM * 2), DM * 2, (jj & 3) * 256, 1024 + (jj & 3) * 32, 512 * xcd, 0, 0);
  const unsigned ol = opaque_lane();
  wso.voff[0] = w_voff(wso, ol, 0); PIN(wso.voff[0]);
#pragma unroll
  for (int e = 0; e < 8; ++e) { a.pk[e >> 2][e & 3] = cvtpk(a.S[0][0][2 * e], a.S[0][0][2 * e + 1]); PIN(a.pk[e >> 2][e & 3]); }
  a.l0 = fadd_s(a.l0, a.S[0][0][14]); a.l1 = fadd_s(a.l1, a.S[0][0][15]);
  if (n1k == 16) wait_vm<8>(); else wait_vm<0>();
  bar();
  constexpr auto KSM = [](int m) constexpr { return m < 8 ? m >> 2 : 2 + (m & 1); };
  constexpr auto DBM = [](int m) constexpr { return m < 8 ? m & 3 : (m - 8) >> 1; };
  auto rdv = [&](auto M_) AI { constexpr int m = decltype(M_)::v; a.fr[m & 7] = lds_b128(a.vb[0] + (DBM(m) * 4 + KSM(m)) * 1024); };
  static_for<6>([&](auto M_) AI { rdv(M_); });
  PIN(fl);
  asm volatile("" ::"v"(a.dummy));
  STAMPG(33);
  const unsigned late = ((unsigned)__builtin_amdgcn_ballot_w64(fl[0] != c.ep0 || fl[1] != c.ep1) & 15u) | kh;
  const bool slow = late != 0 && kh == 0;
  const unsigned stat = ldsb + 544 * WP + rg * 512 + lane * 8, scr = ldsb + rg * 16384 + lane * 16;
  const unsigned xo = WS_CTX + ((mtH * 4 + (w & 3)) * 256 + xcd * 32) * 1024, xv = x_voff(c);
  const unsigned r3 = 384 + 32 * (w & 3);
  float lt = 0.f;
  auto put = [&](auto D_) AI {
    constexpr int d = decltype(D_)::v;
#pragma unroll
    for (int e = 0; e < 4; ++e) *(LAS f32x4*)(size_t)(scr + (d * 4 + e) * 1024) = f32x4{a.o[d][4 * e], a.o[d][4 * e + 1], a.o[d][4 * e + 2], a.o[d][4 * e + 3]};
  };
  if (__builtin_expect(slow, 0)) flag_load(c, fl, fH, 4);
  SB();
  static_for<16>([&](auto M_) AI {
    constexpr int M = decltype(M_)::v;
    a.o[DBM(M)] = MFMA32(a.fr[M & 7], asbf(a.pk[KSM(M)]), a.o[DBM(M)]);
    SB();
    if constexpr (M + 6 < 16) rdv(IC<M + 6>{});
    if constexpr (M < 9) dma16_if(ldsb + r3 * WP + M * 1024, wso.voff[M], wso.r, wso.main + r3 * wso.pitch, kh);
    if constexpr (M < 8) { wso.voff[M + 1] = w_voff(wso, ol, M + 1); PIN(wso.voff[M + 1]); }
    if constexpr (M < 4) {
#pragma unroll
      for (int e = 4 * M; e < 4 * M + 4; ++e) { a.S[0][1][e] = __builtin_amdgcn_exp2f(a.S[0][1][e]); PIN(a.S[0][1][e]); }
    } else if constexpr (M < 8) {
#pragma unroll
      for (int e = 2 * (M - 4); e < 2 * (M - 4) + 2; ++e) {
        a.pk[2 + (e >> 2)][e & 3] = cvtpk(a.S[0][1][2 * e], a.S[0][1][2 * e + 1]); PIN(a.pk[2 + (e >> 2)][e & 3]);
        a.l0 = fadd_s(a.l0, a.S[0][1][2 * e]); a.l1 = fadd_s(a.l1, a.S[0][1][2 * e + 1]);
      }
    }
    if constexpr (M == 8) { lt = xhalf_sum(a.l0 + a.l1); PIN(lt); }
    if constexpr (M >= XS0 && M - XS0 < XS1) x_ld16_if<M - XS0>(c, xr[M - XS0], xv, xo, late);
    if constexpr (M == 12) { if (kh == 0) { *(LAS f32x2*)(size_t)stat = f32x2{a.mhat, lt}; put(IC<0>{}); } }
    if constexpr (M == 14) { if (kh == 0) put(IC<1>{}); }
    SB();
  });
  STAMPG(34);
  if (kh == 0) { put(IC<2>{}); put(IC<3>{}); } else wait_vm<0>();
  STAMP(10);
  STAMPG(35);
  bar();
  STAMPG(36);
  if (kh == 0) {
    if (n1 == 16) flag_store(c, F_CTX + (7 * 32 + head) * 8);
    w_group(c, wso, 2);
    if (__builtin_expect(slow, 0)) { wait_vm<9>(); flag_check(c, fl, fH, 4); }
  }
  static_for<XM - XS1>([&](auto V_) AI { constexpr int v = XS1 + decltype(V_)::v; x_ld16_if<v>(c, xr[v], xv, xo, late); });
  if (kh) {
    f32x4 y[2][4];
    auto get = [&](int d) AI {
#pragma unroll
      for (int e = 0; e < 4; ++e) y[d & 1][e] = *(LAS f32x4*)(size_t)(scr + (d * 4 + e) * 1024);
    };
    const f32x2 ps = *(LAS f32x2*)(size_t)stat;
    get(0); get(1);
    SB();
    const float mm = fmaxf(a.mhat, ps[0]);
    const float fo = __builtin_amdgcn_exp2f(a.mhat - mm), fp = __builtin_amdgcn_exp2f(ps[0] - mm);
    const float inv = __builtin_amdgcn_rcpf(lt * fo + ps[1] * fp), f = fo * inv, f2 = fp * inv;
    const unsigned so = WS_CTX + (((15 - pi) * 4 + rg) * 256 + head * 8) * 1024;
#pragma unroll
    for (int d = 0; d < 4; ++d) {
      f32x16 x = a.o[d];
#pragma unroll
      for (int e = 0; e < 16; ++e) x[e] = x[e] * f + y[d & 1][e >> 2][e & 3] * f2;
      u32x4 p0 = pack8<0>(x), p1 = pack8<1>(x);
      PIN(p0); PIN(p1);
      SB();
      if (d < 2) get(d + 2);
      if (d == 1) { SB(); cnt_add(c, LSYNC + C_MRG); }
      gst16c(p0, c.rws, lane * 16, so + d * 2048);
      gst16c(p1, c.rws, lane * 16, so + d * 2048 + 1024);
      SB();
    }
    STAMPG(37);
    cnt_wait(c, LSYNC + C_MRG, 4);
    w_group(c, wso, 1);
    wait_vm<9>();
    STAMPG(38);
    fput(u32x2{c.ep0, c.ep1}, c.rws, lane == 0 ? F_CX2 + (((7 - pi) * 4 + rg) * 32 + head) * 8 : OOB);
    const unsigned fL = F_CX2 + ((mtH * 4 + (w & 3)) * 32 + xcd * 4) * 8;
    flag_load(c, fl, fL, 4);
    static_for<5>([&](auto P_) AI { park_load<decltype(P_)::v>(c, w1, b1, w2, b2); });
    w_group(c, wso, 0);
    wait_vm<9>(); cnt_add(c, land(0, 1));
    wait_vm<0>(); cnt_add(c, land(0, 0));
    STAMPG(39);
    prio_lo();
    flag_check(c, fl, fL, 4);
  }
  STAMP(11);
  return late;
}

struct TId { unsigned sp, nt, mt, nt32, mb, own, wq; bool isH; };
DI TId tids(Ctx& c) {
  fresh(c);
  TId t;
  const unsigned xcd = c.b & 7, jj = c.b >> 3;
  t.isH = c.w < 4; t.wq = c.w & 3; t.sp = xcd; t.nt = jj & 3; t.mt = (t.isH ? 0 : 8) + (jj >> 2);
  t.nt32 = xcd * 4 + t.nt; t.mb = t.mt * 4 + t.wq; t.own = t.nt * 8 + t.sp;
  return t;
}
DI void acc_zero(f32x4 (&acc)[2][9]) {
#pragma unroll
  for (int u = 0; u < 2; ++u)
#pragma unroll
    for (int a = 0; a < 9; ++a) acc[u][a] = f32x4{};
}
DI void phase_tail(Ctx& c, WSrc& wso, u32x4 (&xr)[XM][2], unsigned xlate, const u16* x, const u16* w1, const u16* b1, const u16* w2, const u16* b2, u16* out) {
  f32x4 acc[2][9]; u32x2 tl[9], tl2[9]; unsigned hw[8]; WSrc ws, wt;
  {
    const TId t = tids(c);
    const unsigned xo = WS_CTX + (t.mb * 256 + t.sp * 32) * 1024;
    prio_hi(c);
    STAMPG(12);
    x_pro_masked16(c, xr, xo, xlate);
    wsrc_setup(c, ws, mk_rsrc(w1, DM * FF * 2), FF * 2, t.nt32 * 256, 8192 + t.nt32 * 32, 0, 512, 1);
    acc_zero(acc);
    const unsigned pb = part_base(c), po = WS_P1 + (((t.mt * 4 + t.nt) * 8 + t.sp) * 8 + 2 * t.wq) * 4608;
    gemm_g<16, 1, 0, 13>(c, acc, xr, tl, tl2, wso, ws, xo, 0, F_P1 + (t.mb * 32 + t.own) * 8, [&](auto I) AI { part_store<decltype(I)::v>(c, acc, pb, po); });
    STAMPG(16);
  }
  {
    const TId t = tids(c);
    u32x2 xres[4]; Red rd;
    row_load(c, xres, mk_rsrc(x, SEQ * DM * 2), t.mt * 128 + t.own * 4 + (c.lane & 3));
    unsigned dummy = 0;
    w_touch<4>(c, dummy, w2, w2, w2, c.w, false);
    poll_srv(c, ws, F_P1 + ((t.mt * 4 + (t.own >> 3)) * 32 + t.wq * 8) * 8, 8, LSRV & 1);
    red_load(c, rd, WS_P1 + (t.mt * 256 + t.wq * 64 + (t.own >> 2)) * 4608 + (t.own & 3) * 1152);
    STAMPG(17);
    asm volatile("" ::"v"(dummy));
    PIN(xres[0]); PIN(xres[1]); PIN(xres[2]); PIN(xres[3]);
    RED_PIN(rd);
    w_flush(c, ws);
    STAMPG(18);
    ln2_job(c, rd, xres, hw, t.mt * 128 + t.own * 4);
#pragma unroll
    for (int e = 0; e < 8; ++e) PIN(hw[e]);
    STAMPG(19);
    flag_srv(c, ws, F_HN + ((t.mt * 32 + t.own) * 4 + t.wq) * 8, LSRV & 2);
    STAMPG(20);
    poll_srv(c, ws, F_HN + t.mb * 256, 32, LSRV & 2);
    STAMPG(21);
    asm volatile("" ::"v"(dummy));
    const unsigned xh = WS_HN + t.mb * 36 * 1024;
    x_pro<18>(c, xr, x_voff(c), xh, xh + 32768);
    wsrc_setup(c, wt, mk_rsrc(w2, FF * DM * 2), DM * 2, t.nt * 256, 1024 + t.nt * 32, 512 * t.sp, 4096 + 64 * t.sp, 2);
#pragma unroll
    for (int a = 0; a < 9; ++a) {
      const u32x2 bias = *(LAS u32x2*)(size_t)(c.ldsb + LB1 + a * 32 + (c.lane >> 4) * 8);
      acc[0][a] = acc[1][a] = f32x4{bflo(bias[0]), bfhi(bias[0]), bflo(bias[1]), bfhi(bias[1])};
    }
    const unsigned vo = x_voff(c), ho = WS_H1 + (t.mb * 288 + 8 * t.nt32) * 1024, hs = WS_H1 + (t.mb * 288 + 256 + t.nt32) * 1024;
    gemm_g<18, 0, 1, 22>(c, acc, xr, tl, tl, ws, wt, xh, xh + 32768, F_H1 + (t.mb * 32 + t.nt32) * 8, [&](auto I_) AI {
      constexpr int I = decltype(I_)::v;
      if constexpr (I == 0) {
        const unsigned g = c.lane >> 4;
        gst16c(pack_single(relu2(acc[0][8]), relu2(acc[1][8])), c.rws, ((g & 1) * 32 + (g >> 1) * 16 + (c.lane & 15)) * 16, hs);
      } else {
        constexpr int i = (I - 1) >> 1, u = (I - 1) & 1;
        gst16c(pack_pair(relu2(acc[u][2 * i]), relu2(acc[u][2 * i + 1])), c.rws, vo, ho + i * 2048 + u * 256);
      }
    });
    STAMPG(25);
  }
  {
    const TId t = tids(c);
    poll_srv(c, wt, F_H1 + (t.mb * 32 + t.sp * 4) * 8, 4, LSRV & 4);
    STAMPG(26);
    const unsigned x1 = WS_H1 + (t.mb * 288 + t.sp * 32) * 1024, x1t = WS_H1 + (t.mb * 288 + 256 + t.sp * 4) * 1024;
    x_pro<18>(c, xr, x_voff(c), x1, x1t);
    acc_zero(acc);
    const unsigned pb = part_base(c), po = WS_P2 + (((t.mt * 4 + t.nt) * 8 + t.sp) * 8 + 2 * t.wq) * 4608;
    gemm_g<18, 0, 2, 27>(c, acc, xr, tl2, tl2, wt, wt, x1, x1t, F_P2 + (t.mb * 32 + t.own) * 8, [&](auto I) AI { part_store<decltype(I)::v>(c, acc, pb, po); });
    STAMPG(30);
  }
  {
    const TId t = tids(c);
    u32x2 bb[4]; Red rd;
    {
      unsigned o0, o2; job_offs(c, 0, o0, o2); o2 = c.lane < 8 ? o2 : 0;
      bb[0] = *(LAS u32x2*)(size_t)(c.ldsb + LB2 + o0); bb[1] = *(LAS u32x2*)(size_t)(c.ldsb + LB2 + o0 + 32);
      bb[2] = *(LAS u32x2*)(size_t)(c.ldsb + LB2 + o2); bb[3] = *(LAS u32x2*)(size_t)(c.ldsb + LB2 + o2 + 16);
    }
    poll_w(c, F_P2 + ((t.mt * 4 + (t.own >> 3)) * 32 + t.wq * 8) * 8, 8);
    STAMPG(31);
    red_load(c, rd, WS_P2 + (t.mt * 256 + t.wq * 64 + (t.own >> 2)) * 4608 + (t.own & 3) * 1152);
    RED_PIN(rd);
    const rsrc_t ro = mk_rsrc(out, SEQ * DM * 2);
    float a[16]; unsigned ow[8];
    red_sum(rd, a);
#pragma unroll
    for (int e = 0; e < 8; ++e) {
      const unsigned bv = bb[e >> 1][e & 1];
      ow[e] = cvtpk(a[2 * e] + bflo(bv) + bflo(hw[e]), a[2 * e + 1] + bfhi(bv) + bfhi(hw[e]));
    }
    unsigned o0, o2; job_offs(c, t.mt * 128 + t.own * 4 + (c.lane & 3), o0, o2);
    __builtin_amdgcn_raw_buffer_store_b64(u32x2{ow[0], ow[1]}, ro, o0, 0, 0);
    __builtin_amdgcn_raw_buffer_store_b64(u32x2{ow[2], ow[3]}, ro, o0, 32, 0);
    __builtin_amdgcn_raw_buffer_store_b64(u32x2{ow[4], ow[5]}, ro, o2, 0, 0);
    __builtin_amdgcn_raw_buffer_store_b64(u32x2{ow[6], ow[7]}, ro, o2, 16, 0);
    STAMPG(32);
    if (c.b == 0 && c.w == 0) gst8c(u32x2{c.ep0, c.ep1}, c.rws, c.lane == 0 ? F_EPOCH : OOB, WS_FLAGS);
  }
}

__global__ __launch_bounds__(512, 2) void fused_block(const u16* __restrict__ x, const u16* __restrict__ ckv, const u16* __restrict__ wq,
                                                      const u16* __restrict__ wuk, const u16* __restrict__ wuv, const u16* __restrict__ wo,
                                                      const u16* __restrict__ w1, const u16* __restrict__ b1, const u16* __restrict__ w2,
                                                      const u16* __restrict__ b2, u16* __restrict__ out, char* __restrict__ ws) {
  extern __shared__ __attribute__((aligned(16))) char lds[];
  Ctx c;
  asm volatile("s_getpc_b64 %0\n.Lfb_begin:" : "=s"(c.pc0));
  c.ldsb = (unsigned)(size_t)lds; c.lane = threadIdx.x & 63; c.w = rfl(threadIdx.x >> 6); c.b = blockIdx.x;
  c.rws = mk_rsrc(ws, WS_END);
  STAMP(0);
  QOut qo;
  WSrc wso; u32x4 xr[XM][2];
  phase_qkv(c, qo, x, ckv, wq, wuk, wuv);
  fresh(c);
  const unsigned xlate = phase_attn(c, qo, wso, xr, wo, w1, b1, w2, b2);
  fresh(c);
  phase_tail(c, wso, xr, xlate, x, w1, b1, w2, b2, out);
  asm volatile(".Lfb_end:");
}

extern "C" void kernel_launch(void* const* d_in, const int* in_sizes, int n_in, void* d_out, int out_size, void* d_ws, size_t ws_size,
                              hipStream_t stream) {
  hipFuncSetAttribute((const void*)fused_block, hipFuncAttributeMaxDynamicSharedMemorySize, 163840);
  fused_block<<<256, 512, 163840, stream>>>((const u16*)d_in[0], (const u16*)d_in[1], (const u16*)d_in[2], (const u16*)d_in[3], (const u16*)d_in[4],
                                            (const u16*)d_in[5], (const u16*)d_in[7], (const u16*)d_in[8], (const u16*)d_in[9], (const u16*)d_in[10],
                                            (u16*)d_out, (char*)d_ws);
}
```

```cpp
#include <hip/hip_runtime.h>
#include <stdint.h>
#include <utility>

typedef __bf16 bf16x8 __attribute__((ext_vector_type(8)));
typedef __bf16 bf16x4 __attribute__((ext_vector_type(4)));
typedef __bf16 bf16x2 __attribute__((ext_vector_type(2)));
typedef float f32x16 __attribute__((ext_vector_type(16)));
typedef float f32x4 __attribute__((ext_vector_type(4)));
typedef float f32x2 __attribute__((ext_vector_type(2)));
typedef unsigned u32x4 __attribute__((ext_vector_type(4)));
typedef unsigned u32x2 __attribute__((ext_vector_type(2)));
typedef unsigned short u16;
typedef unsigned long long u64;
#define LAS __attribute__((address_space(3)))
#define DI __device__ __forceinline__
#define AI __attribute__((always_inline))
typedef __amdgpu_buffer_rsrc_t rsrc_t;

constexpr int SEQ = 2048, DM = 576, NH = 32, HD = 128, DC = 512, FF = 4608, NQ = 4096;

constexpr unsigned WS_FLAGS = 0;
constexpr unsigned WS_XN = 131072;
constexpr unsigned WS_K = WS_XN + SEQ * DM * 2;
constexpr unsigned WS_V = WS_K + SEQ * NQ * 2;
constexpr unsigned WS_CTX = WS_V + SEQ * NQ * 2;
constexpr unsigned WS_P1 = WS_CTX + SEQ * NQ * 2;
constexpr unsigned WS_H = WS_P1 + 8 * SEQ * DM * 2;
constexpr unsigned WS_HN = WS_H + SEQ * DM * 2;
constexpr unsigned WS_H1 = WS_HN + SEQ * DM * 2;
constexpr unsigned WS_P2 = WS_H1 + SEQ * FF * 2;
constexpr unsigned WS_CNT = WS_P2 + 8 * SEQ * DM * 2;
constexpr unsigned WS_END = WS_CNT + 16 * 80 * 128;
constexpr unsigned F_EPOCH = 0, F_XN = 4096, F_KV = 8192, F_CTX = 12288, F_K = 81920, F_V = 90112, F_CX2 = 98304;

constexpr unsigned N_P1 = 0, N_P2 = 16, N_HN = 32, N_H1 = 36;
constexpr unsigned cnt_at(unsigned mt, unsigned i) { return WS_CNT + (mt * 80 + i) * 128; }

template <int... Is> using iseq = std::integer_sequence<int, Is...>;
template <int N> using make_iseq = std::make_integer_sequence<int, N>;
template <int I> struct IC { static constexpr int v = I; };
template <class F, int... Is> DI void static_for_(F&& f, iseq<Is...>) { (f(IC<Is>{}), ...); }
template <int N, class F> DI void static_for(F&& f) { static_for_(f, make_iseq<N>{}); }

#define SB() __builtin_amdgcn_sched_barrier(0)
#define PIN(x) asm volatile("" : "+v"(x))
DI unsigned rfl(unsigned x) { return __builtin_amdgcn_readfirstlane(x); }
DI rsrc_t mk_rsrc(const void* p, unsigned bytes) { return __builtin_amdgcn_make_buffer_rsrc((void*)p, (short)0, (int)bytes, 0x00020000); }
constexpr unsigned OOB = 0xFFFFFF00u;
DI void dma16(unsigned lds_addr, unsigned voff, rsrc_t r, unsigned soff) {
  asm volatile("s_mov_b32 m0, %0\n\ts_nop 3\n\tbuffer_load_dwordx4 %1, %2, %3 offen lds" ::"s"(lds_addr), "v"(voff), "s"(r), "s"(soff) : "memory", "m0");
}
DI void dma16c(unsigned lds_addr, unsigned voff, rsrc_t r, unsigned soff) {
  asm volatile("s_mov_b32 m0, %0\n\ts_nop 3\n\tbuffer_load_dwordx4 %1, %2, %3 offen sc1 lds" ::"s"(lds_addr), "v"(voff), "s"(r), "s"(soff) : "memory", "m0");
}
template <int IMM> DI void gld16c(u32x4& d, unsigned voff, rsrc_t r, unsigned soff) {
  asm volatile("s_nop 4\n\tbuffer_load_dwordx4 %0, %1, %2, %3 offen offset:%4 sc1" : "=v"(d) : "v"(voff), "s"(r), "s"(soff), "i"(IMM) : "memory");
}
template <int IMM> DI void gld16(u32x4& d, unsigned voff, rsrc_t r, unsigned soff) {
  asm volatile("s_nop 4\n\tbuffer_load_dwordx4 %0, %1, %2, %3 offen offset:%4" : "=v"(d) : "v"(voff), "s"(r), "s"(soff), "i"(IMM) : "memory");
}
DI void gld8c(u32x2& d, unsigned voff, rsrc_t r, unsigned soff) {
  asm volatile("s_nop 4\n\tbuffer_load_dwordx2 %0, %1, %2, %3 offen sc1" : "=v"(d) : "v"(voff), "s"(r), "s"(soff) : "memory");
}
DI void gst16c(u32x4 v, rsrc_t r, unsigned voff, unsigned soff) {
  asm volatile("s_nop 4\n\tbuffer_store_dwordx4 %0, %1, %2, %3 offen sc1\n\ts_nop 1" ::"v"(v), "v"(voff), "s"(r), "s"(soff) : "memory");
}
DI void gst8c(u32x2 v, rsrc_t r, unsigned voff, unsigned soff) { __builtin_amdgcn_raw_buffer_store_b64(v, r, voff, soff, 16); }
DI void fput(u32x2 v, rsrc_t r, unsigned voff) { asm volatile("s_nop 4\n\tbuffer_atomic_swap_x2 %0, %1, %2, 0 offen sc1\n\ts_nop 1" ::"v"(v), "v"(voff), "s"(r) : "memory"); }
DI void fadd(rsrc_t r, unsigned voff) { asm volatile("s_nop 4\n\tbuffer_atomic_add_x2 %0, %1, %2, 0 offen sc1\n\ts_nop 1" ::"v"(u32x2{1u, 0u}), "v"(voff), "s"(r) : "memory"); }
DI void fget(u32x2& d, rsrc_t r, unsigned voff) {
  d = u32x2{0u, 0u};
  asm volatile("s_nop 4\n\tbuffer_atomic_add_x2 %0, %1, %2, 0 offen sc0 sc1" : "+v"(d) : "v"(voff), "s"(r) : "memory");
}
template <int N> DI void wait_vm() { asm volatile("s_waitcnt vmcnt(%0)" ::"i"(N) : "memory"); }
DI void bar() { asm volatile("s_waitcnt lgkmcnt(0)" ::: "memory"); __builtin_amdgcn_s_barrier(); asm volatile("" ::: "memory"); }
DI unsigned cvtpk(float lo, float hi) { f32x2 v = {lo, hi}; bf16x2 b = __builtin_convertvector(v, bf16x2); return __builtin_bit_cast(unsigned, b); }
DI float bflo(unsigned x) { return __uint_as_float(x << 16); }
DI float bfhi(unsigned x) { return __uint_as_float(x & 0xffff0000u); }
DI bf16x8 lds_b128(unsigned a) { return *(LAS bf16x8*)(size_t)a; }
DI bf16x4 lds_tr(unsigned a) { return __builtin_amdgcn_ds_read_tr16_b64_v4bf16((LAS bf16x4*)(size_t)a); }
DI bf16x8 cat(bf16x4 a, bf16x4 b) { return __builtin_shufflevector(a, b, 0, 1, 2, 3, 4, 5, 6, 7); }
DI bf16x8 asbf(u32x4 x) { return __builtin_bit_cast(bf16x8, x); }
#define MFMA32(a, b, c) __builtin_amdgcn_mfma_f32_32x32x16_bf16((a), (b), (c), 0, 0, 0)
#define MFMA16(a, b, c) __builtin_amdgcn_mfma_f32_16x16x32_bf16((a), (b), (c), 0, 0, 0)
template <int S> DI u32x4 pack8(const f32x16& x, float sc) {
  u32x4 w;
  w[0] = cvtpk(x[8 * S + 0] * sc, x[8 * S + 1] * sc); w[1] = cvtpk(x[8 * S + 2] * sc, x[8 * S + 3] * sc);
  w[2] = cvtpk(x[8 * S + 4] * sc, x[8 * S + 5] * sc); w[3] = cvtpk(x[8 * S + 6] * sc, x[8 * S + 7] * sc);
  return w;
}
template <int S> DI u32x4 pack8(const f32x16& x) {
  u32x4 w;
  w[0] = cvtpk(x[8 * S + 0], x[8 * S + 1]); w[1] = cvtpk(x[8 * S + 2], x[8 * S + 3]);
  w[2] = cvtpk(x[8 * S + 4], x[8 * S + 5]); w[3] = cvtpk(x[8 * S + 6], x[8 * S + 7]);
  return w;
}
DI float max3f(float a, float b, float c) { float r; asm("v_max3_f32 %0, %1, %2, %3" : "=v"(r) : "v"(a), "v"(b), "v"(c)); return r; }
DI float max2f(float a, float b) { float r; asm("v_max_f32_e32 %0, %1, %2" : "=v"(r) : "v"(a), "v"(b)); return r; }
DI float fadd_s(float a, float b) { float r; asm("v_add_f32_e32 %0, %1, %2" : "=v"(r) : "v"(a), "v"(b)); return r; }
DI float xhalf_sum(float x) {
  auto r = __builtin_amdgcn_permlane32_swap(__float_as_uint(x), __float_as_uint(x), false, false);
  return __uint_as_float(r[0]) + __uint_as_float(r[1]);
}
DI float wave_sum(float x) {
#pragma unroll
  for (int o = 1; o < 64; o <<= 1) x += __shfl_xor(x, o);
  return x;
}

#define STAMP(i)

struct Ctx {
  unsigned ldsb, w, lane, b;
  unsigned ep0, ep1;
  rsrc_t rws;
  u64 pc0;
};

DI void fresh(Ctx& c) {
  asm volatile("v_mbcnt_lo_u32_b32 %0, -1, 0\n\tv_mbcnt_hi_u32_b32 %0, -1, %0" : "=v"(c.lane));
  asm volatile("" : "+s"(c.w), "+s"(c.b), "+s"(c.ldsb));
}
DI void code_touch(Ctx& c, unsigned& dummy) {
  unsigned bytes;
  asm volatile("s_mov_b32 %0, .Lfb_end-.Lfb_begin" : "=s"(bytes));
  const rsrc_t r = mk_rsrc((const void*)c.pc0, bytes);
  asm volatile("s_nop 4\n\tbuffer_load_dword %0, %1, %2, 0 offen" : "+v"(dummy) : "v"(c.w == 0 ? (c.lane * 32 + (c.b >> 3)) * 128 : OOB), "s"(r) : "memory");
}
DI void publish(Ctx& c, unsigned flag_off) {
  wait_vm<0>();
  bar();
  fput(u32x2{c.ep0, c.ep1}, c.rws, (c.w == 0 && c.lane == 0) ? flag_off : OOB);
}
DI void poll_flags(Ctx& c, unsigned flag_off, unsigned n, unsigned stride) {
  const unsigned voff = c.lane < n ? flag_off + c.lane * 8 * stride : OOB;
  const u64 t0 = __builtin_amdgcn_s_memrealtime();
  for (;;) {
    u32x2 f;
    fget(f, c.rws, voff);
    asm volatile("s_waitcnt vmcnt(0)" : "+v"(f)::"memory");
    if (__all((f[0] == c.ep0 && f[1] == c.ep1) || c.lane >= n)) break;
    if (__builtin_amdgcn_s_memrealtime() - t0 > 5000000ull) break;
    __builtin_amdgcn_s_sleep(2);
  }
}
DI void wait_flags(Ctx& c, unsigned flag_off, unsigned n, unsigned stride) {
  if (c.w == 0) poll_flags(c, flag_off, n, stride);
  bar();
}

#define DBG_DRAIN()
#define DBG_NX(x)
#define STAMPG(i)
#define OAUX 0
#define OAUX2 OAUX
DI void flag_store(Ctx& c, unsigned flag_off) {
  fput(u32x2{c.ep0, c.ep1}, c.rws, (c.w == 0 && c.lane == 0) ? flag_off : OOB);
}
DI void flag_load(Ctx& c, u32x2& f, unsigned flag_off, unsigned n) { fget(f, c.rws, c.lane < n ? flag_off + c.lane * 8 : OOB); }
DI void flag_check(Ctx& c, u32x2& f, unsigned flag_off, unsigned n) {
  PIN(f);
  if (__builtin_expect(!__all((f[0] == c.ep0 && f[1] == c.ep1) || c.lane >= n), 0)) poll_flags(c, flag_off, n, 1);
}

constexpr float QSCALE = 0.08838834764831845f * 1.4426950408889634f;
constexpr unsigned LE = 131072;
constexpr unsigned LX = 147456;
constexpr unsigned KW = LX + 12288;
#define ROT 0
constexpr unsigned SLOT = 49152;
constexpr int slot_of(int S) { return (S + 2) % 3; }
constexpr int XQ = 6;
constexpr int LAQ = 6;
constexpr int QB1 = 40, QB2 = 88, QB3 = 116;
constexpr int Q_KPOLL = 70, Q_VPOLL = 134, Q_VST = 12;
#define PRO 0
#define XLS 1
constexpr int XLS1 = XLS > 0 ? XLS : 1, XL0 = 31 - 5 * XLS1, XFL = XLS == 0 ? 19 : ((XL0 - 8) | 1) - ((XL0 & 1) ? 0 : 2);
#define KSP 2
#define TWV 5
#define TWQ 13
#define XT 1
#define LNE 1
#define PRO_N 12
#define PRO_B 5
constexpr int PRO_U[18] = {0, 0, 0, 0, 0, 0, 1, 1, 1, 1, 1, 1, 2, 2, 2, 2, 2, 2}, PRO_I[18] = {0, 4, 1, 2, 5, 3, 0, 4, 1, 2, 5, 3, 0, 1, 2, 3, 4, 5}, PRO_X = LNE ? LNE - 1 : 5;
#define WQL 1
#define WQ2 41
constexpr int wq_piece(int q) { return q >= 1 && q < 25 && (q - 1) % 4 == 0 ? (q - 1) / 4 : q >= WQ2 && q < WQ2 + 24 && (q - WQ2) % 4 == 0 ? 6 + (q - WQ2) / 4 : -1; }
constexpr int vm_wait(int pos, int tag) {
  int seq[512] = {}; int n = 0;
  auto push = [&](int t, int cnt) { for (int i = 0; i < cnt; ++i) seq[n++] = t; };
  for (int i = 0; i < PRO_N; ++i) { push(PRO_U[i], 1); if (i == PRO_X) push(-1, 3); }
  if (PRO_N == 18) push(-12, 1);
  bool done = false;
  for (int T = 0; T < 32 && !done; ++T) {
    if (pos == T) { done = true; break; }
    if (T == 0 && PRO_N < 18) { for (int i = PRO_N; i < 18; ++i) push(PRO_U[i], 1); push(-12, 1); }
    if (T == 3) push(-2, 3);
    if (T == TWV) push(-6, 1);
    if (T == TWQ) push(-6, 1);
    if (T == 7) push(-3, 1);
    if (T == XFL) push(-5, 1);
    if (KSP == 1 && T == 27) push(-8, 1);
    if (T >= 16 && (T - 16) % KSP == 0 && (T - 16) / KSP < 8) push(-7, 1);
    if (T >= 1 && (!WQL || T < 29)) push((T - 1) / 2 + 3, 3);
    if (XLS == 0 && T >= 29) push(200 + 2 * (T - 29), 2), push(201 + 2 * (T - 29), 2);
    if (XLS > 0 && T >= XL0 && (T - XL0) % XLS1 == 0) push(200 + (T - XL0) / XLS1, 2);
  }
  for (int q = 0; q < 144 && !done; ++q) {
    if (pos == 100 + q) break;
    if (q == QB3) push(-11, 1);
    if (KSP == 2 && q == QB1) push(-8, 1);
    if (!WQL && (q == 1 || q == 4 || q == 6)) push(18, 1);
    if (WQL && wq_piece(q) >= 0) push(17 + wq_piece(q) / 6, 1);
    if (tag == -9 && q == Q_KPOLL) push(-9, 1);
    if (q % Q_VST == 2 && q / Q_VST < 8) push(-10, 1);
    if ((q & 7) == 7 && (q >> 3) + XQ < 18) push(200 + (q >> 3) + XQ, 2);
  }
  int last = -1;
  for (int i = 0; i < n; ++i) if (seq[i] == tag) last = i;
  return n - 1 - last;
}
constexpr int imin(int a, int b) { return a < b ? a : b; }
DI unsigned lds_add_rtn(unsigned addr) {
  unsigned r;
  asm volatile("s_mov_b64 exec, 1\n\tds_add_rtn_u32 %0, %1, %2\n\ts_mov_b64 exec, -1\n\ts_waitcnt lgkmcnt(0)" : "=&v"(r) : "v"(addr), "v"(1u) : "memory");
  return rfl(r);
}
DI unsigned lds_word(unsigned addr) { return __hip_atomic_load((LAS unsigned*)(size_t)addr, __ATOMIC_RELAXED, __HIP_MEMORY_SCOPE_WORKGROUP); }
DI void lds_set(unsigned addr, unsigned v) { __hip_atomic_store((LAS unsigned*)(size_t)addr, v, __ATOMIC_RELAXED, __HIP_MEMORY_SCOPE_WORKGROUP); }
DI float wave_sum64(float x) {
  x += __uint_as_float(__builtin_amdgcn_update_dpp(0, __float_as_uint(x), 0xB1, 0xf, 0xf, false));
  x += __uint_as_float(__builtin_amdgcn_update_dpp(0, __float_as_uint(x), 0x4E, 0xf, 0xf, false));
  x += __uint_as_float(__builtin_amdgcn_update_dpp(0, __float_as_uint(x), 0x124, 0xf, 0xf, false));
  x += __uint_as_float(__builtin_amdgcn_update_dpp(0, __float_as_uint(x), 0x128, 0xf, 0xf, false));
  { auto r = __builtin_amdgcn_permlane16_swap(__float_as_uint(x), __float_as_uint(x), false, false); x = __uint_as_float(r[0]) + __uint_as_float(r[1]); }
  return xhalf_sum(x);
}
DI void ln1_row(Ctx& c, const u32x4& xr0, const u32x4& xr1) {
  const unsigned lane = c.lane;
  float v[16];
#pragma unroll
  for (int e = 0; e < 4; ++e) { v[2 * e] = bflo(xr0[e]); v[2 * e + 1] = bfhi(xr0[e]); v[8 + 2 * e] = bflo(xr1[e]); v[9 + 2 * e] = bfhi(xr1[e]); }
  float s = 0.f;
#pragma unroll
  for (int e = 0; e < 16; ++e) s += v[e];
  const float mean = wave_sum64(s) * (1.f / DM);
  float qq = 0.f;
#pragma unroll
  for (int e = 0; e < 16; ++e) { v[e] -= mean; qq += v[e] * v[e]; }
  qq = lane < 36 ? qq : 0.f;
  const float rs = rsqrtf(wave_sum64(qq) * (1.f / DM) + 1e-5f);
  u32x4 o0, o1;
#pragma unroll
  for (int e = 0; e < 4; ++e) { o0[e] = cvtpk(v[2 * e] * rs, v[2 * e + 1] * rs); o1[e] = cvtpk(v[8 + 2 * e] * rs, v[9 + 2 * e] * rs); }
  const unsigned ad = c.ldsb + LX + (lane < 36 ? (2 * lane * 8 + c.w) * 16 : 9216 + (lane - 36) * 16);
  *(LAS u32x4*)(size_t)ad = o0;
  *(LAS u32x4*)(size_t)(ad + 128) = o1;
}
DI void xn_store(Ctx& c) {
  const unsigned w = c.w, lane = c.lane, mb = c.b >> 2, r0 = (c.b & 3) * 8;
  const unsigned c0 = 9 * w + (lane >> 3), c1 = 9 * w + 8;
  const u32x4 a0 = *(LAS u32x4*)(size_t)(c.ldsb + LX + (9 * w * 8 + lane) * 16);
  const u32x4 a1 = *(LAS u32x4*)(size_t)(c.ldsb + LX + (c1 * 8 + (lane & 7)) * 16);
  gst16c(a0, c.rws, ((mb * 36 + (c0 >> 1)) * 64 + (c0 & 1) * 32 + r0 + (lane & 7)) * 16, WS_XN);
  gst16c(a1, c.rws, lane < 8 ? ((mb * 36 + (c1 >> 1)) * 64 + (c1 & 1) * 32 + r0 + lane) * 16 : OOB, WS_XN);
}

template <int WH> DI void w_touch(Ctx& c, unsigned& dummy, const void* wo, const void* w1, const void* w2, unsigned wv, bool two) {
  unsigned lane;
  asm volatile("v_mbcnt_lo_u32_b32 %0, -1, 0\n\tv_mbcnt_hi_u32_b32 %0, -1, %0" : "=v"(lane));
  const unsigned xcd = c.b & 7, jj = (c.b >> 3) - ((lane >> 5) << 2), l = two ? lane & 31 : lane, i = 3 * wv + l - 18;
  const bool mn = l < 18, ex = l < 21 && i < 18;
  auto ld = [&](const void* p, unsigned bytes, unsigned off) AI {
    asm volatile("s_nop 4\n\tbuffer_load_dword %0, %1, %2, 0 offen" : "+v"(dummy) : "v"(off), "s"(mk_rsrc(p, bytes)) : "memory");
  };
  const unsigned o1 = xcd * 589824 + (jj * 144 + wv * 18 + l) * 128;
  if constexpr (WH & 1) ld(wo, NQ * DM * 2, mn ? o1 : OOB);
  if constexpr (WH & 2) ld(w1, FF * DM * 2, mn ? (jj * 18 + l) * (FF * 2) + xcd * 1024 + wv * 128 : ex ? (jj * 18 + i) * (FF * 2) + 8192 + xcd * 128 : OOB);
  if constexpr (WH & 4) ld(w2, FF * DM * 2, mn ? o1 : ex ? (4096 + 64 * xcd) * (DM * 2) + (jj * 18 + i) * 128 : OOB);
}
struct QOut { u32x4 q[8]; bool kdone; };
DI u32x4 pack_pair(const f32x4& r0, const f32x4& r1) { return u32x4{cvtpk(r0[0], r0[1]), cvtpk(r0[2], r0[3]), cvtpk(r1[0], r1[1]), cvtpk(r1[2], r1[3])}; }
template <int IMM> DI void gld16c2(u32x4& d0, u32x4& d1, unsigned voff, rsrc_t r, unsigned soff) {
  asm volatile("s_nop 4\n\tbuffer_load_dwordx4 %0, %2, %3, %4 offen offset:%5 sc1\n\tbuffer_load_dwordx4 %1, %2, %3, %4 offen offset:%6 sc1"
               : "=&v"(d0), "=&v"(d1) : "v"(voff), "s"(r), "s"(soff), "i"(IMM), "i"(IMM + 256) : "memory");
}
DI unsigned x_voff(Ctx& c) { const unsigned g = c.lane >> 4; return (g >> 1) * 1024 + ((g & 1) * 32 + (c.lane & 15)) * 16; }

DI void phase_qkv(Ctx& c, QOut& qo, const u16* x, const u16* ckv, const u16* wq, const u16* wuk, const u16* wuv) {
  const unsigned w = c.w, lane = c.lane, ldsb = c.ldsb, b = c.b;
  const unsigned xcd = b & 7, jj = b >> 3, head = xcd * 4 + (jj >> 3), cb = jj & 7;
  const rsrc_t rxx = mk_rsrc(x, SEQ * DM * 2), rx = mk_rsrc(ckv, SEQ * DC * 2);
  const rsrc_t rwk = mk_rsrc(wuk, DC * NQ * 2), rwv = mk_rsrc(wuv, DC * NQ * 2), rwq = mk_rsrc(wq, DM * NQ * 2);
  unsigned dummy = 0;
  code_touch(c, dummy);
  const unsigned voffX0 = (lane >> 3) * 1024 + (((lane & 7) ^ (lane >> 4)) << 4), voffX1 = voffX0 ^ 64;
  const unsigned voffW = (lane >> 3) * 8192 + (((((lane >> 1) & 3) ^ (((lane >> 4) & 1) | ((w & 1) << 1))) << 5) | ((lane & 1) << 4));
  const unsigned sX = (cb * 256 + 32 * w) * 1024, sW = (8 * (w & 3)) * 8192 + (head * 128 + (w >> 2) * 64) * 2;
  const unsigned rot = ROT == 0 ? 2 * (cb & 3) : ROT == 1 ? 2 * ((cb + head) & 3) : ROT == 2 ? 2 * (cb & 3) + 4 * (head & 1) : (cb + 2 * head) & 7;
  const unsigned dX = ldsb + 4 * w * 1024, dW = ldsb + w * 1024;
  auto issue = [&](auto U_, auto P_) AI {
    constexpr int U = decltype(U_)::v, P = decltype(P_)::v;
    constexpr unsigned so = slot_of(U) * SLOT;
    if constexpr (U < 16) {
      const unsigned tr = (U + rot) & 7;
      constexpr int p = P < 2 ? P : P - 1;
      if constexpr (P % 3 != 2) dma16(dX + so + p * 1024, (p & 1) ? voffX1 : voffX0, rx, sX + 128 * tr + (U < 8 ? 8 * p : 16 * (p & 1) + 8 * (p >> 1)) * 1024);
      else dma16(dW + so + 32768 + (P / 3) * 8192, voffW, U < 8 ? rwk : rwv, sW + (64 * tr + 32 * (P / 3)) * 8192);
    } else {
      dma16(dW + so + (U == 18 ? 5 - P : P) * 8192, voffW, rwq, sW + 32 * (6 * (U - 16) + P) * 8192);
    }
  };
  u32x2 ep; u32x4 xr0, xr1;
    const unsigned hr = head & 3, vA = voffX0 ^ ((hr & 1) << 6), vB = vA ^ 64, wj = (cb >> 2) * 8192;
    auto pro = [&](auto U_, auto I_) AI {
      constexpr int U = decltype(U_)::v, I = decltype(I_)::v;
      constexpr unsigned so = slot_of(U) * SLOT;
      const unsigned tr = (U + rot) & 7;
      if constexpr (I < 4) { const unsigned po = ((I + hr) & 3) * 1024; dma16(dX + so + po, (I & 1) ? vB : vA, rx, sX + 128 * tr + 8 * po); }
      else { const unsigned j = wj ^ ((I - 4) * 8192); dma16(dW + so + 32768 + j, voffW, rwk, sW + 64 * tr * 8192 + 32 * j); }
    };
  {
    static_for<PRO_N>([&](auto N_) AI {
      constexpr int N = decltype(N_)::v;
      pro(IC<PRO_U[N]>{}, IC<PRO_I[N]>{});
      if constexpr (N == PRO_B) __builtin_amdgcn_s_barrier();
      if constexpr (N == PRO_X) {
        const unsigned vo = lane < 36 ? lane * 32 : OOB;
        gld8c(ep, F_EPOCH, c.rws, WS_FLAGS);
        gld16<0>(xr0, vo, rxx, (b * 8 + w) * (DM * 2));
        gld16<16>(xr1, vo, rxx, (b * 8 + w) * (DM * 2));
      }
    });
  }
  unsigned td2 = 0;
  auto xtouch = [&]() AI {
    const unsigned l5 = (lane * 52) >> 8, row = cb * 256 + (head & 3) * 64 + w * 8 + l5, ch = (rot + 3 + lane - 5 * l5) & 7;
    asm volatile("s_nop 4\n\tbuffer_load_dword %0, %1, %2, 0 offen" : "+v"(td2) : "v"(XT && lane < 40 ? row * 1024 + ch * 128 : OOB), "s"(rx) : "memory");
  };
  if constexpr (PRO_N == 18) xtouch();
  const unsigned kh = w >> 2, rg = kh ? 7 - w : w;
  const unsigned qmb = (kh ? cb : 15 - cb) * 4 + rg;
  const unsigned wm = w >> 1, wn = w & 1, g = lane >> 4, l15 = lane & 15, q = l15 >> 2, pp = lane & 3;
  unsigned xb[3][2], wb[3][4], wqb[3][4];
  {
    const unsigned sg = ((q >> 1) & 1) | ((g & 1) << 1);
#pragma unroll
    for (int i = 0; i < 3; ++i) {
      xb[i][0] = ldsb + i * SLOT + (wm * 64 + l15) * 128 + ((g ^ (l15 >> 1)) << 4); xb[i][1] = xb[i][0] ^ 64; PIN(xb[i][0]); PIN(xb[i][1]);
#pragma unroll
      for (int j = 0; j < 4; ++j) { wb[i][j] = ldsb + i * SLOT + wn * 4096 + (8 * g + q) * 128 + ((j ^ sg) << 5) + pp * 8; PIN(wb[i][j]); }
    }
  }
  if constexpr (LNE != 0) { wait_vm<vm_wait(0, -1)>(); PIN(xr0); PIN(xr1); ln1_row(c, xr0, xr1); }
  wait_vm<vm_wait(0, 0)>();
  asm volatile("" ::"v"(dummy));
  bar();
  STAMP(2);
  f32x4 acc[4][4];
  bf16x8 xf[2][4]; bf16x4 wl[2][4][2];
  u32x4 pk[8], pv[8], xq[XQ][2]; u32x2 xnf;
  auto rd = [&](auto T_, auto R_) AI {
    constexpr int T = decltype(T_)::v, R = decltype(R_)::v, B = T & 1, sl = slot_of(T >> 1);
    constexpr int K = R % 3, I = R / 3;
    if constexpr (K == 0) xf[B][I] = lds_b128(xb[sl][B] + I * 2048);
    else wl[B][I][K - 1] = lds_tr(wb[sl][I] + 32768 + B * 8192 + (K - 1) * 512);
  };
  bf16x4 wq8[8][2];
  auto rdq = [&](auto N_, auto H_) AI {
    constexpr int N = decltype(N_)::v, H = decltype(H_)::v, v = N >> 3, a = N & 7, sl = slot_of(16 + v / 6);
    wq8[N & 7][H] = lds_tr(wqb[sl][a & 3] + (v >= 12 ? 17 - v : v % 6) * 8192 + (a >> 2) * 4096 + H * 512);
  };
  const unsigned xvo = x_voff(c), xso = WS_XN + qmb * 36 * 1024;
  auto ldx = [&](auto V_) AI { constexpr int v = decltype(V_)::v; gld16c2<(v & 1) * 2048>(xq[v % XQ][0], xq[v % XQ][1], xvo, c.rws, xso + (v >> 1) * 4096); };
  const unsigned kst = WS_K + ((head * 64 + cb * 8 + wm * 2) * 8 + 4 * wn) * 1024, vst = WS_V + (((head * 32 + cb * 4 + wm) * 4 + 2 * wn) * 4) * 1024;
  static_for<12>([&](auto R) AI { rd(IC<0>{}, R); });
  SB();
  static_for<32>([&](auto T_) AI {
    constexpr int T = decltype(T_)::v, B = T & 1;
    if constexpr (B) { wait_vm<vm_wait(T, (T >> 1) + 1)>(); bar(); }
    if constexpr (T == 1) {
      PIN(ep);
      if constexpr (!LNE) { PIN(xr0); PIN(xr1); }
      u64 z = (((u64)rfl(ep[1]) << 32) | rfl(ep[0])) + 0x9E3779B97F4A7C15ull;
      z = (z ^ (z >> 30)) * 0xBF58476D1CE4E5B9ull; z = (z ^ (z >> 27)) * 0x94D049BB133111EBull; z ^= z >> 31;
      c.ep0 = (unsigned)z; c.ep1 = (unsigned)(z >> 32);
      if (w == 0) lds_set(ldsb + KW + (lane & 3) * 4, 0);
    }
    if constexpr (!LNE && (T == 1 || T == 2)) { if (kh == T - 1) ln1_row(c, xr0, xr1); }
    if constexpr (T == 3) { fput(u32x2{c.ep0, c.ep1}, c.rws, (w == 0 && lane < 5) ? WS_CNT + (b * 5 + lane) * 128 : OOB); xn_store(c); }
    if constexpr (T == TWV) asm volatile("s_nop 4\n\tbuffer_load_dword %0, %1, %2, 0 offen" : "+v"(td2) : "v"(lane < 16 ? (cb * 64 + w * 8 + (lane >> 1)) * 8192 + head * 256 + (lane & 1) * 128 : OOB), "s"(rwv) : "memory");
    if constexpr (T == TWQ) asm volatile("s_nop 4\n\tbuffer_load_dword %0, %1, %2, 0 offen" : "+v"(td2) : "v"(lane < 18 ? (cb * 72 + w * 9 + (lane >> 1)) * 8192 + head * 256 + (lane & 1) * 128 : OOB), "s"(rwq) : "memory");
    if constexpr (T == 31) asm volatile("" ::"v"(td2));
    if constexpr (T == 7) flag_store(c, F_XN + b * 8);
    if constexpr (T == XFL) flag_load(c, xnf, F_XN + qmb * 32, 4);
    if constexpr (T == XFL + 8) flag_check(c, xnf, F_XN + qmb * 32, 4);
    if constexpr (T == 27) { if constexpr (KSP == 1) flag_store(c, F_K + (head * 8 + cb) * 8); }
    if constexpr (T == 31) {
#pragma unroll
      for (int i = 0; i < 3; ++i)
#pragma unroll
        for (int j = 0; j < 4; ++j) { wqb[i][j] = wb[i][j] - wn * 4096; PIN(wqb[i][j]); }
    }
    if constexpr (T == 8 || T == 16 || T == 24) STAMP(100 + T / 2);
    SB();
    static_for<16>([&](auto M_) AI {
      constexpr int M = decltype(M_)::v, i = M >> 2, j = M & 3;
      const bf16x8 wf = cat(wl[B][j][0], wl[B][j][1]);
      const f32x4 cin = (T & 15) == 0 ? f32x4{} : acc[i][j];
      acc[i][j] = T < 16 ? MFMA16(wf, xf[B][i], cin) : MFMA16(xf[B][i], wf, cin);
      SB();
      if constexpr (M < 12) {
        if constexpr (T < 31) rd(IC<T + 1>{}, IC<M>{});
        else rdq(IC<(M >> 1)>{}, IC<(M & 1)>{});
      }
      if constexpr (M == 1 && T >= 16 && (T - 16) % KSP == 0 && (T - 16) / KSP < 8) {
        constexpr int e = (T - 16) / KSP;
        gst16c(pk[e], c.rws, xvo, kst + (e >> 2) * 8192 + (e & 1) * 2048 + ((e >> 1) & 1) * 256);
      }
      if constexpr (T >= 1 && (!WQL || T < 29) && (M == 4 || M == 8 || M == 12)) issue(IC<(T - 1) / 2 + 3>{}, IC<(B ? 0 : 3) + (M >> 2) - 1>{});
      if constexpr (T == 0 && (M & 1) && PRO_N + (M >> 1) < 18) pro(IC<PRO_U[PRO_N + (M >> 1)]>{}, IC<PRO_I[PRO_N + (M >> 1)]>{});
      if constexpr (T == 0 && M == 15 && PRO_N < 18) xtouch();
      if constexpr (XLS == 0 && T >= 29 && (M == 13 || M == 15)) ldx(IC<2 * (T - 29) + (M == 15)>{});
      if constexpr (XLS > 0 && T >= XL0 && (T - XL0) % XLS1 == 0 && M == 14) ldx(IC<(T - XL0) / XLS1>{});
      SB();
    });
    if constexpr (T == 15) {
#pragma unroll
      for (int e = 0; e < 8; ++e) { pk[e] = pack_pair(acc[e >> 1][2 * (e & 1)], acc[e >> 1][2 * (e & 1) + 1]); PIN(pk[e]); }
    }
    if constexpr (T == 31) {
#pragma unroll
      for (int e = 0; e < 8; ++e) { pv[e] = pack_pair(acc[2 * (e >> 2)][e & 3], acc[2 * (e >> 2) + 1][e & 3]); PIN(pv[e]); }
    }
  });
  STAMP(3);
  f32x4 qa[2][8];
  u32x2 kf; bool kdone = false;
  const unsigned kso = WS_K + head * (SEQ * HD * 2) + w * 4096;
  static_for<144>([&](auto N_) AI {
    constexpr int N = decltype(N_)::v, v = N >> 3, a = N & 7;
    if constexpr (a == 0) { wait_vm<vm_wait(100 + N, 200 + v)>(); PIN(xq[v % XQ][0]); PIN(xq[v % XQ][1]); }
    if constexpr (N == QB1) { wait_vm<vm_wait(100 + N, 17)>(); bar(); if constexpr (KSP == 2) flag_store(c, F_K + (head * 8 + cb) * 8); SB(); }
    if constexpr (N == QB2) {
      wait_vm<vm_wait(100 + N, 18)>();
      if (w == 0) wait_vm<vm_wait(100 + N, -9)>();
      PIN(kf);
      if (w == 0 && __all((kf[0] == c.ep0 && kf[1] == c.ep1) || lane >= 8)) lds_set(ldsb + KW + 8, 1);
      bar();
      kdone = rfl(lds_word(ldsb + KW + 8)) != 0;
      SB();
    }
    if constexpr (N == QB3) { wait_vm<vm_wait(100 + N, -10)>(); bar(); flag_store(c, F_V + (head * 8 + cb) * 8); SB(); }
    const bf16x8 wf = cat(wq8[N & 7][0], wq8[N & 7][1]);
    qa[0][a] = MFMA16(wf, asbf(xq[v % XQ][0]), v == 0 ? f32x4{} : qa[0][a]);
    SB();
    if constexpr (N + LAQ < 144) rdq(IC<N + LAQ>{}, IC<0>{});
    SB();
    qa[1][a] = MFMA16(wf, asbf(xq[v % XQ][1]), v == 0 ? f32x4{} : qa[1][a]);
    SB();
    if constexpr (N + LAQ < 144) rdq(IC<N + LAQ>{}, IC<1>{});
    if constexpr (!WQL && (N == 1 || N == 4 || N == 6)) issue(IC<18>{}, IC<(N == 1 ? 3 : N == 4 ? 4 : 5)>{});
    if constexpr (WQL && wq_piece(N) >= 0) issue(IC<17 + wq_piece(N) / 6>{}, IC<wq_piece(N) % 6>{});
    if constexpr (N == Q_KPOLL || N == Q_VPOLL) {
      kf = u32x2{0u, 0u};
      asm volatile("s_mov_b32 exec_lo, %3\n\ts_mov_b32 exec_hi, 0\n\ts_nop 4\n\tbuffer_atomic_add_x2 %0, %1, %2, 0 offen sc0 sc1\n\ts_mov_b64 exec, -1"
                   : "+v"(kf) : "v"((N == Q_KPOLL ? F_K : F_V) + (head * 8 + lane) * 8), "s"(c.rws), "s"(w == 0 ? 0xffu : 0u) : "memory");
    }
    if constexpr (N % Q_VST == 2 && N / Q_VST < 8) {
      constexpr int e = N / Q_VST;
      gst16c(pv[e], c.rws, xvo, vst + ((e >> 1) & 1) * 4096 + (e >> 2) * 2048 + (e & 1) * 256);
    }
    if constexpr (a == 7 && v + XQ < 18) ldx(IC<v + XQ>{});
    if constexpr (N > QB3 && N <= QB3 + 16 && (N - QB3) % 4 == 0) {
      constexpr int p = (N - QB3) / 4 - 1;
      if (kdone) dma16c(ldsb + kh * 16384 + w * 4096 + p * 1024, lane * 16, c.rws, kso + p * 1024);
    }
    SB();
  });
#pragma unroll
  for (int A = 0; A < 4; ++A) {
    u32x4 p0, p1;
#pragma unroll
    for (int e = 0; e < 4; ++e) {
      const int t = 2 * A + (e >> 1), k = 2 * (e & 1);
      const unsigned u0 = cvtpk(qa[0][t][k] * QSCALE, qa[0][t][k + 1] * QSCALE), u1 = cvtpk(qa[1][t][k] * QSCALE, qa[1][t][k + 1] * QSCALE);
      const auto s = __builtin_amdgcn_permlane16_swap(u0, u1, false, false);
      const auto r = __builtin_amdgcn_permlane32_swap(s[0], s[1], false, false);
      p0[e] = r[0]; p1[e] = r[1];
    }
    qo.q[2 * A] = p0; qo.q[2 * A + 1] = p1;
  }
  qo.kdone = kdone;
  STAMP(6);
  if (w == 0) wait_vm<0>();
  flag_check(c, kf, F_V + head * 64, w == 0 ? 8 : 0);
}

constexpr float THR = 32.f;

struct AttnState {
  bf16x8 qf[8];
  f32x16 o[4];
  f32x16 S[2][2];
  f32x16 negm;
  u32x4 pk[4];
  bf16x8 fr[8];
  float mhat, l0, l1, fct, mx0, mx1, t0, t1;
  bool pend;
  unsigned kb[2], vb[2];
  unsigned voff, ldsb, w, kbase, vbase, n1, n1k;
  u64 ta, tm; unsigned dummy;
};

constexpr int RD0[16] = {4, 5, 6, 7, 9, 10, 12, 13, 15, -1, -1, -1, -1, -1, -1, -1};
constexpr int RD1[16] = {-1, -1, -1, 8, -1, 11, -1, 14, -1, -1, -1, -1, -1, -1, -1, -1};

template <int CR, int R> DI void mask4(f32x16& s, int dq, float ninf) {
  float x0 = s[R], x1 = s[R + 1], x2 = s[R + 2], x3 = s[R + 3];
  u64 m0, m1, m2;
  asm volatile("v_cmp_le_i32_e64 %4, %9, %7\n\tv_cmp_le_i32_e64 %5, %10, %7\n\tv_cmp_le_i32_e64 %6, %11, %7\n\tv_cmp_le_i32_e32 vcc, %12, %7\n\t"
               "v_cndmask_b32_e64 %0, %8, %0, %4\n\tv_cndmask_b32_e64 %1, %8, %1, %5\n\tv_cndmask_b32_e64 %2, %8, %2, %6\n\tv_cndmask_b32_e32 %3, %8, %3, vcc"
               : "+v"(x0), "+v"(x1), "+v"(x2), "+v"(x3), "=&s"(m0), "=&s"(m1), "=&s"(m2) : "v"(dq), "v"(ninf), "i"(CR), "i"(CR + 1), "i"(CR + 2), "i"(CR + 3) : "vcc");
  s[R] = x0; s[R + 1] = x1; s[R + 2] = x2; s[R + 3] = x3;
}
template <int B, int... Q> DI void mask_rows(f32x16& s, int dq, float ninf, iseq<Q...>) { (mask4<B + 8 * Q, 4 * Q>(s, dq, ninf), ...); }
struct NoHook { template <class T> DI void operator()(T) const {} };
template <int X, bool QK, bool PV, bool VDMA = true, class Hook = NoHook>
DI void attn_iter(Ctx& c, AttnState& a, const unsigned s, const bool first, const bool diag, const int dq, Hook&& hook = Hook{}) {
  constexpr int Y = X ^ 1;
  auto kaddr = [&](int slot, int n) AI { return a.kb[slot] + ((n & 1) * 8 + (n >> 1)) * 1024; };
  auto vaddr = [&](int slot, int n) AI { return a.vb[slot] + ((n & 3) * 4 + (n >> 2)) * 1024; };
  const unsigned sk = s + 2;
  const unsigned sok = a.kbase + (sk + (sk > a.n1 ? sk - a.n1 : 0)) * 16384, sov = a.vbase + (s + (s > a.n1 ? s - a.n1 : 0)) * 16384;
  const unsigned mk = (sk < a.n1k || sk > 16) ? 0xFFFFFC00u : 0u, mv = s < a.n1k ? 0xFFFFFC00u : 0u;
  unsigned vo = 0;
  const unsigned dk = a.ldsb + X * 32768 + a.w * 4096, dv = dk + 65536;
  static_for<16>([&](auto M_) AI {
    constexpr int M = decltype(M_)::v, d = M >> 1, kb = M & 1;
    if constexpr (QK) {
      if constexpr (d == 0) a.S[X][kb] = MFMA32(a.fr[M & 7], a.qf[d], a.negm);
      else a.S[X][kb] = MFMA32(a.fr[M & 7], a.qf[d], a.S[X][kb]);
      if constexpr (RD0[M] >= 0) a.fr[RD0[M] & 7] = lds_b128(kaddr(X, RD0[M]));
      if constexpr (RD1[M] >= 0) a.fr[RD1[M] & 7] = lds_b128(kaddr(X, RD1[M]));
      if constexpr (M == 0) vo = a.voff | mv;
      if constexpr (VDMA && (M == 0 || M == 2 || M == 6 || M == 9)) { constexpr int P = M == 0 ? 0 : M == 2 ? 1 : M == 6 ? 2 : 3; dma16c(dv + P * 1024, vo, c.rws, sov + P * 1024); }
    }
    if constexpr (PV) {
      if constexpr (M < 8) {
        a.S[Y][1][2 * M] = __builtin_amdgcn_exp2f(a.S[Y][1][2 * M]);
        a.S[Y][1][2 * M + 1] = __builtin_amdgcn_exp2f(a.S[Y][1][2 * M + 1]);
        a.pk[M >> 2][M & 3] = cvtpk(a.S[Y][0][2 * M], a.S[Y][0][2 * M + 1]);
        PIN(a.pk[M >> 2][M & 3]);
        if constexpr (M == 0) { a.l0 = fadd_s(a.l0, a.S[Y][0][14]); a.l1 = fadd_s(a.l1, a.S[Y][0][15]); }
      } else {
        constexpr int e = M - 8;
        a.pk[2 + (e >> 2)][e & 3] = cvtpk(a.S[Y][1][2 * e], a.S[Y][1][2 * e + 1]);
        PIN(a.pk[2 + (e >> 2)][e & 3]);
        a.l0 = fadd_s(a.l0, a.S[Y][1][2 * e]); a.l1 = fadd_s(a.l1, a.S[Y][1][2 * e + 1]);
      }
    }
    if constexpr (M == 11) { SB(); if constexpr (QK) wait_vm<9>(); else wait_vm<0>(); hook(IC<0>{}); bar(); hook(IC<1>{}); SB(); }
    if constexpr (PV && M >= 12) a.fr[(M - 12) & 7] = lds_b128(vaddr(Y, M - 12));
    SB();
  });
  static_for<16>([&](auto M_) AI {
    constexpr int M = decltype(M_)::v, ks = M >> 2, db = M & 3;
    if constexpr (PV) {
      a.o[db] = MFMA32(a.fr[M & 7], asbf(a.pk[ks]), a.o[db]);
      if constexpr (RD0[M] >= 0) a.fr[RD0[M] & 7] = lds_b128(vaddr(Y, RD0[M]));
      if constexpr (RD1[M] >= 0) a.fr[RD1[M] & 7] = lds_b128(vaddr(Y, RD1[M]));
    }
    if constexpr (QK) {
      if constexpr (M == 0) vo = a.voff | mk;
      if constexpr (M == 0 || M == 1 || M == 6 || M == 9) { constexpr int P = M == 0 ? 0 : M == 1 ? 1 : M == 6 ? 2 : 3; dma16c(dk + P * 1024, vo, c.rws, sok + P * 1024); }
      if constexpr (M == 2) {
        asm volatile("s_nop 7\n\ts_nop 3" : "+v"(a.S[X][0]), "+v"(a.S[X][1]));
        if (__builtin_expect(diag, 0)) {
          float ninf;
          asm volatile("v_mov_b32 %0, 0xff800000" : "=v"(ninf));
          const int dq2 = dq + (s + 2 == a.n1k ? 64 : 0);
          mask_rows<0>(a.S[X][0], dq2, ninf, make_iseq<4>{}); mask_rows<32>(a.S[X][1], dq2, ninf, make_iseq<4>{});
        }
      }
      if constexpr (M >= 2 && M < 6) {
        constexpr int r = 4 * (M - 2);
        if constexpr (M == 2) {
          a.mx0 = max3f(a.S[X][0][0], a.S[X][0][1], a.S[X][1][0]); a.mx1 = max3f(a.S[X][0][2], a.S[X][0][3], a.S[X][1][1]);
          a.mx0 = max3f(a.mx0, a.S[X][1][2], a.S[X][1][3]);
        } else {
          a.mx0 = max3f(a.mx0, a.S[X][0][r], a.S[X][0][r + 1]); a.mx1 = max3f(a.mx1, a.S[X][0][r + 2], a.S[X][0][r + 3]);
          a.mx0 = max3f(a.mx0, a.S[X][1][r], a.S[X][1][r + 1]); a.mx1 = max3f(a.mx1, a.S[X][1][r + 2], a.S[X][1][r + 3]);
        }
      }
      if constexpr (M == 6) {
        const float mm = max2f(a.mx0, a.mx1);
        auto pr = __builtin_amdgcn_permlane32_swap(__float_as_uint(mm), __float_as_uint(mm), false, false);
        a.mx0 = max2f(__uint_as_float(pr[0]), __uint_as_float(pr[1]));
      }
      if constexpr (M == 7) {
        if (__builtin_expect(first || __any(a.mx0 > THR), 0)) {
          const float dl = first ? fmaxf(a.mx0, -1e30f) : fmaxf(a.mx0, 0.f);
          a.mhat = first ? dl : a.mhat + dl;
          a.fct = __builtin_amdgcn_exp2f(-dl);
#pragma unroll
          for (int r = 0; r < 16; ++r) { a.S[X][0][r] -= dl; a.S[X][1][r] -= dl; a.negm[r] = -a.mhat; }
          a.pend = !first;
        }
      }
      if constexpr (M >= 8) {
        constexpr int e = M - 8;
        a.S[X][0][2 * e] = __builtin_amdgcn_exp2f(a.S[X][0][2 * e]);
        a.S[X][0][2 * e + 1] = __builtin_amdgcn_exp2f(a.S[X][0][2 * e + 1]);
        PIN(a.S[X][0][2 * e]); PIN(a.S[X][0][2 * e + 1]);
        if constexpr (e > 0) {
          a.t0 = e == 1 ? a.S[X][0][0] : fadd_s(a.t0, a.S[X][0][2 * e - 2]); a.t1 = e == 1 ? a.S[X][0][1] : fadd_s(a.t1, a.S[X][0][2 * e - 1]);
          PIN(a.t0); PIN(a.t1);
        }
      }
      if constexpr (M == 11) { SB(); wait_vm<PV ? 9 : 8>(); bar(); }
      if constexpr (M == 13) { asm volatile("s_mov_b64 exec, %2\n\tglobal_load_dword %0, %1, off\n\ts_mov_b64 exec, -1" : "+v"(a.dummy) : "v"(a.ta), "s"(a.tm) : "memory"); a.tm <<= 4; }
      if constexpr (M >= 12) a.fr[(M - 12) & 7] = lds_b128(kaddr(Y, M - 12));
    }
    SB();
  });
}

constexpr unsigned WP = 288;
constexpr unsigned LSYNC = 161280;
constexpr unsigned C_LAND = 0, C_FREE = 64, C_GB = 128, C_CTXD = 136, C_MRG = 140, C_TICK = 144, LSTAT = LSYNC + 256;
#define ARRV 2
constexpr int XM = 12;
constexpr int XDEP(int NK) { return NK == 16 ? XM : 8; }
DI unsigned land(unsigned ph, unsigned q) { return LSYNC + C_LAND + (ph * 5 + q) * 4; }
DI unsigned fre(unsigned ph, unsigned q) { return LSYNC + C_FREE + (ph * 5 + q) * 4; }
DI void cnt_add(Ctx& c, unsigned off) {
  asm volatile("s_mov_b64 exec, 1\n\tds_add_u32 %0, %1\n\ts_mov_b64 exec, -1" ::"v"(c.ldsb + off), "v"(1u) : "memory");
}
#define wlog(...)
DI unsigned cnt_add_rtn(Ctx& c, unsigned off) {
  unsigned r;
  asm volatile("s_mov_b64 exec, 1\n\tds_add_rtn_u32 %0, %1, %2\n\ts_mov_b64 exec, -1\n\ts_waitcnt lgkmcnt(0)" : "=&v"(r) : "v"(c.ldsb + off), "v"(1u) : "memory");
  return rfl(r);
}
DI unsigned cnt_read(Ctx& c, unsigned off) { return __hip_atomic_load((LAS unsigned*)(size_t)(c.ldsb + off), __ATOMIC_RELAXED, __HIP_MEMORY_SCOPE_WORKGROUP); }
DI void prio_lo() { __builtin_amdgcn_s_setprio(0); }
DI void prio_hi(Ctx& c) { __builtin_amdgcn_s_setprio(3); }
DI void prio_gemm(Ctx& c, bool behind) {
  if (c.w >= 4) __builtin_amdgcn_s_setprio(1); else if (behind) __builtin_amdgcn_s_setprio(0); else __builtin_amdgcn_s_setprio(2);
}
DI void cnt_wait(Ctx& c, unsigned off, unsigned target) {
  asm volatile("" ::: "memory");
  unsigned spins = 0;
#pragma unroll 1
  while (rfl(cnt_read(c, off)) < target) { __builtin_amdgcn_s_sleep(1); if (++spins > (1u << 20)) break; }
  asm volatile("" ::: "memory");
}
DI void flag_w(Ctx& c, unsigned flag_off) {
  wait_vm<0>();
  fadd(c.rws, c.lane == 0 ? flag_off : OOB);
}
DI bool cnt_full(Ctx& c, const u32x2& f, unsigned n) { return (((u64)rfl(f[1]) << 32) | rfl(f[0])) == (((u64)c.ep1 << 32) | c.ep0) + n; }
DI void dma16m(unsigned lds_addr, unsigned voff, rsrc_t r, unsigned soff, u64 mask) {
  asm volatile("s_mov_b32 m0, %0\n\ts_mov_b64 exec, %4\n\ts_nop 3\n\tbuffer_load_dwordx4 %1, %2, %3 offen lds\n\ts_mov_b64 exec, -1" ::"s"(lds_addr), "v"(voff), "s"(r), "s"(soff), "s"(mask)
               : "memory", "m0");
}
struct WSrc { unsigned voff[9]; rsrc_t r; unsigned main, tail, pitch, colA, colB; unsigned idx, pend, ph; u32x2 fa, fb; };
DI unsigned w_voff(const WSrc& s, unsigned lane, int p) {
  const unsigned ci = lane + 64 * p, rr = __umul24(ci, 3641) >> 16, cc = ci - __umul24(rr, 18);
  return __umul24(rr, s.pitch - WP) + ci * 16 + (cc < 16 ? s.colA : s.colB - 256);
}
DI unsigned opaque_lane() {
  unsigned lane;
  asm volatile("v_mbcnt_lo_u32_b32 %0, -1, 0\n\tv_mbcnt_hi_u32_b32 %0, -1, %0" : "=v"(lane));
  return lane;
}
template <int NV = 5> DI void wsrc_voff(Ctx& c, WSrc& s) {
  const unsigned lane = opaque_lane();
#pragma unroll
  for (int p = 0; p < NV; ++p) s.voff[p] = w_voff(s, lane, p);
  if constexpr (NV == 5) {
    s.voff[5] = lane >= 16 ? ~0u : 0u;
    const unsigned l7 = (lane & 7) < 5 ? lane & 7 : 0;
    s.voff[6] = c.ldsb + (lane < 8 ? LSYNC + C_TICK + (s.ph * 5 + l7) * 4 : fre(s.ph - 1, 4) - 4 * l7);
    s.voff[7] = lane == 0 ? 3 : 8;
  }
}
template <int NV = 0> DI void wsrc_setup(Ctx& c, WSrc& s, rsrc_t r, unsigned pitch, unsigned colA, unsigned colB, unsigned rowMain, unsigned rowTail, unsigned ph) {
  s.r = r; s.pitch = pitch; s.main = rowMain * pitch; s.tail = rowTail * pitch;
  s.colA = colA; s.colB = colB; s.ph = ph; s.idx = s.pend = 0;
  if constexpr (NV > 0) wsrc_voff<NV>(c, s);
}
DI void w_group(Ctx& c, const WSrc& s, unsigned q) {
  const unsigned r0 = 128 * q + 32 * (c.w & 3);
#pragma unroll
  for (int p = 0; p < 9; ++p) dma16(c.ldsb + r0 * WP + p * 1024, s.voff[p], s.r, s.main + r0 * s.pitch);
}
DI void poll_w(Ctx& c, unsigned off, unsigned n) {
  const unsigned voff = c.lane == 0 ? off : OOB;
  const u64 t0 = __builtin_amdgcn_s_memrealtime();
  prio_lo();
  for (;;) {
    u32x2 f;
    fget(f, c.rws, voff);
    asm volatile("s_waitcnt vmcnt(0)" : "+v"(f)::"memory");
    if (cnt_full(c, f, n)) break;
    if (__builtin_amdgcn_s_memrealtime() - t0 > 5000000ull) break;
  }
  prio_hi(c);
}
constexpr unsigned nitems(unsigned q) { return q == 4 ? 3 : 8; }
DI bool w_try(Ctx& c, WSrc& s) {
  if (s.idx) return false;
  unsigned idx, t;
#pragma unroll 1
  for (;;) {
    const unsigned v = __hip_atomic_load((LAS unsigned*)(size_t)s.voff[6], __ATOMIC_RELAXED, __HIP_MEMORY_SCOPE_WORKGROUP);
    const unsigned m = (unsigned)__builtin_amdgcn_ballot_w64(v < s.voff[7]);
    if ((m & 31) == 0) s.idx = 1;
    const unsigned av = m & ~(m >> 8) & 31;
    if (av == 0) return false;
    idx = __builtin_ctz(av);
    t = cnt_add_rtn(c, LSYNC + C_TICK + (s.ph * 5 + idx) * 4);
    if (t < (idx ? 8u : 3u)) break;
  }
  s.pend += 1u << (4 * idx);
  wlog(c, s.ph, 1, idx, true); wlog(c, s.ph, 2, idx);
  const unsigned lrow = 128 * (4 - idx) + 16 * t, so = (idx ? s.main + lrow * s.pitch : s.tail + 16 * t * s.pitch), sk = lrow == 544 ? 8 * s.pitch : 0u;
  const unsigned la = c.ldsb + lrow * WP;
  dma16(la, s.voff[0], s.r, so);
  dma16(la + 1024, s.voff[1], s.r, so);
  dma16(la + 2048, s.voff[2] + (s.voff[5] & sk), s.r, so);
  dma16(la + 3072, s.voff[3], s.r, so + sk);
  dma16m(la + 4096, s.voff[4], s.r, so + sk, 0xffffffffull);
  return true;
}
DI void w_signal(Ctx& c, const WSrc& s, unsigned items) {
  if (items) {
    unsigned lane;
    asm volatile("v_mbcnt_lo_u32_b32 %0, -1, 0" : "=v"(lane));
    const unsigned n = (items >> (4 * lane)) & 15, ad = c.ldsb + land(s.ph, 4) - 4 * lane;
    asm volatile("s_mov_b64 exec, 31\n\tds_add_u32 %0, %1\n\ts_mov_b64 exec, -1" ::"v"(ad), "v"(n) : "memory");
  }
}
DI void w_flush(Ctx& c, WSrc& s) { w_signal(c, s, s.pend); s.pend = 0; }
#define PSL 8
#define WMAX 2
#define FMAX 0
#define NMAX WMAX
#define LSRV 7
template <unsigned MX = WMAX> DI void w_try_wait(Ctx& c, WSrc& s, bool en = true) {
  const unsigned old = s.pend;
  unsigned k = 0;
  s.pend = 0;
#pragma unroll 1
  while (en && k < MX && w_try(c, s)) ++k;
  if (k == 0) wait_vm<0>(); else if (k == 1) wait_vm<5>(); else if (k == 2) wait_vm<10>(); else if (k == 3) wait_vm<15>(); else wait_vm<20>();
  w_signal(c, s, old);
}
DI void w_need(Ctx& c, WSrc& s, unsigned q, unsigned n) {
  unsigned spins = 0;
  wsrc_voff(c, s);
#pragma unroll 1
  for (;;) {
    w_try_wait<NMAX>(c, s);
    if (s.pend) { wait_vm<0>(); w_flush(c, s); }
    if (rfl(cnt_read(c, land(s.ph, q))) >= n || ++spins > (1u << 20)) break;
    __builtin_amdgcn_s_sleep(1);
  }
  asm volatile("" ::: "memory");
}
DI void flag_srv(Ctx& c, WSrc& s, unsigned flag_off, bool lsrv) {
  w_try_wait<FMAX>(c, s, lsrv || c.w < 4);
  fadd(c.rws, c.lane == 0 ? flag_off : OOB);
}
DI void poll_srv(Ctx& c, WSrc& s, unsigned flag_off, unsigned n, bool lsrv) {
  const unsigned voff = c.lane == 0 ? flag_off : OOB;
  const u64 t0 = __builtin_amdgcn_s_memrealtime();
  prio_lo();
  for (;;) {
    u32x2 f;
    fget(f, c.rws, voff);
    if (s.pend) { wait_vm<1>(); w_flush(c, s); }
    w_try_wait(c, s, lsrv || c.w < 4);
    PIN(f);
    if (cnt_full(c, f, n)) break;
    if (__builtin_amdgcn_s_memrealtime() - t0 > 5000000ull) break;
  }
  prio_hi(c);
}
DI void poll_done(WSrc& s) {}
constexpr unsigned LB1 = LSTAT + 256, LB2 = LB1 + 320;
template <int P> DI void park_load(Ctx& c, const u16* w1, const u16* b1, const u16* w2, const u16* b2) {
  const unsigned xcd = c.b & 7, nt = (c.b >> 3) & 3, nt32 = xcd * 4 + nt, w = (c.w & 3) * 2 + (P & 1);
  const unsigned rr = c.lane >= 18 ? 1 : 0, cc = c.lane - 18 * rr, r0 = (w < 4 ? 40 : 48) + 2 * w;
  const unsigned c1 = cc < 16 ? nt32 * 256 + cc * 16 : 8192 + nt32 * 32 + (cc - 16) * 16, c2 = cc < 16 ? nt * 256 + cc * 16 : 1024 + nt * 32 + (cc - 16) * 16;
  if constexpr (P < 2) dma16m(c.ldsb + (512 + 2 * w) * WP, rr * (FF * 2) + c1, mk_rsrc(w1, DM * FF * 2), (512 + r0) * (FF * 2), 0xfffffffffull);
  else if constexpr (P < 4) dma16m(c.ldsb + (528 + 2 * w) * WP, rr * (DM * 2) + c2, mk_rsrc(w2, FF * DM * 2), (4096 + 64 * xcd + r0) * (DM * 2), 0xfffffffffull);
  else {
    const bool f = c.w == 4;
    dma16m(c.ldsb + (f ? LB1 : LB2 + (c.w - 5) * 384), f ? c1 : c.lane * 16, mk_rsrc(f ? b1 : b2, f ? FF * 2 : DM * 2), f ? 0 : (c.w - 5) * 384, f ? 0x3ffffull : 0xffffffull);
  }
}
DI void park_read(Ctx& c, u32x2 (&tl)[9], u32x2 (&tl2)[9]) {
  const unsigned ad = c.ldsb + (512 + (c.lane >> 2)) * WP + (c.lane & 3) * 8;
#pragma unroll
  for (int a = 0; a < 9; ++a) { tl[a] = __builtin_bit_cast(u32x2, lds_tr(ad + a * 32)); tl2[a] = __builtin_bit_cast(u32x2, lds_tr(ad + 16 * WP + a * 32)); }
  SB();
  cnt_add(c, fre(0, 4)); wlog(c, 1, 0, 0);
#pragma unroll
  for (int a = 0; a < 9; ++a) { PIN(tl[a]); PIN(tl2[a]); }
}
constexpr int KK(int NK, int v) { return NK == 18 ? (v < 2 ? 16 + v : 4 * (3 - (v - 2) / 4) + (v - 2) % 4) : 4 * (3 - v / 4) + v % 4; }
constexpr bool QSTART(int NK, int v) { return v < NK && v > 0 && (KK(NK, v) & 3) == 0; }
constexpr bool QEND(int NK, int v) { return (KK(NK, v) & 3) == 3 || KK(NK, v) == 17; }
template <int NK, int V> DI void x_ld(Ctx& c, u32x4 (&x)[2], unsigned voff, unsigned so, unsigned so2) {
  constexpr int k = KK(NK, V);
  gld16c2<(k & 1) * 2048>(x[0], x[1], voff, c.rws, k < 16 ? so + (k >> 1) * 4096 : so2);
}
template <int NK, int V0 = 0, int NV = 8> DI void x_pro(Ctx& c, u32x4 (&x)[XM][2], unsigned voff, unsigned so, unsigned so2) {
  static_for<NV>([&](auto V_) AI { x_ld<NK, V0 + decltype(V_)::v>(c, x[V0 + decltype(V_)::v], voff, so, so2); });
}
DI void x_ld4_masked(u32x4& a, u32x4& b, u32x4& d, u32x4& e, unsigned voff, rsrc_t r, unsigned so, unsigned mask) {
  asm volatile("s_cmp_eq_u32 %7, 0\n\ts_cbranch_scc1 .Lxs%=\n\ts_nop 4\n\tbuffer_load_dwordx4 %0, %4, %5, %6 offen sc1\n\tbuffer_load_dwordx4 %1, %4, %5, %6 offen offset:256 sc1\n\t"
               "buffer_load_dwordx4 %2, %4, %5, %6 offen offset:2048 sc1\n\tbuffer_load_dwordx4 %3, %4, %5, %6 offen offset:2304 sc1\n.Lxs%=:"
               : "+v"(a), "+v"(b), "+v"(d), "+v"(e) : "v"(voff), "s"(r), "s"(so), "s"(mask) : "memory", "scc");
}
template <int IMM> DI void x_ld2_if(u32x4& a, u32x4& b, unsigned voff, rsrc_t r, unsigned so, unsigned skip) {
  asm volatile("s_cmp_lg_u32 %5, 0\n\ts_cbranch_scc1 .Lxi%=\n\ts_nop 4\n\tbuffer_load_dwordx4 %0, %2, %3, %4 offen offset:%6 sc1\n\tbuffer_load_dwordx4 %1, %2, %3, %4 offen offset:%7 sc1\n.Lxi%=:"
               : "=&v"(a), "=&v"(b) : "v"(voff), "s"(r), "s"(so), "s"(skip), "i"(IMM), "i"(IMM + 256) : "memory", "scc");
}
template <int V> DI void x_ld16_if(Ctx& c, u32x4 (&x)[2], unsigned voff, unsigned so, unsigned skip) {
  constexpr int k = KK(16, V);
  x_ld2_if<(k & 1) * 2048>(x[0], x[1], voff, c.rws, so + (k >> 1) * 4096, skip);
}
DI void dma16_if(unsigned lds_addr, unsigned voff, rsrc_t r, unsigned soff, unsigned go) {
  asm volatile("s_cmp_eq_u32 %4, 0\n\ts_cbranch_scc1 .Ldi%=\n\ts_mov_b32 m0, %0\n\ts_nop 3\n\tbuffer_load_dwordx4 %1, %2, %3 offen lds\n.Ldi%=:" ::"s"(lds_addr), "v"(voff), "s"(r), "s"(soff), "s"(go)
               : "memory", "m0", "scc");
}
DI void x_pro_masked16(Ctx& c, u32x4 (&x)[XM][2], unsigned so, unsigned mask) {
  const unsigned voff = x_voff(c);
#pragma unroll
  for (int p = 0; p < XM / 2; ++p) x_ld4_masked(x[2 * p][0], x[2 * p][1], x[2 * p + 1][0], x[2 * p + 1][1], voff, c.rws, so + (KK(16, 2 * p) >> 1) * 4096, mask);
}
template <int L> DI void wlane(unsigned& v, unsigned s) { asm volatile("v_writelane_b32 %0, %1, %2" : "+v"(v) : "s"(s), "i"(L)); }
struct Sched {
  int NK, XD;
  constexpr int T0() const { return 9 * (NK - 4); }
  constexpr int sv(int n) const { return n < T0() ? n / 9 : NK - 4 + (n - T0()) % 4; }
  constexpr int sa(int n) const { return n < T0() ? n % 9 : ((n - T0()) / 4 == 0 ? 8 : (n - T0()) / 4 - 1); }
  constexpr int g0(int G) const { return G == 3 ? T0() + 8 : 9 * (NK - 16 + 1 + 4 * G) + 1; }
  constexpr int gn(int G) const { return G == 0 ? 4 : 9; }
  constexpr int wg(int n) const { for (int G = 0; G < 4; ++G) if (n >= g0(G) && (n - g0(G)) % 3 == 0 && (n - g0(G)) / 3 < gn(G)) return G; return -1; }
  constexpr int wp(int n) const { return (n - g0(wg(n))) / 3; }
  constexpr int gsig(int n) const { for (int G = 0; G < 3; ++G) if (n == g0(G) + 3 * (gn(G) - 1) + 27) return G; return -1; }
  constexpr int st(int n) const {
    const int m = n - T0();
    if (m == 6) return 0;
    if (m >= 13 && (m - 13) % 8 == 0) return 1 + 2 * ((m - 13) / 8);
    if (m >= 15 && (m - 15) % 8 == 0) return 2 + 2 * ((m - 15) / 8);
    return -1;
  }
  constexpr bool sp(int n) const { return n < T0() ? (n % 18 == 0 && n >= 18) : ((n - T0()) % 18 == 0); }
  constexpr int between(int n0, int n1) const {
    int cnt = 0;
    for (int i = n0; i < n1; ++i) { if (i < T0() && sa(i) == 8 && sv(i) + XD < NK) cnt += 2; if (st(i) >= 0) cnt += 1; }
    return cnt;
  }
  constexpr int younger(int tag, int n, bool ldr) const {
    int cnt = 0, last = -1;
    for (int v = 0; v < XD; ++v) { if (100 + v == tag) last = cnt + 1; cnt += 2; }
    for (int i = 0; i < n; ++i) {
      if (i < T0() && sa(i) == 8 && sv(i) + XD < NK) { if (100 + sv(i) + XD == tag) last = cnt + 1; cnt += 2; }
      if (ldr && wg(i) >= 0) { if (200 + wg(i) == tag) last = cnt; cnt += 1; }
      if (st(i) >= 0) cnt += 1;
    }
    return cnt - 1 - last;
  }
};
template <int NK, int KORD, int PH, int GST, class St>
DI void gemm_g(Ctx& c, f32x4 (&acc)[2][9], u32x4 (&x)[XM][2], u32x2 (&tl)[9], u32x2 (&tl2)[9], WSrc& ws, WSrc& wn, unsigned so, unsigned so2, unsigned flag_off, St&& st) {
  constexpr unsigned RT = KORD == 0 ? 16 : 8;
  constexpr int LA = 6, NS = NK * 9;
  constexpr int XD = XDEP(NK);
  constexpr Sched S{NK, XD};
  constexpr bool LD = PH < 2;
  const unsigned g = c.lane >> 4, q = (c.lane & 15) >> 2, pp = c.lane & 3;
  const unsigned wrow1 = 16 * (g >> 1) + 4 * (g & 1) + q, wrow = KORD == 0 ? 4 * g + q : wrow1;
  unsigned wb[3], wt[2];
#pragma unroll
  for (int i = 0; i < 3; ++i) { wb[i] = c.ldsb + wrow * WP + pp * 8 + i * 7 * 32 * WP; PIN(wb[i]); }
  if constexpr (NK == 18) {
    wt[0] = c.ldsb + (512 + wrow1) * WP + pp * 8; wt[1] = c.ldsb + (544 + wrow1 - 8 * (g >> 1)) * WP + pp * 8;
    PIN(wt[0]); PIN(wt[1]);
  }
  unsigned voff = x_voff(c); PIN(voff);
  bf16x4 wl[8][2];
  auto rdw = [&](auto N_, auto H_) AI {
    constexpr int N = decltype(N_)::v, H = decltype(H_)::v, v = S.sv(N), a = S.sa(N), k = KK(NK, v);
    if constexpr (k < 16) wl[N & 7][H] = lds_tr(wb[k / 7] + ((k % 7) * 32 + H * RT) * WP + a * 32);
    else if constexpr (k == 16) wl[N & 7][H] = lds_tr(wt[0] + H * 8 * WP + a * 32);
    else if constexpr (H == 0) wl[N & 7][H] = lds_tr(wt[1] + a * 32);
    else {
      u32x2 t;
      asm volatile("s_nop 2\n\tv_mov_b64 %0, %1" : "=v"(t) : "v"(tl[a]));
      wl[N & 7][1] = __builtin_bit_cast(bf16x4, t);
    }
    if constexpr (LD && H == 1 && ((N < S.T0() && a == 8 && QEND(NK, v)) || N == NS - 1)) { SB(); cnt_add(c, fre(PH, k >> 2)); wlog(c, PH + 1, 0, 4 - (k >> 2)); }
  };
  bool behind = false;
  if constexpr (PH > 0) {
    if (ws.pend) { wait_vm<2 * XD>(); w_flush(c, ws); }
    const unsigned l4 = cnt_read(c, land(PH, 4)), f0 = cnt_read(c, fre(PH - 1, 0));
    behind = (c.w < 4) & (rfl(f0) < 8);
    if (__builtin_expect(rfl(l4) < 3, 0)) w_need(c, ws, 4, 3);
  }
  prio_gemm(c, behind);
  static_for<LA>([&](auto N) AI { rdw(N, IC<0>{}); rdw(N, IC<1>{}); });
  unsigned lc = 0;
  SB();
  static_for<NS>([&](auto N_) AI {
    constexpr int N = decltype(N_)::v, v = S.sv(N), a = S.sa(N);
    if constexpr (N < S.T0() ? a == 0 : N < S.T0() + 4) {
      wait_vm<S.younger(100 + v, N, false)>();
      PIN(x[v % XD][0]); PIN(x[v % XD][1]);
    }
    if constexpr (N < S.T0() && a == 0) {
      if constexpr (v == 0 || v == 8) STAMPG(GST + (v >> 3));
      if constexpr (PH > 0 && v == 0) poll_done(ws);
      if constexpr (PH == 0 && v == ARRV) { if (c.w < 4) { wait_vm<S.between(0, N)>(); cnt_add(c, land(0, 2)); } }
      constexpr bool chk2 = QSTART(NK, v + 2), chk1 = QSTART(NK, v + 1);
      if constexpr (chk1) {
        if (__builtin_expect(rfl(lc) < (PH > 0 ? 8 : 4), 0)) { if constexpr (PH > 0) { prio_hi(c); w_need(c, ws, KK(NK, v + 1) >> 2, 8); prio_gemm(c, behind); } else cnt_wait(c, land(0, KK(NK, v + 1) >> 2), 4); }
      }
      if constexpr (PH == 0 && v == 7) park_read(c, tl, tl2);
      if constexpr (chk2) lc = cnt_read(c, land(PH, KK(NK, v + 2) >> 2));
    }
    if constexpr (PH > 0 && S.sp(N) && N < S.T0()) {
      if (__builtin_expect(behind, 0)) { behind = rfl(cnt_read(c, fre(PH - 1, 0))) < 8; prio_gemm(c, behind); }
    }
    SB();
    const bf16x8 wf = cat(wl[N & 7][0], wl[N & 7][1]);
    acc[0][a] = MFMA16(wf, asbf(x[v % XD][0]), acc[0][a]);
    SB();
    if constexpr (N + LA < NS) rdw(IC<N + LA>{}, IC<0>{});
    SB();
    acc[1][a] = MFMA16(wf, asbf(x[v % XD][1]), acc[1][a]);
    SB();
    if constexpr (N + LA < NS) rdw(IC<N + LA>{}, IC<1>{});
    if constexpr (N < S.T0() && a == 8 && v + XD < NK) x_ld<NK, v + XD>(c, x[v % XD], voff, so, so2);
    if constexpr (S.st(N) >= 0) st(IC<S.st(N)>{});
    SB();
  });
  prio_hi(c);
  st(IC<7>{}); st(IC<8>{});
  STAMPG(GST + 2);
  if constexpr (LD) { wsrc_voff(c, wn); flag_srv(c, wn, flag_off, LSRV & (PH ? 4 : 1)); } else flag_w(c, flag_off);
}
DI f32x4 relu2(const f32x4& a) {
  const float inf = 3.4028234664e38f;
  const float r0 = __builtin_amdgcn_fmed3f(a[0], 0.f, inf), r1 = __builtin_amdgcn_fmed3f(a[1], 0.f, inf), r2 = __builtin_amdgcn_fmed3f(a[2], 0.f, inf), r3 = __builtin_amdgcn_fmed3f(a[3], 0.f, inf);
  return f32x4{r0 * r0, r1 * r1, r2 * r2, r3 * r3};
}
DI u32x4 pack_single(const f32x4& r0, const f32x4& r1) {
  const auto a = __builtin_amdgcn_permlane32_swap(cvtpk(r0[0], r0[1]), cvtpk(r1[0], r1[1]), false, false);
  const auto b = __builtin_amdgcn_permlane32_swap(cvtpk(r0[2], r0[3]), cvtpk(r1[2], r1[3]), false, false);
  return u32x4{a[0], b[0], a[1], b[1]};
}
template <int I> DI void part_store(Ctx& c, const f32x4 (&acc)[2][9], unsigned base, unsigned so) {
  const unsigned g = c.lane >> 4;
  if constexpr (I == 0) gst16c(pack_single(acc[0][8], acc[1][8]), c.rws, (g >> 1) * (4608 - 128) + base + 1024, so);
  else { constexpr int i = (I - 1) >> 1, u = (I - 1) & 1; gst16c(pack_pair(acc[u][2 * i], acc[u][2 * i + 1]), c.rws, base, so + u * 4608 + i * 256); }
}
DI unsigned part_base(Ctx& c) { const unsigned l15 = c.lane & 15; return (l15 >> 2) * 1152 + (l15 & 3) * 16 + (c.lane >> 4) * 64; }
struct Red { u32x4 p[8]; u32x4 s; u32x4 alo, ahi; };
DI void red_sel(Ctx& c, Red& d) {
  const unsigned i = c.lane & 15, one = 0x3F80u << ((i & 1) * 16);
#pragma unroll
  for (int e = 0; e < 4; ++e) { d.alo[e] = (i >> 1) == (unsigned)e ? one : 0u; d.ahi[e] = (i >> 1) == (unsigned)e + 4 ? one : 0u; }
  PIN(d.alo); PIN(d.ahi);
}
DI void red_load(Ctx& c, Red& d, unsigned so) {
  const unsigned vo = (c.lane >> 4) * 36864 + (c.lane & 15) * 16;
  gld16c<1024>(d.s, vo + ((c.lane >> 3) & 1) * (4 * 36864 - 128), c.rws, so);
  static_for<4>([&](auto P_) AI {
    constexpr int P = decltype(P_)::v;
    gld16c<P * 256>(d.p[2 * P], vo, c.rws, so);
    gld16c<P * 256>(d.p[2 * P + 1], vo, c.rws, so + 4 * 36864);
  });
}
#define RED_PIN(d) asm volatile("s_waitcnt vmcnt(0)" : "+v"(d.p[0]), "+v"(d.p[1]), "+v"(d.p[2]), "+v"(d.p[3]), "+v"(d.p[4]), "+v"(d.p[5]), "+v"(d.p[6]), "+v"(d.p[7]), "+v"(d.s)::"memory")
DI void gld8(u32x2& d, unsigned voff, rsrc_t r, unsigned soff) { asm volatile("s_nop 4\n\tbuffer_load_dwordx2 %0, %1, %2, %3 offen" : "=v"(d) : "v"(voff), "s"(r), "s"(soff) : "memory"); }
DI bool sing_lane(Ctx& c) { return (c.lane & 40) == 0; }
DI void job_offs(Ctx& c, unsigned row, unsigned& o0, unsigned& o2) {
  const unsigned pr = c.lane >> 4, g = (c.lane >> 2) & 3, nt = c.w & 3;
  o0 = row * (DM * 2) + (nt * 128 + pr * 32 + 4 * g) * 2;
  o2 = row * (DM * 2) + (512 + nt * 16 + 4 * (g & 1) + 8 * (pr & 1)) * 2;
}
DI void row_load(Ctx& c, u32x2 (&r)[3], rsrc_t rs, unsigned row) {
  unsigned o0, o2; job_offs(c, row, o0, o2);
  gld8(r[0], o0, rs, 0); gld8(r[1], o0, rs, 32); gld8(r[2], o2, rs, 0);
}
DI void red_sum(const Red& d, float (&a)[12]) {
  f32x4 s0 = MFMA16(asbf(d.alo), asbf(d.p[0]), f32x4{}), s1 = MFMA16(asbf(d.alo), asbf(d.p[2]), f32x4{}), s2 = MFMA16(asbf(d.alo), asbf(d.s), f32x4{});
  s0 = MFMA16(asbf(d.alo), asbf(d.p[1]), s0); s1 = MFMA16(asbf(d.alo), asbf(d.p[3]), s1);
  s0 = MFMA16(asbf(d.ahi), asbf(d.p[4]), s0); s1 = MFMA16(asbf(d.ahi), asbf(d.p[6]), s1);
  s0 = MFMA16(asbf(d.ahi), asbf(d.p[5]), s0); s1 = MFMA16(asbf(d.ahi), asbf(d.p[7]), s1);
#pragma unroll
  for (int e = 0; e < 4; ++e) {
    a[8 + e] = s2[e] + __uint_as_float(__builtin_amdgcn_update_dpp(0, __float_as_uint(s2[e]), 0x128, 0xf, 0xf, false));
    const auto r = __builtin_amdgcn_permlane16_swap(__float_as_uint(s0[e]), __float_as_uint(s1[e]), false, false);
    a[e] = __uint_as_float(r[0]); a[4 + e] = __uint_as_float(r[1]);
  }
}
DI float sum_tok4(float x) {
  x += __uint_as_float(__builtin_amdgcn_update_dpp(0, __float_as_uint(x), 0x124, 0xf, 0xf, false));
  x += __uint_as_float(__builtin_amdgcn_update_dpp(0, __float_as_uint(x), 0x128, 0xf, 0xf, false));
  { auto r = __builtin_amdgcn_permlane16_swap(__float_as_uint(x), __float_as_uint(x), false, false); x = __uint_as_float(r[0]) + __uint_as_float(r[1]); }
  return xhalf_sum(x);
}
DI void ln2_job(Ctx& c, const Red& d, const u32x2 (&xr)[3], unsigned (&hw)[6], unsigned row0) {
  const unsigned lane = c.lane, tok = lane & 3, g = (lane >> 2) & 3, pr = lane >> 4, nt = c.w & 3;
  const unsigned stat = c.ldsb + LSTAT + (c.w >> 2) * 128;
  const bool sing = sing_lane(c);
  float a[12], v[12];
  red_sum(d, a);
#pragma unroll
  for (int e = 0; e < 6; ++e) {
    const unsigned xx = xr[e >> 1][e & 1];
    hw[e] = cvtpk(bflo(xx) + a[2 * e], bfhi(xx) + a[2 * e + 1]); v[2 * e] = bflo(hw[e]); v[2 * e + 1] = bfhi(hw[e]);
  }
  const float s1 = ((v[0] + v[1]) + (v[2] + v[3])) + ((v[4] + v[5]) + (v[6] + v[7])), s0 = (v[8] + v[9]) + (v[10] + v[11]);
  const float mw = sum_tok4(sing ? s1 + s0 : s1) * (1.f / 144);
  float s2 = 0.f, s3 = 0.f;
#pragma unroll
  for (int e = 0; e < 8; ++e) { const float t = v[e] - mw; s2 += t * t; }
#pragma unroll
  for (int e = 0; e < 4; ++e) { const float t = v[8 + e] - mw; s3 += t * t; }
  s2 = sum_tok4(sing ? s2 + s3 : s2);
  if (lane < 4) *(LAS f32x2*)(size_t)(stat + (lane * 4 + nt) * 8) = f32x2{mw, s2};
  SB();
  cnt_add(c, LSYNC + C_GB + (c.w >> 2) * 4);
  const unsigned row = row0 + tok, mb = row >> 5, r32 = row & 31;
  unsigned o0 = ((mb * 36 + 2 * (nt * 4 + pr) + (g >> 1)) * 64 + (g & 1) * 32 + r32) * 16;
  unsigned o1 = lane < 8 ? ((mb * 36 + 32 + nt) * 64 + g * 32 + r32) * 16 : OOB;
  PIN(o0); PIN(o1);
  cnt_wait(c, LSYNC + C_GB + (c.w >> 2) * 4, 4);
  const f32x4 sa = *(LAS f32x4*)(size_t)(stat + tok * 32), sb = *(LAS f32x4*)(size_t)(stat + tok * 32 + 16);
  const float mean = ((sa[0] + sa[2]) + (sb[0] + sb[2])) * 0.25f;
  const float t0 = sa[0] - mean, t1 = sa[2] - mean, t2 = sb[0] - mean, t3 = sb[2] - mean;
  const float m2 = ((sa[1] + sa[3]) + (sb[1] + sb[3])) + 144.f * ((t0 * t0 + t1 * t1) + (t2 * t2 + t3 * t3));
  const float rs = rsqrtf(m2 * (1.f / DM) + 1e-5f);
#pragma unroll
  for (int e = 0; e < 12; ++e) v[e] = (v[e] - mean) * rs;
  gst16c(u32x4{cvtpk(v[0], v[1]), cvtpk(v[2], v[3]), cvtpk(v[4], v[5]), cvtpk(v[6], v[7])}, c.rws, o0, WS_HN);
  const unsigned u0 = cvtpk(v[8], v[9]), u1 = cvtpk(v[10], v[11]);
  const auto ra = __builtin_amdgcn_permlane16_swap(u0, u0, false, false), rb = __builtin_amdgcn_permlane16_swap(u1, u1, false, false);
  gst16c(u32x4{ra[0], rb[0], ra[1], rb[1]}, c.rws, o1, WS_HN);
}

#define XS0 4
#define XS1 12
DI unsigned phase_attn(Ctx& c, QOut& qo, WSrc& wso, u32x4 (&xr)[XM][2], const u16* wo, const u16* w1, const u16* b1, const u16* w2, const u16* b2) {
  AttnState a;
  const unsigned ldsb = c.ldsb, lane = c.lane, w = c.w, b = c.b;
  const unsigned xcd = b & 7, jj = b >> 3, head = xcd * 4 + (jj >> 3), pi = jj & 7;
  const unsigned l31 = lane & 31, h = lane >> 5;
  const unsigned kh = w >> 2, rg = kh ? 7 - w : w, partner = 7 - w;
  const unsigned n1 = 2 * pi + 2, n1k = kh ? n1 : 0, n1t = kh ? n1 : 99;
  a.voff = lane * 16; a.ldsb = ldsb; a.w = w; a.n1 = n1; a.n1k = n1k;
  a.kbase = WS_K + head * (SEQ * HD * 2) + w * 4096; a.vbase = WS_V + head * (SEQ * HD * 2) + w * 4096;
  a.kb[0] = ldsb + lane * 16; a.kb[1] = a.kb[0] + 32768; a.vb[0] = a.kb[0] + 65536; a.vb[1] = a.kb[0] + 98304;
  PIN(a.kb[0]); PIN(a.kb[1]); PIN(a.vb[0]); PIN(a.vb[1]);
  if (kh == 0) {
#pragma unroll
    for (int d = 0; d < 8; ++d) *(LAS u32x4*)(size_t)(ldsb + LE + rg * 8192 + d * 1024 + lane * 16) = qo.q[d];
  }
  if (__builtin_expect(!qo.kdone, 0)) {
    wait_flags(c, F_K + head * 64, 8, 1);
#pragma unroll
    for (int p = 0; p < 4; ++p) dma16c(ldsb + kh * 16384 + w * 4096 + p * 1024, a.voff, c.rws, a.kbase + p * 1024);
  }
  STAMP(8);
#pragma unroll
  for (int d = 0; d < 4; ++d) a.o[d] = f32x16{};
#pragma unroll
  for (int d = 0; d < 8; ++d) a.qf[d] = asbf(qo.q[d]);
  a.negm = f32x16{}; a.mhat = 0.f; a.l0 = a.l1 = 0.f; a.fct = 1.f; a.pend = false;
  {
    unsigned bytes;
    asm volatile("s_mov_b32 %0, .Lfb_end-.Lfb_begin" : "=s"(bytes));
    const unsigned g = lane * 8 + w, sel = g < 162 ? 0 : g < 324 ? 1 : 2, li = g - 162 * sel, r = (li * 57) >> 9, cl = li - 9 * r;
    const unsigned o2 = (r < 16 ? 512 * xcd + jj * 16 + r : 4096 + 64 * xcd + jj * 2 + r - 16) * (DM * 2) + cl * 128;
    const unsigned o1 = (jj * 18 + r) * (FF * 2) + (cl < 8 ? xcd * 1024 + cl * 128 : 8192 + xcd * 128);
    const unsigned oc = ((g - 468) * 32 + jj) * 128;
    const bool code = g >= 468;
    const u64 base = code ? c.pc0 : sel == 0 ? (u64)w2 : sel == 1 ? (u64)w1 : (u64)wo;
    a.ta = base + (code ? (oc < bytes ? oc : 0) : sel == 1 ? o1 : o2);
    a.dummy = 0; a.tm = 15;
    PIN(a.ta);
  }
  if (kh == 0) wait_vm<0>();
  bar();
  STAMP(9);
#pragma unroll
  for (int n = 0; n < 4; ++n) a.fr[n] = lds_b128(a.kb[0] + ((n & 1) * 8 + (n >> 1)) * 1024);
  const int dq = (int)(rg * 32 + l31) - (int)(kh * 64) - 4 * (int)h;
  const unsigned dA = rg < 2 ? n1t - 2 : 99, dB = n1t - 1, dC = (kh || rg < 2) ? 16 : 99;

  const unsigned mtH = jj >> 2, fH = F_CTX + (mtH * 32 + xcd * 4) * 8;
  u32x2 fl = {0u, 0u};
  auto post = [&](auto X_, unsigned s) AI {
    constexpr int X = decltype(X_)::v;
    if constexpr (X == 0) {
      if (__builtin_expect(s == n1t, 0)) {
        const float inv = __builtin_amdgcn_rcpf(xhalf_sum(a.l0 + a.l1));
        const unsigned so = WS_CTX + ((pi * 4 + rg) * 256 + head * 8) * 1024;
#pragma unroll
        for (int d = 0; d < 4; ++d) {
          gst16c(pack8<0>(a.o[d], inv), c.rws, a.voff, so + d * 2048);
          gst16c(pack8<1>(a.o[d], inv), c.rws, a.voff, so + d * 2048 + 1024);
          a.o[d] = f32x16{};
        }
        a.l0 = a.l1 = 0.f;
        a.vb[1] += 16384;
      }
      if (__builtin_expect(s + 2 == n1t, 0)) a.kb[0] += 16384;
    } else {
      if (__builtin_expect(s == n1 + 1, 0)) flag_store(c, F_CTX + (pi * 32 + head) * 8);
      if (__builtin_expect(s == 15, 0)) flag_load(c, fl, fH, kh ? 0 : 4);
    }
    if (__builtin_expect(a.pend, 0)) {
#pragma unroll
      for (int d = 0; d < 4; ++d)
#pragma unroll
        for (int r = 0; r < 16; ++r) a.o[d][r] *= a.fct;
      a.l0 *= a.fct; a.l1 *= a.fct; a.pend = false;
    }
    a.l0 = fadd_s(a.l0, a.t0); a.l1 = fadd_s(a.l1, a.t1);
    if constexpr (X == 1) {
      if (__builtin_expect(s + 1 == n1t, 0)) {
#pragma unroll
        for (int d = 0; d < 8; ++d) a.qf[d] = lds_b128(ldsb + LE + rg * 8192 + d * 1024 + lane * 16);
        a.negm = f32x16{};
        a.kb[1] += 16384; a.vb[0] += 16384;
      }
    }
    SB();
  };
  attn_iter<0, true, false>(c, a, 0, true, dA == 0, dq);
  post(IC<0>{}, 0);
  for (unsigned s = 1; s < 17; s += 2) {
    attn_iter<1, true, true>(c, a, s, false, s == dB, dq); post(IC<1>{}, s);
    attn_iter<0, true, true>(c, a, s + 1, s + 1 == n1t, s + 1 == dA || s + 1 == dC, dq); post(IC<0>{}, s + 1);
  }
  if (w == 0) *(LAS unsigned*)(size_t)(ldsb + LSYNC + lane * 4) = 0;
  wsrc_setup<0>(c, wso, mk_rsrc(wo, NQ * DM * 2), DM * 2, (jj & 3) * 256, 1024 + (jj & 3) * 32, 512 * xcd, 0, 0);
  const unsigned ol = opaque_lane();
  wso.voff[0] = w_voff(wso, ol, 0); PIN(wso.voff[0]);
#pragma unroll
  for (int e = 0; e < 8; ++e) { a.pk[e >> 2][e & 3] = cvtpk(a.S[0][0][2 * e], a.S[0][0][2 * e + 1]); PIN(a.pk[e >> 2][e & 3]); }
  a.l0 = fadd_s(a.l0, a.S[0][0][14]); a.l1 = fadd_s(a.l1, a.S[0][0][15]);
  if (n1k == 16) wait_vm<8>(); else wait_vm<0>();
  bar();
  constexpr auto KSM = [](int m) constexpr { return m < 8 ? m >> 2 : 2 + (m & 1); };
  constexpr auto DBM = [](int m) constexpr { return m < 8 ? m & 3 : (m - 8) >> 1; };
  auto rdv = [&](auto M_) AI { constexpr int m = decltype(M_)::v; a.fr[m & 7] = lds_b128(a.vb[0] + (DBM(m) * 4 + KSM(m)) * 1024); };
  static_for<6>([&](auto M_) AI { rdv(M_); });
  PIN(fl);
  asm volatile("" ::"v"(a.dummy));
  STAMPG(33);
  const unsigned late = ((unsigned)__builtin_amdgcn_ballot_w64(fl[0] != c.ep0 || fl[1] != c.ep1) & 15u) | kh;
  const bool slow = late != 0 && kh == 0;
  const unsigned stat = ldsb + 544 * WP + rg * 512 + lane * 8, scr = ldsb + rg * 16384 + lane * 16;
  const unsigned xo = WS_CTX + ((mtH * 4 + (w & 3)) * 256 + xcd * 32) * 1024, xv = x_voff(c);
  const unsigned r3 = 384 + 32 * (w & 3);
  float lt = 0.f;
  auto put = [&](auto D_) AI {
    constexpr int d = decltype(D_)::v;
#pragma unroll
    for (int e = 0; e < 4; ++e) *(LAS f32x4*)(size_t)(scr + (d * 4 + e) * 1024) = f32x4{a.o[d][4 * e], a.o[d][4 * e + 1], a.o[d][4 * e + 2], a.o[d][4 * e + 3]};
  };
  if (__builtin_expect(slow, 0)) flag_load(c, fl, fH, 4);
  SB();
  static_for<16>([&](auto M_) AI {
    constexpr int M = decltype(M_)::v;
    a.o[DBM(M)] = MFMA32(a.fr[M & 7], asbf(a.pk[KSM(M)]), a.o[DBM(M)]);
    SB();
    if constexpr (M + 6 < 16) rdv(IC<M + 6>{});
    if constexpr (M < 9) dma16_if(ldsb + r3 * WP + M * 1024, wso.voff[M], wso.r, wso.main + r3 * wso.pitch, kh);
    if constexpr (M < 8) { wso.voff[M + 1] = w_voff(wso, ol, M + 1); PIN(wso.voff[M + 1]); }
    if constexpr (M < 4) {
#pragma unroll
      for (int e = 4 * M; e < 4 * M + 4; ++e) { a.S[0][1][e] = __builtin_amdgcn_exp2f(a.S[0][1][e]); PIN(a.S[0][1][e]); }
    } else if constexpr (M < 8) {
#pragma unroll
      for (int e = 2 * (M - 4); e < 2 * (M - 4) + 2; ++e) {
        a.pk[2 + (e >> 2)][e & 3] = cvtpk(a.S[0][1][2 * e], a.S[0][1][2 * e + 1]); PIN(a.pk[2 + (e >> 2)][e & 3]);
        a.l0 = fadd_s(a.l0, a.S[0][1][2 * e]); a.l1 = fadd_s(a.l1, a.S[0][1][2 * e + 1]);
      }
    }
    if constexpr (M == 8) { lt = xhalf_sum(a.l0 + a.l1); PIN(lt); }
    if constexpr (M >= XS0 && M - XS0 < XS1) x_ld16_if<M - XS0>(c, xr[M - XS0], xv, xo, late);
    if constexpr (M == 12) { if (kh == 0) { *(LAS f32x2*)(size_t)stat = f32x2{a.mhat, lt}; put(IC<0>{}); } }
    if constexpr (M == 14) { if (kh == 0) put(IC<1>{}); }
    SB();
  });
  STAMPG(34);
  if (kh == 0) { put(IC<2>{}); put(IC<3>{}); } else wait_vm<0>();
  STAMP(10);
  STAMPG(35);
  bar();
  STAMPG(36);
  if (kh == 0) {
    if (n1 == 16) flag_store(c, F_CTX + (7 * 32 + head) * 8);
    w_group(c, wso, 2);
    if (__builtin_expect(slow, 0)) { wait_vm<9>(); flag_check(c, fl, fH, 4); }
  }
  static_for<XM - XS1>([&](auto V_) AI { constexpr int v = XS1 + decltype(V_)::v; x_ld16_if<v>(c, xr[v], xv, xo, late); });
  if (kh) {
    f32x4 y[2][4];
    auto get = [&](int d) AI {
#pragma unroll
      for (int e = 0; e < 4; ++e) y[d & 1][e] = *(LAS f32x4*)(size_t)(scr + (d * 4 + e) * 1024);
    };
    const f32x2 ps = *(LAS f32x2*)(size_t)stat;
    get(0); get(1);
    SB();
    const float mm = fmaxf(a.mhat, ps[0]);
    const float fo = __builtin_amdgcn_exp2f(a.mhat - mm), fp = __builtin_amdgcn_exp2f(ps[0] - mm);
    const float inv = __builtin_amdgcn_rcpf(lt * fo + ps[1] * fp), f = fo * inv, f2 = fp * inv;
    const unsigned so = WS_CTX + (((15 - pi) * 4 + rg) * 256 + head * 8) * 1024;
#pragma unroll
    for (int d = 0; d < 4; ++d) {
      f32x16 x = a.o[d];
#pragma unroll
      for (int e = 0; e < 16; ++e) x[e] = x[e] * f + y[d & 1][e >> 2][e & 3] * f2;
      u32x4 p0 = pack8<0>(x), p1 = pack8<1>(x);
      PIN(p0); PIN(p1);
      SB();
      if (d < 2) get(d + 2);
      if (d == 1) { SB(); cnt_add(c, LSYNC + C_MRG); }
      gst16c(p0, c.rws, lane * 16, so + d * 2048);
      gst16c(p1, c.rws, lane * 16, so + d * 2048 + 1024);
      SB();
    }
    STAMPG(37);
    cnt_wait(c, LSYNC + C_MRG, 4);
    w_group(c, wso, 1);
    wait_vm<9>();
    STAMPG(38);
    fput(u32x2{c.ep0, c.ep1}, c.rws, lane == 0 ? F_CX2 + (((7 - pi) * 4 + rg) * 32 + head) * 8 : OOB);
    const unsigned fL = F_CX2 + ((mtH * 4 + (w & 3)) * 32 + xcd * 4) * 8;
    flag_load(c, fl, fL, 4);
    static_for<5>([&](auto P_) AI { park_load<decltype(P_)::v>(c, w1, b1, w2, b2); });
    w_group(c, wso, 0);
    wait_vm<9>(); cnt_add(c, land(0, 1));
    wait_vm<0>(); cnt_add(c, land(0, 0));
    STAMPG(39);
    prio_lo();
    flag_check(c, fl, fL, 4);
  }
  STAMP(11);
  return late;
}

struct TId { unsigned sp, nt, mt, nt32, mb, own, wq; bool isH; };
DI TId tids(Ctx& c) {
  fresh(c);
  TId t;
  const unsigned xcd = c.b & 7, jj = c.b >> 3;
  t.isH = c.w < 4; t.wq = c.w & 3; t.sp = xcd; t.nt = jj & 3; t.mt = (t.isH ? 0 : 8) + (jj >> 2);
  t.nt32 = xcd * 4 + t.nt; t.mb = t.mt * 4 + t.wq; t.own = t.nt * 8 + t.sp;
  return t;
}
DI void acc_zero(f32x4 (&acc)[2][9]) {
#pragma unroll
  for (int u = 0; u < 2; ++u)
#pragma unroll
    for (int a = 0; a < 9; ++a) acc[u][a] = f32x4{};
}
DI void phase_tail(Ctx& c, WSrc& wso, u32x4 (&xr)[XM][2], unsigned xlate, const u16* x, const u16* w1, const u16* b1, const u16* w2, const u16* b2, u16* out) {
  f32x4 acc[2][9]; u32x2 tl[9], tl2[9]; unsigned hw[6]; WSrc ws, wt;
  {
    const TId t = tids(c);
    const unsigned xo = WS_CTX + (t.mb * 256 + t.sp * 32) * 1024;
    prio_hi(c);
    STAMPG(12);
    x_pro_masked16(c, xr, xo, xlate);
    wsrc_setup(c, ws, mk_rsrc(w1, DM * FF * 2), FF * 2, t.nt32 * 256, 8192 + t.nt32 * 32, 0, 512, 1);
    acc_zero(acc);
    const unsigned pb = part_base(c), po = WS_P1 + (((t.mt * 4 + t.nt) * 8 + t.sp) * 8 + 2 * t.wq) * 4608;
    gemm_g<16, 1, 0, 13>(c, acc, xr, tl, tl2, wso, ws, xo, 0, cnt_at(t.mt, N_P1 + t.wq * 4 + t.nt), [&](auto I) AI { part_store<decltype(I)::v>(c, acc, pb, po); });
    STAMPG(16);
  }
  {
    const TId t = tids(c);
    u32x2 xres[3]; Red rd;
    row_load(c, xres, mk_rsrc(x, SEQ * DM * 2), t.mt * 128 + t.own * 4 + (c.lane & 3));
    unsigned dummy = 0;
    w_touch<4>(c, dummy, w2, w2, w2, c.w, false);
    poll_srv(c, ws, cnt_at(t.mt, N_P1 + t.nt * 4 + t.wq), 8, LSRV & 1);
    red_load(c, rd, WS_P1 + (t.mt * 256 + t.wq * 64 + (t.own >> 2)) * 4608 + (t.own & 3) * 1152);
    red_sel(c, rd);
    STAMPG(17);
    asm volatile("" ::"v"(dummy));
    PIN(xres[0]); PIN(xres[1]); PIN(xres[2]);
    RED_PIN(rd);
    poll_done(ws);
    w_flush(c, ws);
    STAMPG(18);
    ln2_job(c, rd, xres, hw, t.mt * 128 + t.own * 4);
#pragma unroll
    for (int e = 0; e < 6; ++e) PIN(hw[e]);
    STAMPG(19);
    flag_srv(c, ws, cnt_at(t.mt, N_HN + t.nt), LSRV & 2);
    STAMPG(20);
    poll_srv(c, ws, cnt_at(t.mt, N_HN + t.wq), 32, LSRV & 2);
    STAMPG(21);
    asm volatile("" ::"v"(dummy));
    const unsigned xh = WS_HN + t.mb * 36 * 1024;
    x_pro<18>(c, xr, x_voff(c), xh, xh + 32768);
    wsrc_setup(c, wt, mk_rsrc(w2, FF * DM * 2), DM * 2, t.nt * 256, 1024 + t.nt * 32, 512 * t.sp, 4096 + 64 * t.sp, 2);
#pragma unroll
    for (int a = 0; a < 9; ++a) {
      const u32x2 bias = *(LAS u32x2*)(size_t)(c.ldsb + LB1 + a * 32 + (c.lane >> 4) * 8);
      acc[0][a] = acc[1][a] = f32x4{bflo(bias[0]), bfhi(bias[0]), bflo(bias[1]), bfhi(bias[1])};
    }
    const unsigned vo = x_voff(c), ho = WS_H1 + (t.mb * 288 + 8 * t.nt32) * 1024, hs = WS_H1 + (t.mb * 288 + 256 + t.nt32) * 1024;
    gemm_g<18, 0, 1, 22>(c, acc, xr, tl, tl, ws, wt, xh, xh + 32768, cnt_at(t.mt, N_H1 + t.wq * 8 + t.sp), [&](auto I_) AI {
      constexpr int I = decltype(I_)::v;
      if constexpr (I == 0) {
        const unsigned g = c.lane >> 4;
        gst16c(pack_single(relu2(acc[0][8]), relu2(acc[1][8])), c.rws, ((g & 1) * 32 + (g >> 1) * 16 + (c.lane & 15)) * 16, hs);
      } else {
        constexpr int i = (I - 1) >> 1, u = (I - 1) & 1;
        gst16c(pack_pair(relu2(acc[u][2 * i]), relu2(acc[u][2 * i + 1])), c.rws, vo, ho + i * 2048 + u * 256);
      }
    });
    STAMPG(25);
  }
  {
    const TId t = tids(c);
    poll_srv(c, wt, cnt_at(t.mt, N_H1 + t.wq * 8 + t.sp), 4, LSRV & 4);
    STAMPG(26);
    const unsigned x1 = WS_H1 + (t.mb * 288 + t.sp * 32) * 1024, x1t = WS_H1 + (t.mb * 288 + 256 + t.sp * 4) * 1024;
    x_pro<18>(c, xr, x_voff(c), x1, x1t);
    acc_zero(acc);
    const unsigned pb = part_base(c), po = WS_P2 + (((t.mt * 4 + t.nt) * 8 + t.sp) * 8 + 2 * t.wq) * 4608;
    gemm_g<18, 0, 2, 27>(c, acc, xr, tl2, tl2, wt, wt, x1, x1t, cnt_at(t.mt, N_P2 + t.wq * 4 + t.nt), [&](auto I) AI { part_store<decltype(I)::v>(c, acc, pb, po); });
    STAMPG(30);
  }
  {
    const TId t = tids(c);
    float hb[12]; Red rd;
    {
      unsigned o0, o2; job_offs(c, 0, o0, o2);
      const u32x2 bb[3] = {*(LAS u32x2*)(size_t)(c.ldsb + LB2 + o0), *(LAS u32x2*)(size_t)(c.ldsb + LB2 + o0 + 32), *(LAS u32x2*)(size_t)(c.ldsb + LB2 + o2)};
#pragma unroll
      for (int e = 0; e < 6; ++e) { const unsigned bv = bb[e >> 1][e & 1]; hb[2 * e] = bflo(bv) + bflo(hw[e]); hb[2 * e + 1] = bfhi(bv) + bfhi(hw[e]); PIN(hb[2 * e]); PIN(hb[2 * e + 1]); }
      red_sel(c, rd);
    }
    poll_w(c, cnt_at(t.mt, N_P2 + t.nt * 4 + t.wq), 8);
    STAMPG(31);
    red_load(c, rd, WS_P2 + (t.mt * 256 + t.wq * 64 + (t.own >> 2)) * 4608 + (t.own & 3) * 1152);
    RED_PIN(rd);
    poll_done(wt);
    const rsrc_t ro = mk_rsrc(out, SEQ * DM * 2);
    float a[12]; unsigned ow[6];
    red_sum(rd, a);
#pragma unroll
    for (int e = 0; e < 6; ++e) ow[e] = cvtpk(a[2 * e] + hb[2 * e], a[2 * e + 1] + hb[2 * e + 1]);
    {
      const unsigned g = (c.lane >> 2) & 3, ro0 = (t.mt * 128 + t.own * 4 + (c.lane & 3)) * (DM * 2);
      u32x4 q, q2;
      q[0] = __builtin_amdgcn_update_dpp(ow[0], ow[2], 0x114, 0xf, 0xa, false); q[1] = __builtin_amdgcn_update_dpp(ow[1], ow[3], 0x114, 0xf, 0xa, false);
      q[2] = __builtin_amdgcn_update_dpp(ow[2], ow[0], 0x104, 0xf, 0x5, false); q[3] = __builtin_amdgcn_update_dpp(ow[3], ow[1], 0x104, 0xf, 0x5, false);
      q2[0] = ow[4]; q2[1] = ow[5];
      q2[2] = __builtin_amdgcn_update_dpp(0, ow[4], 0x104, 0xf, 0xf, false); q2[3] = __builtin_amdgcn_update_dpp(0, ow[5], 0x104, 0xf, 0xf, false);
      __builtin_amdgcn_raw_buffer_store_b128(q, ro, ro0 + t.wq * 256 + (c.lane >> 4) * 64 + (g & 1) * 32 + (g >> 1) * 16, 0, 0);
      __builtin_amdgcn_raw_buffer_store_b128(q2, ro, (c.lane & 44) == 0 ? ro0 + 1024 + t.wq * 32 + (c.lane >> 4) * 16 : OOB, 0, 0);
    }
    STAMPG(32);
    if (c.b == 0 && c.w == 0) gst8c(u32x2{c.ep0, c.ep1}, c.rws, c.lane == 0 ? F_EPOCH : OOB, WS_FLAGS);
  }
}

__global__ __launch_bounds__(512, 2) void fused_block(const u16* __restrict__ x, const u16* __restrict__ ckv, const u16* __restrict__ wq,
                                                      const u16* __restrict__ wuk, const u16* __restrict__ wuv, const u16* __restrict__ wo,
                                                      const u16* __restrict__ w1, const u16* __restrict__ b1, const u16* __restrict__ w2,
                                                      const u16* __restrict__ b2, u16* __restrict__ out, char* __restrict__ ws) {
  extern __shared__ __attribute__((aligned(16))) char lds[];
  Ctx c;
  asm volatile("s_getpc_b64 %0\n.Lfb_begin:" : "=s"(c.pc0));
  c.ldsb = (unsigned)(size_t)lds; c.lane = threadIdx.x & 63; c.w = rfl(threadIdx.x >> 6); c.b = blockIdx.x;
  c.rws = mk_rsrc(ws, WS_END);
  STAMP(0);
  QOut qo;
  WSrc wso; u32x4 xr[XM][2];
  phase_qkv(c, qo, x, ckv, wq, wuk, wuv);
  fresh(c);
  const unsigned xlate = phase_attn(c, qo, wso, xr, wo, w1, b1, w2, b2);
  fresh(c);
  phase_tail(c, wso, xr, xlate, x, w1, b1, w2, b2, out);
  asm volatile(".Lfb_end:");
}

extern "C" void kernel_launch(void* const* d_in, const int* in_sizes, int n_in, void* d_out, int out_size, void* d_ws, size_t ws_size,
                              hipStream_t stream) {
  hipFuncSetAttribute((const void*)fused_block, hipFuncAttributeMaxDynamicSharedMemorySize, 163840);
  fused_block<<<256, 512, 163840, stream>>>((const u16*)d_in[0], (const u16*)d_in[1], (const u16*)d_in[2], (const u16*)d_in[3], (const u16*)d_in[4],
                                            (const u16*)d_in[5], (const u16*)d_in[7], (const u16*)d_in[8], (const u16*)d_in[9], (const u16*)d_in[10],
                                            (u16*)d_out, (char*)d_ws);
}
```

```cpp
#include <hip/hip_runtime.h>
#include <stdint.h>
#include <utility>

typedef __bf16 bf16x8 __attribute__((ext_vector_type(8)));
typedef __bf16 bf16x4 __attribute__((ext_vector_type(4)));
typedef __bf16 bf16x2 __attribute__((ext_vector_type(2)));
typedef float f32x16 __attribute__((ext_vector_type(16)));
typedef float f32x4 __attribute__((ext_vector_type(4)));
typedef float f32x2 __attribute__((ext_vector_type(2)));
typedef unsigned u32x4 __attribute__((ext_vector_type(4)));
typedef unsigned u32x2 __attribute__((ext_vector_type(2)));
typedef unsigned short u16;
typedef unsigned long long u64;
#define LAS __attribute__((address_space(3)))
#define DI __device__ __forceinline__
#define AI __attribute__((always_inline))
typedef __amdgpu_buffer_rsrc_t rsrc_t;

constexpr int SEQ = 2048, DM = 576, NH = 32, HD = 128, DC = 512, FF = 4608, NQ = 4096;

constexpr unsigned WS_FLAGS = 0;
constexpr unsigned WS_XN = 131072;
constexpr unsigned WS_K = WS_XN + SEQ * DM * 2;
constexpr unsigned WS_V = WS_K + SEQ * NQ * 2;
constexpr unsigned WS_CTX = WS_V + SEQ * NQ * 2;
constexpr unsigned WS_P1 = WS_CTX + SEQ * NQ * 2;
constexpr unsigned WS_H = WS_P1 + 8 * SEQ * DM * 2;
constexpr unsigned WS_HN = WS_H + SEQ * DM * 2;
constexpr unsigned WS_H1 = WS_HN + SEQ * DM * 2;
constexpr unsigned WS_P2 = WS_H1 + SEQ * FF * 2;
constexpr unsigned WS_END = WS_P2 + 8 * SEQ * DM * 2;
constexpr unsigned F_EPOCH = 0, F_XN = 4096, F_KV = 8192, F_CTX = 12288, F_P1 = 16384, F_HN = 32768, F_H1 = 49152, F_P2 = 65536, F_K = 81920, F_V = 90112, F_CX2 = 98304;

template <int... Is> using iseq = std::integer_sequence<int, Is...>;
template <int N> using make_iseq = std::make_integer_sequence<int, N>;
template <int I> struct IC { static constexpr int v = I; };
template <class F, int... Is> DI void static_for_(F&& f, iseq<Is...>) { (f(IC<Is>{}), ...); }
template <int N, class F> DI void static_for(F&& f) { static_for_(f, make_iseq<N>{}); }

#define SB() __builtin_amdgcn_sched_barrier(0)
#define PIN(x) asm volatile("" : "+v"(x))
DI unsigned rfl(unsigned x) { return __builtin_amdgcn_readfirstlane(x); }
DI rsrc_t mk_rsrc(const void* p, unsigned bytes) { return __builtin_amdgcn_make_buffer_rsrc((void*)p, (short)0, (int)bytes, 0x00020000); }
constexpr unsigned OOB = 0xFFFFFF00u;
DI void dma16(unsigned lds_addr, unsigned voff, rsrc_t r, unsigned soff) {
  asm volatile("s_mov_b32 m0, %0\n\ts_nop 3\n\tbuffer_load_dwordx4 %1, %2, %3 offen lds" ::"s"(lds_addr), "v"(voff), "s"(r), "s"(soff) : "memory", "m0");
}
DI void dma16c(unsigned lds_addr, unsigned voff, rsrc_t r, unsigned soff) {
  asm volatile("s_mov_b32 m0, %0\n\ts_nop 3\n\tbuffer_load_dwordx4 %1, %2, %3 offen sc1 lds" ::"s"(lds_addr), "v"(voff), "s"(r), "s"(soff) : "memory", "m0");
}
template <int IMM> DI void gld16c(u32x4& d, unsigned voff, rsrc_t r, unsigned soff) {
  asm volatile("s_nop 4\n\tbuffer_load_dwordx4 %0, %1, %2, %3 offen offset:%4 sc1" : "=v"(d) : "v"(voff), "s"(r), "s"(soff), "i"(IMM) : "memory");
}
template <int IMM> DI void gld16(u32x4& d, unsigned voff, rsrc_t r, unsigned soff) {
  asm volatile("s_nop 4\n\tbuffer_load_dwordx4 %0, %1, %2, %3 offen offset:%4" : "=v"(d) : "v"(voff), "s"(r), "s"(soff), "i"(IMM) : "memory");
}
DI void gld8c(u32x2& d, unsigned voff, rsrc_t r, unsigned soff) {
  asm volatile("s_nop 4\n\tbuffer_load_dwordx2 %0, %1, %2, %3 offen sc1" : "=v"(d) : "v"(voff), "s"(r), "s"(soff) : "memory");
}
DI void gst16c(u32x4 v, rsrc_t r, unsigned voff, unsigned soff) {
  asm volatile("s_nop 4\n\tbuffer_store_dwordx4 %0, %1, %2, %3 offen sc1\n\ts_nop 1" ::"v"(v), "v"(voff), "s"(r), "s"(soff) : "memory");
}
DI void gst8c(u32x2 v, rsrc_t r, unsigned voff, unsigned soff) { __builtin_amdgcn_raw_buffer_store_b64(v, r, voff, soff, 16); }
DI void fput(u32x2 v, rsrc_t r, unsigned voff) { asm volatile("s_nop 4\n\tbuffer_atomic_swap_x2 %0, %1, %2, 0 offen sc1\n\ts_nop 1" ::"v"(v), "v"(voff), "s"(r) : "memory"); }
DI void fget(u32x2& d, rsrc_t r, unsigned voff) {
  d = u32x2{0u, 0u};
  asm volatile("s_nop 4\n\tbuffer_atomic_add_x2 %0, %1, %2, 0 offen sc0 sc1" : "+v"(d) : "v"(voff), "s"(r) : "memory");
}
template <int N> DI void wait_vm() { asm volatile("s_waitcnt vmcnt(%0)" ::"i"(N) : "memory"); }
DI void bar() { asm volatile("s_waitcnt lgkmcnt(0)" ::: "memory"); __builtin_amdgcn_s_barrier(); asm volatile("" ::: "memory"); }
DI unsigned cvtpk(float lo, float hi) { f32x2 v = {lo, hi}; bf16x2 b = __builtin_convertvector(v, bf16x2); return __builtin_bit_cast(unsigned, b); }
DI float bflo(unsigned x) { return __uint_as_float(x << 16); }
DI float bfhi(unsigned x) { return __uint_as_float(x & 0xffff0000u); }
DI bf16x8 lds_b128(unsigned a) { return *(LAS bf16x8*)(size_t)a; }
DI bf16x4 lds_tr(unsigned a) { return __builtin_amdgcn_ds_read_tr16_b64_v4bf16((LAS bf16x4*)(size_t)a); }
DI bf16x8 cat(bf16x4 a, bf16x4 b) { return __builtin_shufflevector(a, b, 0, 1, 2, 3, 4, 5, 6, 7); }
DI bf16x8 asbf(u32x4 x) { return __builtin_bit_cast(bf16x8, x); }
#define MFMA32(a, b, c) __builtin_amdgcn_mfma_f32_32x32x16_bf16((a), (b), (c), 0, 0, 0)
#define MFMA16(a, b, c) __builtin_amdgcn_mfma_f32_16x16x32_bf16((a), (b), (c), 0, 0, 0)
template <int S> DI u32x4 pack8(const f32x16& x, float sc) {
  u32x4 w;
  w[0] = cvtpk(x[8 * S + 0] * sc, x[8 * S + 1] * sc); w[1] = cvtpk(x[8 * S + 2] * sc, x[8 * S + 3] * sc);
  w[2] = cvtpk(x[8 * S + 4] * sc, x[8 * S + 5] * sc); w[3] = cvtpk(x[8 * S + 6] * sc, x[8 * S + 7] * sc);
  return w;
}
template <int S> DI u32x4 pack8(const f32x16& x) {
  u32x4 w;
  w[0] = cvtpk(x[8 * S + 0], x[8 * S + 1]); w[1] = cvtpk(x[8 * S + 2], x[8 * S + 3]);
  w[2] = cvtpk(x[8 * S + 4], x[8 * S + 5]); w[3] = cvtpk(x[8 * S + 6], x[8 * S + 7]);
  return w;
}
DI float max3f(float a, float b, float c) { float r; asm("v_max3_f32 %0, %1, %2, %3" : "=v"(r) : "v"(a), "v"(b), "v"(c)); return r; }
DI float max2f(float a, float b) { float r; asm("v_max_f32_e32 %0, %1, %2" : "=v"(r) : "v"(a), "v"(b)); return r; }
DI float fadd_s(float a, float b) { float r; asm("v_add_f32_e32 %0, %1, %2" : "=v"(r) : "v"(a), "v"(b)); return r; }
DI float xhalf_sum(float x) {
  auto r = __builtin_amdgcn_permlane32_swap(__float_as_uint(x), __float_as_uint(x), false, false);
  return __uint_as_float(r[0]) + __uint_as_float(r[1]);
}
DI float wave_sum(float x) {
#pragma unroll
  for (int o = 1; o < 64; o <<= 1) x += __shfl_xor(x, o);
  return x;
}

#define STAMP(i)

struct Ctx {
  unsigned ldsb, w, lane, b;
  unsigned ep0, ep1;
  rsrc_t rws;
  u64 pc0;
};

DI void fresh(Ctx& c) {
  asm volatile("v_mbcnt_lo_u32_b32 %0, -1, 0\n\tv_mbcnt_hi_u32_b32 %0, -1, %0" : "=v"(c.lane));
  asm volatile("" : "+s"(c.w), "+s"(c.b), "+s"(c.ldsb));
}
DI void code_touch(Ctx& c, unsigned& dummy) {
  unsigned bytes;
  asm volatile("s_mov_b32 %0, .Lfb_end-.Lfb_begin" : "=s"(bytes));
  const rsrc_t r = mk_rsrc((const void*)c.pc0, bytes);
  asm volatile("s_nop 4\n\tbuffer_load_dword %0, %1, %2, 0 offen" : "+v"(dummy) : "v"(c.w == 0 ? (c.lane * 32 + (c.b >> 3)) * 128 : OOB), "s"(r) : "memory");
}
DI void publish(Ctx& c, unsigned flag_off) {
  wait_vm<0>();
  bar();
  fput(u32x2{c.ep0, c.ep1}, c.rws, (c.w == 0 && c.lane == 0) ? flag_off : OOB);
}
DI void poll_flags(Ctx& c, unsigned flag_off, unsigned n, unsigned stride) {
  const unsigned voff = c.lane < n ? flag_off + c.lane * 8 * stride : OOB;
  const u64 t0 = __builtin_amdgcn_s_memrealtime();
  for (;;) {
    u32x2 f;
    fget(f, c.rws, voff);
    asm volatile("s_waitcnt vmcnt(0)" : "+v"(f)::"memory");
    if (__all((f[0] == c.ep0 && f[1] == c.ep1) || c.lane >= n)) break;
    if (__builtin_amdgcn_s_memrealtime() - t0 > 5000000ull) break;
    __builtin_amdgcn_s_sleep(2);
  }
}
DI void wait_flags(Ctx& c, unsigned flag_off, unsigned n, unsigned stride) {
  if (c.w == 0) poll_flags(c, flag_off, n, stride);
  bar();
}

#define DBG_DRAIN()
#define DBG_NX(x)
#define STAMPG(i)
#define OAUX 0
#define OAUX2 OAUX
DI void flag_store(Ctx& c, unsigned flag_off) {
  fput(u32x2{c.ep0, c.ep1}, c.rws, (c.w == 0 && c.lane == 0) ? flag_off : OOB);
}
DI void flag_load(Ctx& c, u32x2& f, unsigned flag_off, unsigned n) { fget(f, c.rws, c.lane < n ? flag_off + c.lane * 8 : OOB); }
DI void flag_check(Ctx& c, u32x2& f, unsigned flag_off, unsigned n) {
  PIN(f);
  if (__builtin_expect(!__all((f[0] == c.ep0 && f[1] == c.ep1) || c.lane >= n), 0)) poll_flags(c, flag_off, n, 1);
}

constexpr float QSCALE = 0.08838834764831845f * 1.4426950408889634f;
constexpr unsigned LE = 131072;
constexpr unsigned LX = 147456;
constexpr unsigned KW = LX + 12288;
#define ROT 0
constexpr unsigned SLOT = 49152;
constexpr int slot_of(int S) { return (S + 2) % 3; }
constexpr int XQ = 6;
constexpr int LAQ = 6;
constexpr int QB1 = 40, QB2 = 88, QB3 = 116;
constexpr int Q_KPOLL = 70, Q_VPOLL = 134, Q_VST = 12;
#define PRO 0
#define XLS 1
constexpr int XLS1 = XLS > 0 ? XLS : 1, XL0 = 31 - 5 * XLS1, XFL = XLS == 0 ? 19 : ((XL0 - 8) | 1) - ((XL0 & 1) ? 0 : 2);
#define KSP 2
#define TWV 5
#define TWQ 13
#define XT 1
#define LNE 1
#define PRO_N 12
#define PRO_B 5
constexpr int PRO_U[18] = {0, 0, 0, 0, 0, 0, 1, 1, 1, 1, 1, 1, 2, 2, 2, 2, 2, 2}, PRO_I[18] = {0, 4, 1, 2, 5, 3, 0, 4, 1, 2, 5, 3, 0, 1, 2, 3, 4, 5}, PRO_X = LNE ? LNE - 1 : 5;
#define WQL 1
#define WQ2 41
constexpr int wq_piece(int q) { return q >= 1 && q < 25 && (q - 1) % 4 == 0 ? (q - 1) / 4 : q >= WQ2 && q < WQ2 + 24 && (q - WQ2) % 4 == 0 ? 6 + (q - WQ2) / 4 : -1; }
constexpr int vm_wait(int pos, int tag) {
  int seq[512] = {}; int n = 0;
  auto push = [&](int t, int cnt) { for (int i = 0; i < cnt; ++i) seq[n++] = t; };
  for (int i = 0; i < PRO_N; ++i) { push(PRO_U[i], 1); if (i == PRO_X) push(-1, 3); }
  if (PRO_N == 18) push(-12, 1);
  bool done = false;
  for (int T = 0; T < 32 && !done; ++T) {
    if (pos == T) { done = true; break; }
    if (T == 0 && PRO_N < 18) { for (int i = PRO_N; i < 18; ++i) push(PRO_U[i], 1); push(-12, 1); }
    if (T == 3) push(-2, 2);
    if (T == TWV) push(-6, 1);
    if (T == TWQ) push(-6, 1);
    if (T == 7) push(-3, 1);
    if (T == XFL) push(-5, 1);
    if (KSP == 1 && T == 27) push(-8, 1);
    if (T >= 16 && (T - 16) % KSP == 0 && (T - 16) / KSP < 8) push(-7, 1);
    if (T >= 1 && (!WQL || T < 29)) push((T - 1) / 2 + 3, 3);
    if (XLS == 0 && T >= 29) push(200 + 2 * (T - 29), 2), push(201 + 2 * (T - 29), 2);
    if (XLS > 0 && T >= XL0 && (T - XL0) % XLS1 == 0) push(200 + (T - XL0) / XLS1, 2);
  }
  for (int q = 0; q < 144 && !done; ++q) {
    if (pos == 100 + q) break;
    if (q == QB3) push(-11, 1);
    if (KSP == 2 && q == QB1) push(-8, 1);
    if (!WQL && (q == 1 || q == 4 || q == 6)) push(18, 1);
    if (WQL && wq_piece(q) >= 0) push(17 + wq_piece(q) / 6, 1);
    if (tag == -9 && q == Q_KPOLL) push(-9, 1);
    if (q % Q_VST == 2 && q / Q_VST < 8) push(-10, 1);
    if ((q & 7) == 7 && (q >> 3) + XQ < 18) push(200 + (q >> 3) + XQ, 2);
  }
  int last = -1;
  for (int i = 0; i < n; ++i) if (seq[i] == tag) last = i;
  return n - 1 - last;
}
constexpr int imin(int a, int b) { return a < b ? a : b; }
DI unsigned lds_add_rtn(unsigned addr) {
  unsigned r;
  asm volatile("s_mov_b64 exec, 1\n\tds_add_rtn_u32 %0, %1, %2\n\ts_mov_b64 exec, -1\n\ts_waitcnt lgkmcnt(0)" : "=&v"(r) : "v"(addr), "v"(1u) : "memory");
  return rfl(r);
}
DI unsigned lds_word(unsigned addr) { return __hip_atomic_load((LAS unsigned*)(size_t)addr, __ATOMIC_RELAXED, __HIP_MEMORY_SCOPE_WORKGROUP); }
DI void lds_set(unsigned addr, unsigned v) { __hip_atomic_store((LAS unsigned*)(size_t)addr, v, __ATOMIC_RELAXED, __HIP_MEMORY_SCOPE_WORKGROUP); }
DI float wave_sum64(float x) {
  x += __uint_as_float(__builtin_amdgcn_update_dpp(0, __float_as_uint(x), 0xB1, 0xf, 0xf, false));
  x += __uint_as_float(__builtin_amdgcn_update_dpp(0, __float_as_uint(x), 0x4E, 0xf, 0xf, false));
  x += __uint_as_float(__builtin_amdgcn_update_dpp(0, __float_as_uint(x), 0x124, 0xf, 0xf, false));
  x += __uint_as_float(__builtin_amdgcn_update_dpp(0, __float_as_uint(x), 0x128, 0xf, 0xf, false));
  { auto r = __builtin_amdgcn_permlane16_swap(__float_as_uint(x), __float_as_uint(x), false, false); x = __uint_as_float(r[0]) + __uint_as_float(r[1]); }
  return xhalf_sum(x);
}
DI void ln1_row(Ctx& c, const u32x4& xr0, const u32x4& xr1) {
  const unsigned lane = c.lane;
  float v[16];
#pragma unroll
  for (int e = 0; e < 4; ++e) { v[2 * e] = bflo(xr0[e]); v[2 * e + 1] = bfhi(xr0[e]); v[8 + 2 * e] = bflo(xr1[e]); v[9 + 2 * e] = bfhi(xr1[e]); }
  float s = 0.f;
#pragma unroll
  for (int e = 0; e < 16; ++e) s += v[e];
  const float mean = wave_sum64(s) * (1.f / DM);
  float qq = 0.f;
#pragma unroll
  for (int e = 0; e < 16; ++e) { v[e] -= mean; qq += v[e] * v[e]; }
  qq = lane < 36 ? qq : 0.f;
  const float rs = rsqrtf(wave_sum64(qq) * (1.f / DM) + 1e-5f);
  u32x4 o0, o1;
#pragma unroll
  for (int e = 0; e < 4; ++e) { o0[e] = cvtpk(v[2 * e] * rs, v[2 * e + 1] * rs); o1[e] = cvtpk(v[8 + 2 * e] * rs, v[9 + 2 * e] * rs); }
  const unsigned ad = c.ldsb + LX + (lane < 36 ? (2 * lane * 8 + c.w) * 16 : 9216 + (lane - 36) * 16);
  *(LAS u32x4*)(size_t)ad = o0;
  *(LAS u32x4*)(size_t)(ad + 128) = o1;
}
DI void xn_store(Ctx& c) {
  const unsigned w = c.w, lane = c.lane, mb = c.b >> 2, r0 = (c.b & 3) * 8;
  const unsigned c0 = 9 * w + (lane >> 3), c1 = 9 * w + 8;
  const u32x4 a0 = *(LAS u32x4*)(size_t)(c.ldsb + LX + (9 * w * 8 + lane) * 16);
  const u32x4 a1 = *(LAS u32x4*)(size_t)(c.ldsb + LX + (c1 * 8 + (lane & 7)) * 16);
  gst16c(a0, c.rws, ((mb * 36 + (c0 >> 1)) * 64 + (c0 & 1) * 32 + r0 + (lane & 7)) * 16, WS_XN);
  gst16c(a1, c.rws, lane < 8 ? ((mb * 36 + (c1 >> 1)) * 64 + (c1 & 1) * 32 + r0 + lane) * 16 : OOB, WS_XN);
}

template <int WH> DI void w_touch(Ctx& c, unsigned& dummy, const void* wo, const void* w1, const void* w2, unsigned wv, bool two) {
  unsigned lane;
  asm volatile("v_mbcnt_lo_u32_b32 %0, -1, 0\n\tv_mbcnt_hi_u32_b32 %0, -1, %0" : "=v"(lane));
  const unsigned xcd = c.b & 7, jj = (c.b >> 3) - ((lane >> 5) << 2), l = two ? lane & 31 : lane, i = 3 * wv + l - 18;
  const bool mn = l < 18, ex = l < 21 && i < 18;
  auto ld = [&](const void* p, unsigned bytes, unsigned off) AI {
    asm volatile("s_nop 4\n\tbuffer_load_dword %0, %1, %2, 0 offen" : "+v"(dummy) : "v"(off), "s"(mk_rsrc(p, bytes)) : "memory");
  };
  const unsigned o1 = xcd * 589824 + (jj * 144 + wv * 18 + l) * 128;
  if constexpr (WH & 1) ld(wo, NQ * DM * 2, mn ? o1 : OOB);
  if constexpr (WH & 2) ld(w1, FF * DM * 2, mn ? (jj * 18 + l) * (FF * 2) + xcd * 1024 + wv * 128 : ex ? (jj * 18 + i) * (FF * 2) + 8192 + xcd * 128 : OOB);
  if constexpr (WH & 4) ld(w2, FF * DM * 2, mn ? o1 : ex ? (4096 + 64 * xcd) * (DM * 2) + (jj * 18 + i) * 128 : OOB);
}
struct QOut { u32x4 q[8]; bool kdone; };
DI u32x4 pack_pair(const f32x4& r0, const f32x4& r1) { return u32x4{cvtpk(r0[0], r0[1]), cvtpk(r0[2], r0[3]), cvtpk(r1[0], r1[1]), cvtpk(r1[2], r1[3])}; }
template <int IMM> DI void gld16c2(u32x4& d0, u32x4& d1, unsigned voff, rsrc_t r, unsigned soff) {
  asm volatile("s_nop 4\n\tbuffer_load_dwordx4 %0, %2, %3, %4 offen offset:%5 sc1\n\tbuffer_load_dwordx4 %1, %2, %3, %4 offen offset:%6 sc1"
               : "=&v"(d0), "=&v"(d1) : "v"(voff), "s"(r), "s"(soff), "i"(IMM), "i"(IMM + 256) : "memory");
}
DI unsigned x_voff(Ctx& c) { const unsigned g = c.lane >> 4; return (g >> 1) * 1024 + ((g & 1) * 32 + (c.lane & 15)) * 16; }

DI void phase_qkv(Ctx& c, QOut& qo, const u16* x, const u16* ckv, const u16* wq, const u16* wuk, const u16* wuv) {
  const unsigned w = c.w, lane = c.lane, ldsb = c.ldsb, b = c.b;
  const unsigned xcd = b & 7, jj = b >> 3, head = xcd * 4 + (jj >> 3), cb = jj & 7;
  const rsrc_t rxx = mk_rsrc(x, SEQ * DM * 2), rx = mk_rsrc(ckv, SEQ * DC * 2);
  const rsrc_t rwk = mk_rsrc(wuk, DC * NQ * 2), rwv = mk_rsrc(wuv, DC * NQ * 2), rwq = mk_rsrc(wq, DM * NQ * 2);
  unsigned dummy = 0;
  code_touch(c, dummy);
  const unsigned voffX0 = (lane >> 3) * 1024 + (((lane & 7) ^ (lane >> 4)) << 4), voffX1 = voffX0 ^ 64;
  const unsigned voffW = (lane >> 3) * 8192 + (((((lane >> 1) & 3) ^ (((lane >> 4) & 1) | ((w & 1) << 1))) << 5) | ((lane & 1) << 4));
  const unsigned sX = (cb * 256 + 32 * w) * 1024, sW = (8 * (w & 3)) * 8192 + (head * 128 + (w >> 2) * 64) * 2;
  const unsigned rot = ROT == 0 ? 2 * (cb & 3) : ROT == 1 ? 2 * ((cb + head) & 3) : ROT == 2 ? 2 * (cb & 3) + 4 * (head & 1) : (cb + 2 * head) & 7;
  const unsigned dX = ldsb + 4 * w * 1024, dW = ldsb + w * 1024;
  auto issue = [&](auto U_, auto P_) AI {
    constexpr int U = decltype(U_)::v, P = decltype(P_)::v;
    constexpr unsigned so = slot_of(U) * SLOT;
    if constexpr (U < 16) {
      const unsigned tr = (U + rot) & 7;
      constexpr int p = P < 2 ? P : P - 1;
      if constexpr (P % 3 != 2) dma16(dX + so + p * 1024, (p & 1) ? voffX1 : voffX0, rx, sX + 128 * tr + (U < 8 ? 8 * p : 16 * (p & 1) + 8 * (p >> 1)) * 1024);
      else dma16(dW + so + 32768 + (P / 3) * 8192, voffW, U < 8 ? rwk : rwv, sW + (64 * tr + 32 * (P / 3)) * 8192);
    } else {
      dma16(dW + so + (U == 18 ? 5 - P : P) * 8192, voffW, rwq, sW + 32 * (6 * (U - 16) + P) * 8192);
    }
  };
  u32x2 ep; u32x4 xr0, xr1;
    const unsigned hr = head & 3, vA = voffX0 ^ ((hr & 1) << 6), vB = vA ^ 64, wj = (cb >> 2) * 8192;
    auto pro = [&](auto U_, auto I_) AI {
      constexpr int U = decltype(U_)::v, I = decltype(I_)::v;
      constexpr unsigned so = slot_of(U) * SLOT;
      const unsigned tr = (U + rot) & 7;
      if constexpr (I < 4) { const unsigned po = ((I + hr) & 3) * 1024; dma16(dX + so + po, (I & 1) ? vB : vA, rx, sX + 128 * tr + 8 * po); }
      else { const unsigned j = wj ^ ((I - 4) * 8192); dma16(dW + so + 32768 + j, voffW, rwk, sW + 64 * tr * 8192 + 32 * j); }
    };
  {
    static_for<PRO_N>([&](auto N_) AI {
      constexpr int N = decltype(N_)::v;
      pro(IC<PRO_U[N]>{}, IC<PRO_I[N]>{});
      if constexpr (N == PRO_B) __builtin_amdgcn_s_barrier();
      if constexpr (N == PRO_X) {
        const unsigned vo = lane < 36 ? lane * 32 : OOB;
        gld8c(ep, F_EPOCH, c.rws, WS_FLAGS);
        gld16<0>(xr0, vo, rxx, (b * 8 + w) * (DM * 2));
        gld16<16>(xr1, vo, rxx, (b * 8 + w) * (DM * 2));
      }
    });
  }
  unsigned td2 = 0;
  auto xtouch = [&]() AI {
    const unsigned l5 = (lane * 52) >> 8, row = cb * 256 + (head & 3) * 64 + w * 8 + l5, ch = (rot + 3 + lane - 5 * l5) & 7;
    asm volatile("s_nop 4\n\tbuffer_load_dword %0, %1, %2, 0 offen" : "+v"(td2) : "v"(XT && lane < 40 ? row * 1024 + ch * 128 : OOB), "s"(rx) : "memory");
  };
  if constexpr (PRO_N == 18) xtouch();
  const unsigned kh = w >> 2, rg = kh ? 7 - w : w;
  const unsigned qmb = (kh ? cb : 15 - cb) * 4 + rg;
  const unsigned wm = w >> 1, wn = w & 1, g = lane >> 4, l15 = lane & 15, q = l15 >> 2, pp = lane & 3;
  unsigned xb[3][2], wb[3][4], wqb[3][4];
  {
    const unsigned sg = ((q >> 1) & 1) | ((g & 1) << 1);
#pragma unroll
    for (int i = 0; i < 3; ++i) {
      xb[i][0] = ldsb + i * SLOT + (wm * 64 + l15) * 128 + ((g ^ (l15 >> 1)) << 4); xb[i][1] = xb[i][0] ^ 64; PIN(xb[i][0]); PIN(xb[i][1]);
#pragma unroll
      for (int j = 0; j < 4; ++j) { wb[i][j] = ldsb + i * SLOT + wn * 4096 + (8 * g + q) * 128 + ((j ^ sg) << 5) + pp * 8; PIN(wb[i][j]); }
    }
  }
  if constexpr (LNE != 0) { wait_vm<vm_wait(0, -1)>(); PIN(xr0); PIN(xr1); ln1_row(c, xr0, xr1); }
  wait_vm<vm_wait(0, 0)>();
  asm volatile("" ::"v"(dummy));
  bar();
  STAMP(2);
  f32x4 acc[4][4];
  bf16x8 xf[2][4]; bf16x4 wl[2][4][2];
  u32x4 pk[8], pv[8], xq[XQ][2]; u32x2 xnf;
  auto rd = [&](auto T_, auto R_) AI {
    constexpr int T = decltype(T_)::v, R = decltype(R_)::v, B = T & 1, sl = slot_of(T >> 1);
    constexpr int K = R % 3, I = R / 3;
    if constexpr (K == 0) xf[B][I] = lds_b128(xb[sl][B] + I * 2048);
    else wl[B][I][K - 1] = lds_tr(wb[sl][I] + 32768 + B * 8192 + (K - 1) * 512);
  };
  bf16x4 wq8[8][2];
  auto rdq = [&](auto N_, auto H_) AI {
    constexpr int N = decltype(N_)::v, H = decltype(H_)::v, v = N >> 3, a = N & 7, sl = slot_of(16 + v / 6);
    wq8[N & 7][H] = lds_tr(wqb[sl][a & 3] + (v >= 12 ? 17 - v : v % 6) * 8192 + (a >> 2) * 4096 + H * 512);
  };
  const unsigned xvo = x_voff(c), xso = WS_XN + qmb * 36 * 1024;
  auto ldx = [&](auto V_) AI { constexpr int v = decltype(V_)::v; gld16c2<(v & 1) * 2048>(xq[v % XQ][0], xq[v % XQ][1], xvo, c.rws, xso + (v >> 1) * 4096); };
  const unsigned kst = WS_K + ((head * 64 + cb * 8 + wm * 2) * 8 + 4 * wn) * 1024, vst = WS_V + (((head * 32 + cb * 4 + wm) * 4 + 2 * wn) * 4) * 1024;
  static_for<12>([&](auto R) AI { rd(IC<0>{}, R); });
  SB();
  static_for<32>([&](auto T_) AI {
    constexpr int T = decltype(T_)::v, B = T & 1;
    if constexpr (B) { wait_vm<vm_wait(T, (T >> 1) + 1)>(); bar(); }
    if constexpr (T == 1) {
      PIN(ep);
      if constexpr (!LNE) { PIN(xr0); PIN(xr1); }
      u64 z = (((u64)rfl(ep[1]) << 32) | rfl(ep[0])) + 0x9E3779B97F4A7C15ull;
      z = (z ^ (z >> 30)) * 0xBF58476D1CE4E5B9ull; z = (z ^ (z >> 27)) * 0x94D049BB133111EBull; z ^= z >> 31;
      c.ep0 = (unsigned)z; c.ep1 = (unsigned)(z >> 32);
      if (w == 0) lds_set(ldsb + KW + (lane & 3) * 4, 0);
    }
    if constexpr (!LNE && (T == 1 || T == 2)) { if (kh == T - 1) ln1_row(c, xr0, xr1); }
    if constexpr (T == 3) xn_store(c);
    if constexpr (T == TWV) asm volatile("s_nop 4\n\tbuffer_load_dword %0, %1, %2, 0 offen" : "+v"(td2) : "v"(lane < 16 ? (cb * 64 + w * 8 + (lane >> 1)) * 8192 + head * 256 + (lane & 1) * 128 : OOB), "s"(rwv) : "memory");
    if constexpr (T == TWQ) asm volatile("s_nop 4\n\tbuffer_load_dword %0, %1, %2, 0 offen" : "+v"(td2) : "v"(lane < 18 ? (cb * 72 + w * 9 + (lane >> 1)) * 8192 + head * 256 + (lane & 1) * 128 : OOB), "s"(rwq) : "memory");
    if constexpr (T == 31) asm volatile("" ::"v"(td2));
    if constexpr (T == 7) flag_store(c, F_XN + b * 8);
    if constexpr (T == XFL) flag_load(c, xnf, F_XN + qmb * 32, 4);
    if constexpr (T == XFL + 8) flag_check(c, xnf, F_XN + qmb * 32, 4);
    if constexpr (T == 27) { if constexpr (KSP == 1) flag_store(c, F_K + (head * 8 + cb) * 8); }
    if constexpr (T == 31) {
#pragma unroll
      for (int i = 0; i < 3; ++i)
#pragma unroll
        for (int j = 0; j < 4; ++j) { wqb[i][j] = wb[i][j] - wn * 4096; PIN(wqb[i][j]); }
    }
    if constexpr (T == 8 || T == 16 || T == 24) STAMP(100 + T / 2);
    SB();
    static_for<16>([&](auto M_) AI {
      constexpr int M = decltype(M_)::v, i = M >> 2, j = M & 3;
      const bf16x8 wf = cat(wl[B][j][0], wl[B][j][1]);
      const f32x4 cin = (T & 15) == 0 ? f32x4{} : acc[i][j];
      acc[i][j] = T < 16 ? MFMA16(wf, xf[B][i], cin) : MFMA16(xf[B][i], wf, cin);
      SB();
      if constexpr (M < 12) {
        if constexpr (T < 31) rd(IC<T + 1>{}, IC<M>{});
        else rdq(IC<(M >> 1)>{}, IC<(M & 1)>{});
      }
      if constexpr (M == 1 && T >= 16 && (T - 16) % KSP == 0 && (T - 16) / KSP < 8) {
        constexpr int e = (T - 16) / KSP;
        gst16c(pk[e], c.rws, xvo, kst + (e >> 2) * 8192 + (e & 1) * 2048 + ((e >> 1) & 1) * 256);
      }
      if constexpr (T >= 1 && (!WQL || T < 29) && (M == 4 || M == 8 || M == 12)) issue(IC<(T - 1) / 2 + 3>{}, IC<(B ? 0 : 3) + (M >> 2) - 1>{});
      if constexpr (T == 0 && (M & 1) && PRO_N + (M >> 1) < 18) pro(IC<PRO_U[PRO_N + (M >> 1)]>{}, IC<PRO_I[PRO_N + (M >> 1)]>{});
      if constexpr (T == 0 && M == 15 && PRO_N < 18) xtouch();
      if constexpr (XLS == 0 && T >= 29 && (M == 13 || M == 15)) ldx(IC<2 * (T - 29) + (M == 15)>{});
      if constexpr (XLS > 0 && T >= XL0 && (T - XL0) % XLS1 == 0 && M == 14) ldx(IC<(T - XL0) / XLS1>{});
      SB();
    });
    if constexpr (T == 15) {
#pragma unroll
      for (int e = 0; e < 8; ++e) { pk[e] = pack_pair(acc[e >> 1][2 * (e & 1)], acc[e >> 1][2 * (e & 1) + 1]); PIN(pk[e]); }
    }
    if constexpr (T == 31) {
#pragma unroll
      for (int e = 0; e < 8; ++e) { pv[e] = pack_pair(acc[2 * (e >> 2)][e & 3], acc[2 * (e >> 2) + 1][e & 3]); PIN(pv[e]); }
    }
  });
  STAMP(3);
  f32x4 qa[2][8];
  u32x2 kf; bool kdone = false;
  const unsigned kso = WS_K + head * (SEQ * HD * 2) + w * 4096;
  static_for<144>([&](auto N_) AI {
    constexpr int N = decltype(N_)::v, v = N >> 3, a = N & 7;
    if constexpr (a == 0) { wait_vm<vm_wait(100 + N, 200 + v)>(); PIN(xq[v % XQ][0]); PIN(xq[v % XQ][1]); }
    if constexpr (N == QB1) { wait_vm<vm_wait(100 + N, 17)>(); bar(); if constexpr (KSP == 2) flag_store(c, F_K + (head * 8 + cb) * 8); SB(); }
    if constexpr (N == QB2) {
      wait_vm<vm_wait(100 + N, 18)>();
      if (w == 0) wait_vm<vm_wait(100 + N, -9)>();
      PIN(kf);
      if (w == 0 && __all((kf[0] == c.ep0 && kf[1] == c.ep1) || lane >= 8)) lds_set(ldsb + KW + 8, 1);
      bar();
      kdone = rfl(lds_word(ldsb + KW + 8)) != 0;
      SB();
    }
    if constexpr (N == QB3) { wait_vm<vm_wait(100 + N, -10)>(); bar(); flag_store(c, F_V + (head * 8 + cb) * 8); SB(); }
    const bf16x8 wf = cat(wq8[N & 7][0], wq8[N & 7][1]);
    qa[0][a] = MFMA16(wf, asbf(xq[v % XQ][0]), v == 0 ? f32x4{} : qa[0][a]);
    SB();
    if constexpr (N + LAQ < 144) rdq(IC<N + LAQ>{}, IC<0>{});
    SB();
    qa[1][a] = MFMA16(wf, asbf(xq[v % XQ][1]), v == 0 ? f32x4{} : qa[1][a]);
    SB();
    if constexpr (N + LAQ < 144) rdq(IC<N + LAQ>{}, IC<1>{});
    if constexpr (!WQL && (N == 1 || N == 4 || N == 6)) issue(IC<18>{}, IC<(N == 1 ? 3 : N == 4 ? 4 : 5)>{});
    if constexpr (WQL && wq_piece(N) >= 0) issue(IC<17 + wq_piece(N) / 6>{}, IC<wq_piece(N) % 6>{});
    if constexpr (N == Q_KPOLL || N == Q_VPOLL) {
      kf = u32x2{0u, 0u};
      asm volatile("s_mov_b32 exec_lo, %3\n\ts_mov_b32 exec_hi, 0\n\ts_nop 4\n\tbuffer_atomic_add_x2 %0, %1, %2, 0 offen sc0 sc1\n\ts_mov_b64 exec, -1"
                   : "+v"(kf) : "v"((N == Q_KPOLL ? F_K : F_V) + (head * 8 + lane) * 8), "s"(c.rws), "s"(w == 0 ? 0xffu : 0u) : "memory");
    }
    if constexpr (N % Q_VST == 2 && N / Q_VST < 8) {
      constexpr int e = N / Q_VST;
      gst16c(pv[e], c.rws, xvo, vst + ((e >> 1) & 1) * 4096 + (e >> 2) * 2048 + (e & 1) * 256);
    }
    if constexpr (a == 7 && v + XQ < 18) ldx(IC<v + XQ>{});
    if constexpr (N > QB3 && N <= QB3 + 16 && (N - QB3) % 4 == 0) {
      constexpr int p = (N - QB3) / 4 - 1;
      if (kdone) dma16c(ldsb + kh * 16384 + w * 4096 + p * 1024, lane * 16, c.rws, kso + p * 1024);
    }
    SB();
  });
#pragma unroll
  for (int A = 0; A < 4; ++A) {
    u32x4 p0, p1;
#pragma unroll
    for (int e = 0; e < 4; ++e) {
      const int t = 2 * A + (e >> 1), k = 2 * (e & 1);
      const unsigned u0 = cvtpk(qa[0][t][k] * QSCALE, qa[0][t][k + 1] * QSCALE), u1 = cvtpk(qa[1][t][k] * QSCALE, qa[1][t][k + 1] * QSCALE);
      const auto s = __builtin_amdgcn_permlane16_swap(u0, u1, false, false);
      const auto r = __builtin_amdgcn_permlane32_swap(s[0], s[1], false, false);
      p0[e] = r[0]; p1[e] = r[1];
    }
    qo.q[2 * A] = p0; qo.q[2 * A + 1] = p1;
  }
  qo.kdone = kdone;
  STAMP(6);
  if (w == 0) wait_vm<0>();
  flag_check(c, kf, F_V + head * 64, w == 0 ? 8 : 0);
}

constexpr float THR = 32.f;

struct AttnState {
  bf16x8 qf[8];
  f32x16 o[4];
  f32x16 S[2][2];
  f32x16 negm;
  u32x4 pk[4];
  bf16x8 fr[8];
  float mhat, l0, l1, fct, mx0, mx1, t0, t1;
  bool pend;
  unsigned kb[2], vb[2];
  unsigned voff, ldsb, w, kbase, vbase, n1, n1k;
  u64 ta, tm; unsigned dummy;
};

constexpr int RD0[16] = {4, 5, 6, 7, 9, 10, 12, 13, 15, -1, -1, -1, -1, -1, -1, -1};
constexpr int RD1[16] = {-1, -1, -1, 8, -1, 11, -1, 14, -1, -1, -1, -1, -1, -1, -1, -1};

template <int CR> DI float mask1(float x, int dq, float ninf) {
  asm volatile("v_cmp_le_i32_e32 vcc, %2, %1\n\tv_cndmask_b32_e32 %0, %3, %0, vcc" : "+v"(x) : "v"(dq), "i"(CR), "v"(ninf) : "vcc");
  return x;
}
template <int B, int... R> DI void mask_rows(f32x16& s, int dq, float ninf, iseq<R...>) { ((s[R] = mask1<B + (R & 3) + 8 * (R >> 2)>(s[R], dq, ninf)), ...); }
struct NoHook { template <class T> DI void operator()(T) const {} };
template <int X, bool QK, bool PV, bool VDMA = true, class Hook = NoHook>
DI void attn_iter(Ctx& c, AttnState& a, const unsigned s, const bool first, const bool diag, const int dq, Hook&& hook = Hook{}) {
  constexpr int Y = X ^ 1;
  auto kaddr = [&](int slot, int n) AI { return a.kb[slot] + ((n & 1) * 8 + (n >> 1)) * 1024; };
  auto vaddr = [&](int slot, int n) AI { return a.vb[slot] + ((n & 3) * 4 + (n >> 2)) * 1024; };
  const unsigned sk = s + 2;
  const unsigned sok = a.kbase + (sk + (sk > a.n1 ? sk - a.n1 : 0)) * 16384, sov = a.vbase + (s + (s > a.n1 ? s - a.n1 : 0)) * 16384;
  const unsigned mk = (sk < a.n1k || sk > 16) ? 0xFFFFFC00u : 0u, mv = s < a.n1k ? 0xFFFFFC00u : 0u;
  unsigned vo = 0;
  const unsigned dk = a.ldsb + X * 32768 + a.w * 4096, dv = dk + 65536;
  static_for<16>([&](auto M_) AI {
    constexpr int M = decltype(M_)::v, d = M >> 1, kb = M & 1;
    if constexpr (QK) {
      if constexpr (d == 0) a.S[X][kb] = MFMA32(a.fr[M & 7], a.qf[d], a.negm);
      else a.S[X][kb] = MFMA32(a.fr[M & 7], a.qf[d], a.S[X][kb]);
      if constexpr (RD0[M] >= 0) a.fr[RD0[M] & 7] = lds_b128(kaddr(X, RD0[M]));
      if constexpr (RD1[M] >= 0) a.fr[RD1[M] & 7] = lds_b128(kaddr(X, RD1[M]));
      if constexpr (M == 0) vo = a.voff | mv;
      if constexpr (VDMA && (M == 0 || M == 2 || M == 6 || M == 9)) { constexpr int P = M == 0 ? 0 : M == 2 ? 1 : M == 6 ? 2 : 3; dma16c(dv + P * 1024, vo, c.rws, sov + P * 1024); }
    }
    if constexpr (PV) {
      if constexpr (M < 8) {
        a.S[Y][1][2 * M] = __builtin_amdgcn_exp2f(a.S[Y][1][2 * M]);
        a.S[Y][1][2 * M + 1] = __builtin_amdgcn_exp2f(a.S[Y][1][2 * M + 1]);
        a.pk[M >> 2][M & 3] = cvtpk(a.S[Y][0][2 * M], a.S[Y][0][2 * M + 1]);
        PIN(a.pk[M >> 2][M & 3]);
        if constexpr (M == 0) { a.l0 = fadd_s(a.l0, a.S[Y][0][14]); a.l1 = fadd_s(a.l1, a.S[Y][0][15]); }
      } else {
        constexpr int e = M - 8;
        a.pk[2 + (e >> 2)][e & 3] = cvtpk(a.S[Y][1][2 * e], a.S[Y][1][2 * e + 1]);
        PIN(a.pk[2 + (e >> 2)][e & 3]);
        a.l0 = fadd_s(a.l0, a.S[Y][1][2 * e]); a.l1 = fadd_s(a.l1, a.S[Y][1][2 * e + 1]);
      }
    }
    if constexpr (M == 11) { SB(); if constexpr (QK) wait_vm<9>(); else wait_vm<0>(); hook(IC<0>{}); bar(); hook(IC<1>{}); SB(); }
    if constexpr (PV && M >= 12) a.fr[(M - 12) & 7] = lds_b128(vaddr(Y, M - 12));
    SB();
  });
  static_for<16>([&](auto M_) AI {
    constexpr int M = decltype(M_)::v, ks = M >> 2, db = M & 3;
    if constexpr (PV) {
      a.o[db] = MFMA32(a.fr[M & 7], asbf(a.pk[ks]), a.o[db]);
      if constexpr (RD0[M] >= 0) a.fr[RD0[M] & 7] = lds_b128(vaddr(Y, RD0[M]));
      if constexpr (RD1[M] >= 0) a.fr[RD1[M] & 7] = lds_b128(vaddr(Y, RD1[M]));
    }
    if constexpr (QK) {
      if constexpr (M == 0) vo = a.voff | mk;
      if constexpr (M == 0 || M == 1 || M == 6 || M == 9) { constexpr int P = M == 0 ? 0 : M == 1 ? 1 : M == 6 ? 2 : 3; dma16c(dk + P * 1024, vo, c.rws, sok + P * 1024); }
      if constexpr (M == 2) {
        asm volatile("s_nop 7\n\ts_nop 3" : "+v"(a.S[X][0]), "+v"(a.S[X][1]));
        if (__builtin_expect(diag, 0)) {
          float ninf;
          asm volatile("v_mov_b32 %0, 0xff800000" : "=v"(ninf));
          const int dq2 = dq + (s + 2 == a.n1k ? 64 : 0);
          mask_rows<0>(a.S[X][0], dq2, ninf, make_iseq<16>{}); mask_rows<32>(a.S[X][1], dq2, ninf, make_iseq<16>{});
        }
      }
      if constexpr (M >= 2 && M < 6) {
        constexpr int r = 4 * (M - 2);
        if constexpr (M == 2) {
          a.mx0 = max3f(a.S[X][0][0], a.S[X][0][1], a.S[X][1][0]); a.mx1 = max3f(a.S[X][0][2], a.S[X][0][3], a.S[X][1][1]);
          a.mx0 = max3f(a.mx0, a.S[X][1][2], a.S[X][1][3]);
        } else {
          a.mx0 = max3f(a.mx0, a.S[X][0][r], a.S[X][0][r + 1]); a.mx1 = max3f(a.mx1, a.S[X][0][r + 2], a.S[X][0][r + 3]);
          a.mx0 = max3f(a.mx0, a.S[X][1][r], a.S[X][1][r + 1]); a.mx1 = max3f(a.mx1, a.S[X][1][r + 2], a.S[X][1][r + 3]);
        }
      }
      if constexpr (M == 6) {
        const float mm = max2f(a.mx0, a.mx1);
        auto pr = __builtin_amdgcn_permlane32_swap(__float_as_uint(mm), __float_as_uint(mm), false, false);
        a.mx0 = max2f(__uint_as_float(pr[0]), __uint_as_float(pr[1]));
      }
      if constexpr (M == 7) {
        if (__builtin_expect(first || __any(a.mx0 > THR), 0)) {
          const float dl = first ? fmaxf(a.mx0, -1e30f) : fmaxf(a.mx0, 0.f);
          a.mhat = first ? dl : a.mhat + dl;
          a.fct = __builtin_amdgcn_exp2f(-dl);
#pragma unroll
          for (int r = 0; r < 16; ++r) { a.S[X][0][r] -= dl; a.S[X][1][r] -= dl; a.negm[r] = -a.mhat; }
          a.pend = !first;
        }
      }
      if constexpr (M >= 8) {
        constexpr int e = M - 8;
        a.S[X][0][2 * e] = __builtin_amdgcn_exp2f(a.S[X][0][2 * e]);
        a.S[X][0][2 * e + 1] = __builtin_amdgcn_exp2f(a.S[X][0][2 * e + 1]);
        PIN(a.S[X][0][2 * e]); PIN(a.S[X][0][2 * e + 1]);
        if constexpr (e > 0) {
          a.t0 = e == 1 ? a.S[X][0][0] : fadd_s(a.t0, a.S[X][0][2 * e - 2]); a.t1 = e == 1 ? a.S[X][0][1] : fadd_s(a.t1, a.S[X][0][2 * e - 1]);
          PIN(a.t0); PIN(a.t1);
        }
      }
      if constexpr (M == 11) { SB(); wait_vm<PV ? 9 : 8>(); bar(); }
      if constexpr (M == 13) { asm volatile("s_mov_b64 exec, %2\n\tglobal_load_dword %0, %1, off\n\ts_mov_b64 exec, -1" : "+v"(a.dummy) : "v"(a.ta), "s"(a.tm) : "memory"); a.tm <<= 4; }
      if constexpr (M >= 12) a.fr[(M - 12) & 7] = lds_b128(kaddr(Y, M - 12));
    }
    SB();
  });
}

constexpr unsigned WP = 288;
constexpr unsigned LSYNC = 161280;
constexpr unsigned C_LAND = 0, C_FREE = 64, C_GB = 128, C_CTXD = 136, C_MRG = 140, C_TICK = 144, LSTAT = LSYNC + 256;
#define ARRV 2
constexpr int XM = 12;
constexpr int XDEP(int NK) { return NK == 16 ? XM : 8; }
DI unsigned land(unsigned ph, unsigned q) { return LSYNC + C_LAND + (ph * 5 + q) * 4; }
DI unsigned fre(unsigned ph, unsigned q) { return LSYNC + C_FREE + (ph * 5 + q) * 4; }
DI void cnt_add(Ctx& c, unsigned off) {
  asm volatile("s_mov_b64 exec, 1\n\tds_add_u32 %0, %1\n\ts_mov_b64 exec, -1" ::"v"(c.ldsb + off), "v"(1u) : "memory");
}
#define wlog(...)
DI unsigned cnt_add_rtn(Ctx& c, unsigned off) {
  unsigned r;
  asm volatile("s_mov_b64 exec, 1\n\tds_add_rtn_u32 %0, %1, %2\n\ts_mov_b64 exec, -1\n\ts_waitcnt lgkmcnt(0)" : "=&v"(r) : "v"(c.ldsb + off), "v"(1u) : "memory");
  return rfl(r);
}
DI unsigned cnt_read(Ctx& c, unsigned off) { return __hip_atomic_load((LAS unsigned*)(size_t)(c.ldsb + off), __ATOMIC_RELAXED, __HIP_MEMORY_SCOPE_WORKGROUP); }
DI void prio_lo() { __builtin_amdgcn_s_setprio(0); }
DI void prio_hi(Ctx& c) { __builtin_amdgcn_s_setprio(3); }
DI void prio_gemm(Ctx& c, bool behind) {
  if (c.w >= 4) __builtin_amdgcn_s_setprio(1); else if (behind) __builtin_amdgcn_s_setprio(0); else __builtin_amdgcn_s_setprio(2);
}
DI void cnt_wait(Ctx& c, unsigned off, unsigned target) {
  asm volatile("" ::: "memory");
  unsigned spins = 0;
#pragma unroll 1
  while (rfl(cnt_read(c, off)) < target) { __builtin_amdgcn_s_sleep(1); if (++spins > (1u << 20)) break; }
  asm volatile("" ::: "memory");
}
DI void flag_w(Ctx& c, unsigned flag_off) {
  wait_vm<0>();
  fput(u32x2{c.ep0, c.ep1}, c.rws, c.lane == 0 ? flag_off : OOB);
}
DI void dma16m(unsigned lds_addr, unsigned voff, rsrc_t r, unsigned soff, u64 mask) {
  asm volatile("s_mov_b32 m0, %0\n\ts_mov_b64 exec, %4\n\ts_nop 3\n\tbuffer_load_dwordx4 %1, %2, %3 offen lds\n\ts_mov_b64 exec, -1" ::"s"(lds_addr), "v"(voff), "s"(r), "s"(soff), "s"(mask)
               : "memory", "m0");
}
struct WSrc { unsigned voff[9]; rsrc_t r; unsigned main, tail, pitch, colA, colB; unsigned idx, pend, ph; };
DI unsigned w_voff(const WSrc& s, unsigned lane, int p) {
  const unsigned ci = lane + 64 * p, rr = __umul24(ci, 3641) >> 16, cc = ci - __umul24(rr, 18);
  return __umul24(rr, s.pitch - WP) + ci * 16 + (cc < 16 ? s.colA : s.colB - 256);
}
DI unsigned opaque_lane() {
  unsigned lane;
  asm volatile("v_mbcnt_lo_u32_b32 %0, -1, 0\n\tv_mbcnt_hi_u32_b32 %0, -1, %0" : "=v"(lane));
  return lane;
}
template <int NV = 5> DI void wsrc_voff(Ctx& c, WSrc& s) {
  const unsigned lane = opaque_lane();
#pragma unroll
  for (int p = 0; p < NV; ++p) s.voff[p] = w_voff(s, lane, p);
  if constexpr (NV == 5) {
    s.voff[5] = lane >= 16 ? ~0u : 0u;
    const unsigned l7 = (lane & 7) < 5 ? lane & 7 : 0;
    s.voff[6] = c.ldsb + (lane < 8 ? LSYNC + C_TICK + (s.ph * 5 + l7) * 4 : fre(s.ph - 1, 4) - 4 * l7);
    s.voff[7] = lane == 0 ? 3 : 8;
  }
}
template <int NV = 0> DI void wsrc_setup(Ctx& c, WSrc& s, rsrc_t r, unsigned pitch, unsigned colA, unsigned colB, unsigned rowMain, unsigned rowTail, unsigned ph) {
  s.r = r; s.pitch = pitch; s.main = rowMain * pitch; s.tail = rowTail * pitch;
  s.colA = colA; s.colB = colB; s.ph = ph; s.idx = s.pend = 0;
  if constexpr (NV > 0) wsrc_voff<NV>(c, s);
}
DI void w_group(Ctx& c, const WSrc& s, unsigned q) {
  const unsigned r0 = 128 * q + 32 * (c.w & 3);
#pragma unroll
  for (int p = 0; p < 9; ++p) dma16(c.ldsb + r0 * WP + p * 1024, s.voff[p], s.r, s.main + r0 * s.pitch);
}
DI void poll_w(Ctx& c, unsigned flag_off, unsigned n) {
  prio_lo();
  poll_flags(c, flag_off, n, 1);
  prio_hi(c);
}
constexpr unsigned nitems(unsigned q) { return q == 4 ? 3 : 8; }
DI bool w_try(Ctx& c, WSrc& s) {
  if (s.idx) return false;
  unsigned idx, t;
#pragma unroll 1
  for (;;) {
    const unsigned v = __hip_atomic_load((LAS unsigned*)(size_t)s.voff[6], __ATOMIC_RELAXED, __HIP_MEMORY_SCOPE_WORKGROUP);
    const unsigned m = (unsigned)__builtin_amdgcn_ballot_w64(v < s.voff[7]);
    if ((m & 31) == 0) s.idx = 1;
    const unsigned av = m & ~(m >> 8) & 31;
    if (av == 0) return false;
    idx = __builtin_ctz(av);
    t = cnt_add_rtn(c, LSYNC + C_TICK + (s.ph * 5 + idx) * 4);
    if (t < (idx ? 8u : 3u)) break;
  }
  s.pend += 1u << (4 * idx);
  wlog(c, s.ph, 1, idx, true); wlog(c, s.ph, 2, idx);
  const unsigned lrow = 128 * (4 - idx) + 16 * t, so = (idx ? s.main + lrow * s.pitch : s.tail + 16 * t * s.pitch), sk = lrow == 544 ? 8 * s.pitch : 0u;
  const unsigned la = c.ldsb + lrow * WP;
  dma16(la, s.voff[0], s.r, so);
  dma16(la + 1024, s.voff[1], s.r, so);
  dma16(la + 2048, s.voff[2] + (s.voff[5] & sk), s.r, so);
  dma16(la + 3072, s.voff[3], s.r, so + sk);
  dma16m(la + 4096, s.voff[4], s.r, so + sk, 0xffffffffull);
  return true;
}
DI void w_signal(Ctx& c, const WSrc& s, unsigned items) {
  if (items) {
    unsigned lane;
    asm volatile("v_mbcnt_lo_u32_b32 %0, -1, 0" : "=v"(lane));
    const unsigned n = (items >> (4 * lane)) & 15, ad = c.ldsb + land(s.ph, 4) - 4 * lane;
    asm volatile("s_mov_b64 exec, 31\n\tds_add_u32 %0, %1\n\ts_mov_b64 exec, -1" ::"v"(ad), "v"(n) : "memory");
  }
}
DI void w_flush(Ctx& c, WSrc& s) { w_signal(c, s, s.pend); s.pend = 0; }
#define WMAX 2
#define FMAX 0
#define NMAX WMAX
#define LSRV 7
template <unsigned MX = WMAX> DI void w_try_wait(Ctx& c, WSrc& s, bool en = true) {
  const unsigned old = s.pend;
  unsigned k = 0;
  s.pend = 0;
#pragma unroll 1
  while (en && k < MX && w_try(c, s)) ++k;
  if (k == 0) wait_vm<0>(); else if (k == 1) wait_vm<5>(); else if (k == 2) wait_vm<10>(); else if (k == 3) wait_vm<15>(); else wait_vm<20>();
  w_signal(c, s, old);
}
DI void w_need(Ctx& c, WSrc& s, unsigned q, unsigned n) {
  unsigned spins = 0;
  wsrc_voff(c, s);
#pragma unroll 1
  for (;;) {
    w_try_wait<NMAX>(c, s);
    if (s.pend) { wait_vm<0>(); w_flush(c, s); }
    if (rfl(cnt_read(c, land(s.ph, q))) >= n || ++spins > (1u << 20)) break;
    __builtin_amdgcn_s_sleep(1);
  }
  asm volatile("" ::: "memory");
}
DI void flag_srv(Ctx& c, WSrc& s, unsigned flag_off, bool lsrv) {
  w_try_wait<FMAX>(c, s, lsrv || c.w < 4);
  fput(u32x2{c.ep0, c.ep1}, c.rws, c.lane == 0 ? flag_off : OOB);
}
DI void poll_srv(Ctx& c, WSrc& s, unsigned flag_off, unsigned n, bool lsrv) {
  const unsigned voff = c.lane < n ? flag_off + c.lane * 8 : OOB;
  const u64 t0 = __builtin_amdgcn_s_memrealtime();
  prio_lo();
  for (;;) {
    u32x2 f;
    fget(f, c.rws, voff);
    if (s.pend) { wait_vm<1>(); w_flush(c, s); }
    w_try_wait(c, s, lsrv || c.w < 4);
    PIN(f);
    if (__all((f[0] == c.ep0 && f[1] == c.ep1) || c.lane >= n)) break;
    if (__builtin_amdgcn_s_memrealtime() - t0 > 5000000ull) break;
    if (s.pend == 0) __builtin_amdgcn_s_sleep(1);
  }
  prio_hi(c);
}
constexpr unsigned LB1 = LSTAT + 256, LB2 = LB1 + 320;
template <int P> DI void park_load(Ctx& c, const u16* w1, const u16* b1, const u16* w2, const u16* b2) {
  const unsigned xcd = c.b & 7, nt = (c.b >> 3) & 3, nt32 = xcd * 4 + nt, w = (c.w & 3) * 2 + (P & 1);
  const unsigned rr = c.lane >= 18 ? 1 : 0, cc = c.lane - 18 * rr, r0 = (w < 4 ? 40 : 48) + 2 * w;
  const unsigned c1 = cc < 16 ? nt32 * 256 + cc * 16 : 8192 + nt32 * 32 + (cc - 16) * 16, c2 = cc < 16 ? nt * 256 + cc * 16 : 1024 + nt * 32 + (cc - 16) * 16;
  if constexpr (P < 2) dma16m(c.ldsb + (512 + 2 * w) * WP, rr * (FF * 2) + c1, mk_rsrc(w1, DM * FF * 2), (512 + r0) * (FF * 2), 0xfffffffffull);
  else if constexpr (P < 4) dma16m(c.ldsb + (528 + 2 * w) * WP, rr * (DM * 2) + c2, mk_rsrc(w2, FF * DM * 2), (4096 + 64 * xcd + r0) * (DM * 2), 0xfffffffffull);
  else {
    const bool f = c.w == 4;
    dma16m(c.ldsb + (f ? LB1 : LB2 + (c.w - 5) * 384), f ? c1 : c.lane * 16, mk_rsrc(f ? b1 : b2, f ? FF * 2 : DM * 2), f ? 0 : (c.w - 5) * 384, f ? 0x3ffffull : 0xffffffull);
  }
}
DI void park_read(Ctx& c, u32x2 (&tl)[9], u32x2 (&tl2)[9]) {
  const unsigned ad = c.ldsb + (512 + (c.lane >> 2)) * WP + (c.lane & 3) * 8;
#pragma unroll
  for (int a = 0; a < 9; ++a) { tl[a] = __builtin_bit_cast(u32x2, lds_tr(ad + a * 32)); tl2[a] = __builtin_bit_cast(u32x2, lds_tr(ad + 16 * WP + a * 32)); }
  SB();
  cnt_add(c, fre(0, 4)); wlog(c, 1, 0, 0);
#pragma unroll
  for (int a = 0; a < 9; ++a) { PIN(tl[a]); PIN(tl2[a]); }
}
constexpr int KK(int NK, int v) { return NK == 18 ? (v < 2 ? 16 + v : 4 * (3 - (v - 2) / 4) + (v - 2) % 4) : 4 * (3 - v / 4) + v % 4; }
constexpr bool QSTART(int NK, int v) { return v < NK && v > 0 && (KK(NK, v) & 3) == 0; }
constexpr bool QEND(int NK, int v) { return (KK(NK, v) & 3) == 3 || KK(NK, v) == 17; }
template <int NK, int V> DI void x_ld(Ctx& c, u32x4 (&x)[2], unsigned voff, unsigned so, unsigned so2) {
  constexpr int k = KK(NK, V);
  gld16c2<(k & 1) * 2048>(x[0], x[1], voff, c.rws, k < 16 ? so + (k >> 1) * 4096 : so2);
}
template <int NK, int V0 = 0, int NV = 8> DI void x_pro(Ctx& c, u32x4 (&x)[XM][2], unsigned voff, unsigned so, unsigned so2) {
  static_for<NV>([&](auto V_) AI { x_ld<NK, V0 + decltype(V_)::v>(c, x[V0 + decltype(V_)::v], voff, so, so2); });
}
DI void x_ld4_masked(u32x4& a, u32x4& b, u32x4& d, u32x4& e, unsigned voff, rsrc_t r, unsigned so, unsigned mask) {
  asm volatile("s_cmp_eq_u32 %7, 0\n\ts_cbranch_scc1 .Lxs%=\n\ts_nop 4\n\tbuffer_load_dwordx4 %0, %4, %5, %6 offen sc1\n\tbuffer_load_dwordx4 %1, %4, %5, %6 offen offset:256 sc1\n\t"
               "buffer_load_dwordx4 %2, %4, %5, %6 offen offset:2048 sc1\n\tbuffer_load_dwordx4 %3, %4, %5, %6 offen offset:2304 sc1\n.Lxs%=:"
               : "+v"(a), "+v"(b), "+v"(d), "+v"(e) : "v"(voff), "s"(r), "s"(so), "s"(mask) : "memory", "scc");
}
template <int IMM> DI void x_ld2_if(u32x4& a, u32x4& b, unsigned voff, rsrc_t r, unsigned so, unsigned skip) {
  asm volatile("s_cmp_lg_u32 %5, 0\n\ts_cbranch_scc1 .Lxi%=\n\ts_nop 4\n\tbuffer_load_dwordx4 %0, %2, %3, %4 offen offset:%6 sc1\n\tbuffer_load_dwordx4 %1, %2, %3, %4 offen offset:%7 sc1\n.Lxi%=:"
               : "=&v"(a), "=&v"(b) : "v"(voff), "s"(r), "s"(so), "s"(skip), "i"(IMM), "i"(IMM + 256) : "memory", "scc");
}
template <int V> DI void x_ld16_if(Ctx& c, u32x4 (&x)[2], unsigned voff, unsigned so, unsigned skip) {
  constexpr int k = KK(16, V);
  x_ld2_if<(k & 1) * 2048>(x[0], x[1], voff, c.rws, so + (k >> 1) * 4096, skip);
}
DI void dma16_if(unsigned lds_addr, unsigned voff, rsrc_t r, unsigned soff, unsigned go) {
  asm volatile("s_cmp_eq_u32 %4, 0\n\ts_cbranch_scc1 .Ldi%=\n\ts_mov_b32 m0, %0\n\ts_nop 3\n\tbuffer_load_dwordx4 %1, %2, %3 offen lds\n.Ldi%=:" ::"s"(lds_addr), "v"(voff), "s"(r), "s"(soff), "s"(go)
               : "memory", "m0", "scc");
}
DI void x_pro_masked16(Ctx& c, u32x4 (&x)[XM][2], unsigned so, unsigned mask) {
  const unsigned voff = x_voff(c);
#pragma unroll
  for (int p = 0; p < XM / 2; ++p) x_ld4_masked(x[2 * p][0], x[2 * p][1], x[2 * p + 1][0], x[2 * p + 1][1], voff, c.rws, so + (KK(16, 2 * p) >> 1) * 4096, mask);
}
template <int L> DI void wlane(unsigned& v, unsigned s) { asm volatile("v_writelane_b32 %0, %1, %2" : "+v"(v) : "s"(s), "i"(L)); }
struct Sched {
  int NK, XD;
  constexpr int T0() const { return 9 * (NK - 4); }
  constexpr int sv(int n) const { return n < T0() ? n / 9 : NK - 4 + (n - T0()) % 4; }
  constexpr int sa(int n) const { return n < T0() ? n % 9 : ((n - T0()) / 4 == 0 ? 8 : (n - T0()) / 4 - 1); }
  constexpr int g0(int G) const { return G == 3 ? T0() + 8 : 9 * (NK - 16 + 1 + 4 * G) + 1; }
  constexpr int gn(int G) const { return G == 0 ? 4 : 9; }
  constexpr int wg(int n) const { for (int G = 0; G < 4; ++G) if (n >= g0(G) && (n - g0(G)) % 3 == 0 && (n - g0(G)) / 3 < gn(G)) return G; return -1; }
  constexpr int wp(int n) const { return (n - g0(wg(n))) / 3; }
  constexpr int gsig(int n) const { for (int G = 0; G < 3; ++G) if (n == g0(G) + 3 * (gn(G) - 1) + 27) return G; return -1; }
  constexpr int st(int n) const {
    const int m = n - T0();
    if (m == 6) return 0;
    if (m >= 13 && (m - 13) % 8 == 0) return 1 + 2 * ((m - 13) / 8);
    if (m >= 15 && (m - 15) % 8 == 0) return 2 + 2 * ((m - 15) / 8);
    return -1;
  }
  constexpr bool sp(int n) const { return n < T0() ? (n % 18 == 0 && n >= 18) : ((n - T0()) % 18 == 0); }
  constexpr int between(int n0, int n1) const {
    int cnt = 0;
    for (int i = n0; i < n1; ++i) { if (i < T0() && sa(i) == 8 && sv(i) + XD < NK) cnt += 2; if (st(i) >= 0) cnt += 1; }
    return cnt;
  }
  constexpr int younger(int tag, int n, bool ldr) const {
    int cnt = 0, last = -1;
    for (int v = 0; v < XD; ++v) { if (100 + v == tag) last = cnt + 1; cnt += 2; }
    for (int i = 0; i < n; ++i) {
      if (i < T0() && sa(i) == 8 && sv(i) + XD < NK) { if (100 + sv(i) + XD == tag) last = cnt + 1; cnt += 2; }
      if (ldr && wg(i) >= 0) { if (200 + wg(i) == tag) last = cnt; cnt += 1; }
      if (st(i) >= 0) cnt += 1;
    }
    return cnt - 1 - last;
  }
};
template <int NK, int KORD, int PH, int GST, class St>
DI void gemm_g(Ctx& c, f32x4 (&acc)[2][9], u32x4 (&x)[XM][2], u32x2 (&tl)[9], u32x2 (&tl2)[9], WSrc& ws, WSrc& wn, unsigned so, unsigned so2, unsigned flag_off, St&& st) {
  constexpr unsigned RT = KORD == 0 ? 16 : 8;
  constexpr int LA = 6, NS = NK * 9;
  constexpr int XD = XDEP(NK);
  constexpr Sched S{NK, XD};
  constexpr bool LD = PH < 2;
  const unsigned g = c.lane >> 4, q = (c.lane & 15) >> 2, pp = c.lane & 3;
  const unsigned wrow1 = 16 * (g >> 1) + 4 * (g & 1) + q, wrow = KORD == 0 ? 4 * g + q : wrow1;
  unsigned wb[3], wt[2];
#pragma unroll
  for (int i = 0; i < 3; ++i) { wb[i] = c.ldsb + wrow * WP + pp * 8 + i * 7 * 32 * WP; PIN(wb[i]); }
  if constexpr (NK == 18) {
    wt[0] = c.ldsb + (512 + wrow1) * WP + pp * 8; wt[1] = c.ldsb + (544 + wrow1 - 8 * (g >> 1)) * WP + pp * 8;
    PIN(wt[0]); PIN(wt[1]);
  }
  unsigned voff = x_voff(c); PIN(voff);
  bf16x4 wl[8][2];
  auto rdw = [&](auto N_, auto H_) AI {
    constexpr int N = decltype(N_)::v, H = decltype(H_)::v, v = S.sv(N), a = S.sa(N), k = KK(NK, v);
    if constexpr (k < 16) wl[N & 7][H] = lds_tr(wb[k / 7] + ((k % 7) * 32 + H * RT) * WP + a * 32);
    else if constexpr (k == 16) wl[N & 7][H] = lds_tr(wt[0] + H * 8 * WP + a * 32);
    else if constexpr (H == 0) wl[N & 7][H] = lds_tr(wt[1] + a * 32);
    else {
      u32x2 t;
      asm volatile("v_mov_b64 %0, %1" : "=v"(t) : "v"(tl[a]));
      wl[N & 7][1] = __builtin_bit_cast(bf16x4, t);
    }
    if constexpr (LD && H == 1 && ((N < S.T0() && a == 8 && QEND(NK, v)) || N == NS - 1)) { SB(); cnt_add(c, fre(PH, k >> 2)); wlog(c, PH + 1, 0, 4 - (k >> 2)); }
  };
  bool behind = false;
  if constexpr (PH > 0) {
    if (ws.pend) { wait_vm<2 * XD>(); w_flush(c, ws); }
    const unsigned l4 = cnt_read(c, land(PH, 4)), f0 = cnt_read(c, fre(PH - 1, 0));
    behind = (c.w < 4) & (rfl(f0) < 8);
    if (__builtin_expect(rfl(l4) < 3, 0)) w_need(c, ws, 4, 3);
  }
  prio_gemm(c, behind);
  static_for<LA>([&](auto N) AI { rdw(N, IC<0>{}); rdw(N, IC<1>{}); });
  unsigned lc = 0;
  SB();
  static_for<NS>([&](auto N_) AI {
    constexpr int N = decltype(N_)::v, v = S.sv(N), a = S.sa(N);
    if constexpr (N < S.T0() ? a == 0 : N < S.T0() + 4) {
      wait_vm<S.younger(100 + v, N, false)>();
      PIN(x[v % XD][0]); PIN(x[v % XD][1]);
    }
    if constexpr (N < S.T0() && a == 0) {
      if constexpr (v == 0 || v == 8) STAMPG(GST + (v >> 3));
      if constexpr (PH == 0 && v == ARRV) { if (c.w < 4) { wait_vm<S.between(0, N)>(); cnt_add(c, land(0, 2)); } }
      constexpr bool chk2 = QSTART(NK, v + 2), chk1 = QSTART(NK, v + 1);
      if constexpr (chk1) {
        if (__builtin_expect(rfl(lc) < (PH > 0 ? 8 : 4), 0)) { if constexpr (PH > 0) { prio_hi(c); w_need(c, ws, KK(NK, v + 1) >> 2, 8); prio_gemm(c, behind); } else cnt_wait(c, land(0, KK(NK, v + 1) >> 2), 4); }
      }
      if constexpr (PH == 0 && v == 7) park_read(c, tl, tl2);
      if constexpr (chk2) lc = cnt_read(c, land(PH, KK(NK, v + 2) >> 2));
    }
    if constexpr (PH > 0 && S.sp(N) && N < S.T0()) {
      if (__builtin_expect(behind, 0)) { behind = rfl(cnt_read(c, fre(PH - 1, 0))) < 8; prio_gemm(c, behind); }
    }
    SB();
    const bf16x8 wf = cat(wl[N & 7][0], wl[N & 7][1]);
    acc[0][a] = MFMA16(wf, asbf(x[v % XD][0]), acc[0][a]);
    SB();
    if constexpr (N + LA < NS) rdw(IC<N + LA>{}, IC<0>{});
    SB();
    acc[1][a] = MFMA16(wf, asbf(x[v % XD][1]), acc[1][a]);
    SB();
    if constexpr (N + LA < NS) rdw(IC<N + LA>{}, IC<1>{});
    if constexpr (N < S.T0() && a == 8 && v + XD < NK) x_ld<NK, v + XD>(c, x[v % XD], voff, so, so2);
    if constexpr (S.st(N) >= 0) st(IC<S.st(N)>{});
    SB();
  });
  prio_hi(c);
  st(IC<7>{}); st(IC<8>{});
  STAMPG(GST + 2);
  if constexpr (LD) { wsrc_voff(c, wn); flag_srv(c, wn, flag_off, LSRV & (PH ? 4 : 1)); } else flag_w(c, flag_off);
}
DI f32x4 relu2(const f32x4& a) {
  const float inf = 3.4028234664e38f;
  const float r0 = __builtin_amdgcn_fmed3f(a[0], 0.f, inf), r1 = __builtin_amdgcn_fmed3f(a[1], 0.f, inf), r2 = __builtin_amdgcn_fmed3f(a[2], 0.f, inf), r3 = __builtin_amdgcn_fmed3f(a[3], 0.f, inf);
  return f32x4{r0 * r0, r1 * r1, r2 * r2, r3 * r3};
}
DI u32x4 pack_single(const f32x4& r0, const f32x4& r1) {
  const auto a = __builtin_amdgcn_permlane32_swap(cvtpk(r0[0], r0[1]), cvtpk(r1[0], r1[1]), false, false);
  const auto b = __builtin_amdgcn_permlane32_swap(cvtpk(r0[2], r0[3]), cvtpk(r1[2], r1[3]), false, false);
  return u32x4{a[0], b[0], a[1], b[1]};
}
template <int I> DI void part_store(Ctx& c, const f32x4 (&acc)[2][9], unsigned base, unsigned so) {
  const unsigned g = c.lane >> 4;
  if constexpr (I == 0) gst16c(pack_single(acc[0][8], acc[1][8]), c.rws, (g >> 1) * (4608 - 128) + base + 1024, so);
  else { constexpr int i = (I - 1) >> 1, u = (I - 1) & 1; gst16c(pack_pair(acc[u][2 * i], acc[u][2 * i + 1]), c.rws, base, so + u * 4608 + i * 256); }
}
DI unsigned part_base(Ctx& c) { const unsigned l15 = c.lane & 15; return (l15 >> 2) * 1152 + (l15 & 3) * 16 + (c.lane >> 4) * 64; }
struct Red { u32x4 p[8]; u32x4 s; };
DI void red_load(Ctx& c, Red& d, unsigned so) {
  gld16c<1024>(d.s, (c.lane >> 3) * 36864 + (c.lane & 7) * 16, c.rws, so);
#pragma unroll
  for (int s = 0; s < 8; ++s) gld16c<0>(d.p[s], c.lane * 16, c.rws, so + s * 36864);
}
#define RED_PIN(d) asm volatile("s_waitcnt vmcnt(0)" : "+v"(d.p[0]), "+v"(d.p[1]), "+v"(d.p[2]), "+v"(d.p[3]), "+v"(d.p[4]), "+v"(d.p[5]), "+v"(d.p[6]), "+v"(d.p[7]), "+v"(d.s)::"memory")
DI void gld8(u32x2& d, unsigned voff, rsrc_t r, unsigned soff) { asm volatile("s_nop 4\n\tbuffer_load_dwordx2 %0, %1, %2, %3 offen" : "=v"(d) : "v"(voff), "s"(r), "s"(soff) : "memory"); }
DI void job_offs(Ctx& c, unsigned row, unsigned& o0, unsigned& o2) {
  const unsigned pr = c.lane >> 4, g = (c.lane >> 2) & 3, nt = c.w & 3;
  o0 = row * (DM * 2) + (nt * 128 + pr * 32 + 4 * g) * 2;
  o2 = c.lane < 8 ? row * (DM * 2) + (512 + nt * 16 + 4 * g) * 2 : OOB;
}
DI void row_load(Ctx& c, u32x2 (&r)[4], rsrc_t rs, unsigned row) {
  unsigned o0, o2; job_offs(c, row, o0, o2);
  gld8(r[0], o0, rs, 0); gld8(r[1], o0, rs, 32); gld8(r[2], o2, rs, 0); gld8(r[3], o2, rs, 16);
}
DI float sum_hi3(float x) {
  x += __uint_as_float(__builtin_amdgcn_update_dpp(0, __float_as_uint(x), 0x128, 0xf, 0xf, false));
  { auto r = __builtin_amdgcn_permlane16_swap(__float_as_uint(x), __float_as_uint(x), false, false); x = __uint_as_float(r[0]) + __uint_as_float(r[1]); }
  return xhalf_sum(x);
}
DI void red_sum(const Red& d, float (&a)[16]) {
#pragma unroll
  for (int e = 0; e < 8; ++e) a[e] = 0.f;
#pragma unroll
  for (int s = 0; s < 8; ++s) {
#pragma unroll
    for (int e = 0; e < 4; ++e) { a[2 * e] += bflo(d.p[s][e]); a[2 * e + 1] += bfhi(d.p[s][e]); }
  }
#pragma unroll
  for (int e = 0; e < 4; ++e) { a[8 + 2 * e] = sum_hi3(bflo(d.s[e])); a[9 + 2 * e] = sum_hi3(bfhi(d.s[e])); }
}
DI float sum_tok4(float x) {
  x += __uint_as_float(__builtin_amdgcn_update_dpp(0, __float_as_uint(x), 0x124, 0xf, 0xf, false));
  x += __uint_as_float(__builtin_amdgcn_update_dpp(0, __float_as_uint(x), 0x128, 0xf, 0xf, false));
  { auto r = __builtin_amdgcn_permlane16_swap(__float_as_uint(x), __float_as_uint(x), false, false); x = __uint_as_float(r[0]) + __uint_as_float(r[1]); }
  return xhalf_sum(x);
}
DI void ln2_job(Ctx& c, const Red& d, const u32x2 (&xr)[4], unsigned (&hw)[8], unsigned row0) {
  const unsigned lane = c.lane, tok = lane & 3, g = (lane >> 2) & 3, pr = lane >> 4, nt = c.w & 3;
  const unsigned stat = c.ldsb + LSTAT + (c.w >> 2) * 128;
  const bool sing = lane < 8;
  float a[16], v[16];
  red_sum(d, a);
#pragma unroll
  for (int e = 0; e < 8; ++e) {
    const unsigned xx = xr[e >> 1][e & 1];
    hw[e] = cvtpk(bflo(xx) + a[2 * e], bfhi(xx) + a[2 * e + 1]); v[2 * e] = bflo(hw[e]); v[2 * e + 1] = bfhi(hw[e]);
  }
  float s1 = 0.f, s0 = 0.f;
#pragma unroll
  for (int e = 0; e < 8; ++e) { s1 += v[e]; s0 += v[8 + e]; }
  const float mw = sum_tok4(sing ? s1 + s0 : s1) * (1.f / 144);
  float s2 = 0.f, s3 = 0.f;
#pragma unroll
  for (int e = 0; e < 8; ++e) { const float t = v[e] - mw, t2 = v[8 + e] - mw; s2 += t * t; s3 += t2 * t2; }
  s2 = sum_tok4(sing ? s2 + s3 : s2);
  if (lane < 4) *(LAS f32x2*)(size_t)(stat + (nt * 4 + lane) * 8) = f32x2{mw, s2};
  SB();
  cnt_add(c, LSYNC + C_GB + (c.w >> 2) * 4);
  cnt_wait(c, LSYNC + C_GB + (c.w >> 2) * 4, 4);
  f32x2 st[4];
#pragma unroll
  for (int i = 0; i < 4; ++i) st[i] = *(LAS f32x2*)(size_t)(stat + (i * 4 + tok) * 8);
  const float mean = (st[0][0] + st[1][0] + st[2][0] + st[3][0]) * 0.25f;
  float m2 = st[0][1] + st[1][1] + st[2][1] + st[3][1];
#pragma unroll
  for (int i = 0; i < 4; ++i) { const float t = st[i][0] - mean; m2 += 144.f * t * t; }
  const float rs = rsqrtf(m2 * (1.f / DM) + 1e-5f);
#pragma unroll
  for (int e = 0; e < 16; ++e) v[e] = (v[e] - mean) * rs;
  const unsigned row = row0 + tok, mb = row >> 5, r32 = row & 31;
  const unsigned o0 = ((mb * 36 + 2 * (nt * 4 + pr) + (g >> 1)) * 64 + (g & 1) * 32 + r32) * 16;
  const unsigned o1 = sing ? ((mb * 36 + 32 + nt) * 64 + g * 32 + r32) * 16 : OOB;
  gst16c(u32x4{cvtpk(v[0], v[1]), cvtpk(v[2], v[3]), cvtpk(v[4], v[5]), cvtpk(v[6], v[7])}, c.rws, o0, WS_HN);
  gst16c(u32x4{cvtpk(v[8], v[9]), cvtpk(v[10], v[11]), cvtpk(v[12], v[13]), cvtpk(v[14], v[15])}, c.rws, o1, WS_HN);
}

#define XS0 4
#define XS1 12
DI unsigned phase_attn(Ctx& c, QOut& qo, WSrc& wso, u32x4 (&xr)[XM][2], const u16* wo, const u16* w1, const u16* b1, const u16* w2, const u16* b2) {
  AttnState a;
  const unsigned ldsb = c.ldsb, lane = c.lane, w = c.w, b = c.b;
  const unsigned xcd = b & 7, jj = b >> 3, head = xcd * 4 + (jj >> 3), pi = jj & 7;
  const unsigned l31 = lane & 31, h = lane >> 5;
  const unsigned kh = w >> 2, rg = kh ? 7 - w : w, partner = 7 - w;
  const unsigned n1 = 2 * pi + 2, n1k = kh ? n1 : 0, n1t = kh ? n1 : 99;
  a.voff = lane * 16; a.ldsb = ldsb; a.w = w; a.n1 = n1; a.n1k = n1k;
  a.kbase = WS_K + head * (SEQ * HD * 2) + w * 4096; a.vbase = WS_V + head * (SEQ * HD * 2) + w * 4096;
  a.kb[0] = ldsb + lane * 16; a.kb[1] = a.kb[0] + 32768; a.vb[0] = a.kb[0] + 65536; a.vb[1] = a.kb[0] + 98304;
  PIN(a.kb[0]); PIN(a.kb[1]); PIN(a.vb[0]); PIN(a.vb[1]);
  if (kh == 0) {
#pragma unroll
    for (int d = 0; d < 8; ++d) *(LAS u32x4*)(size_t)(ldsb + LE + rg * 8192 + d * 1024 + lane * 16) = qo.q[d];
  }
  if (__builtin_expect(!qo.kdone, 0)) {
    wait_flags(c, F_K + head * 64, 8, 1);
#pragma unroll
    for (int p = 0; p < 4; ++p) dma16c(ldsb + kh * 16384 + w * 4096 + p * 1024, a.voff, c.rws, a.kbase + p * 1024);
  }
  STAMP(8);
#pragma unroll
  for (int d = 0; d < 4; ++d) a.o[d] = f32x16{};
#pragma unroll
  for (int d = 0; d < 8; ++d) a.qf[d] = asbf(qo.q[d]);
  a.negm = f32x16{}; a.mhat = 0.f; a.l0 = a.l1 = 0.f; a.fct = 1.f; a.pend = false;
  {
    unsigned bytes;
    asm volatile("s_mov_b32 %0, .Lfb_end-.Lfb_begin" : "=s"(bytes));
    const unsigned g = lane * 8 + w, sel = g < 162 ? 0 : g < 324 ? 1 : 2, li = g - 162 * sel, r = (li * 57) >> 9, cl = li - 9 * r;
    const unsigned o2 = (r < 16 ? 512 * xcd + jj * 16 + r : 4096 + 64 * xcd + jj * 2 + r - 16) * (DM * 2) + cl * 128;
    const unsigned o1 = (jj * 18 + r) * (FF * 2) + (cl < 8 ? xcd * 1024 + cl * 128 : 8192 + xcd * 128);
    const unsigned oc = ((g - 468) * 32 + jj) * 128;
    const bool code = g >= 468;
    const u64 base = code ? c.pc0 : sel == 0 ? (u64)w2 : sel == 1 ? (u64)w1 : (u64)wo;
    a.ta = base + (code ? (oc < bytes ? oc : 0) : sel == 1 ? o1 : o2);
    a.dummy = 0; a.tm = 15;
    PIN(a.ta);
  }
  if (kh == 0) wait_vm<0>();
  bar();
  STAMP(9);
#pragma unroll
  for (int n = 0; n < 4; ++n) a.fr[n] = lds_b128(a.kb[0] + ((n & 1) * 8 + (n >> 1)) * 1024);
  const int dq = (int)(rg * 32 + l31) - (int)(kh * 64) - 4 * (int)h;
  const unsigned dA = rg < 2 ? n1t - 2 : 99, dB = n1t - 1, dC = (kh || rg < 2) ? 16 : 99;

  const unsigned mtH = jj >> 2, fH = F_CTX + (mtH * 32 + xcd * 4) * 8;
  u32x2 fl = {0u, 0u};
  auto post = [&](auto X_, unsigned s) AI {
    constexpr int X = decltype(X_)::v;
    if constexpr (X == 0) {
      if (__builtin_expect(s == n1t, 0)) {
        const float inv = __builtin_amdgcn_rcpf(xhalf_sum(a.l0 + a.l1));
        const unsigned so = WS_CTX + ((pi * 4 + rg) * 256 + head * 8) * 1024;
#pragma unroll
        for (int d = 0; d < 4; ++d) {
          gst16c(pack8<0>(a.o[d], inv), c.rws, a.voff, so + d * 2048);
          gst16c(pack8<1>(a.o[d], inv), c.rws, a.voff, so + d * 2048 + 1024);
          a.o[d] = f32x16{};
        }
        a.l0 = a.l1 = 0.f;
        a.vb[1] += 16384;
      }
      if (__builtin_expect(s + 2 == n1t, 0)) a.kb[0] += 16384;
    } else {
      if (__builtin_expect(s == n1 + 1, 0)) flag_store(c, F_CTX + (pi * 32 + head) * 8);
      if (__builtin_expect(s == 15, 0)) flag_load(c, fl, fH, kh ? 0 : 4);
    }
    if (__builtin_expect(a.pend, 0)) {
#pragma unroll
      for (int d = 0; d < 4; ++d)
#pragma unroll
        for (int r = 0; r < 16; ++r) a.o[d][r] *= a.fct;
      a.l0 *= a.fct; a.l1 *= a.fct; a.pend = false;
    }
    a.l0 = fadd_s(a.l0, a.t0); a.l1 = fadd_s(a.l1, a.t1);
    if constexpr (X == 1) {
      if (__builtin_expect(s + 1 == n1t, 0)) {
#pragma unroll
        for (int d = 0; d < 8; ++d) a.qf[d] = lds_b128(ldsb + LE + rg * 8192 + d * 1024 + lane * 16);
        a.negm = f32x16{};
        a.kb[1] += 16384; a.vb[0] += 16384;
      }
    }
    SB();
  };
  attn_iter<0, true, false>(c, a, 0, true, dA == 0, dq);
  post(IC<0>{}, 0);
  for (unsigned s = 1; s < 17; s += 2) {
    attn_iter<1, true, true>(c, a, s, false, s == dB, dq); post(IC<1>{}, s);
    attn_iter<0, true, true>(c, a, s + 1, s + 1 == n1t, s + 1 == dA || s + 1 == dC, dq); post(IC<0>{}, s + 1);
  }
  if (w == 0) *(LAS unsigned*)(size_t)(ldsb + LSYNC + lane * 4) = 0;
  wsrc_setup<0>(c, wso, mk_rsrc(wo, NQ * DM * 2), DM * 2, (jj & 3) * 256, 1024 + (jj & 3) * 32, 512 * xcd, 0, 0);
  const unsigned ol = opaque_lane();
  wso.voff[0] = w_voff(wso, ol, 0); PIN(wso.voff[0]);
#pragma unroll
  for (int e = 0; e < 8; ++e) { a.pk[e >> 2][e & 3] = cvtpk(a.S[0][0][2 * e], a.S[0][0][2 * e + 1]); PIN(a.pk[e >> 2][e & 3]); }
  a.l0 = fadd_s(a.l0, a.S[0][0][14]); a.l1 = fadd_s(a.l1, a.S[0][0][15]);
  if (n1k == 16) wait_vm<8>(); else wait_vm<0>();
  bar();
  constexpr auto KSM = [](int m) constexpr { return m < 8 ? m >> 2 : 2 + (m & 1); };
  constexpr auto DBM = [](int m) constexpr { return m < 8 ? m & 3 : (m - 8) >> 1; };
  auto rdv = [&](auto M_) AI { constexpr int m = decltype(M_)::v; a.fr[m & 7] = lds_b128(a.vb[0] + (DBM(m) * 4 + KSM(m)) * 1024); };
  static_for<6>([&](auto M_) AI { rdv(M_); });
  PIN(fl);
  asm volatile("" ::"v"(a.dummy));
  STAMPG(33);
  const unsigned late = ((unsigned)__builtin_amdgcn_ballot_w64(fl[0] != c.ep0 || fl[1] != c.ep1) & 15u) | kh;
  const bool slow = late != 0 && kh == 0;
  const unsigned stat = ldsb + 544 * WP + rg * 512 + lane * 8, scr = ldsb + rg * 16384 + lane * 16;
  const unsigned xo = WS_CTX + ((mtH * 4 + (w & 3)) * 256 + xcd * 32) * 1024, xv = x_voff(c);
  const unsigned r3 = 384 + 32 * (w & 3);
  float lt = 0.f;
  auto put = [&](auto D_) AI {
    constexpr int d = decltype(D_)::v;
#pragma unroll
    for (int e = 0; e < 4; ++e) *(LAS f32x4*)(size_t)(scr + (d * 4 + e) * 1024) = f32x4{a.o[d][4 * e], a.o[d][4 * e + 1], a.o[d][4 * e + 2], a.o[d][4 * e + 3]};
  };
  if (__builtin_expect(slow, 0)) flag_load(c, fl, fH, 4);
  SB();
  static_for<16>([&](auto M_) AI {
    constexpr int M = decltype(M_)::v;
    a.o[DBM(M)] = MFMA32(a.fr[M & 7], asbf(a.pk[KSM(M)]), a.o[DBM(M)]);
    SB();
    if constexpr (M + 6 < 16) rdv(IC<M + 6>{});
    if constexpr (M < 9) dma16_if(ldsb + r3 * WP + M * 1024, wso.voff[M], wso.r, wso.main + r3 * wso.pitch, kh);
    if constexpr (M < 8) { wso.voff[M + 1] = w_voff(wso, ol, M + 1); PIN(wso.voff[M + 1]); }
    if constexpr (M < 4) {
#pragma unroll
      for (int e = 4 * M; e < 4 * M + 4; ++e) { a.S[0][1][e] = __builtin_amdgcn_exp2f(a.S[0][1][e]); PIN(a.S[0][1][e]); }
    } else if constexpr (M < 8) {
#pragma unroll
      for (int e = 2 * (M - 4); e < 2 * (M - 4) + 2; ++e) {
        a.pk[2 + (e >> 2)][e & 3] = cvtpk(a.S[0][1][2 * e], a.S[0][1][2 * e + 1]); PIN(a.pk[2 + (e >> 2)][e & 3]);
        a.l0 = fadd_s(a.l0, a.S[0][1][2 * e]); a.l1 = fadd_s(a.l1, a.S[0][1][2 * e + 1]);
      }
    }
    if constexpr (M == 8) { lt = xhalf_sum(a.l0 + a.l1); PIN(lt); }
    if constexpr (M >= XS0 && M - XS0 < XS1) x_ld16_if<M - XS0>(c, xr[M - XS0], xv, xo, late);
    if constexpr (M == 12) { if (kh == 0) { *(LAS f32x2*)(size_t)stat = f32x2{a.mhat, lt}; put(IC<0>{}); } }
    if constexpr (M == 14) { if (kh == 0) put(IC<1>{}); }
    SB();
  });
  STAMPG(34);
  if (kh == 0) { put(IC<2>{}); put(IC<3>{}); } else wait_vm<0>();
  STAMP(10);
  STAMPG(35);
  bar();
  STAMPG(36);
  if (kh == 0) {
    if (n1 == 16) flag_store(c, F_CTX + (7 * 32 + head) * 8);
    w_group(c, wso, 2);
    if (__builtin_expect(slow, 0)) { wait_vm<9>(); flag_check(c, fl, fH, 4); }
  }
  static_for<XM - XS1>([&](auto V_) AI { constexpr int v = XS1 + decltype(V_)::v; x_ld16_if<v>(c, xr[v], xv, xo, late); });
  if (kh) {
    f32x4 y[2][4];
    auto get = [&](int d) AI {
#pragma unroll
      for (int e = 0; e < 4; ++e) y[d & 1][e] = *(LAS f32x4*)(size_t)(scr + (d * 4 + e) * 1024);
    };
    const f32x2 ps = *(LAS f32x2*)(size_t)stat;
    get(0); get(1);
    SB();
    const float mm = fmaxf(a.mhat, ps[0]);
    const float fo = __builtin_amdgcn_exp2f(a.mhat - mm), fp = __builtin_amdgcn_exp2f(ps[0] - mm);
    const float inv = __builtin_amdgcn_rcpf(lt * fo + ps[1] * fp), f = fo * inv, f2 = fp * inv;
    const unsigned so = WS_CTX + (((15 - pi) * 4 + rg) * 256 + head * 8) * 1024;
#pragma unroll
    for (int d = 0; d < 4; ++d) {
      f32x16 x = a.o[d];
#pragma unroll
      for (int e = 0; e < 16; ++e) x[e] = x[e] * f + y[d & 1][e >> 2][e & 3] * f2;
      u32x4 p0 = pack8<0>(x), p1 = pack8<1>(x);
      PIN(p0); PIN(p1);
      SB();
      if (d < 2) get(d + 2);
      if (d == 1) { SB(); cnt_add(c, LSYNC + C_MRG); }
      gst16c(p0, c.rws, lane * 16, so + d * 2048);
      gst16c(p1, c.rws, lane * 16, so + d * 2048 + 1024);
      SB();
    }
    STAMPG(37);
    cnt_wait(c, LSYNC + C_MRG, 4);
    w_group(c, wso, 1);
    wait_vm<9>();
    STAMPG(38);
    fput(u32x2{c.ep0, c.ep1}, c.rws, lane == 0 ? F_CX2 + (((7 - pi) * 4 + rg) * 32 + head) * 8 : OOB);
    const unsigned fL = F_CX2 + ((mtH * 4 + (w & 3)) * 32 + xcd * 4) * 8;
    flag_load(c, fl, fL, 4);
    static_for<5>([&](auto P_) AI { park_load<decltype(P_)::v>(c, w1, b1, w2, b2); });
    w_group(c, wso, 0);
    wait_vm<9>(); cnt_add(c, land(0, 1));
    wait_vm<0>(); cnt_add(c, land(0, 0));
    STAMPG(39);
    prio_lo();
    flag_check(c, fl, fL, 4);
  }
  STAMP(11);
  return late;
}

struct TId { unsigned sp, nt, mt, nt32, mb, own, wq; bool isH; };
DI TId tids(Ctx& c) {
  fresh(c);
  TId t;
  const unsigned xcd = c.b & 7, jj = c.b >> 3;
  t.isH = c.w < 4; t.wq = c.w & 3; t.sp = xcd; t.nt = jj & 3; t.mt = (t.isH ? 0 : 8) + (jj >> 2);
  t.nt32 = xcd * 4 + t.nt; t.mb = t.mt * 4 + t.wq; t.own = t.nt * 8 + t.sp;
  return t;
}
DI void acc_zero(f32x4 (&acc)[2][9]) {
#pragma unroll
  for (int u = 0; u < 2; ++u)
#pragma unroll
    for (int a = 0; a < 9; ++a) acc[u][a] = f32x4{};
}
DI void phase_tail(Ctx& c, WSrc& wso, u32x4 (&xr)[XM][2], unsigned xlate, const u16* x, const u16* w1, const u16* b1, const u16* w2, const u16* b2, u16* out) {
  f32x4 acc[2][9]; u32x2 tl[9], tl2[9]; unsigned hw[8]; WSrc ws, wt;
  {
    const TId t = tids(c);
    const unsigned xo = WS_CTX + (t.mb * 256 + t.sp * 32) * 1024;
    prio_hi(c);
    STAMPG(12);
    x_pro_masked16(c, xr, xo, xlate);
    wsrc_setup(c, ws, mk_rsrc(w1, DM * FF * 2), FF * 2, t.nt32 * 256, 8192 + t.nt32 * 32, 0, 512, 1);
    acc_zero(acc);
    const unsigned pb = part_base(c), po = WS_P1 + (((t.mt * 4 + t.nt) * 8 + t.sp) * 8 + 2 * t.wq) * 4608;
    gemm_g<16, 1, 0, 13>(c, acc, xr, tl, tl2, wso, ws, xo, 0, F_P1 + (t.mb * 32 + t.own) * 8, [&](auto I) AI { part_store<decltype(I)::v>(c, acc, pb, po); });
    STAMPG(16);
  }
  {
    const TId t = tids(c);
    u32x2 xres[4]; Red rd;
    row_load(c, xres, mk_rsrc(x, SEQ * DM * 2), t.mt * 128 + t.own * 4 + (c.lane & 3));
    unsigned dummy = 0;
    w_touch<4>(c, dummy, w2, w2, w2, c.w, false);
    poll_srv(c, ws, F_P1 + ((t.mt * 4 + (t.own >> 3)) * 32 + t.wq * 8) * 8, 8, LSRV & 1);
    red_load(c, rd, WS_P1 + (t.mt * 256 + t.wq * 64 + (t.own >> 2)) * 4608 + (t.own & 3) * 1152);
    STAMPG(17);
    asm volatile("" ::"v"(dummy));
    PIN(xres[0]); PIN(xres[1]); PIN(xres[2]); PIN(xres[3]);
    RED_PIN(rd);
    w_flush(c, ws);
    STAMPG(18);
    ln2_job(c, rd, xres, hw, t.mt * 128 + t.own * 4);
#pragma unroll
    for (int e = 0; e < 8; ++e) PIN(hw[e]);
    STAMPG(19);
    flag_srv(c, ws, F_HN + ((t.mt * 32 + t.own) * 4 + t.wq) * 8, LSRV & 2);
    STAMPG(20);
    poll_srv(c, ws, F_HN + t.mb * 256, 32, LSRV & 2);
    STAMPG(21);
    asm volatile("" ::"v"(dummy));
    const unsigned xh = WS_HN + t.mb * 36 * 1024;
    x_pro<18>(c, xr, x_voff(c), xh, xh + 32768);
    wsrc_setup(c, wt, mk_rsrc(w2, FF * DM * 2), DM * 2, t.nt * 256, 1024 + t.nt * 32, 512 * t.sp, 4096 + 64 * t.sp, 2);
#pragma unroll
    for (int a = 0; a < 9; ++a) {
      const u32x2 bias = *(LAS u32x2*)(size_t)(c.ldsb + LB1 + a * 32 + (c.lane >> 4) * 8);
      acc[0][a] = acc[1][a] = f32x4{bflo(bias[0]), bfhi(bias[0]), bflo(bias[1]), bfhi(bias[1])};
    }
    const unsigned vo = x_voff(c), ho = WS_H1 + (t.mb * 288 + 8 * t.nt32) * 1024, hs = WS_H1 + (t.mb * 288 + 256 + t.nt32) * 1024;
    gemm_g<18, 0, 1, 22>(c, acc, xr, tl, tl, ws, wt, xh, xh + 32768, F_H1 + (t.mb * 32 + t.nt32) * 8, [&](auto I_) AI {
      constexpr int I = decltype(I_)::v;
      if constexpr (I == 0) {
        const unsigned g = c.lane >> 4;
        gst16c(pack_single(relu2(acc[0][8]), relu2(acc[1][8])), c.rws, ((g & 1) * 32 + (g >> 1) * 16 + (c.lane & 15)) * 16, hs);
      } else {
        constexpr int i = (I - 1) >> 1, u = (I - 1) & 1;
        gst16c(pack_pair(relu2(acc[u][2 * i]), relu2(acc[u][2 * i + 1])), c.rws, vo, ho + i * 2048 + u * 256);
      }
    });
    STAMPG(25);
  }
  {
    const TId t = tids(c);
    poll_srv(c, wt, F_H1 + (t.mb * 32 + t.sp * 4) * 8, 4, LSRV & 4);
    STAMPG(26);
    const unsigned x1 = WS_H1 + (t.mb * 288 + t.sp * 32) * 1024, x1t = WS_H1 + (t.mb * 288 + 256 + t.sp * 4) * 1024;
    x_pro<18>(c, xr, x_voff(c), x1, x1t);
    acc_zero(acc);
    const unsigned pb = part_base(c), po = WS_P2 + (((t.mt * 4 + t.nt) * 8 + t.sp) * 8 + 2 * t.wq) * 4608;
    gemm_g<18, 0, 2, 27>(c, acc, xr, tl2, tl2, wt, wt, x1, x1t, F_P2 + (t.mb * 32 + t.own) * 8, [&](auto I) AI { part_store<decltype(I)::v>(c, acc, pb, po); });
    STAMPG(30);
  }
  {
    const TId t = tids(c);
    u32x2 bb[4]; Red rd;
    {
      unsigned o0, o2; job_offs(c, 0, o0, o2); o2 = c.lane < 8 ? o2 : 0;
      bb[0] = *(LAS u32x2*)(size_t)(c.ldsb + LB2 + o0); bb[1] = *(LAS u32x2*)(size_t)(c.ldsb + LB2 + o0 + 32);
      bb[2] = *(LAS u32x2*)(size_t)(c.ldsb + LB2 + o2); bb[3] = *(LAS u32x2*)(size_t)(c.ldsb + LB2 + o2 + 16);
    }
    poll_w(c, F_P2 + ((t.mt * 4 + (t.own >> 3)) * 32 + t.wq * 8) * 8, 8);
    STAMPG(31);
    red_load(c, rd, WS_P2 + (t.mt * 256 + t.wq * 64 + (t.own >> 2)) * 4608 + (t.own & 3) * 1152);
    RED_PIN(rd);
    const rsrc_t ro = mk_rsrc(out, SEQ * DM * 2);
    float a[16]; unsigned ow[8];
    red_sum(rd, a);
#pragma unroll
    for (int e = 0; e < 8; ++e) {
      const unsigned bv = bb[e >> 1][e & 1];
      ow[e] = cvtpk(a[2 * e] + bflo(bv) + bflo(hw[e]), a[2 * e + 1] + bfhi(bv) + bfhi(hw[e]));
    }
    {
      const unsigned g = (c.lane >> 2) & 3, ro0 = (t.mt * 128 + t.own * 4 + (c.lane & 3)) * (DM * 2);
      u32x4 q[2];
#pragma unroll
      for (int i = 0; i < 2; ++i) {
        q[i][0] = __builtin_amdgcn_update_dpp(ow[4 * i], ow[4 * i + 2], 0x114, 0xf, 0xa, false);
        q[i][1] = __builtin_amdgcn_update_dpp(ow[4 * i + 1], ow[4 * i + 3], 0x114, 0xf, 0xa, false);
        q[i][2] = __builtin_amdgcn_update_dpp(ow[4 * i + 2], ow[4 * i], 0x104, 0xf, 0x5, false);
        q[i][3] = __builtin_amdgcn_update_dpp(ow[4 * i + 3], ow[4 * i + 1], 0x104, 0xf, 0x5, false);
      }
      __builtin_amdgcn_raw_buffer_store_b128(q[0], ro, ro0 + t.wq * 256 + (c.lane >> 4) * 64 + (g & 1) * 32 + (g >> 1) * 16, 0, OAUX);
      __builtin_amdgcn_raw_buffer_store_b128(q[1], ro, c.lane < 8 ? ro0 + 1024 + t.wq * 32 + g * 16 : OOB, 0, OAUX2);
    }
    STAMPG(32);
    if (c.b == 0 && c.w == 0) gst8c(u32x2{c.ep0, c.ep1}, c.rws, c.lane == 0 ? F_EPOCH : OOB, WS_FLAGS);
  }
}

__global__ __launch_bounds__(512, 2) void fused_block(const u16* __restrict__ x, const u16* __restrict__ ckv, const u16* __restrict__ wq,
                                                      const u16* __restrict__ wuk, const u16* __restrict__ wuv, const u16* __restrict__ wo,
                                                      const u16* __restrict__ w1, const u16* __restrict__ b1, const u16* __restrict__ w2,
                                                      const u16* __restrict__ b2, u16* __restrict__ out, char* __restrict__ ws) {
  extern __shared__ __attribute__((aligned(16))) char lds[];
  Ctx c;
  asm volatile("s_getpc_b64 %0\n.Lfb_begin:" : "=s"(c.pc0));
  c.ldsb = (unsigned)(size_t)lds; c.lane = threadIdx.x & 63; c.w = rfl(threadIdx.x >> 6); c.b = blockIdx.x;
  c.rws = mk_rsrc(ws, WS_END);
  STAMP(0);
  QOut qo;
  WSrc wso; u32x4 xr[XM][2];
  phase_qkv(c, qo, x, ckv, wq, wuk, wuv);
  fresh(c);
  const unsigned xlate = phase_attn(c, qo, wso, xr, wo, w1, b1, w2, b2);
  fresh(c);
  phase_tail(c, wso, xr, xlate, x, w1, b1, w2, b2, out);
  asm volatile(".Lfb_end:");
}

extern "C" void kernel_launch(void* const* d_in, const int* in_sizes, int n_in, void* d_out, int out_size, void* d_ws, size_t ws_size,
                              hipStream_t stream) {
  hipFuncSetAttribute((const void*)fused_block, hipFuncAttributeMaxDynamicSharedMemorySize, 163840);
  fused_block<<<256, 512, 163840, stream>>>((const u16*)d_in[0], (const u16*)d_in[1], (const u16*)d_in[2], (const u16*)d_in[3], (const u16*)d_in[4],
                                            (const u16*)d_in[5], (const u16*)d_in[7], (const u16*)d_in[8], (const u16*)d_in[9], (const u16*)d_in[10],
                                            (u16*)d_out, (char*)d_ws);
}
```

```cpp
#define PROBE -1
#include <hip/hip_runtime.h>
#include <stdint.h>
#include <utility>

typedef __bf16 bf16x8 __attribute__((ext_vector_type(8)));
typedef __bf16 bf16x4 __attribute__((ext_vector_type(4)));
typedef __bf16 bf16x2 __attribute__((ext_vector_type(2)));
typedef float f32x16 __attribute__((ext_vector_type(16)));
typedef float f32x4 __attribute__((ext_vector_type(4)));
typedef float f32x2 __attribute__((ext_vector_type(2)));
typedef unsigned u32x4 __attribute__((ext_vector_type(4)));
typedef unsigned u32x2 __attribute__((ext_vector_type(2)));
typedef unsigned short u16;
typedef unsigned long long u64;
#define LAS __attribute__((address_space(3)))
#define DI __device__ __forceinline__
#define AI __attribute__((always_inline))
typedef __amdgpu_buffer_rsrc_t rsrc_t;

constexpr int SEQ = 2048, DM = 576, NH = 32, HD = 128, DC = 512, FF = 4608, NQ = 4096;

constexpr unsigned WS_FLAGS = 0;
constexpr unsigned WS_XN = 131072;
constexpr unsigned WS_K = WS_XN + SEQ * DM * 2;
constexpr unsigned WS_V = WS_K + SEQ * NQ * 2;
constexpr unsigned WS_CTX = WS_V + SEQ * NQ * 2;
constexpr unsigned WS_P1 = WS_CTX + SEQ * NQ * 2;
constexpr unsigned WS_H = WS_P1 + 8 * SEQ * DM * 2;
constexpr unsigned WS_HN = WS_H + SEQ * DM * 2;
constexpr unsigned WS_H1 = WS_HN + SEQ * DM * 2;
constexpr unsigned WS_P2 = WS_H1 + SEQ * FF * 2;
constexpr unsigned WS_CNT = WS_P2 + 8 * SEQ * DM * 2;
constexpr unsigned WS_END = WS_CNT + 16 * 80 * 128;
constexpr unsigned F_EPOCH = 0, F_XN = 4096, F_KV = 8192, F_CTX = 12288, F_K = 81920, F_V = 90112, F_CX2 = 98304;

constexpr unsigned N_P1 = 0, N_P2 = 16, N_HN = 32, N_H1 = 36;
constexpr unsigned cnt_at(unsigned mt, unsigned i) { return WS_CNT + (mt * 80 + i) * 128; }

template <int... Is> using iseq = std::integer_sequence<int, Is...>;
template <int N> using make_iseq = std::make_integer_sequence<int, N>;
template <int I> struct IC { static constexpr int v = I; };
template <class F, int... Is> DI void static_for_(F&& f, iseq<Is...>) { (f(IC<Is>{}), ...); }
template <int N, class F> DI void static_for(F&& f) { static_for_(f, make_iseq<N>{}); }

#define SB() __builtin_amdgcn_sched_barrier(0)
#define PIN(x) asm volatile("" : "+v"(x))
DI unsigned rfl(unsigned x) { return __builtin_amdgcn_readfirstlane(x); }
DI rsrc_t mk_rsrc(const void* p, unsigned bytes) { return __builtin_amdgcn_make_buffer_rsrc((void*)p, (short)0, (int)bytes, 0x00020000); }
constexpr unsigned OOB = 0xFFFFFF00u;
DI void dma16(unsigned lds_addr, unsigned voff, rsrc_t r, unsigned soff) {
  asm volatile("s_mov_b32 m0, %0\n\ts_nop 3\n\tbuffer_load_dwordx4 %1, %2, %3 offen lds" ::"s"(lds_addr), "v"(voff), "s"(r), "s"(soff) : "memory", "m0");
}
DI void dma16c(unsigned lds_addr, unsigned voff, rsrc_t r, unsigned soff) {
  asm volatile("s_mov_b32 m0, %0\n\ts_nop 3\n\tbuffer_load_dwordx4 %1, %2, %3 offen sc1 lds" ::"s"(lds_addr), "v"(voff), "s"(r), "s"(soff) : "memory", "m0");
}
template <int IMM> DI void gld16c(u32x4& d, unsigned voff, rsrc_t r, unsigned soff) {
  asm volatile("s_nop 4\n\tbuffer_load_dwordx4 %0, %1, %2, %3 offen offset:%4 sc1" : "=v"(d) : "v"(voff), "s"(r), "s"(soff), "i"(IMM) : "memory");
}
template <int IMM> DI void gld16(u32x4& d, unsigned voff, rsrc_t r, unsigned soff) {
  asm volatile("s_nop 4\n\tbuffer_load_dwordx4 %0, %1, %2, %3 offen offset:%4" : "=v"(d) : "v"(voff), "s"(r), "s"(soff), "i"(IMM) : "memory");
}
DI void gld8c(u32x2& d, unsigned voff, rsrc_t r, unsigned soff) {
  asm volatile("s_nop 4\n\tbuffer_load_dwordx2 %0, %1, %2, %3 offen sc1" : "=v"(d) : "v"(voff), "s"(r), "s"(soff) : "memory");
}
DI void gst16c(u32x4 v, rsrc_t r, unsigned voff, unsigned soff) {
  asm volatile("s_nop 4\n\tbuffer_store_dwordx4 %0, %1, %2, %3 offen sc1\n\ts_nop 1" ::"v"(v), "v"(voff), "s"(r), "s"(soff) : "memory");
}
DI void gst8c(u32x2 v, rsrc_t r, unsigned voff, unsigned soff) { __builtin_amdgcn_raw_buffer_store_b64(v, r, voff, soff, 16); }
DI void fput(u32x2 v, rsrc_t r, unsigned voff) { asm volatile("s_nop 4\n\tbuffer_atomic_swap_x2 %0, %1, %2, 0 offen sc1\n\ts_nop 1" ::"v"(v), "v"(voff), "s"(r) : "memory"); }
DI void fadd(rsrc_t r, unsigned voff) { asm volatile("s_nop 4\n\tbuffer_atomic_add_x2 %0, %1, %2, 0 offen sc1\n\ts_nop 1" ::"v"(u32x2{1u, 0u}), "v"(voff), "s"(r) : "memory"); }
DI void fget(u32x2& d, rsrc_t r, unsigned voff) {
  d = u32x2{0u, 0u};
  asm volatile("s_nop 4\n\tbuffer_atomic_add_x2 %0, %1, %2, 0 offen sc0 sc1" : "+v"(d) : "v"(voff), "s"(r) : "memory");
}
template <int N> DI void wait_vm() { asm volatile("s_waitcnt vmcnt(%0)" ::"i"(N) : "memory"); }
DI void bar() { asm volatile("s_waitcnt lgkmcnt(0)" ::: "memory"); __builtin_amdgcn_s_barrier(); asm volatile("" ::: "memory"); }
DI unsigned cvtpk(float lo, float hi) { f32x2 v = {lo, hi}; bf16x2 b = __builtin_convertvector(v, bf16x2); return __builtin_bit_cast(unsigned, b); }
DI float bflo(unsigned x) { return __uint_as_float(x << 16); }
DI float bfhi(unsigned x) { return __uint_as_float(x & 0xffff0000u); }
DI bf16x8 lds_b128(unsigned a) { return *(LAS bf16x8*)(size_t)a; }
DI bf16x4 lds_tr(unsigned a) { return __builtin_amdgcn_ds_read_tr16_b64_v4bf16((LAS bf16x4*)(size_t)a); }
DI bf16x8 cat(bf16x4 a, bf16x4 b) { return __builtin_shufflevector(a, b, 0, 1, 2, 3, 4, 5, 6, 7); }
DI bf16x8 asbf(u32x4 x) { return __builtin_bit_cast(bf16x8, x); }
#define MFMA32(a, b, c) __builtin_amdgcn_mfma_f32_32x32x16_bf16((a), (b), (c), 0, 0, 0)
#define MFMA16(a, b, c) __builtin_amdgcn_mfma_f32_16x16x32_bf16((a), (b), (c), 0, 0, 0)
template <int S> DI u32x4 pack8(const f32x16& x, float sc) {
  u32x4 w;
  w[0] = cvtpk(x[8 * S + 0] * sc, x[8 * S + 1] * sc); w[1] = cvtpk(x[8 * S + 2] * sc, x[8 * S + 3] * sc);
  w[2] = cvtpk(x[8 * S + 4] * sc, x[8 * S + 5] * sc); w[3] = cvtpk(x[8 * S + 6] * sc, x[8 * S + 7] * sc);
  return w;
}
template <int S> DI u32x4 pack8(const f32x16& x) {
  u32x4 w;
  w[0] = cvtpk(x[8 * S + 0], x[8 * S + 1]); w[1] = cvtpk(x[8 * S + 2], x[8 * S + 3]);
  w[2] = cvtpk(x[8 * S + 4], x[8 * S + 5]); w[3] = cvtpk(x[8 * S + 6], x[8 * S + 7]);
  return w;
}
DI float max3f(float a, float b, float c) { float r; asm("v_max3_f32 %0, %1, %2, %3" : "=v"(r) : "v"(a), "v"(b), "v"(c)); return r; }
DI float max2f(float a, float b) { float r; asm("v_max_f32_e32 %0, %1, %2" : "=v"(r) : "v"(a), "v"(b)); return r; }
DI float fadd_s(float a, float b) { float r; asm("v_add_f32_e32 %0, %1, %2" : "=v"(r) : "v"(a), "v"(b)); return r; }
DI float xhalf_sum(float x) {
  auto r = __builtin_amdgcn_permlane32_swap(__float_as_uint(x), __float_as_uint(x), false, false);
  return __uint_as_float(r[0]) + __uint_as_float(r[1]);
}
DI float wave_sum(float x) {
#pragma unroll
  for (int o = 1; o < 64; o <<= 1) x += __shfl_xor(x, o);
  return x;
}

#define STAMP(i) do { if ((i) == PROBE) { SB(); c.px = (unsigned)__builtin_amdgcn_s_memrealtime(); SB(); } } while (0)

struct Ctx {
  unsigned px;
  unsigned ldsb, w, lane, b;
  unsigned ep0, ep1;
  rsrc_t rws;
  u64 pc0;
};

DI void fresh(Ctx& c) {
  asm volatile("v_mbcnt_lo_u32_b32 %0, -1, 0\n\tv_mbcnt_hi_u32_b32 %0, -1, %0" : "=v"(c.lane));
  asm volatile("" : "+s"(c.w), "+s"(c.b), "+s"(c.ldsb));
}
DI void code_touch(Ctx& c, unsigned& dummy) {
  unsigned bytes;
  asm volatile("s_mov_b32 %0, .Lfb_end-.Lfb_begin" : "=s"(bytes));
  const rsrc_t r = mk_rsrc((const void*)c.pc0, bytes);
  asm volatile("s_nop 4\n\tbuffer_load_dword %0, %1, %2, 0 offen" : "+v"(dummy) : "v"(c.w == 0 ? (c.lane * 32 + (c.b >> 3)) * 128 : OOB), "s"(r) : "memory");
}
DI void publish(Ctx& c, unsigned flag_off) {
  wait_vm<0>();
  bar();
  fput(u32x2{c.ep0, c.ep1}, c.rws, (c.w == 0 && c.lane == 0) ? flag_off : OOB);
}
DI void poll_flags(Ctx& c, unsigned flag_off, unsigned n, unsigned stride) {
  const unsigned voff = c.lane < n ? flag_off + c.lane * 8 * stride : OOB;
  const u64 t0 = __builtin_amdgcn_s_memrealtime();
  for (;;) {
    u32x2 f;
    fget(f, c.rws, voff);
    asm volatile("s_waitcnt vmcnt(0)" : "+v"(f)::"memory");
    if (__all((f[0] == c.ep0 && f[1] == c.ep1) || c.lane >= n)) break;
    if (__builtin_amdgcn_s_memrealtime() - t0 > 5000000ull) break;
    __builtin_amdgcn_s_sleep(2);
  }
}
DI void wait_flags(Ctx& c, unsigned flag_off, unsigned n, unsigned stride) {
  if (c.w == 0) poll_flags(c, flag_off, n, stride);
  bar();
}

#define DBG_DRAIN()
#define DBG_NX(x)
#define STAMPG(i) STAMP(i)
#define OAUX 0
#define OAUX2 OAUX
DI void flag_store(Ctx& c, unsigned flag_off) {
  fput(u32x2{c.ep0, c.ep1}, c.rws, (c.w == 0 && c.lane == 0) ? flag_off : OOB);
}
DI void flag_load(Ctx& c, u32x2& f, unsigned flag_off, unsigned n) { fget(f, c.rws, c.lane < n ? flag_off + c.lane * 8 : OOB); }
DI void flag_check(Ctx& c, u32x2& f, unsigned flag_off, unsigned n) {
  PIN(f);
  if (__builtin_expect(!__all((f[0] == c.ep0 && f[1] == c.ep1) || c.lane >= n), 0)) poll_flags(c, flag_off, n, 1);
}

constexpr float QSCALE = 0.08838834764831845f * 1.4426950408889634f;
constexpr unsigned LE = 131072;
constexpr unsigned LX = 147456;
constexpr unsigned KW = LX + 12288;
#define ROT 0
constexpr unsigned SLOT = 49152;
constexpr int slot_of(int S) { return (S + 2) % 3; }
constexpr int XQ = 6;
constexpr int LAQ = 6;
constexpr int QB1 = 40, QB2 = 88, QB3 = 116;
constexpr int Q_KPOLL = 70, Q_VPOLL = 134, Q_VST = 12;
#define PRO 0
#define XLS 1
constexpr int XLS1 = XLS > 0 ? XLS : 1, XL0 = 31 - 5 * XLS1, XFL = XLS == 0 ? 19 : ((XL0 - 8) | 1) - ((XL0 & 1) ? 0 : 2);
#define KSP 2
#define TWV 5
#define TWQ 13
#define XT 1
#define LNE 1
#define PRO_N 12
#define PRO_B 5
constexpr int PRO_U[18] = {0, 0, 0, 0, 0, 0, 1, 1, 1, 1, 1, 1, 2, 2, 2, 2, 2, 2}, PRO_I[18] = {0, 4, 1, 2, 5, 3, 0, 4, 1, 2, 5, 3, 0, 1, 2, 3, 4, 5}, PRO_X = LNE ? LNE - 1 : 5;
#define WQL 1
#define WQ2 41
constexpr int wq_piece(int q) { return q >= 1 && q < 25 && (q - 1) % 4 == 0 ? (q - 1) / 4 : q >= WQ2 && q < WQ2 + 24 && (q - WQ2) % 4 == 0 ? 6 + (q - WQ2) / 4 : -1; }
constexpr int vm_wait(int pos, int tag) {
  int seq[512] = {}; int n = 0;
  auto push = [&](int t, int cnt) { for (int i = 0; i < cnt; ++i) seq[n++] = t; };
  for (int i = 0; i < PRO_N; ++i) { push(PRO_U[i], 1); if (i == PRO_X) push(-1, 3); }
  if (PRO_N == 18) push(-12, 1);
  bool done = false;
  for (int T = 0; T < 32 && !done; ++T) {
    if (pos == T) { done = true; break; }
    if (T == 0 && PRO_N < 18) { for (int i = PRO_N; i < 18; ++i) push(PRO_U[i], 1); push(-12, 1); }
    if (T == 3) push(-2, 3);
    if (T == TWV) push(-6, 1);
    if (T == TWQ) push(-6, 1);
    if (T == 7) push(-3, 1);
    if (T == XFL) push(-5, 1);
    if (KSP == 1 && T == 27) push(-8, 1);
    if (T >= 16 && (T - 16) % KSP == 0 && (T - 16) / KSP < 8) push(-7, 1);
    if (T >= 1 && (!WQL || T < 29)) push((T - 1) / 2 + 3, 3);
    if (XLS == 0 && T >= 29) push(200 + 2 * (T - 29), 2), push(201 + 2 * (T - 29), 2);
    if (XLS > 0 && T >= XL0 && (T - XL0) % XLS1 == 0) push(200 + (T - XL0) / XLS1, 2);
  }
  for (int q = 0; q < 144 && !done; ++q) {
    if (pos == 100 + q) break;
    if (q == QB3) push(-11, 1);
    if (KSP == 2 && q == QB1) push(-8, 1);
    if (!WQL && (q == 1 || q == 4 || q == 6)) push(18, 1);
    if (WQL && wq_piece(q) >= 0) push(17 + wq_piece(q) / 6, 1);
    if (tag == -9 && q == Q_KPOLL) push(-9, 1);
    if (q % Q_VST == 2 && q / Q_VST < 8) push(-10, 1);
    if ((q & 7) == 7 && (q >> 3) + XQ < 18) push(200 + (q >> 3) + XQ, 2);
  }
  int last = -1;
  for (int i = 0; i < n; ++i) if (seq[i] == tag) last = i;
  return n - 1 - last;
}
constexpr int imin(int a, int b) { return a < b ? a : b; }
DI unsigned lds_add_rtn(unsigned addr) {
  unsigned r;
  asm volatile("s_mov_b64 exec, 1\n\tds_add_rtn_u32 %0, %1, %2\n\ts_mov_b64 exec, -1\n\ts_waitcnt lgkmcnt(0)" : "=&v"(r) : "v"(addr), "v"(1u) : "memory");
  return rfl(r);
}
DI unsigned lds_word(unsigned addr) { return __hip_atomic_load((LAS unsigned*)(size_t)addr, __ATOMIC_RELAXED, __HIP_MEMORY_SCOPE_WORKGROUP); }
DI void lds_set(unsigned addr, unsigned v) { __hip_atomic_store((LAS unsigned*)(size_t)addr, v, __ATOMIC_RELAXED, __HIP_MEMORY_SCOPE_WORKGROUP); }
DI float wave_sum64(float x) {
  x += __uint_as_float(__builtin_amdgcn_update_dpp(0, __float_as_uint(x), 0xB1, 0xf, 0xf, false));
  x += __uint_as_float(__builtin_amdgcn_update_dpp(0, __float_as_uint(x), 0x4E, 0xf, 0xf, false));
  x += __uint_as_float(__builtin_amdgcn_update_dpp(0, __float_as_uint(x), 0x124, 0xf, 0xf, false));
  x += __uint_as_float(__builtin_amdgcn_update_dpp(0, __float_as_uint(x), 0x128, 0xf, 0xf, false));
  { auto r = __builtin_amdgcn_permlane16_swap(__float_as_uint(x), __float_as_uint(x), false, false); x = __uint_as_float(r[0]) + __uint_as_float(r[1]); }
  return xhalf_sum(x);
}
DI void ln1_row(Ctx& c, const u32x4& xr0, const u32x4& xr1) {
  const unsigned lane = c.lane;
  float v[16];
#pragma unroll
  for (int e = 0; e < 4; ++e) { v[2 * e] = bflo(xr0[e]); v[2 * e + 1] = bfhi(xr0[e]); v[8 + 2 * e] = bflo(xr1[e]); v[9 + 2 * e] = bfhi(xr1[e]); }
  float s = 0.f;
#pragma unroll
  for (int e = 0; e < 16; ++e) s += v[e];
  const float mean = wave_sum64(s) * (1.f / DM);
  float qq = 0.f;
#pragma unroll
  for (int e = 0; e < 16; ++e) { v[e] -= mean; qq += v[e] * v[e]; }
  qq = lane < 36 ? qq : 0.f;
  const float rs = rsqrtf(wave_sum64(qq) * (1.f / DM) + 1e-5f);
  u32x4 o0, o1;
#pragma unroll
  for (int e = 0; e < 4; ++e) { o0[e] = cvtpk(v[2 * e] * rs, v[2 * e + 1] * rs); o1[e] = cvtpk(v[8 + 2 * e] * rs, v[9 + 2 * e] * rs); }
  const unsigned ad = c.ldsb + LX + (lane < 36 ? (2 * lane * 8 + c.w) * 16 : 9216 + (lane - 36) * 16);
  *(LAS u32x4*)(size_t)ad = o0;
  *(LAS u32x4*)(size_t)(ad + 128) = o1;
}
DI void xn_store(Ctx& c) {
  const unsigned w = c.w, lane = c.lane, mb = c.b >> 2, r0 = (c.b & 3) * 8;
  const unsigned c0 = 9 * w + (lane >> 3), c1 = 9 * w + 8;
  const u32x4 a0 = *(LAS u32x4*)(size_t)(c.ldsb + LX + (9 * w * 8 + lane) * 16);
  const u32x4 a1 = *(LAS u32x4*)(size_t)(c.ldsb + LX + (c1 * 8 + (lane & 7)) * 16);
  gst16c(a0, c.rws, ((mb * 36 + (c0 >> 1)) * 64 + (c0 & 1) * 32 + r0 + (lane & 7)) * 16, WS_XN);
  gst16c(a1, c.rws, lane < 8 ? ((mb * 36 + (c1 >> 1)) * 64 + (c1 & 1) * 32 + r0 + lane) * 16 : OOB, WS_XN);
}

template <int WH> DI void w_touch(Ctx& c, unsigned& dummy, const void* wo, const void* w1, const void* w2, unsigned wv, bool two) {
  unsigned lane;
  asm volatile("v_mbcnt_lo_u32_b32 %0, -1, 0\n\tv_mbcnt_hi_u32_b32 %0, -1, %0" : "=v"(lane));
  const unsigned xcd = c.b & 7, jj = (c.b >> 3) - ((lane >> 5) << 2), l = two ? lane & 31 : lane, i = 3 * wv + l - 18;
  const bool mn = l < 18, ex = l < 21 && i < 18;
  auto ld = [&](const void* p, unsigned bytes, unsigned off) AI {
    asm volatile("s_nop 4\n\tbuffer_load_dword %0, %1, %2, 0 offen" : "+v"(dummy) : "v"(off), "s"(mk_rsrc(p, bytes)) : "memory");
  };
  const unsigned o1 = xcd * 589824 + (jj * 144 + wv * 18 + l) * 128;
  if constexpr (WH & 1) ld(wo, NQ * DM * 2, mn ? o1 : OOB);
  if constexpr (WH & 2) ld(w1, FF * DM * 2, mn ? (jj * 18 + l) * (FF * 2) + xcd * 1024 + wv * 128 : ex ? (jj * 18 + i) * (FF * 2) + 8192 + xcd * 128 : OOB);
  if constexpr (WH & 4) ld(w2, FF * DM * 2, mn ? o1 : ex ? (4096 + 64 * xcd) * (DM * 2) + (jj * 18 + i) * 128 : OOB);
}
struct QOut { u32x4 q[8]; bool kdone; };
DI u32x4 pack_pair(const f32x4& r0, const f32x4& r1) { return u32x4{cvtpk(r0[0], r0[1]), cvtpk(r0[2], r0[3]), cvtpk(r1[0], r1[1]), cvtpk(r1[2], r1[3])}; }
template <int IMM> DI void gld16c2(u32x4& d0, u32x4& d1, unsigned voff, rsrc_t r, unsigned soff) {
  asm volatile("s_nop 4\n\tbuffer_load_dwordx4 %0, %2, %3, %4 offen offset:%5 sc1\n\tbuffer_load_dwordx4 %1, %2, %3, %4 offen offset:%6 sc1"
               : "=&v"(d0), "=&v"(d1) : "v"(voff), "s"(r), "s"(soff), "i"(IMM), "i"(IMM + 256) : "memory");
}
DI unsigned x_voff(Ctx& c) { const unsigned g = c.lane >> 4; return (g >> 1) * 1024 + ((g & 1) * 32 + (c.lane & 15)) * 16; }

DI void phase_qkv(Ctx& c, QOut& qo, const u16* x, const u16* ckv, const u16* wq, const u16* wuk, const u16* wuv) {
  const unsigned w = c.w, lane = c.lane, ldsb = c.ldsb, b = c.b;
  const unsigned xcd = b & 7, jj = b >> 3, head = xcd * 4 + (jj >> 3), cb = jj & 7;
  const rsrc_t rxx = mk_rsrc(x, SEQ * DM * 2), rx = mk_rsrc(ckv, SEQ * DC * 2);
  const rsrc_t rwk = mk_rsrc(wuk, DC * NQ * 2), rwv = mk_rsrc(wuv, DC * NQ * 2), rwq = mk_rsrc(wq, DM * NQ * 2);
  unsigned dummy = 0;
  code_touch(c, dummy);
  const unsigned voffX0 = (lane >> 3) * 1024 + (((lane & 7) ^ (lane >> 4)) << 4), voffX1 = voffX0 ^ 64;
  const unsigned voffW = (lane >> 3) * 8192 + (((((lane >> 1) & 3) ^ (((lane >> 4) & 1) | ((w & 1) << 1))) << 5) | ((lane & 1) << 4));
  const unsigned sX = (cb * 256 + 32 * w) * 1024, sW = (8 * (w & 3)) * 8192 + (head * 128 + (w >> 2) * 64) * 2;
  const unsigned rot = ROT == 0 ? 2 * (cb & 3) : ROT == 1 ? 2 * ((cb + head) & 3) : ROT == 2 ? 2 * (cb & 3) + 4 * (head & 1) : (cb + 2 * head) & 7;
  const unsigned dX = ldsb + 4 * w * 1024, dW = ldsb + w * 1024;
  auto issue = [&](auto U_, auto P_) AI {
    constexpr int U = decltype(U_)::v, P = decltype(P_)::v;
    constexpr unsigned so = slot_of(U) * SLOT;
    if constexpr (U < 16) {
      const unsigned tr = (U + rot) & 7;
      constexpr int p = P < 2 ? P : P - 1;
      if constexpr (P % 3 != 2) dma16(dX + so + p * 1024, (p & 1) ? voffX1 : voffX0, rx, sX + 128 * tr + (U < 8 ? 8 * p : 16 * (p & 1) + 8 * (p >> 1)) * 1024);
      else dma16(dW + so + 32768 + (P / 3) * 8192, voffW, U < 8 ? rwk : rwv, sW + (64 * tr + 32 * (P / 3)) * 8192);
    } else {
      dma16(dW + so + (U == 18 ? 5 - P : P) * 8192, voffW, rwq, sW + 32 * (6 * (U - 16) + P) * 8192);
    }
  };
  u32x2 ep; u32x4 xr0, xr1;
    const unsigned hr = head & 3, vA = voffX0 ^ ((hr & 1) << 6), vB = vA ^ 64, wj = (cb >> 2) * 8192;
    auto pro = [&](auto U_, auto I_) AI {
      constexpr int U = decltype(U_)::v, I = decltype(I_)::v;
      constexpr unsigned so = slot_of(U) * SLOT;
      const unsigned tr = (U + rot) & 7;
      if constexpr (I < 4) { const unsigned po = ((I + hr) & 3) * 1024; dma16(dX + so + po, (I & 1) ? vB : vA, rx, sX + 128 * tr + 8 * po); }
      else { const unsigned j = wj ^ ((I - 4) * 8192); dma16(dW + so + 32768 + j, voffW, rwk, sW + 64 * tr * 8192 + 32 * j); }
    };
  {
    static_for<PRO_N>([&](auto N_) AI {
      constexpr int N = decltype(N_)::v;
      pro(IC<PRO_U[N]>{}, IC<PRO_I[N]>{});
      if constexpr (N == PRO_B) __builtin_amdgcn_s_barrier();
      if constexpr (N == PRO_X) {
        const unsigned vo = lane < 36 ? lane * 32 : OOB;
        gld8c(ep, F_EPOCH, c.rws, WS_FLAGS);
        gld16<0>(xr0, vo, rxx, (b * 8 + w) * (DM * 2));
        gld16<16>(xr1, vo, rxx, (b * 8 + w) * (DM * 2));
      }
    });
  }
  unsigned td2 = 0;
  auto xtouch = [&]() AI {
    const unsigned l5 = (lane * 52) >> 8, row = cb * 256 + (head & 3) * 64 + w * 8 + l5, ch = (rot + 3 + lane - 5 * l5) & 7;
    asm volatile("s_nop 4\n\tbuffer_load_dword %0, %1, %2, 0 offen" : "+v"(td2) : "v"(XT && lane < 40 ? row * 1024 + ch * 128 : OOB), "s"(rx) : "memory");
  };
  if constexpr (PRO_N == 18) xtouch();
  const unsigned kh = w >> 2, rg = kh ? 7 - w : w;
  const unsigned qmb = (kh ? cb : 15 - cb) * 4 + rg;
  const unsigned wm = w >> 1, wn = w & 1, g = lane >> 4, l15 = lane & 15, q = l15 >> 2, pp = lane & 3;
  unsigned xb[3][2], wb[3][4], wqb[3][4];
  {
    const unsigned sg = ((q >> 1) & 1) | ((g & 1) << 1);
#pragma unroll
    for (int i = 0; i < 3; ++i) {
      xb[i][0] = ldsb + i * SLOT + (wm * 64 + l15) * 128 + ((g ^ (l15 >> 1)) << 4); xb[i][1] = xb[i][0] ^ 64; PIN(xb[i][0]); PIN(xb[i][1]);
#pragma unroll
      for (int j = 0; j < 4; ++j) { wb[i][j] = ldsb + i * SLOT + wn * 4096 + (8 * g + q) * 128 + ((j ^ sg) << 5) + pp * 8; PIN(wb[i][j]); }
    }
  }
  if constexpr (LNE != 0) { wait_vm<vm_wait(0, -1)>(); PIN(xr0); PIN(xr1); ln1_row(c, xr0, xr1); }
  wait_vm<vm_wait(0, 0)>();
  asm volatile("" ::"v"(dummy));
  bar();
  STAMP(2);
  f32x4 acc[4][4];
  bf16x8 xf[2][4]; bf16x4 wl[2][4][2];
  u32x4 pk[8], pv[8], xq[XQ][2]; u32x2 xnf;
  auto rd = [&](auto T_, auto R_) AI {
    constexpr int T = decltype(T_)::v, R = decltype(R_)::v, B = T & 1, sl = slot_of(T >> 1);
    constexpr int K = R % 3, I = R / 3;
    if constexpr (K == 0) xf[B][I] = lds_b128(xb[sl][B] + I * 2048);
    else wl[B][I][K - 1] = lds_tr(wb[sl][I] + 32768 + B * 8192 + (K - 1) * 512);
  };
  bf16x4 wq8[8][2];
  auto rdq = [&](auto N_, auto H_) AI {
    constexpr int N = decltype(N_)::v, H = decltype(H_)::v, v = N >> 3, a = N & 7, sl = slot_of(16 + v / 6);
    wq8[N & 7][H] = lds_tr(wqb[sl][a & 3] + (v >= 12 ? 17 - v : v % 6) * 8192 + (a >> 2) * 4096 + H * 512);
  };
  const unsigned xvo = x_voff(c), xso = WS_XN + qmb * 36 * 1024;
  auto ldx = [&](auto V_) AI { constexpr int v = decltype(V_)::v; gld16c2<(v & 1) * 2048>(xq[v % XQ][0], xq[v % XQ][1], xvo, c.rws, xso + (v >> 1) * 4096); };
  const unsigned kst = WS_K + ((head * 64 + cb * 8 + wm * 2) * 8 + 4 * wn) * 1024, vst = WS_V + (((head * 32 + cb * 4 + wm) * 4 + 2 * wn) * 4) * 1024;
  static_for<12>([&](auto R) AI { rd(IC<0>{}, R); });
  SB();
  static_for<32>([&](auto T_) AI {
    constexpr int T = decltype(T_)::v, B = T & 1;
    if constexpr (B) { wait_vm<vm_wait(T, (T >> 1) + 1)>(); bar(); }
    if constexpr (T == 1) {
      PIN(ep);
      if constexpr (!LNE) { PIN(xr0); PIN(xr1); }
      u64 z = (((u64)rfl(ep[1]) << 32) | rfl(ep[0])) + 0x9E3779B97F4A7C15ull;
      z = (z ^ (z >> 30)) * 0xBF58476D1CE4E5B9ull; z = (z ^ (z >> 27)) * 0x94D049BB133111EBull; z ^= z >> 31;
      c.ep0 = (unsigned)z; c.ep1 = (unsigned)(z >> 32);
      if (w == 0) lds_set(ldsb + KW + (lane & 3) * 4, 0);
    }
    if constexpr (!LNE && (T == 1 || T == 2)) { if (kh == T - 1) ln1_row(c, xr0, xr1); }
    if constexpr (T == 3) { fput(u32x2{c.ep0, c.ep1}, c.rws, (w == 0 && lane < 5) ? WS_CNT + (b * 5 + lane) * 128 : OOB); xn_store(c); }
    if constexpr (T == TWV) asm volatile("s_nop 4\n\tbuffer_load_dword %0, %1, %2, 0 offen" : "+v"(td2) : "v"(lane < 16 ? (cb * 64 + w * 8 + (lane >> 1)) * 8192 + head * 256 + (lane & 1) * 128 : OOB), "s"(rwv) : "memory");
    if constexpr (T == TWQ) asm volatile("s_nop 4\n\tbuffer_load_dword %0, %1, %2, 0 offen" : "+v"(td2) : "v"(lane < 18 ? (cb * 72 + w * 9 + (lane >> 1)) * 8192 + head * 256 + (lane & 1) * 128 : OOB), "s"(rwq) : "memory");
    if constexpr (T == 31) asm volatile("" ::"v"(td2));
    if constexpr (T == 7) flag_store(c, F_XN + b * 8);
    if constexpr (T == XFL) flag_load(c, xnf, F_XN + qmb * 32, 4);
    if constexpr (T == XFL + 8) flag_check(c, xnf, F_XN + qmb * 32, 4);
    if constexpr (T == 27) { if constexpr (KSP == 1) flag_store(c, F_K + (head * 8 + cb) * 8); }
    if constexpr (T == 31) {
#pragma unroll
      for (int i = 0; i < 3; ++i)
#pragma unroll
        for (int j = 0; j < 4; ++j) { wqb[i][j] = wb[i][j] - wn * 4096; PIN(wqb[i][j]); }
    }
    if constexpr (T == 8 || T == 16 || T == 24) STAMP(100 + T / 2);
    SB();
    static_for<16>([&](auto M_) AI {
      constexpr int M = decltype(M_)::v, i = M >> 2, j = M & 3;
      const bf16x8 wf = cat(wl[B][j][0], wl[B][j][1]);
      const f32x4 cin = (T & 15) == 0 ? f32x4{} : acc[i][j];
      acc[i][j] = T < 16 ? MFMA16(wf, xf[B][i], cin) : MFMA16(xf[B][i], wf, cin);
      SB();
      if constexpr (M < 12) {
        if constexpr (T < 31) rd(IC<T + 1>{}, IC<M>{});
        else rdq(IC<(M >> 1)>{}, IC<(M & 1)>{});
      }
      if constexpr (M == 1 && T >= 16 && (T - 16) % KSP == 0 && (T - 16) / KSP < 8) {
        constexpr int e = (T - 16) / KSP;
        gst16c(pk[e], c.rws, xvo, kst + (e >> 2) * 8192 + (e & 1) * 2048 + ((e >> 1) & 1) * 256);
      }
      if constexpr (T >= 1 && (!WQL || T < 29) && (M == 4 || M == 8 || M == 12)) issue(IC<(T - 1) / 2 + 3>{}, IC<(B ? 0 : 3) + (M >> 2) - 1>{});
      if constexpr (T == 0 && (M & 1) && PRO_N + (M >> 1) < 18) pro(IC<PRO_U[PRO_N + (M >> 1)]>{}, IC<PRO_I[PRO_N + (M >> 1)]>{});
      if constexpr (T == 0 && M == 15 && PRO_N < 18) xtouch();
      if constexpr (XLS == 0 && T >= 29 && (M == 13 || M == 15)) ldx(IC<2 * (T - 29) + (M == 15)>{});
      if constexpr (XLS > 0 && T >= XL0 && (T - XL0) % XLS1 == 0 && M == 14) ldx(IC<(T - XL0) / XLS1>{});
      SB();
    });
    if constexpr (T == 15) {
#pragma unroll
      for (int e = 0; e < 8; ++e) { pk[e] = pack_pair(acc[e >> 1][2 * (e & 1)], acc[e >> 1][2 * (e & 1) + 1]); PIN(pk[e]); }
    }
    if constexpr (T == 31) {
#pragma unroll
      for (int e = 0; e < 8; ++e) { pv[e] = pack_pair(acc[2 * (e >> 2)][e & 3], acc[2 * (e >> 2) + 1][e & 3]); PIN(pv[e]); }
    }
  });
  STAMP(3);
  f32x4 qa[2][8];
  u32x2 kf; bool kdone = false;
  const unsigned kso = WS_K + head * (SEQ * HD * 2) + w * 4096;
  static_for<144>([&](auto N_) AI {
    constexpr int N = decltype(N_)::v, v = N >> 3, a = N & 7;
    if constexpr (a == 0) { wait_vm<vm_wait(100 + N, 200 + v)>(); PIN(xq[v % XQ][0]); PIN(xq[v % XQ][1]); }
    if constexpr (N == QB1) { wait_vm<vm_wait(100 + N, 17)>(); bar(); if constexpr (KSP == 2) flag_store(c, F_K + (head * 8 + cb) * 8); SB(); }
    if constexpr (N == QB2) {
      wait_vm<vm_wait(100 + N, 18)>();
      if (w == 0) wait_vm<vm_wait(100 + N, -9)>();
      PIN(kf);
      if (w == 0 && __all((kf[0] == c.ep0 && kf[1] == c.ep1) || lane >= 8)) lds_set(ldsb + KW + 8, 1);
      bar();
      kdone = rfl(lds_word(ldsb + KW + 8)) != 0;
      SB();
    }
    if constexpr (N == QB3) { wait_vm<vm_wait(100 + N, -10)>(); bar(); flag_store(c, F_V + (head * 8 + cb) * 8); SB(); }
    const bf16x8 wf = cat(wq8[N & 7][0], wq8[N & 7][1]);
    qa[0][a] = MFMA16(wf, asbf(xq[v % XQ][0]), v == 0 ? f32x4{} : qa[0][a]);
    SB();
    if constexpr (N + LAQ < 144) rdq(IC<N + LAQ>{}, IC<0>{});
    SB();
    qa[1][a] = MFMA16(wf, asbf(xq[v % XQ][1]), v == 0 ? f32x4{} : qa[1][a]);
    SB();
    if constexpr (N + LAQ < 144) rdq(IC<N + LAQ>{}, IC<1>{});
    if constexpr (!WQL && (N == 1 || N == 4 || N == 6)) issue(IC<18>{}, IC<(N == 1 ? 3 : N == 4 ? 4 : 5)>{});
    if constexpr (WQL && wq_piece(N) >= 0) issue(IC<17 + wq_piece(N) / 6>{}, IC<wq_piece(N) % 6>{});
    if constexpr (N == Q_KPOLL || N == Q_VPOLL) {
      kf = u32x2{0u, 0u};
      asm volatile("s_mov_b32 exec_lo, %3\n\ts_mov_b32 exec_hi, 0\n\ts_nop 4\n\tbuffer_atomic_add_x2 %0, %1, %2, 0 offen sc0 sc1\n\ts_mov_b64 exec, -1"
                   : "+v"(kf) : "v"((N == Q_KPOLL ? F_K : F_V) + (head * 8 + lane) * 8), "s"(c.rws), "s"(w == 0 ? 0xffu : 0u) : "memory");
    }
    if constexpr (N % Q_VST == 2 && N / Q_VST < 8) {
      constexpr int e = N / Q_VST;
      gst16c(pv[e], c.rws, xvo, vst + ((e >> 1) & 1) * 4096 + (e >> 2) * 2048 + (e & 1) * 256);
    }
    if constexpr (a == 7 && v + XQ < 18) ldx(IC<v + XQ>{});
    if constexpr (N > QB3 && N <= QB3 + 16 && (N - QB3) % 4 == 0) {
      constexpr int p = (N - QB3) / 4 - 1;
      if (kdone) dma16c(ldsb + kh * 16384 + w * 4096 + p * 1024, lane * 16, c.rws, kso + p * 1024);
    }
    SB();
  });
#pragma unroll
  for (int A = 0; A < 4; ++A) {
    u32x4 p0, p1;
#pragma unroll
    for (int e = 0; e < 4; ++e) {
      const int t = 2 * A + (e >> 1), k = 2 * (e & 1);
      const unsigned u0 = cvtpk(qa[0][t][k] * QSCALE, qa[0][t][k + 1] * QSCALE), u1 = cvtpk(qa[1][t][k] * QSCALE, qa[1][t][k + 1] * QSCALE);
      const auto s = __builtin_amdgcn_permlane16_swap(u0, u1, false, false);
      const auto r = __builtin_amdgcn_permlane32_swap(s[0], s[1], false, false);
      p0[e] = r[0]; p1[e] = r[1];
    }
    qo.q[2 * A] = p0; qo.q[2 * A + 1] = p1;
  }
  qo.kdone = kdone;
  STAMP(6);
  if (w == 0) wait_vm<0>();
  flag_check(c, kf, F_V + head * 64, w == 0 ? 8 : 0);
}

constexpr float THR = 32.f;

struct AttnState {
  bf16x8 qf[8];
  f32x16 o[4];
  f32x16 S[2][2];
  f32x16 negm;
  u32x4 pk[4];
  bf16x8 fr[8];
  float mhat, l0, l1, fct, mx0, mx1, t0, t1;
  bool pend;
  unsigned kb[2], vb[2];
  unsigned voff, ldsb, w, kbase, vbase, n1, n1k;
  u64 ta, tm; unsigned dummy;
};

constexpr int RD0[16] = {4, 5, 6, 7, 9, 10, 12, 13, 15, -1, -1, -1, -1, -1, -1, -1};
constexpr int RD1[16] = {-1, -1, -1, 8, -1, 11, -1, 14, -1, -1, -1, -1, -1, -1, -1, -1};

template <int CR, int R> DI void mask4(f32x16& s, int dq, float ninf) {
  float x0 = s[R], x1 = s[R + 1], x2 = s[R + 2], x3 = s[R + 3];
  u64 m0, m1, m2;
  asm volatile("v_cmp_le_i32_e64 %4, %9, %7\n\tv_cmp_le_i32_e64 %5, %10, %7\n\tv_cmp_le_i32_e64 %6, %11, %7\n\tv_cmp_le_i32_e32 vcc, %12, %7\n\t"
               "v_cndmask_b32_e64 %0, %8, %0, %4\n\tv_cndmask_b32_e64 %1, %8, %1, %5\n\tv_cndmask_b32_e64 %2, %8, %2, %6\n\tv_cndmask_b32_e32 %3, %8, %3, vcc"
               : "+v"(x0), "+v"(x1), "+v"(x2), "+v"(x3), "=&s"(m0), "=&s"(m1), "=&s"(m2) : "v"(dq), "v"(ninf), "i"(CR), "i"(CR + 1), "i"(CR + 2), "i"(CR + 3) : "vcc");
  s[R] = x0; s[R + 1] = x1; s[R + 2] = x2; s[R + 3] = x3;
}
template <int B, int... Q> DI void mask_rows(f32x16& s, int dq, float ninf, iseq<Q...>) { (mask4<B + 8 * Q, 4 * Q>(s, dq, ninf), ...); }
struct NoHook { template <class T> DI void operator()(T) const {} };
template <int X, bool QK, bool PV, bool VDMA = true, class Hook = NoHook>
DI void attn_iter(Ctx& c, AttnState& a, const unsigned s, const bool first, const bool diag, const int dq, Hook&& hook = Hook{}) {
  constexpr int Y = X ^ 1;
  auto kaddr = [&](int slot, int n) AI { return a.kb[slot] + ((n & 1) * 8 + (n >> 1)) * 1024; };
  auto vaddr = [&](int slot, int n) AI { return a.vb[slot] + ((n & 3) * 4 + (n >> 2)) * 1024; };
  const unsigned sk = s + 2;
  const unsigned sok = a.kbase + (sk + (sk > a.n1 ? sk - a.n1 : 0)) * 16384, sov = a.vbase + (s + (s > a.n1 ? s - a.n1 : 0)) * 16384;
  const unsigned mk = (sk < a.n1k || sk > 16) ? 0xFFFFFC00u : 0u, mv = s < a.n1k ? 0xFFFFFC00u : 0u;
  unsigned vo = 0;
  const unsigned dk = a.ldsb + X * 32768 + a.w * 4096, dv = dk + 65536;
  static_for<16>([&](auto M_) AI {
    constexpr int M = decltype(M_)::v, d = M >> 1, kb = M & 1;
    if constexpr (QK) {
      if constexpr (d == 0) a.S[X][kb] = MFMA32(a.fr[M & 7], a.qf[d], a.negm);
      else a.S[X][kb] = MFMA32(a.fr[M & 7], a.qf[d], a.S[X][kb]);
      if constexpr (RD0[M] >= 0) a.fr[RD0[M] & 7] = lds_b128(kaddr(X, RD0[M]));
      if constexpr (RD1[M] >= 0) a.fr[RD1[M] & 7] = lds_b128(kaddr(X, RD1[M]));
      if constexpr (M == 0) vo = a.voff | mv;
      if constexpr (VDMA && (M == 0 || M == 2 || M == 6 || M == 9)) { constexpr int P = M == 0 ? 0 : M == 2 ? 1 : M == 6 ? 2 : 3; dma16c(dv + P * 1024, vo, c.rws, sov + P * 1024); }
    }
    if constexpr (PV) {
      if constexpr (M < 8) {
        a.S[Y][1][2 * M] = __builtin_amdgcn_exp2f(a.S[Y][1][2 * M]);
        a.S[Y][1][2 * M + 1] = __builtin_amdgcn_exp2f(a.S[Y][1][2 * M + 1]);
        a.pk[M >> 2][M & 3] = cvtpk(a.S[Y][0][2 * M], a.S[Y][0][2 * M + 1]);
        PIN(a.pk[M >> 2][M & 3]);
        if constexpr (M == 0) { a.l0 = fadd_s(a.l0, a.S[Y][0][14]); a.l1 = fadd_s(a.l1, a.S[Y][0][15]); }
      } else {
        constexpr int e = M - 8;
        a.pk[2 + (e >> 2)][e & 3] = cvtpk(a.S[Y][1][2 * e], a.S[Y][1][2 * e + 1]);
        PIN(a.pk[2 + (e >> 2)][e & 3]);
        a.l0 = fadd_s(a.l0, a.S[Y][1][2 * e]); a.l1 = fadd_s(a.l1, a.S[Y][1][2 * e + 1]);
      }
    }
    if constexpr (M == 11) { SB(); if constexpr (QK) wait_vm<9>(); else wait_vm<0>(); hook(IC<0>{}); bar(); hook(IC<1>{}); SB(); }
    if constexpr (PV && M >= 12) a.fr[(M - 12) & 7] = lds_b128(vaddr(Y, M - 12));
    SB();
  });
  static_for<16>([&](auto M_) AI {
    constexpr int M = decltype(M_)::v, ks = M >> 2, db = M & 3;
    if constexpr (PV) {
      a.o[db] = MFMA32(a.fr[M & 7], asbf(a.pk[ks]), a.o[db]);
      if constexpr (RD0[M] >= 0) a.fr[RD0[M] & 7] = lds_b128(vaddr(Y, RD0[M]));
      if constexpr (RD1[M] >= 0) a.fr[RD1[M] & 7] = lds_b128(vaddr(Y, RD1[M]));
    }
    if constexpr (QK) {
      if constexpr (M == 0) vo = a.voff | mk;
      if constexpr (M == 0 || M == 1 || M == 6 || M == 9) { constexpr int P = M == 0 ? 0 : M == 1 ? 1 : M == 6 ? 2 : 3; dma16c(dk + P * 1024, vo, c.rws, sok + P * 1024); }
      if constexpr (M == 2) {
        asm volatile("s_nop 7\n\ts_nop 3" : "+v"(a.S[X][0]), "+v"(a.S[X][1]));
        if (__builtin_expect(diag, 0)) {
          float ninf;
          asm volatile("v_mov_b32 %0, 0xff800000" : "=v"(ninf));
          const int dq2 = dq + (s + 2 == a.n1k ? 64 : 0);
          mask_rows<0>(a.S[X][0], dq2, ninf, make_iseq<4>{}); mask_rows<32>(a.S[X][1], dq2, ninf, make_iseq<4>{});
        }
      }
      if constexpr (M >= 2 && M < 6) {
        constexpr int r = 4 * (M - 2);
        if constexpr (M == 2) {
          a.mx0 = max3f(a.S[X][0][0], a.S[X][0][1], a.S[X][1][0]); a.mx1 = max3f(a.S[X][0][2], a.S[X][0][3], a.S[X][1][1]);
          a.mx0 = max3f(a.mx0, a.S[X][1][2], a.S[X][1][3]);
        } else {
          a.mx0 = max3f(a.mx0, a.S[X][0][r], a.S[X][0][r + 1]); a.mx1 = max3f(a.mx1, a.S[X][0][r + 2], a.S[X][0][r + 3]);
          a.mx0 = max3f(a.mx0, a.S[X][1][r], a.S[X][1][r + 1]); a.mx1 = max3f(a.mx1, a.S[X][1][r + 2], a.S[X][1][r + 3]);
        }
      }
      if constexpr (M == 6) {
        const float mm = max2f(a.mx0, a.mx1);
        auto pr = __builtin_amdgcn_permlane32_swap(__float_as_uint(mm), __float_as_uint(mm), false, false);
        a.mx0 = max2f(__uint_as_float(pr[0]), __uint_as_float(pr[1]));
      }
      if constexpr (M == 7) {
        if (__builtin_expect(first || __any(a.mx0 > THR), 0)) {
          const float dl = first ? fmaxf(a.mx0, -1e30f) : fmaxf(a.mx0, 0.f);
          a.mhat = first ? dl : a.mhat + dl;
          a.fct = __builtin_amdgcn_exp2f(-dl);
#pragma unroll
          for (int r = 0; r < 16; ++r) { a.S[X][0][r] -= dl; a.S[X][1][r] -= dl; a.negm[r] = -a.mhat; }
          a.pend = !first;
        }
      }
      if constexpr (M >= 8) {
        constexpr int e = M - 8;
        a.S[X][0][2 * e] = __builtin_amdgcn_exp2f(a.S[X][0][2 * e]);
        a.S[X][0][2 * e + 1] = __builtin_amdgcn_exp2f(a.S[X][0][2 * e + 1]);
        PIN(a.S[X][0][2 * e]); PIN(a.S[X][0][2 * e + 1]);
        if constexpr (e > 0) {
          a.t0 = e == 1 ? a.S[X][0][0] : fadd_s(a.t0, a.S[X][0][2 * e - 2]); a.t1 = e == 1 ? a.S[X][0][1] : fadd_s(a.t1, a.S[X][0][2 * e - 1]);
          PIN(a.t0); PIN(a.t1);
        }
      }
      if constexpr (M == 11) { SB(); wait_vm<PV ? 9 : 8>(); bar(); }
      if constexpr (M == 13) { asm volatile("s_mov_b64 exec, %2\n\tglobal_load_dword %0, %1, off\n\ts_mov_b64 exec, -1" : "+v"(a.dummy) : "v"(a.ta), "s"(a.tm) : "memory"); a.tm <<= 4; }
      if constexpr (M >= 12) a.fr[(M - 12) & 7] = lds_b128(kaddr(Y, M - 12));
    }
    SB();
  });
}

constexpr unsigned WP = 288;
constexpr unsigned LSYNC = 161280;
constexpr unsigned C_LAND = 0, C_FREE = 64, C_GB = 128, C_CTXD = 136, C_MRG = 140, C_TICK = 144, LSTAT = LSYNC + 256;
#define ARRV 2
constexpr int XM = 12;
constexpr int XDEP(int NK) { return NK == 16 ? XM : 8; }
DI unsigned land(unsigned ph, unsigned q) { return LSYNC + C_LAND + (ph * 5 + q) * 4; }
DI unsigned fre(unsigned ph, unsigned q) { return LSYNC + C_FREE + (ph * 5 + q) * 4; }
DI void cnt_add(Ctx& c, unsigned off) {
  asm volatile("s_mov_b64 exec, 1\n\tds_add_u32 %0, %1\n\ts_mov_b64 exec, -1" ::"v"(c.ldsb + off), "v"(1u) : "memory");
}
#define wlog(...)
DI unsigned cnt_add_rtn(Ctx& c, unsigned off) {
  unsigned r;
  asm volatile("s_mov_b64 exec, 1\n\tds_add_rtn_u32 %0, %1, %2\n\ts_mov_b64 exec, -1\n\ts_waitcnt lgkmcnt(0)" : "=&v"(r) : "v"(c.ldsb + off), "v"(1u) : "memory");
  return rfl(r);
}
DI unsigned cnt_read(Ctx& c, unsigned off) { return __hip_atomic_load((LAS unsigned*)(size_t)(c.ldsb + off), __ATOMIC_RELAXED, __HIP_MEMORY_SCOPE_WORKGROUP); }
DI void prio_lo() { __builtin_amdgcn_s_setprio(0); }
DI void prio_hi(Ctx& c) { __builtin_amdgcn_s_setprio(3); }
DI void prio_gemm(Ctx& c, bool behind) {
  if (c.w >= 4) __builtin_amdgcn_s_setprio(1); else if (behind) __builtin_amdgcn_s_setprio(0); else __builtin_amdgcn_s_setprio(2);
}
DI void cnt_wait(Ctx& c, unsigned off, unsigned target) {
  asm volatile("" ::: "memory");
  unsigned spins = 0;
#pragma unroll 1
  while (rfl(cnt_read(c, off)) < target) { __builtin_amdgcn_s_sleep(1); if (++spins > (1u << 20)) break; }
  asm volatile("" ::: "memory");
}
DI void flag_w(Ctx& c, unsigned flag_off) {
  wait_vm<0>();
  fadd(c.rws, c.lane == 0 ? flag_off : OOB);
}
DI bool cnt_full(Ctx& c, const u32x2& f, unsigned n) { return (((u64)rfl(f[1]) << 32) | rfl(f[0])) == (((u64)c.ep1 << 32) | c.ep0) + n; }
DI void dma16m(unsigned lds_addr, unsigned voff, rsrc_t r, unsigned soff, u64 mask) {
  asm volatile("s_mov_b32 m0, %0\n\ts_mov_b64 exec, %4\n\ts_nop 3\n\tbuffer_load_dwordx4 %1, %2, %3 offen lds\n\ts_mov_b64 exec, -1" ::"s"(lds_addr), "v"(voff), "s"(r), "s"(soff), "s"(mask)
               : "memory", "m0");
}
struct WSrc { unsigned voff[9]; rsrc_t r; unsigned main, tail, pitch, colA, colB; unsigned idx, pend, ph; u32x2 fa, fb; };
DI unsigned w_voff(const WSrc& s, unsigned lane, int p) {
  const unsigned ci = lane + 64 * p, rr = __umul24(ci, 3641) >> 16, cc = ci - __umul24(rr, 18);
  return __umul24(rr, s.pitch - WP) + ci * 16 + (cc < 16 ? s.colA : s.colB - 256);
}
DI unsigned opaque_lane() {
  unsigned lane;
  asm volatile("v_mbcnt_lo_u32_b32 %0, -1, 0\n\tv_mbcnt_hi_u32_b32 %0, -1, %0" : "=v"(lane));
  return lane;
}
template <int NV = 5> DI void wsrc_voff(Ctx& c, WSrc& s) {
  const unsigned lane = opaque_lane();
#pragma unroll
  for (int p = 0; p < NV; ++p) s.voff[p] = w_voff(s, lane, p);
  if constexpr (NV == 5) {
    s.voff[5] = lane >= 16 ? ~0u : 0u;
    const unsigned l7 = (lane & 7) < 5 ? lane & 7 : 0;
    s.voff[6] = c.ldsb + (lane < 8 ? LSYNC + C_TICK + (s.ph * 5 + l7) * 4 : fre(s.ph - 1, 4) - 4 * l7);
    s.voff[7] = lane == 0 ? 3 : 8;
  }
}
template <int NV = 0> DI void wsrc_setup(Ctx& c, WSrc& s, rsrc_t r, unsigned pitch, unsigned colA, unsigned colB, unsigned rowMain, unsigned rowTail, unsigned ph) {
  s.r = r; s.pitch = pitch; s.main = rowMain * pitch; s.tail = rowTail * pitch;
  s.colA = colA; s.colB = colB; s.ph = ph; s.idx = s.pend = 0;
  if constexpr (NV > 0) wsrc_voff<NV>(c, s);
}
DI void w_group(Ctx& c, const WSrc& s, unsigned q) {
  const unsigned r0 = 128 * q + 32 * (c.w & 3);
#pragma unroll
  for (int p = 0; p < 9; ++p) dma16(c.ldsb + r0 * WP + p * 1024, s.voff[p], s.r, s.main + r0 * s.pitch);
}
DI void poll_w(Ctx& c, unsigned off, unsigned n) {
  const unsigned voff = c.lane == 0 ? off : OOB;
  const u64 t0 = __builtin_amdgcn_s_memrealtime();
  prio_lo();
  for (;;) {
    u32x2 f;
    fget(f, c.rws, voff);
    asm volatile("s_waitcnt vmcnt(0)" : "+v"(f)::"memory");
    if (cnt_full(c, f, n)) break;
    if (__builtin_amdgcn_s_memrealtime() - t0 > 5000000ull) break;
  }
  prio_hi(c);
}
constexpr unsigned nitems(unsigned q) { return q == 4 ? 3 : 8; }
DI bool w_try(Ctx& c, WSrc& s) {
  if (s.idx) return false;
  unsigned idx, t;
#pragma unroll 1
  for (;;) {
    const unsigned v = __hip_atomic_load((LAS unsigned*)(size_t)s.voff[6], __ATOMIC_RELAXED, __HIP_MEMORY_SCOPE_WORKGROUP);
    const unsigned m = (unsigned)__builtin_amdgcn_ballot_w64(v < s.voff[7]);
    if ((m & 31) == 0) s.idx = 1;
    const unsigned av = m & ~(m >> 8) & 31;
    if (av == 0) return false;
    idx = __builtin_ctz(av);
    t = cnt_add_rtn(c, LSYNC + C_TICK + (s.ph * 5 + idx) * 4);
    if (t < (idx ? 8u : 3u)) break;
  }
  s.pend += 1u << (4 * idx);
  wlog(c, s.ph, 1, idx, true); wlog(c, s.ph, 2, idx);
  const unsigned lrow = 128 * (4 - idx) + 16 * t, so = (idx ? s.main + lrow * s.pitch : s.tail + 16 * t * s.pitch), sk = lrow == 544 ? 8 * s.pitch : 0u;
  const unsigned la = c.ldsb + lrow * WP;
  dma16(la, s.voff[0], s.r, so);
  dma16(la + 1024, s.voff[1], s.r, so);
  dma16(la + 2048, s.voff[2] + (s.voff[5] & sk), s.r, so);
  dma16(la + 3072, s.voff[3], s.r, so + sk);
  dma16m(la + 4096, s.voff[4], s.r, so + sk, 0xffffffffull);
  return true;
}
DI void w_signal(Ctx& c, const WSrc& s, unsigned items) {
  if (items) {
    unsigned lane;
    asm volatile("v_mbcnt_lo_u32_b32 %0, -1, 0" : "=v"(lane));
    const unsigned n = (items >> (4 * lane)) & 15, ad = c.ldsb + land(s.ph, 4) - 4 * lane;
    asm volatile("s_mov_b64 exec, 31\n\tds_add_u32 %0, %1\n\ts_mov_b64 exec, -1" ::"v"(ad), "v"(n) : "memory");
  }
}
DI void w_flush(Ctx& c, WSrc& s) { w_signal(c, s, s.pend); s.pend = 0; }
#define PSL 8
#define WMAX 2
#define FMAX 0
#define NMAX WMAX
#define LSRV 7
template <unsigned MX = WMAX> DI void w_try_wait(Ctx& c, WSrc& s, bool en = true) {
  const unsigned old = s.pend;
  unsigned k = 0;
  s.pend = 0;
#pragma unroll 1
  while (en && k < MX && w_try(c, s)) ++k;
  if (k == 0) wait_vm<0>(); else if (k == 1) wait_vm<5>(); else if (k == 2) wait_vm<10>(); else if (k == 3) wait_vm<15>(); else wait_vm<20>();
  w_signal(c, s, old);
}
DI void w_need(Ctx& c, WSrc& s, unsigned q, unsigned n) {
  unsigned spins = 0;
  wsrc_voff(c, s);
#pragma unroll 1
  for (;;) {
    w_try_wait<NMAX>(c, s);
    if (s.pend) { wait_vm<0>(); w_flush(c, s); }
    if (rfl(cnt_read(c, land(s.ph, q))) >= n || ++spins > (1u << 20)) break;
    __builtin_amdgcn_s_sleep(1);
  }
  asm volatile("" ::: "memory");
}
DI void flag_srv(Ctx& c, WSrc& s, unsigned flag_off, bool lsrv) {
  w_try_wait<FMAX>(c, s, lsrv || c.w < 4);
  fadd(c.rws, c.lane == 0 ? flag_off : OOB);
}
DI void poll_srv(Ctx& c, WSrc& s, unsigned flag_off, unsigned n, bool lsrv) {
  const unsigned voff = c.lane == 0 ? flag_off : OOB;
  const u64 t0 = __builtin_amdgcn_s_memrealtime();
  prio_lo();
  for (;;) {
    u32x2 f;
    fget(f, c.rws, voff);
    if (s.pend) { wait_vm<1>(); w_flush(c, s); }
    w_try_wait(c, s, lsrv || c.w < 4);
    PIN(f);
    if (cnt_full(c, f, n)) break;
    if (__builtin_amdgcn_s_memrealtime() - t0 > 5000000ull) break;
  }
  prio_hi(c);
}
DI void poll_done(WSrc& s) {}
constexpr unsigned LB1 = LSTAT + 256, LB2 = LB1 + 320;
template <int P> DI void park_load(Ctx& c, const u16* w1, const u16* b1, const u16* w2, const u16* b2) {
  const unsigned xcd = c.b & 7, nt = (c.b >> 3) & 3, nt32 = xcd * 4 + nt, w = (c.w & 3) * 2 + (P & 1);
  const unsigned rr = c.lane >= 18 ? 1 : 0, cc = c.lane - 18 * rr, r0 = (w < 4 ? 40 : 48) + 2 * w;
  const unsigned c1 = cc < 16 ? nt32 * 256 + cc * 16 : 8192 + nt32 * 32 + (cc - 16) * 16, c2 = cc < 16 ? nt * 256 + cc * 16 : 1024 + nt * 32 + (cc - 16) * 16;
  if constexpr (P < 2) dma16m(c.ldsb + (512 + 2 * w) * WP, rr * (FF * 2) + c1, mk_rsrc(w1, DM * FF * 2), (512 + r0) * (FF * 2), 0xfffffffffull);
  else if constexpr (P < 4) dma16m(c.ldsb + (528 + 2 * w) * WP, rr * (DM * 2) + c2, mk_rsrc(w2, FF * DM * 2), (4096 + 64 * xcd + r0) * (DM * 2), 0xfffffffffull);
  else {
    const bool f = c.w == 4;
    dma16m(c.ldsb + (f ? LB1 : LB2 + (c.w - 5) * 384), f ? c1 : c.lane * 16, mk_rsrc(f ? b1 : b2, f ? FF * 2 : DM * 2), f ? 0 : (c.w - 5) * 384, f ? 0x3ffffull : 0xffffffull);
  }
}
DI void park_read(Ctx& c, u32x2 (&tl)[9], u32x2 (&tl2)[9]) {
  const unsigned ad = c.ldsb + (512 + (c.lane >> 2)) * WP + (c.lane & 3) * 8;
#pragma unroll
  for (int a = 0; a < 9; ++a) { tl[a] = __builtin_bit_cast(u32x2, lds_tr(ad + a * 32)); tl2[a] = __builtin_bit_cast(u32x2, lds_tr(ad + 16 * WP + a * 32)); }
  SB();
  cnt_add(c, fre(0, 4)); wlog(c, 1, 0, 0);
#pragma unroll
  for (int a = 0; a < 9; ++a) { PIN(tl[a]); PIN(tl2[a]); }
}
constexpr int KK(int NK, int v) { return NK == 18 ? (v < 2 ? 16 + v : 4 * (3 - (v - 2) / 4) + (v - 2) % 4) : 4 * (3 - v / 4) + v % 4; }
constexpr bool QSTART(int NK, int v) { return v < NK && v > 0 && (KK(NK, v) & 3) == 0; }
constexpr bool QEND(int NK, int v) { return (KK(NK, v) & 3) == 3 || KK(NK, v) == 17; }
template <int NK, int V> DI void x_ld(Ctx& c, u32x4 (&x)[2], unsigned voff, unsigned so, unsigned so2) {
  constexpr int k = KK(NK, V);
  gld16c2<(k & 1) * 2048>(x[0], x[1], voff, c.rws, k < 16 ? so + (k >> 1) * 4096 : so2);
}
template <int NK, int V0 = 0, int NV = 8> DI void x_pro(Ctx& c, u32x4 (&x)[XM][2], unsigned voff, unsigned so, unsigned so2) {
  static_for<NV>([&](auto V_) AI { x_ld<NK, V0 + decltype(V_)::v>(c, x[V0 + decltype(V_)::v], voff, so, so2); });
}
DI void x_ld4_masked(u32x4& a, u32x4& b, u32x4& d, u32x4& e, unsigned voff, rsrc_t r, unsigned so, unsigned mask) {
  asm volatile("s_cmp_eq_u32 %7, 0\n\ts_cbranch_scc1 .Lxs%=\n\ts_nop 4\n\tbuffer_load_dwordx4 %0, %4, %5, %6 offen sc1\n\tbuffer_load_dwordx4 %1, %4, %5, %6 offen offset:256 sc1\n\t"
               "buffer_load_dwordx4 %2, %4, %5, %6 offen offset:2048 sc1\n\tbuffer_load_dwordx4 %3, %4, %5, %6 offen offset:2304 sc1\n.Lxs%=:"
               : "+v"(a), "+v"(b), "+v"(d), "+v"(e) : "v"(voff), "s"(r), "s"(so), "s"(mask) : "memory", "scc");
}
template <int IMM> DI void x_ld2_if(u32x4& a, u32x4& b, unsigned voff, rsrc_t r, unsigned so, unsigned skip) {
  asm volatile("s_cmp_lg_u32 %5, 0\n\ts_cbranch_scc1 .Lxi%=\n\ts_nop 4\n\tbuffer_load_dwordx4 %0, %2, %3, %4 offen offset:%6 sc1\n\tbuffer_load_dwordx4 %1, %2, %3, %4 offen offset:%7 sc1\n.Lxi%=:"
               : "=&v"(a), "=&v"(b) : "v"(voff), "s"(r), "s"(so), "s"(skip), "i"(IMM), "i"(IMM + 256) : "memory", "scc");
}
template <int V> DI void x_ld16_if(Ctx& c, u32x4 (&x)[2], unsigned voff, unsigned so, unsigned skip) {
  constexpr int k = KK(16, V);
  x_ld2_if<(k & 1) * 2048>(x[0], x[1], voff, c.rws, so + (k >> 1) * 4096, skip);
}
DI void dma16_if(unsigned lds_addr, unsigned voff, rsrc_t r, unsigned soff, unsigned go) {
  asm volatile("s_cmp_eq_u32 %4, 0\n\ts_cbranch_scc1 .Ldi%=\n\ts_mov_b32 m0, %0\n\ts_nop 3\n\tbuffer_load_dwordx4 %1, %2, %3 offen lds\n.Ldi%=:" ::"s"(lds_addr), "v"(voff), "s"(r), "s"(soff), "s"(go)
               : "memory", "m0", "scc");
}
DI void x_pro_masked16(Ctx& c, u32x4 (&x)[XM][2], unsigned so, unsigned mask) {
  const unsigned voff = x_voff(c);
#pragma unroll
  for (int p = 0; p < XM / 2; ++p) x_ld4_masked(x[2 * p][0], x[2 * p][1], x[2 * p + 1][0], x[2 * p + 1][1], voff, c.rws, so + (KK(16, 2 * p) >> 1) * 4096, mask);
}
template <int L> DI void wlane(unsigned& v, unsigned s) { asm volatile("v_writelane_b32 %0, %1, %2" : "+v"(v) : "s"(s), "i"(L)); }
struct Sched {
  int NK, XD;
  constexpr int T0() const { return 9 * (NK - 4); }
  constexpr int sv(int n) const { return n < T0() ? n / 9 : NK - 4 + (n - T0()) % 4; }
  constexpr int sa(int n) const { return n < T0() ? n % 9 : ((n - T0()) / 4 == 0 ? 8 : (n - T0()) / 4 - 1); }
  constexpr int g0(int G) const { return G == 3 ? T0() + 8 : 9 * (NK - 16 + 1 + 4 * G) + 1; }
  constexpr int gn(int G) const { return G == 0 ? 4 : 9; }
  constexpr int wg(int n) const { for (int G = 0; G < 4; ++G) if (n >= g0(G) && (n - g0(G)) % 3 == 0 && (n - g0(G)) / 3 < gn(G)) return G; return -1; }
  constexpr int wp(int n) const { return (n - g0(wg(n))) / 3; }
  constexpr int gsig(int n) const { for (int G = 0; G < 3; ++G) if (n == g0(G) + 3 * (gn(G) - 1) + 27) return G; return -1; }
  constexpr int st(int n) const {
    const int m = n - T0();
    if (m == 6) return 0;
    if (m >= 13 && (m - 13) % 8 == 0) return 1 + 2 * ((m - 13) / 8);
    if (m >= 15 && (m - 15) % 8 == 0) return 2 + 2 * ((m - 15) / 8);
    return -1;
  }
  constexpr bool sp(int n) const { return n < T0() ? (n % 18 == 0 && n >= 18) : ((n - T0()) % 18 == 0); }
  constexpr int between(int n0, int n1) const {
    int cnt = 0;
    for (int i = n0; i < n1; ++i) { if (i < T0() && sa(i) == 8 && sv(i) + XD < NK) cnt += 2; if (st(i) >= 0) cnt += 1; }
    return cnt;
  }
  constexpr int younger(int tag, int n, bool ldr) const {
    int cnt = 0, last = -1;
    for (int v = 0; v < XD; ++v) { if (100 + v == tag) last = cnt + 1; cnt += 2; }
    for (int i = 0; i < n; ++i) {
      if (i < T0() && sa(i) == 8 && sv(i) + XD < NK) { if (100 + sv(i) + XD == tag) last = cnt + 1; cnt += 2; }
      if (ldr && wg(i) >= 0) { if (200 + wg(i) == tag) last = cnt; cnt += 1; }
      if (st(i) >= 0) cnt += 1;
    }
    return cnt - 1 - last;
  }
};
template <int NK, int KORD, int PH, int GST, class St>
DI void gemm_g(Ctx& c, f32x4 (&acc)[2][9], u32x4 (&x)[XM][2], u32x2 (&tl)[9], u32x2 (&tl2)[9], WSrc& ws, WSrc& wn, unsigned so, unsigned so2, unsigned flag_off, St&& st) {
  constexpr unsigned RT = KORD == 0 ? 16 : 8;
  constexpr int LA = 6, NS = NK * 9;
  constexpr int XD = XDEP(NK);
  constexpr Sched S{NK, XD};
  constexpr bool LD = PH < 2;
  const unsigned g = c.lane >> 4, q = (c.lane & 15) >> 2, pp = c.lane & 3;
  const unsigned wrow1 = 16 * (g >> 1) + 4 * (g & 1) + q, wrow = KORD == 0 ? 4 * g + q : wrow1;
  unsigned wb[3], wt[2];
#pragma unroll
  for (int i = 0; i < 3; ++i) { wb[i] = c.ldsb + wrow * WP + pp * 8 + i * 7 * 32 * WP; PIN(wb[i]); }
  if constexpr (NK == 18) {
    wt[0] = c.ldsb + (512 + wrow1) * WP + pp * 8; wt[1] = c.ldsb + (544 + wrow1 - 8 * (g >> 1)) * WP + pp * 8;
    PIN(wt[0]); PIN(wt[1]);
  }
  unsigned voff = x_voff(c); PIN(voff);
  bf16x4 wl[8][2];
  auto rdw = [&](auto N_, auto H_) AI {
    constexpr int N = decltype(N_)::v, H = decltype(H_)::v, v = S.sv(N), a = S.sa(N), k = KK(NK, v);
    if constexpr (k < 16) wl[N & 7][H] = lds_tr(wb[k / 7] + ((k % 7) * 32 + H * RT) * WP + a * 32);
    else if constexpr (k == 16) wl[N & 7][H] = lds_tr(wt[0] + H * 8 * WP + a * 32);
    else if constexpr (H == 0) wl[N & 7][H] = lds_tr(wt[1] + a * 32);
    else {
      u32x2 t;
      asm volatile("s_nop 2\n\tv_mov_b64 %0, %1" : "=v"(t) : "v"(tl[a]));
      wl[N & 7][1] = __builtin_bit_cast(bf16x4, t);
    }
    if constexpr (LD && H == 1 && ((N < S.T0() && a == 8 && QEND(NK, v)) || N == NS - 1)) { SB(); cnt_add(c, fre(PH, k >> 2)); wlog(c, PH + 1, 0, 4 - (k >> 2)); }
  };
  bool behind = false;
  if constexpr (PH > 0) {
    if (ws.pend) { wait_vm<2 * XD>(); w_flush(c, ws); }
    const unsigned l4 = cnt_read(c, land(PH, 4)), f0 = cnt_read(c, fre(PH - 1, 0));
    behind = (c.w < 4) & (rfl(f0) < 8);
    if (__builtin_expect(rfl(l4) < 3, 0)) w_need(c, ws, 4, 3);
  }
  prio_gemm(c, behind);
  static_for<LA>([&](auto N) AI { rdw(N, IC<0>{}); rdw(N, IC<1>{}); });
  unsigned lc = 0;
  SB();
  static_for<NS>([&](auto N_) AI {
    constexpr int N = decltype(N_)::v, v = S.sv(N), a = S.sa(N);
    if constexpr (N < S.T0() ? a == 0 : N < S.T0() + 4) {
      wait_vm<S.younger(100 + v, N, false)>();
      PIN(x[v % XD][0]); PIN(x[v % XD][1]);
    }
    if constexpr (N < S.T0() && a == 0) {
      if constexpr (v == 0 || v == 8) STAMPG(GST + (v >> 3));
      if constexpr (PH > 0 && v == 0) poll_done(ws);
      if constexpr (PH == 0 && v == ARRV) { if (c.w < 4) { wait_vm<S.between(0, N)>(); cnt_add(c, land(0, 2)); } }
      constexpr bool chk2 = QSTART(NK, v + 2), chk1 = QSTART(NK, v + 1);
      if constexpr (chk1) {
        if (__builtin_expect(rfl(lc) < (PH > 0 ? 8 : 4), 0)) { if constexpr (PH > 0) { prio_hi(c); w_need(c, ws, KK(NK, v + 1) >> 2, 8); prio_gemm(c, behind); } else cnt_wait(c, land(0, KK(NK, v + 1) >> 2), 4); }
      }
      if constexpr (PH == 0 && v == 7) park_read(c, tl, tl2);
      if constexpr (chk2) lc = cnt_read(c, land(PH, KK(NK, v + 2) >> 2));
    }
    if constexpr (PH > 0 && S.sp(N) && N < S.T0()) {
      if (__builtin_expect(behind, 0)) { behind = rfl(cnt_read(c, fre(PH - 1, 0))) < 8; prio_gemm(c, behind); }
    }
    SB();
    const bf16x8 wf = cat(wl[N & 7][0], wl[N & 7][1]);
    acc[0][a] = MFMA16(wf, asbf(x[v % XD][0]), acc[0][a]);
    SB();
    if constexpr (N + LA < NS) rdw(IC<N + LA>{}, IC<0>{});
    SB();
    acc[1][a] = MFMA16(wf, asbf(x[v % XD][1]), acc[1][a]);
    SB();
    if constexpr (N + LA < NS) rdw(IC<N + LA>{}, IC<1>{});
    if constexpr (N < S.T0() && a == 8 && v + XD < NK) x_ld<NK, v + XD>(c, x[v % XD], voff, so, so2);
    if constexpr (S.st(N) >= 0) st(IC<S.st(N)>{});
    SB();
  });
  prio_hi(c);
  st(IC<7>{}); st(IC<8>{});
  STAMPG(GST + 2);
  if constexpr (LD) { wsrc_voff(c, wn); flag_srv(c, wn, flag_off, LSRV & (PH ? 4 : 1)); } else flag_w(c, flag_off);
}
DI f32x4 relu2(const f32x4& a) {
  const float inf = 3.4028234664e38f;
  const float r0 = __builtin_amdgcn_fmed3f(a[0], 0.f, inf), r1 = __builtin_amdgcn_fmed3f(a[1], 0.f, inf), r2 = __builtin_amdgcn_fmed3f(a[2], 0.f, inf), r3 = __builtin_amdgcn_fmed3f(a[3], 0.f, inf);
  return f32x4{r0 * r0, r1 * r1, r2 * r2, r3 * r3};
}
DI u32x4 pack_single(const f32x4& r0, const f32x4& r1) {
  const auto a = __builtin_amdgcn_permlane32_swap(cvtpk(r0[0], r0[1]), cvtpk(r1[0], r1[1]), false, false);
  const auto b = __builtin_amdgcn_permlane32_swap(cvtpk(r0[2], r0[3]), cvtpk(r1[2], r1[3]), false, false);
  return u32x4{a[0], b[0], a[1], b[1]};
}
template <int I> DI void part_store(Ctx& c, const f32x4 (&acc)[2][9], unsigned base, unsigned so) {
  const unsigned g = c.lane >> 4;
  if constexpr (I == 0) gst16c(pack_single(acc[0][8], acc[1][8]), c.rws, (g >> 1) * (4608 - 128) + base + 1024, so);
  else { constexpr int i = (I - 1) >> 1, u = (I - 1) & 1; gst16c(pack_pair(acc[u][2 * i], acc[u][2 * i + 1]), c.rws, base, so + u * 4608 + i * 256); }
}
DI unsigned part_base(Ctx& c) { const unsigned l15 = c.lane & 15; return (l15 >> 2) * 1152 + (l15 & 3) * 16 + (c.lane >> 4) * 64; }
struct Red { u32x4 p[8]; u32x4 s; u32x4 alo, ahi; };
DI void red_sel(Ctx& c, Red& d) {
  const unsigned i = c.lane & 15, one = 0x3F80u << ((i & 1) * 16);
#pragma unroll
  for (int e = 0; e < 4; ++e) { d.alo[e] = (i >> 1) == (unsigned)e ? one : 0u; d.ahi[e] = (i >> 1) == (unsigned)e + 4 ? one : 0u; }
  PIN(d.alo); PIN(d.ahi);
}
DI void red_load(Ctx& c, Red& d, unsigned so) {
  const unsigned vo = (c.lane >> 4) * 36864 + (c.lane & 15) * 16;
  gld16c<1024>(d.s, vo + ((c.lane >> 3) & 1) * (4 * 36864 - 128), c.rws, so);
  static_for<4>([&](auto P_) AI {
    constexpr int P = decltype(P_)::v;
    gld16c<P * 256>(d.p[2 * P], vo, c.rws, so);
    gld16c<P * 256>(d.p[2 * P + 1], vo, c.rws, so + 4 * 36864);
  });
}
#define RED_PIN(d) asm volatile("s_waitcnt vmcnt(0)" : "+v"(d.p[0]), "+v"(d.p[1]), "+v"(d.p[2]), "+v"(d.p[3]), "+v"(d.p[4]), "+v"(d.p[5]), "+v"(d.p[6]), "+v"(d.p[7]), "+v"(d.s)::"memory")
DI void gld8(u32x2& d, unsigned voff, rsrc_t r, unsigned soff) { asm volatile("s_nop 4\n\tbuffer_load_dwordx2 %0, %1, %2, %3 offen" : "=v"(d) : "v"(voff), "s"(r), "s"(soff) : "memory"); }
DI bool sing_lane(Ctx& c) { return (c.lane & 40) == 0; }
DI void job_offs(Ctx& c, unsigned row, unsigned& o0, unsigned& o2) {
  const unsigned pr = c.lane >> 4, g = (c.lane >> 2) & 3, nt = c.w & 3;
  o0 = row * (DM * 2) + (nt * 128 + pr * 32 + 4 * g) * 2;
  o2 = row * (DM * 2) + (512 + nt * 16 + 4 * (g & 1) + 8 * (pr & 1)) * 2;
}
DI void row_load(Ctx& c, u32x2 (&r)[3], rsrc_t rs, unsigned row) {
  unsigned o0, o2; job_offs(c, row, o0, o2);
  gld8(r[0], o0, rs, 0); gld8(r[1], o0, rs, 32); gld8(r[2], o2, rs, 0);
}
DI void red_sum(const Red& d, float (&a)[12]) {
  f32x4 s0 = MFMA16(asbf(d.alo), asbf(d.p[0]), f32x4{}), s1 = MFMA16(asbf(d.alo), asbf(d.p[2]), f32x4{}), s2 = MFMA16(asbf(d.alo), asbf(d.s), f32x4{});
  s0 = MFMA16(asbf(d.alo), asbf(d.p[1]), s0); s1 = MFMA16(asbf(d.alo), asbf(d.p[3]), s1);
  s0 = MFMA16(asbf(d.ahi), asbf(d.p[4]), s0); s1 = MFMA16(asbf(d.ahi), asbf(d.p[6]), s1);
  s0 = MFMA16(asbf(d.ahi), asbf(d.p[5]), s0); s1 = MFMA16(asbf(d.ahi), asbf(d.p[7]), s1);
#pragma unroll
  for (int e = 0; e < 4; ++e) {
    a[8 + e] = s2[e] + __uint_as_float(__builtin_amdgcn_update_dpp(0, __float_as_uint(s2[e]), 0x128, 0xf, 0xf, false));
    const auto r = __builtin_amdgcn_permlane16_swap(__float_as_uint(s0[e]), __float_as_uint(s1[e]), false, false);
    a[e] = __uint_as_float(r[0]); a[4 + e] = __uint_as_float(r[1]);
  }
}
DI float sum_tok4(float x) {
  x += __uint_as_float(__builtin_amdgcn_update_dpp(0, __float_as_uint(x), 0x124, 0xf, 0xf, false));
  x += __uint_as_float(__builtin_amdgcn_update_dpp(0, __float_as_uint(x), 0x128, 0xf, 0xf, false));
  { auto r = __builtin_amdgcn_permlane16_swap(__float_as_uint(x), __float_as_uint(x), false, false); x = __uint_as_float(r[0]) + __uint_as_float(r[1]); }
  return xhalf_sum(x);
}
DI void ln2_job(Ctx& c, const Red& d, const u32x2 (&xr)[3], unsigned (&hw)[6], unsigned row0) {
  const unsigned lane = c.lane, tok = lane & 3, g = (lane >> 2) & 3, pr = lane >> 4, nt = c.w & 3;
  const unsigned stat = c.ldsb + LSTAT + (c.w >> 2) * 128;
  const bool sing = sing_lane(c);
  float a[12], v[12];
  red_sum(d, a);
#pragma unroll
  for (int e = 0; e < 6; ++e) {
    const unsigned xx = xr[e >> 1][e & 1];
    hw[e] = cvtpk(bflo(xx) + a[2 * e], bfhi(xx) + a[2 * e + 1]); v[2 * e] = bflo(hw[e]); v[2 * e + 1] = bfhi(hw[e]);
  }
  const float s1 = ((v[0] + v[1]) + (v[2] + v[3])) + ((v[4] + v[5]) + (v[6] + v[7])), s0 = (v[8] + v[9]) + (v[10] + v[11]);
  const float mw = sum_tok4(sing ? s1 + s0 : s1) * (1.f / 144);
  float s2 = 0.f, s3 = 0.f;
#pragma unroll
  for (int e = 0; e < 8; ++e) { const float t = v[e] - mw; s2 += t * t; }
#pragma unroll
  for (int e = 0; e < 4; ++e) { const float t = v[8 + e] - mw; s3 += t * t; }
  s2 = sum_tok4(sing ? s2 + s3 : s2);
  if (lane < 4) *(LAS f32x2*)(size_t)(stat + (lane * 4 + nt) * 8) = f32x2{mw, s2};
  SB();
  cnt_add(c, LSYNC + C_GB + (c.w >> 2) * 4);
  const unsigned row = row0 + tok, mb = row >> 5, r32 = row & 31;
  unsigned o0 = ((mb * 36 + 2 * (nt * 4 + pr) + (g >> 1)) * 64 + (g & 1) * 32 + r32) * 16;
  unsigned o1 = lane < 8 ? ((mb * 36 + 32 + nt) * 64 + g * 32 + r32) * 16 : OOB;
  PIN(o0); PIN(o1);
  cnt_wait(c, LSYNC + C_GB + (c.w >> 2) * 4, 4);
  const f32x4 sa = *(LAS f32x4*)(size_t)(stat + tok * 32), sb = *(LAS f32x4*)(size_t)(stat + tok * 32 + 16);
  const float mean = ((sa[0] + sa[2]) + (sb[0] + sb[2])) * 0.25f;
  const float t0 = sa[0] - mean, t1 = sa[2] - mean, t2 = sb[0] - mean, t3 = sb[2] - mean;
  const float m2 = ((sa[1] + sa[3]) + (sb[1] + sb[3])) + 144.f * ((t0 * t0 + t1 * t1) + (t2 * t2 + t3 * t3));
  const float rs = rsqrtf(m2 * (1.f / DM) + 1e-5f);
#pragma unroll
  for (int e = 0; e < 12; ++e) v[e] = (v[e] - mean) * rs;
  gst16c(u32x4{cvtpk(v[0], v[1]), cvtpk(v[2], v[3]), cvtpk(v[4], v[5]), cvtpk(v[6], v[7])}, c.rws, o0, WS_HN);
  const unsigned u0 = cvtpk(v[8], v[9]), u1 = cvtpk(v[10], v[11]);
  const auto ra = __builtin_amdgcn_permlane16_swap(u0, u0, false, false), rb = __builtin_amdgcn_permlane16_swap(u1, u1, false, false);
  gst16c(u32x4{ra[0], rb[0], ra[1], rb[1]}, c.rws, o1, WS_HN);
}

#define XS0 4
#define XS1 12
DI unsigned phase_attn(Ctx& c, QOut& qo, WSrc& wso, u32x4 (&xr)[XM][2], const u16* wo, const u16* w1, const u16* b1, const u16* w2, const u16* b2) {
  AttnState a;
  const unsigned ldsb = c.ldsb, lane = c.lane, w = c.w, b = c.b;
  const unsigned xcd = b & 7, jj = b >> 3, head = xcd * 4 + (jj >> 3), pi = jj & 7;
  const unsigned l31 = lane & 31, h = lane >> 5;
  const unsigned kh = w >> 2, rg = kh ? 7 - w : w, partner = 7 - w;
  const unsigned n1 = 2 * pi + 2, n1k = kh ? n1 : 0, n1t = kh ? n1 : 99;
  a.voff = lane * 16; a.ldsb = ldsb; a.w = w; a.n1 = n1; a.n1k = n1k;
  a.kbase = WS_K + head * (SEQ * HD * 2) + w * 4096; a.vbase = WS_V + head * (SEQ * HD * 2) + w * 4096;
  a.kb[0] = ldsb + lane * 16; a.kb[1] = a.kb[0] + 32768; a.vb[0] = a.kb[0] + 65536; a.vb[1] = a.kb[0] + 98304;
  PIN(a.kb[0]); PIN(a.kb[1]); PIN(a.vb[0]); PIN(a.vb[1]);
  if (kh == 0) {
#pragma unroll
    for (int d = 0; d < 8; ++d) *(LAS u32x4*)(size_t)(ldsb + LE + rg * 8192 + d * 1024 + lane * 16) = qo.q[d];
  }
  if (__builtin_expect(!qo.kdone, 0)) {
    wait_flags(c, F_K + head * 64, 8, 1);
#pragma unroll
    for (int p = 0; p < 4; ++p) dma16c(ldsb + kh * 16384 + w * 4096 + p * 1024, a.voff, c.rws, a.kbase + p * 1024);
  }
  STAMP(8);
#pragma unroll
  for (int d = 0; d < 4; ++d) a.o[d] = f32x16{};
#pragma unroll
  for (int d = 0; d < 8; ++d) a.qf[d] = asbf(qo.q[d]);
  a.negm = f32x16{}; a.mhat = 0.f; a.l0 = a.l1 = 0.f; a.fct = 1.f; a.pend = false;
  {
    unsigned bytes;
    asm volatile("s_mov_b32 %0, .Lfb_end-.Lfb_begin" : "=s"(bytes));
    const unsigned g = lane * 8 + w, sel = g < 162 ? 0 : g < 324 ? 1 : 2, li = g - 162 * sel, r = (li * 57) >> 9, cl = li - 9 * r;
    const unsigned o2 = (r < 16 ? 512 * xcd + jj * 16 + r : 4096 + 64 * xcd + jj * 2 + r - 16) * (DM * 2) + cl * 128;
    const unsigned o1 = (jj * 18 + r) * (FF * 2) + (cl < 8 ? xcd * 1024 + cl * 128 : 8192 + xcd * 128);
    const unsigned oc = ((g - 468) * 32 + jj) * 128;
    const bool code = g >= 468;
    const u64 base = code ? c.pc0 : sel == 0 ? (u64)w2 : sel == 1 ? (u64)w1 : (u64)wo;
    a.ta = base + (code ? (oc < bytes ? oc : 0) : sel == 1 ? o1 : o2);
    a.dummy = 0; a.tm = 15;
    PIN(a.ta);
  }
  if (kh == 0) wait_vm<0>();
  bar();
  STAMP(9);
#pragma unroll
  for (int n = 0; n < 4; ++n) a.fr[n] = lds_b128(a.kb[0] + ((n & 1) * 8 + (n >> 1)) * 1024);
  const int dq = (int)(rg * 32 + l31) - (int)(kh * 64) - 4 * (int)h;
  const unsigned dA = rg < 2 ? n1t - 2 : 99, dB = n1t - 1, dC = (kh || rg < 2) ? 16 : 99;

  const unsigned mtH = jj >> 2, fH = F_CTX + (mtH * 32 + xcd * 4) * 8;
  u32x2 fl = {0u, 0u};
  auto post = [&](auto X_, unsigned s) AI {
    constexpr int X = decltype(X_)::v;
    if constexpr (X == 0) {
      if (__builtin_expect(s == n1t, 0)) {
        const float inv = __builtin_amdgcn_rcpf(xhalf_sum(a.l0 + a.l1));
        const unsigned so = WS_CTX + ((pi * 4 + rg) * 256 + head * 8) * 1024;
#pragma unroll
        for (int d = 0; d < 4; ++d) {
          gst16c(pack8<0>(a.o[d], inv), c.rws, a.voff, so + d * 2048);
          gst16c(pack8<1>(a.o[d], inv), c.rws, a.voff, so + d * 2048 + 1024);
          a.o[d] = f32x16{};
        }
        a.l0 = a.l1 = 0.f;
        a.vb[1] += 16384;
      }
      if (__builtin_expect(s + 2 == n1t, 0)) a.kb[0] += 16384;
    } else {
      if (__builtin_expect(s == n1 + 1, 0)) flag_store(c, F_CTX + (pi * 32 + head) * 8);
      if (__builtin_expect(s == 15, 0)) flag_load(c, fl, fH, kh ? 0 : 4);
    }
    if (__builtin_expect(a.pend, 0)) {
#pragma unroll
      for (int d = 0; d < 4; ++d)
#pragma unroll
        for (int r = 0; r < 16; ++r) a.o[d][r] *= a.fct;
      a.l0 *= a.fct; a.l1 *= a.fct; a.pend = false;
    }
    a.l0 = fadd_s(a.l0, a.t0); a.l1 = fadd_s(a.l1, a.t1);
    if constexpr (X == 1) {
      if (__builtin_expect(s + 1 == n1t, 0)) {
#pragma unroll
        for (int d = 0; d < 8; ++d) a.qf[d] = lds_b128(ldsb + LE + rg * 8192 + d * 1024 + lane * 16);
        a.negm = f32x16{};
        a.kb[1] += 16384; a.vb[0] += 16384;
      }
    }
    SB();
  };
  attn_iter<0, true, false>(c, a, 0, true, dA == 0, dq);
  post(IC<0>{}, 0);
  for (unsigned s = 1; s < 17; s += 2) {
    attn_iter<1, true, true>(c, a, s, false, s == dB, dq); post(IC<1>{}, s);
    attn_iter<0, true, true>(c, a, s + 1, s + 1 == n1t, s + 1 == dA || s + 1 == dC, dq); post(IC<0>{}, s + 1);
  }
  if (w == 0) *(LAS unsigned*)(size_t)(ldsb + LSYNC + lane * 4) = 0;
  wsrc_setup<0>(c, wso, mk_rsrc(wo, NQ * DM * 2), DM * 2, (jj & 3) * 256, 1024 + (jj & 3) * 32, 512 * xcd, 0, 0);
  const unsigned ol = opaque_lane();
  wso.voff[0] = w_voff(wso, ol, 0); PIN(wso.voff[0]);
#pragma unroll
  for (int e = 0; e < 8; ++e) { a.pk[e >> 2][e & 3] = cvtpk(a.S[0][0][2 * e], a.S[0][0][2 * e + 1]); PIN(a.pk[e >> 2][e & 3]); }
  a.l0 = fadd_s(a.l0, a.S[0][0][14]); a.l1 = fadd_s(a.l1, a.S[0][0][15]);
  if (n1k == 16) wait_vm<8>(); else wait_vm<0>();
  bar();
  constexpr auto KSM = [](int m) constexpr { return m < 8 ? m >> 2 : 2 + (m & 1); };
  constexpr auto DBM = [](int m) constexpr { return m < 8 ? m & 3 : (m - 8) >> 1; };
  auto rdv = [&](auto M_) AI { constexpr int m = decltype(M_)::v; a.fr[m & 7] = lds_b128(a.vb[0] + (DBM(m) * 4 + KSM(m)) * 1024); };
  static_for<6>([&](auto M_) AI { rdv(M_); });
  PIN(fl);
  asm volatile("" ::"v"(a.dummy));
  STAMPG(33);
  const unsigned late = ((unsigned)__builtin_amdgcn_ballot_w64(fl[0] != c.ep0 || fl[1] != c.ep1) & 15u) | kh;
  const bool slow = late != 0 && kh == 0;
  const unsigned stat = ldsb + 544 * WP + rg * 512 + lane * 8, scr = ldsb + rg * 16384 + lane * 16;
  const unsigned xo = WS_CTX + ((mtH * 4 + (w & 3)) * 256 + xcd * 32) * 1024, xv = x_voff(c);
  const unsigned r3 = 384 + 32 * (w & 3);
  float lt = 0.f;
  auto put = [&](auto D_) AI {
    constexpr int d = decltype(D_)::v;
#pragma unroll
    for (int e = 0; e < 4; ++e) *(LAS f32x4*)(size_t)(scr + (d * 4 + e) * 1024) = f32x4{a.o[d][4 * e], a.o[d][4 * e + 1], a.o[d][4 * e + 2], a.o[d][4 * e + 3]};
  };
  if (__builtin_expect(slow, 0)) flag_load(c, fl, fH, 4);
  SB();
  static_for<16>([&](auto M_) AI {
    constexpr int M = decltype(M_)::v;
    a.o[DBM(M)] = MFMA32(a.fr[M & 7], asbf(a.pk[KSM(M)]), a.o[DBM(M)]);
    SB();
    if constexpr (M + 6 < 16) rdv(IC<M + 6>{});
    if constexpr (M < 9) dma16_if(ldsb + r3 * WP + M * 1024, wso.voff[M], wso.r, wso.main + r3 * wso.pitch, kh);
    if constexpr (M < 8) { wso.voff[M + 1] = w_voff(wso, ol, M + 1); PIN(wso.voff[M + 1]); }
    if constexpr (M < 4) {
#pragma unroll
      for (int e = 4 * M; e < 4 * M + 4; ++e) { a.S[0][1][e] = __builtin_amdgcn_exp2f(a.S[0][1][e]); PIN(a.S[0][1][e]); }
    } else if constexpr (M < 8) {
#pragma unroll
      for (int e = 2 * (M - 4); e < 2 * (M - 4) + 2; ++e) {
        a.pk[2 + (e >> 2)][e & 3] = cvtpk(a.S[0][1][2 * e], a.S[0][1][2 * e + 1]); PIN(a.pk[2 + (e >> 2)][e & 3]);
        a.l0 = fadd_s(a.l0, a.S[0][1][2 * e]); a.l1 = fadd_s(a.l1, a.S[0][1][2 * e + 1]);
      }
    }
    if constexpr (M == 8) { lt = xhalf_sum(a.l0 + a.l1); PIN(lt); }
    if constexpr (M >= XS0 && M - XS0 < XS1) x_ld16_if<M - XS0>(c, xr[M - XS0], xv, xo, late);
    if constexpr (M == 12) { if (kh == 0) { *(LAS f32x2*)(size_t)stat = f32x2{a.mhat, lt}; put(IC<0>{}); } }
    if constexpr (M == 14) { if (kh == 0) put(IC<1>{}); }
    SB();
  });
  STAMPG(34);
  if (kh == 0) { put(IC<2>{}); put(IC<3>{}); } else wait_vm<0>();
  STAMP(10);
  STAMPG(35);
  bar();
  STAMPG(36);
  if (kh == 0) {
    if (n1 == 16) flag_store(c, F_CTX + (7 * 32 + head) * 8);
    w_group(c, wso, 2);
    if (__builtin_expect(slow, 0)) { wait_vm<9>(); flag_check(c, fl, fH, 4); }
  }
  static_for<XM - XS1>([&](auto V_) AI { constexpr int v = XS1 + decltype(V_)::v; x_ld16_if<v>(c, xr[v], xv, xo, late); });
  if (kh) {
    f32x4 y[2][4];
    auto get = [&](int d) AI {
#pragma unroll
      for (int e = 0; e < 4; ++e) y[d & 1][e] = *(LAS f32x4*)(size_t)(scr + (d * 4 + e) * 1024);
    };
    const f32x2 ps = *(LAS f32x2*)(size_t)stat;
    get(0); get(1);
    SB();
    const float mm = fmaxf(a.mhat, ps[0]);
    const float fo = __builtin_amdgcn_exp2f(a.mhat - mm), fp = __builtin_amdgcn_exp2f(ps[0] - mm);
    const float inv = __builtin_amdgcn_rcpf(lt * fo + ps[1] * fp), f = fo * inv, f2 = fp * inv;
    const unsigned so = WS_CTX + (((15 - pi) * 4 + rg) * 256 + head * 8) * 1024;
#pragma unroll
    for (int d = 0; d < 4; ++d) {
      f32x16 x = a.o[d];
#pragma unroll
      for (int e = 0; e < 16; ++e) x[e] = x[e] * f + y[d & 1][e >> 2][e & 3] * f2;
      u32x4 p0 = pack8<0>(x), p1 = pack8<1>(x);
      PIN(p0); PIN(p1);
      SB();
      if (d < 2) get(d + 2);
      if (d == 1) { SB(); cnt_add(c, LSYNC + C_MRG); }
      gst16c(p0, c.rws, lane * 16, so + d * 2048);
      gst16c(p1, c.rws, lane * 16, so + d * 2048 + 1024);
      SB();
    }
    STAMPG(37);
    cnt_wait(c, LSYNC + C_MRG, 4);
    w_group(c, wso, 1);
    wait_vm<9>();
    STAMPG(38);
    fput(u32x2{c.ep0, c.ep1}, c.rws, lane == 0 ? F_CX2 + (((7 - pi) * 4 + rg) * 32 + head) * 8 : OOB);
    const unsigned fL = F_CX2 + ((mtH * 4 + (w & 3)) * 32 + xcd * 4) * 8;
    flag_load(c, fl, fL, 4);
    static_for<5>([&](auto P_) AI { park_load<decltype(P_)::v>(c, w1, b1, w2, b2); });
    w_group(c, wso, 0);
    wait_vm<9>(); cnt_add(c, land(0, 1));
    wait_vm<0>(); cnt_add(c, land(0, 0));
    STAMPG(39);
    prio_lo();
    flag_check(c, fl, fL, 4);
  }
  STAMP(11);
  return late;
}

struct TId { unsigned sp, nt, mt, nt32, mb, own, wq; bool isH; };
DI TId tids(Ctx& c) {
  fresh(c);
  TId t;
  const unsigned xcd = c.b & 7, jj = c.b >> 3;
  t.isH = c.w < 4; t.wq = c.w & 3; t.sp = xcd; t.nt = jj & 3; t.mt = (t.isH ? 0 : 8) + (jj >> 2);
  t.nt32 = xcd * 4 + t.nt; t.mb = t.mt * 4 + t.wq; t.own = t.nt * 8 + t.sp;
  return t;
}
DI void acc_zero(f32x4 (&acc)[2][9]) {
#pragma unroll
  for (int u = 0; u < 2; ++u)
#pragma unroll
    for (int a = 0; a < 9; ++a) acc[u][a] = f32x4{};
}
DI void phase_tail(Ctx& c, WSrc& wso, u32x4 (&xr)[XM][2], unsigned xlate, const u16* x, const u16* w1, const u16* b1, const u16* w2, const u16* b2, u16* out) {
  f32x4 acc[2][9]; u32x2 tl[9], tl2[9]; unsigned hw[6]; WSrc ws, wt;
  {
    const TId t = tids(c);
    const unsigned xo = WS_CTX + (t.mb * 256 + t.sp * 32) * 1024;
    prio_hi(c);
    STAMPG(12);
    x_pro_masked16(c, xr, xo, xlate);
    wsrc_setup(c, ws, mk_rsrc(w1, DM * FF * 2), FF * 2, t.nt32 * 256, 8192 + t.nt32 * 32, 0, 512, 1);
    acc_zero(acc);
    const unsigned pb = part_base(c), po = WS_P1 + (((t.mt * 4 + t.nt) * 8 + t.sp) * 8 + 2 * t.wq) * 4608;
    gemm_g<16, 1, 0, 13>(c, acc, xr, tl, tl2, wso, ws, xo, 0, cnt_at(t.mt, N_P1 + t.wq * 4 + t.nt), [&](auto I) AI { part_store<decltype(I)::v>(c, acc, pb, po); });
    STAMPG(16);
  }
  {
    const TId t = tids(c);
    u32x2 xres[3]; Red rd;
    row_load(c, xres, mk_rsrc(x, SEQ * DM * 2), t.mt * 128 + t.own * 4 + (c.lane & 3));
    unsigned dummy = 0;
    w_touch<4>(c, dummy, w2, w2, w2, c.w, false);
    poll_srv(c, ws, cnt_at(t.mt, N_P1 + t.nt * 4 + t.wq), 8, LSRV & 1);
    red_load(c, rd, WS_P1 + (t.mt * 256 + t.wq * 64 + (t.own >> 2)) * 4608 + (t.own & 3) * 1152);
    red_sel(c, rd);
    STAMPG(17);
    asm volatile("" ::"v"(dummy));
    PIN(xres[0]); PIN(xres[1]); PIN(xres[2]);
    RED_PIN(rd);
    poll_done(ws);
    w_flush(c, ws);
    STAMPG(18);
    ln2_job(c, rd, xres, hw, t.mt * 128 + t.own * 4);
#pragma unroll
    for (int e = 0; e < 6; ++e) PIN(hw[e]);
    STAMPG(19);
    flag_srv(c, ws, cnt_at(t.mt, N_HN + t.nt), LSRV & 2);
    STAMPG(20);
    poll_srv(c, ws, cnt_at(t.mt, N_HN + t.wq), 32, LSRV & 2);
    STAMPG(21);
    asm volatile("" ::"v"(dummy));
    const unsigned xh = WS_HN + t.mb * 36 * 1024;
    x_pro<18>(c, xr, x_voff(c), xh, xh + 32768);
    wsrc_setup(c, wt, mk_rsrc(w2, FF * DM * 2), DM * 2, t.nt * 256, 1024 + t.nt * 32, 512 * t.sp, 4096 + 64 * t.sp, 2);
#pragma unroll
    for (int a = 0; a < 9; ++a) {
      const u32x2 bias = *(LAS u32x2*)(size_t)(c.ldsb + LB1 + a * 32 + (c.lane >> 4) * 8);
      acc[0][a] = acc[1][a] = f32x4{bflo(bias[0]), bfhi(bias[0]), bflo(bias[1]), bfhi(bias[1])};
    }
    const unsigned vo = x_voff(c), ho = WS_H1 + (t.mb * 288 + 8 * t.nt32) * 1024, hs = WS_H1 + (t.mb * 288 + 256 + t.nt32) * 1024;
    gemm_g<18, 0, 1, 22>(c, acc, xr, tl, tl, ws, wt, xh, xh + 32768, cnt_at(t.mt, N_H1 + t.wq * 8 + t.sp), [&](auto I_) AI {
      constexpr int I = decltype(I_)::v;
      if constexpr (I == 0) {
        const unsigned g = c.lane >> 4;
        gst16c(pack_single(relu2(acc[0][8]), relu2(acc[1][8])), c.rws, ((g & 1) * 32 + (g >> 1) * 16 + (c.lane & 15)) * 16, hs);
      } else {
        constexpr int i = (I - 1) >> 1, u = (I - 1) & 1;
        gst16c(pack_pair(relu2(acc[u][2 * i]), relu2(acc[u][2 * i + 1])), c.rws, vo, ho + i * 2048 + u * 256);
      }
    });
    STAMPG(25);
  }
  {
    const TId t = tids(c);
    poll_srv(c, wt, cnt_at(t.mt, N_H1 + t.wq * 8 + t.sp), 4, LSRV & 4);
    STAMPG(26);
    const unsigned x1 = WS_H1 + (t.mb * 288 + t.sp * 32) * 1024, x1t = WS_H1 + (t.mb * 288 + 256 + t.sp * 4) * 1024;
    x_pro<18>(c, xr, x_voff(c), x1, x1t);
    acc_zero(acc);
    const unsigned pb = part_base(c), po = WS_P2 + (((t.mt * 4 + t.nt) * 8 + t.sp) * 8 + 2 * t.wq) * 4608;
    gemm_g<18, 0, 2, 27>(c, acc, xr, tl2, tl2, wt, wt, x1, x1t, cnt_at(t.mt, N_P2 + t.wq * 4 + t.nt), [&](auto I) AI { part_store<decltype(I)::v>(c, acc, pb, po); });
    STAMPG(30);
  }
  {
    const TId t = tids(c);
    float hb[12]; Red rd;
    {
      unsigned o0, o2; job_offs(c, 0, o0, o2);
      const u32x2 bb[3] = {*(LAS u32x2*)(size_t)(c.ldsb + LB2 + o0), *(LAS u32x2*)(size_t)(c.ldsb + LB2 + o0 + 32), *(LAS u32x2*)(size_t)(c.ldsb + LB2 + o2)};
#pragma unroll
      for (int e = 0; e < 6; ++e) { const unsigned bv = bb[e >> 1][e & 1]; hb[2 * e] = bflo(bv) + bflo(hw[e]); hb[2 * e + 1] = bfhi(bv) + bfhi(hw[e]); PIN(hb[2 * e]); PIN(hb[2 * e + 1]); }
      red_sel(c, rd);
    }
    poll_w(c, cnt_at(t.mt, N_P2 + t.nt * 4 + t.wq), 8);
    STAMPG(31);
    red_load(c, rd, WS_P2 + (t.mt * 256 + t.wq * 64 + (t.own >> 2)) * 4608 + (t.own & 3) * 1152);
    RED_PIN(rd);
    poll_done(wt);
    const rsrc_t ro = mk_rsrc(out, SEQ * DM * 2);
    float a[12]; unsigned ow[6];
    red_sum(rd, a);
#pragma unroll
    for (int e = 0; e < 6; ++e) ow[e] = cvtpk(a[2 * e] + hb[2 * e], a[2 * e + 1] + hb[2 * e + 1]);
    {
      const unsigned g = (c.lane >> 2) & 3, ro0 = (t.mt * 128 + t.own * 4 + (c.lane & 3)) * (DM * 2);
      u32x4 q, q2;
      q[0] = __builtin_amdgcn_update_dpp(ow[0], ow[2], 0x114, 0xf, 0xa, false); q[1] = __builtin_amdgcn_update_dpp(ow[1], ow[3], 0x114, 0xf, 0xa, false);
      q[2] = __builtin_amdgcn_update_dpp(ow[2], ow[0], 0x104, 0xf, 0x5, false); q[3] = __builtin_amdgcn_update_dpp(ow[3], ow[1], 0x104, 0xf, 0x5, false);
      q2[0] = ow[4]; q2[1] = ow[5];
      q2[2] = __builtin_amdgcn_update_dpp(0, ow[4], 0x104, 0xf, 0xf, false); q2[3] = __builtin_amdgcn_update_dpp(0, ow[5], 0x104, 0xf, 0xf, false);
      __builtin_amdgcn_raw_buffer_store_b128(q, ro, ro0 + t.wq * 256 + (c.lane >> 4) * 64 + (g & 1) * 32 + (g >> 1) * 16, 0, 0);
      __builtin_amdgcn_raw_buffer_store_b128(q2, ro, (c.lane & 44) == 0 ? ro0 + 1024 + t.wq * 32 + (c.lane >> 4) * 16 : OOB, 0, 0);
    }
    STAMPG(32);
    if (c.b == 0 && c.w == 0) gst8c(u32x2{c.ep0, c.ep1}, c.rws, c.lane == 0 ? F_EPOCH : OOB, WS_FLAGS);
  }
}

__global__ __launch_bounds__(512, 2) void fused_block(const u16* __restrict__ x, const u16* __restrict__ ckv, const u16* __restrict__ wq,
                                                      const u16* __restrict__ wuk, const u16* __restrict__ wuv, const u16* __restrict__ wo,
                                                      const u16* __restrict__ w1, const u16* __restrict__ b1, const u16* __restrict__ w2,
                                                      const u16* __restrict__ b2, u16* __restrict__ out, char* __restrict__ ws) {
  extern __shared__ __attribute__((aligned(16))) char lds[];
  Ctx c;
  asm volatile("s_getpc_b64 %0\n.Lfb_begin:" : "=s"(c.pc0));
  c.ldsb = (unsigned)(size_t)lds; c.lane = threadIdx.x & 63; c.w = rfl(threadIdx.x >> 6); c.b = blockIdx.x;
  c.rws = mk_rsrc(ws, WS_END);
  const unsigned pr0 = (unsigned)__builtin_amdgcn_s_memrealtime(), pc0 = (unsigned)__builtin_amdgcn_s_memtime();
  c.px = pr0;
  STAMP(0);
  QOut qo;
  WSrc wso; u32x4 xr[XM][2];
  phase_qkv(c, qo, x, ckv, wq, wuk, wuv);
  fresh(c);
  const unsigned xlate = phase_attn(c, qo, wso, xr, wo, w1, b1, w2, b2);
  fresh(c);
  phase_tail(c, wso, xr, xlate, x, w1, b1, w2, b2, out);
  asm volatile(".Lfb_end:");
  if (PROBE == -2) c.px = (unsigned)__builtin_amdgcn_s_memrealtime();
  if (PROBE == -3) c.px = pr0 + ((unsigned)__builtin_amdgcn_s_memtime() - pc0) / 20;
  {
    const unsigned lim = 15000 + (c.px - pr0);
    while (c.w == 0 && c.b == 255 && (unsigned)__builtin_amdgcn_s_memrealtime() - pr0 < lim) __builtin_amdgcn_s_sleep(8);
  }
}

extern "C" void kernel_launch(void* const* d_in, const int* in_sizes, int n_in, void* d_out, int out_size, void* d_ws, size_t ws_size,
                              hipStream_t stream) {
  hipFuncSetAttribute((const void*)fused_block, hipFuncAttributeMaxDynamicSharedMemorySize, 163840);
  fused_block<<<256, 512, 163840, stream>>>((const u16*)d_in[0], (const u16*)d_in[1], (const u16*)d_in[2], (const u16*)d_in[3], (const u16*)d_in[4],
                                            (const u16*)d_in[5], (const u16*)d_in[7], (const u16*)d_in[8], (const u16*)d_in[9], (const u16*)d_in[10],
                                            (u16*)d_out, (char*)d_ws);
}
```

```cpp
#include <hip/hip_runtime.h>
#include <stdint.h>
#include <utility>

typedef __bf16 bf16x8 __attribute__((ext_vector_type(8)));
typedef __bf16 bf16x4 __attribute__((ext_vector_type(4)));
typedef __bf16 bf16x2 __attribute__((ext_vector_type(2)));
typedef float f32x16 __attribute__((ext_vector_type(16)));
typedef float f32x4 __attribute__((ext_vector_type(4)));
typedef float f32x2 __attribute__((ext_vector_type(2)));
typedef unsigned u32x4 __attribute__((ext_vector_type(4)));
typedef unsigned u32x2 __attribute__((ext_vector_type(2)));
typedef unsigned short u16;
typedef unsigned long long u64;
#define LAS __attribute__((address_space(3)))
#define DI __device__ __forceinline__
#define AI __attribute__((always_inline))
typedef __amdgpu_buffer_rsrc_t rsrc_t;

constexpr int SEQ = 2048, DM = 576, NH = 32, HD = 128, DC = 512, FF = 4608, NQ = 4096;

constexpr unsigned WS_FLAGS = 0;
constexpr unsigned WS_XN = 131072;
constexpr unsigned WS_K = WS_XN + SEQ * DM * 2;
constexpr unsigned WS_V = WS_K + SEQ * NQ * 2;
constexpr unsigned WS_CTX = WS_V + SEQ * NQ * 2;
constexpr unsigned WS_P1 = WS_CTX + SEQ * NQ * 2;
constexpr unsigned WS_H = WS_P1 + 8 * SEQ * DM * 2;
constexpr unsigned WS_HN = WS_H + SEQ * DM * 2;
constexpr unsigned WS_H1 = WS_HN + SEQ * DM * 2;
constexpr unsigned WS_P2 = WS_H1 + SEQ * FF * 2;
constexpr unsigned WS_CNT = WS_P2 + 8 * SEQ * DM * 2;
constexpr unsigned WS_END = WS_CNT + 16 * 80 * 128;
constexpr unsigned F_EPOCH = 0, F_XN = 4096, F_KV = 8192, F_CTX = 12288, F_K = 81920, F_V = 90112, F_CX2 = 98304;

constexpr unsigned N_P1 = 0, N_P2 = 16, N_HN = 32, N_H1 = 36;
constexpr unsigned cnt_at(unsigned mt, unsigned i) { return WS_CNT + (mt * 80 + i) * 128; }

template <int... Is> using iseq = std::integer_sequence<int, Is...>;
template <int N> using make_iseq = std::make_integer_sequence<int, N>;
template <int I> struct IC { static constexpr int v = I; };
template <class F, int... Is> DI void static_for_(F&& f, iseq<Is...>) { (f(IC<Is>{}), ...); }
template <int N, class F> DI void static_for(F&& f) { static_for_(f, make_iseq<N>{}); }

#define SB() __builtin_amdgcn_sched_barrier(0)
#define PIN(x) asm volatile("" : "+v"(x))
DI unsigned rfl(unsigned x) { return __builtin_amdgcn_readfirstlane(x); }
DI rsrc_t mk_rsrc(const void* p, unsigned bytes) { return __builtin_amdgcn_make_buffer_rsrc((void*)p, (short)0, (int)bytes, 0x00020000); }
#define VPD
#define VP
constexpr unsigned OOB = 0xFFFFFF00u;
DI void dma16(unsigned lds_addr, unsigned voff, rsrc_t r, unsigned soff) {
  asm volatile(VP VPD "s_mov_b32 m0, %0\n\ts_nop 0\n\tbuffer_load_dwordx4 %1, %2, %3 offen lds" ::"s"(lds_addr), "v"(voff), "s"(r), "s"(soff) : "memory", "m0");
}
DI void dma16c(unsigned lds_addr, unsigned voff, rsrc_t r, unsigned soff) {
  asm volatile(VP "s_mov_b32 m0, %0\n\ts_nop 0\n\tbuffer_load_dwordx4 %1, %2, %3 offen sc1 lds" ::"s"(lds_addr), "v"(voff), "s"(r), "s"(soff) : "memory", "m0");
}
template <int IMM> DI void gld16c(u32x4& d, unsigned voff, rsrc_t r, unsigned soff) {
  asm volatile(VP "buffer_load_dwordx4 %0, %1, %2, %3 offen offset:%4 sc1" : "=v"(d) : "v"(voff), "s"(r), "s"(soff), "i"(IMM) : "memory");
}
template <int IMM> DI void gld16(u32x4& d, unsigned voff, rsrc_t r, unsigned soff) {
  asm volatile(VP "buffer_load_dwordx4 %0, %1, %2, %3 offen offset:%4" : "=v"(d) : "v"(voff), "s"(r), "s"(soff), "i"(IMM) : "memory");
}
DI void gld8c(u32x2& d, unsigned voff, rsrc_t r, unsigned soff) {
  asm volatile(VP "buffer_load_dwordx2 %0, %1, %2, %3 offen sc1" : "=v"(d) : "v"(voff), "s"(r), "s"(soff) : "memory");
}
DI void gst16c(u32x4 v, rsrc_t r, unsigned voff, unsigned soff) {
  asm volatile(VP "buffer_store_dwordx4 %0, %1, %2, %3 offen sc1\n\ts_nop 1" ::"v"(v), "v"(voff), "s"(r), "s"(soff) : "memory");
}
DI void gst8c(u32x2 v, rsrc_t r, unsigned voff, unsigned soff) { __builtin_amdgcn_raw_buffer_store_b64(v, r, voff, soff, 16); }
DI void fput(u32x2 v, rsrc_t r, unsigned voff) { asm volatile("s_nop 4\n\tbuffer_atomic_swap_x2 %0, %1, %2, 0 offen sc1\n\ts_nop 1" ::"v"(v), "v"(voff), "s"(r) : "memory"); }
DI void fadd(rsrc_t r, unsigned voff) { asm volatile("s_nop 4\n\tbuffer_atomic_add_x2 %0, %1, %2, 0 offen sc1\n\ts_nop 1" ::"v"(u32x2{1u, 0u}), "v"(voff), "s"(r) : "memory"); }
DI void fget(u32x2& d, rsrc_t r, unsigned voff) {
  d = u32x2{0u, 0u};
  asm volatile("s_nop 4\n\tbuffer_atomic_add_x2 %0, %1, %2, 0 offen sc0 sc1" : "+v"(d) : "v"(voff), "s"(r) : "memory");
}
template <int N> DI void wait_vm() { asm volatile("s_waitcnt vmcnt(%0)" ::"i"(N) : "memory"); }
DI void bar() { asm volatile("" ::: "memory"); __builtin_amdgcn_s_waitcnt(0xC07F); __builtin_amdgcn_s_barrier(); asm volatile("" ::: "memory"); }
DI unsigned cvtpk(float lo, float hi) { f32x2 v = {lo, hi}; bf16x2 b = __builtin_convertvector(v, bf16x2); return __builtin_bit_cast(unsigned, b); }
DI float bflo(unsigned x) { return __uint_as_float(x << 16); }
DI float bfhi(unsigned x) { return __uint_as_float(x & 0xffff0000u); }
DI bf16x8 lds_b128(unsigned a) { return *(LAS bf16x8*)(size_t)a; }
DI bf16x4 lds_tr(unsigned a) { return __builtin_amdgcn_ds_read_tr16_b64_v4bf16((LAS bf16x4*)(size_t)a); }
DI bf16x8 cat(bf16x4 a, bf16x4 b) { return __builtin_shufflevector(a, b, 0, 1, 2, 3, 4, 5, 6, 7); }
DI bf16x8 asbf(u32x4 x) { return __builtin_bit_cast(bf16x8, x); }
#define MFMA32(a, b, c) __builtin_amdgcn_mfma_f32_32x32x16_bf16((a), (b), (c), 0, 0, 0)
#define MFMA16(a, b, c) __builtin_amdgcn_mfma_f32_16x16x32_bf16((a), (b), (c), 0, 0, 0)
template <int S> DI u32x4 pack8(const f32x16& x, float sc) {
  u32x4 w;
  w[0] = cvtpk(x[8 * S + 0] * sc, x[8 * S + 1] * sc); w[1] = cvtpk(x[8 * S + 2] * sc, x[8 * S + 3] * sc);
  w[2] = cvtpk(x[8 * S + 4] * sc, x[8 * S + 5] * sc); w[3] = cvtpk(x[8 * S + 6] * sc, x[8 * S + 7] * sc);
  return w;
}
template <int S> DI u32x4 pack8(const f32x16& x) {
  u32x4 w;
  w[0] = cvtpk(x[8 * S + 0], x[8 * S + 1]); w[1] = cvtpk(x[8 * S + 2], x[8 * S + 3]);
  w[2] = cvtpk(x[8 * S + 4], x[8 * S + 5]); w[3] = cvtpk(x[8 * S + 6], x[8 * S + 7]);
  return w;
}
DI float max3f(float a, float b, float c) { float r; asm("v_max3_f32 %0, %1, %2, %3" : "=v"(r) : "v"(a), "v"(b), "v"(c)); return r; }
DI float max2f(float a, float b) { float r; asm("v_max_f32_e32 %0, %1, %2" : "=v"(r) : "v"(a), "v"(b)); return r; }
DI float fadd_s(float a, float b) { float r; asm("v_add_f32_e32 %0, %1, %2" : "=v"(r) : "v"(a), "v"(b)); return r; }
DI float xhalf_sum(float x) {
  auto r = __builtin_amdgcn_permlane32_swap(__float_as_uint(x), __float_as_uint(x), false, false);
  return __uint_as_float(r[0]) + __uint_as_float(r[1]);
}
DI float wave_sum(float x) {
#pragma unroll
  for (int o = 1; o < 64; o <<= 1) x += __shfl_xor(x, o);
  return x;
}

#define STAMP(i)

struct Ctx {
  unsigned ldsb, w, lane, b;
  unsigned ep0, ep1;
  rsrc_t rws;
  u64 pc0;
};

DI void fresh(Ctx& c) {
  asm volatile("v_mbcnt_lo_u32_b32 %0, -1, 0\n\tv_mbcnt_hi_u32_b32 %0, -1, %0" : "=v"(c.lane));
  asm volatile("" : "+s"(c.w), "+s"(c.b), "+s"(c.ldsb));
}
DI void code_touch(Ctx& c, unsigned& dummy) {
  unsigned bytes;
  asm volatile("s_mov_b32 %0, .Lfb_end-.Lfb_begin" : "=s"(bytes));
  const rsrc_t r = mk_rsrc((const void*)c.pc0, bytes);
  asm volatile("s_nop 4\n\tbuffer_load_dword %0, %1, %2, 0 offen" : "+v"(dummy) : "v"(c.w == 0 ? (c.lane * 32 + (c.b >> 3)) * 128 : OOB), "s"(r) : "memory");
}
DI void publish(Ctx& c, unsigned flag_off) {
  wait_vm<0>();
  bar();
  fput(u32x2{c.ep0, c.ep1}, c.rws, (c.w == 0 && c.lane == 0) ? flag_off : OOB);
}
DI void poll_flags(Ctx& c, unsigned flag_off, unsigned n, unsigned stride) {
  const unsigned voff = c.lane < n ? flag_off + c.lane * 8 * stride : OOB;
  const u64 t0 = __builtin_amdgcn_s_memrealtime();
  for (;;) {
    u32x2 f;
    fget(f, c.rws, voff);
    asm volatile("s_waitcnt vmcnt(0)" : "+v"(f)::"memory");
    if (__all((f[0] == c.ep0 && f[1] == c.ep1) || c.lane >= n)) break;
    if (__builtin_amdgcn_s_memrealtime() - t0 > 5000000ull) break;
    __builtin_amdgcn_s_sleep(2);
  }
}
DI void wait_flags(Ctx& c, unsigned flag_off, unsigned n, unsigned stride) {
  if (c.w == 0) poll_flags(c, flag_off, n, stride);
  bar();
}

#define DBG_DRAIN()
#define DBG_NX(x)
#define STAMPG(i)
#define OAUX 0
#define OAUX2 OAUX
#define SK1 1
#define MXA 2
#define MXB 2
#define MXC 2
#define MXD 2
DI void flag_store(Ctx& c, unsigned flag_off) {
  fput(u32x2{c.ep0, c.ep1}, c.rws, (c.w == 0 && c.lane == 0) ? flag_off : OOB);
}
DI void flag_load(Ctx& c, u32x2& f, unsigned flag_off, unsigned n) { fget(f, c.rws, c.lane < n ? flag_off + c.lane * 8 : OOB); }
DI void flag_check(Ctx& c, u32x2& f, unsigned flag_off, unsigned n) {
  PIN(f);
  if (__builtin_expect(!__all((f[0] == c.ep0 && f[1] == c.ep1) || c.lane >= n), 0)) poll_flags(c, flag_off, n, 1);
}

constexpr float QSCALE = 0.08838834764831845f * 1.4426950408889634f;
constexpr unsigned LE = 131072;
constexpr unsigned LX = 147456;
constexpr unsigned KW = LX + 12288;
#define ROT 0
constexpr unsigned SLOT = 49152;
constexpr int slot_of(int S) { return (S + 2) % 3; }
constexpr int XQ = 6;
constexpr int LAQ = 6;
constexpr int QB1 = 40, QB2 = 88, QB3 = 116;
constexpr int Q_KPOLL = 70, Q_VPOLL = 134, Q_VST = 12;
#define PRO 0
#define XLS 1
constexpr int XLS1 = XLS > 0 ? XLS : 1, XL0 = 31 - 5 * XLS1, XFL = XLS == 0 ? 19 : ((XL0 - 8) | 1) - ((XL0 & 1) ? 0 : 2);
#define KSP 2
#define TWV 5
#define TWQ 13
#define XT 1
#define LNE 1
#define PRO_N 12
#define PRO_B 5
constexpr int PRO_U[18] = {0, 0, 0, 0, 0, 0, 1, 1, 1, 1, 1, 1, 2, 2, 2, 2, 2, 2}, PRO_I[18] = {0, 4, 1, 2, 5, 3, 0, 4, 1, 2, 5, 3, 0, 1, 2, 3, 4, 5}, PRO_X = LNE ? LNE - 1 : 5;
#define WQL 1
#define WQ2 41
constexpr int wq_piece(int q) { return q >= 1 && q < 25 && (q - 1) % 4 == 0 ? (q - 1) / 4 : q >= WQ2 && q < WQ2 + 24 && (q - WQ2) % 4 == 0 ? 6 + (q - WQ2) / 4 : -1; }
constexpr int vm_wait(int pos, int tag) {
  int seq[512] = {}; int n = 0;
  auto push = [&](int t, int cnt) { for (int i = 0; i < cnt; ++i) seq[n++] = t; };
  for (int i = 0; i < PRO_N; ++i) { push(PRO_U[i], 1); if (i == PRO_X) push(-1, 3); }
  if (PRO_N == 18) push(-12, 1);
  bool done = false;
  for (int T = 0; T < 32 && !done; ++T) {
    if (pos == T) { done = true; break; }
    if (T == 0 && PRO_N < 18) { for (int i = PRO_N; i < 18; ++i) push(PRO_U[i], 1); push(-12, 1); }
    if (T == 3) push(-2, 3);
    if (T == TWV) push(-6, 1);
    if (T == TWQ) push(-6, 1);
    if (T == 7) push(-3, 1);
    if (T == XFL) push(-5, 1);
    if (KSP == 1 && T == 27) push(-8, 1);
    if (T >= 16 && (T - 16) % KSP == 0 && (T - 16) / KSP < 8) push(-7, 1);
    if (T >= 1 && (!WQL || T < 29)) push((T - 1) / 2 + 3, 3);
    if (XLS == 0 && T >= 29) push(200 + 2 * (T - 29), 2), push(201 + 2 * (T - 29), 2);
    if (XLS > 0 && T >= XL0 && (T - XL0) % XLS1 == 0) push(200 + (T - XL0) / XLS1, 2);
  }
  for (int q = 0; q < 144 && !done; ++q) {
    if (pos == 100 + q) break;
    if (q == QB3) push(-11, 1);
    if (KSP == 2 && q == QB1) push(-8, 1);
    if (!WQL && (q == 1 || q == 4 || q == 6)) push(18, 1);
    if (WQL && wq_piece(q) >= 0) push(17 + wq_piece(q) / 6, 1);
    if (tag == -9 && q == Q_KPOLL) push(-9, 1);
    if (q % Q_VST == 2 && q / Q_VST < 8) push(-10, 1);
    if ((q & 7) == 7 && (q >> 3) + XQ < 18) push(200 + (q >> 3) + XQ, 2);
  }
  int last = -1;
  for (int i = 0; i < n; ++i) if (seq[i] == tag) last = i;
  return n - 1 - last;
}
constexpr int imin(int a, int b) { return a < b ? a : b; }
DI unsigned lds_add_rtn(unsigned addr) {
  unsigned r;
  asm volatile("s_mov_b64 exec, 1\n\tds_add_rtn_u32 %0, %1, %2\n\ts_mov_b64 exec, -1\n\ts_waitcnt lgkmcnt(0)" : "=&v"(r) : "v"(addr), "v"(1u) : "memory");
  return rfl(r);
}
DI unsigned lds_word(unsigned addr) { return __hip_atomic_load((LAS unsigned*)(size_t)addr, __ATOMIC_RELAXED, __HIP_MEMORY_SCOPE_WORKGROUP); }
DI void lds_set(unsigned addr, unsigned v) { __hip_atomic_store((LAS unsigned*)(size_t)addr, v, __ATOMIC_RELAXED, __HIP_MEMORY_SCOPE_WORKGROUP); }
DI float wave_sum64(float x) {
  x += __uint_as_float(__builtin_amdgcn_update_dpp(0, __float_as_uint(x), 0xB1, 0xf, 0xf, false));
  x += __uint_as_float(__builtin_amdgcn_update_dpp(0, __float_as_uint(x), 0x4E, 0xf, 0xf, false));
  x += __uint_as_float(__builtin_amdgcn_update_dpp(0, __float_as_uint(x), 0x124, 0xf, 0xf, false));
  x += __uint_as_float(__builtin_amdgcn_update_dpp(0, __float_as_uint(x), 0x128, 0xf, 0xf, false));
  { auto r = __builtin_amdgcn_permlane16_swap(__float_as_uint(x), __float_as_uint(x), false, false); x = __uint_as_float(r[0]) + __uint_as_float(r[1]); }
  return xhalf_sum(x);
}
DI void ln1_row(Ctx& c, const u32x4& xr0, const u32x4& xr1) {
  const unsigned lane = c.lane;
  float v[16];
#pragma unroll
  for (int e = 0; e < 4; ++e) { v[2 * e] = bflo(xr0[e]); v[2 * e + 1] = bfhi(xr0[e]); v[8 + 2 * e] = bflo(xr1[e]); v[9 + 2 * e] = bfhi(xr1[e]); }
  float s = 0.f;
#pragma unroll
  for (int e = 0; e < 16; ++e) s += v[e];
  const float mean = wave_sum64(s) * (1.f / DM);
  float qq = 0.f;
#pragma unroll
  for (int e = 0; e < 16; ++e) { v[e] -= mean; qq += v[e] * v[e]; }
  qq = lane < 36 ? qq : 0.f;
  const float rs = rsqrtf(wave_sum64(qq) * (1.f / DM) + 1e-5f);
  u32x4 o0, o1;
#pragma unroll
  for (int e = 0; e < 4; ++e) { o0[e] = cvtpk(v[2 * e] * rs, v[2 * e + 1] * rs); o1[e] = cvtpk(v[8 + 2 * e] * rs, v[9 + 2 * e] * rs); }
  const unsigned ad = c.ldsb + LX + (lane < 36 ? (2 * lane * 8 + c.w) * 16 : 9216 + (lane - 36) * 16);
  *(LAS u32x4*)(size_t)ad = o0;
  *(LAS u32x4*)(size_t)(ad + 128) = o1;
}
DI void xn_store(Ctx& c) {
  const unsigned w = c.w, lane = c.lane, mb = c.b >> 2, r0 = (c.b & 3) * 8;
  const unsigned c0 = 9 * w + (lane >> 3), c1 = 9 * w + 8;
  const u32x4 a0 = *(LAS u32x4*)(size_t)(c.ldsb + LX + (9 * w * 8 + lane) * 16);
  const u32x4 a1 = *(LAS u32x4*)(size_t)(c.ldsb + LX + (c1 * 8 + (lane & 7)) * 16);
  gst16c(a0, c.rws, ((mb * 36 + (c0 >> 1)) * 64 + (c0 & 1) * 32 + r0 + (lane & 7)) * 16, WS_XN);
  gst16c(a1, c.rws, lane < 8 ? ((mb * 36 + (c1 >> 1)) * 64 + (c1 & 1) * 32 + r0 + lane) * 16 : OOB, WS_XN);
}

template <int WH> DI void w_touch(Ctx& c, unsigned& dummy, const void* wo, const void* w1, const void* w2, unsigned wv, bool two) {
  unsigned lane;
  asm volatile("v_mbcnt_lo_u32_b32 %0, -1, 0\n\tv_mbcnt_hi_u32_b32 %0, -1, %0" : "=v"(lane));
  const unsigned xcd = c.b & 7, jj = (c.b >> 3) - ((lane >> 5) << 2), l = two ? lane & 31 : lane, i = 3 * wv + l - 18;
  const bool mn = l < 18, ex = l < 21 && i < 18;
  auto ld = [&](const void* p, unsigned bytes, unsigned off) AI {
    asm volatile("s_nop 4\n\tbuffer_load_dword %0, %1, %2, 0 offen" : "+v"(dummy) : "v"(off), "s"(mk_rsrc(p, bytes)) : "memory");
  };
  const unsigned o1 = xcd * 589824 + (jj * 144 + wv * 18 + l) * 128;
  if constexpr (WH & 1) ld(wo, NQ * DM * 2, mn ? o1 : OOB);
  if constexpr (WH & 2) ld(w1, FF * DM * 2, mn ? (jj * 18 + l) * (FF * 2) + xcd * 1024 + wv * 128 : ex ? (jj * 18 + i) * (FF * 2) + 8192 + xcd * 128 : OOB);
  if constexpr (WH & 4) ld(w2, FF * DM * 2, mn ? o1 : ex ? (4096 + 64 * xcd) * (DM * 2) + (jj * 18 + i) * 128 : OOB);
}
struct QOut { u32x4 q[8]; bool kdone; };
DI u32x4 pack_pair(const f32x4& r0, const f32x4& r1) { return u32x4{cvtpk(r0[0], r0[1]), cvtpk(r0[2], r0[3]), cvtpk(r1[0], r1[1]), cvtpk(r1[2], r1[3])}; }
template <int IMM> DI void gld16c2(u32x4& d0, u32x4& d1, unsigned voff, rsrc_t r, unsigned soff) {
  asm volatile(VP "buffer_load_dwordx4 %0, %2, %3, %4 offen offset:%5 sc1\n\tbuffer_load_dwordx4 %1, %2, %3, %4 offen offset:%6 sc1"
               : "=&v"(d0), "=&v"(d1) : "v"(voff), "s"(r), "s"(soff), "i"(IMM), "i"(IMM + 256) : "memory");
}
DI unsigned x_voff(Ctx& c) { const unsigned g = c.lane >> 4; return (g >> 1) * 1024 + ((g & 1) * 32 + (c.lane & 15)) * 16; }

DI void phase_qkv(Ctx& c, QOut& qo, const u16* x, const u16* ckv, const u16* wq, const u16* wuk, const u16* wuv) {
  const unsigned w = c.w, lane = c.lane, ldsb = c.ldsb, b = c.b;
  const unsigned xcd = b & 7, jj = b >> 3, head = xcd * 4 + (jj >> 3), cb = jj & 7;
  const rsrc_t rxx = mk_rsrc(x, SEQ * DM * 2), rx = mk_rsrc(ckv, SEQ * DC * 2);
  const rsrc_t rwk = mk_rsrc(wuk, DC * NQ * 2), rwv = mk_rsrc(wuv, DC * NQ * 2), rwq = mk_rsrc(wq, DM * NQ * 2);
  unsigned dummy = 0;
  code_touch(c, dummy);
  const unsigned voffX0 = (lane >> 3) * 1024 + (((lane & 7) ^ (lane >> 4)) << 4), voffX1 = voffX0 ^ 64;
  const unsigned voffW = (lane >> 3) * 8192 + (((((lane >> 1) & 3) ^ (((lane >> 4) & 1) | ((w & 1) << 1))) << 5) | ((lane & 1) << 4));
  const unsigned sX = (cb * 256 + 32 * w) * 1024, sW = (8 * (w & 3)) * 8192 + (head * 128 + (w >> 2) * 64) * 2;
  const unsigned rot = ROT == 0 ? 2 * (cb & 3) : ROT == 1 ? 2 * ((cb + head) & 3) : ROT == 2 ? 2 * (cb & 3) + 4 * (head & 1) : (cb + 2 * head) & 7;
  const unsigned dX = ldsb + 4 * w * 1024, dW = ldsb + w * 1024;
  auto issue = [&](auto U_, auto P_) AI {
    constexpr int U = decltype(U_)::v, P = decltype(P_)::v;
    constexpr unsigned so = slot_of(U) * SLOT;
    if constexpr (U < 16) {
      const unsigned tr = (U + rot) & 7;
      constexpr int p = P < 2 ? P : P - 1;
      if constexpr (P % 3 != 2) dma16(dX + so + p * 1024, (p & 1) ? voffX1 : voffX0, rx, sX + 128 * tr + (U < 8 ? 8 * p : 16 * (p & 1) + 8 * (p >> 1)) * 1024);
      else dma16(dW + so + 32768 + (P / 3) * 8192, voffW, U < 8 ? rwk : rwv, sW + (64 * tr + 32 * (P / 3)) * 8192);
    } else {
      dma16(dW + so + (U == 18 ? 5 - P : P) * 8192, voffW, rwq, sW + 32 * (6 * (U - 16) + P) * 8192);
    }
  };
  u32x2 ep; u32x4 xr0, xr1;
    const unsigned hr = head & 3, vA = voffX0 ^ ((hr & 1) << 6), vB = vA ^ 64, wj = (cb >> 2) * 8192;
    auto pro = [&](auto U_, auto I_) AI {
      constexpr int U = decltype(U_)::v, I = decltype(I_)::v;
      constexpr unsigned so = slot_of(U) * SLOT;
      const unsigned tr = (U + rot) & 7;
      if constexpr (I < 4) { const unsigned po = ((I + hr) & 3) * 1024; dma16(dX + so + po, (I & 1) ? vB : vA, rx, sX + 128 * tr + 8 * po); }
      else { const unsigned j = wj ^ ((I - 4) * 8192); dma16(dW + so + 32768 + j, voffW, rwk, sW + 64 * tr * 8192 + 32 * j); }
    };
  {
    static_for<PRO_N>([&](auto N_) AI {
      constexpr int N = decltype(N_)::v;
      pro(IC<PRO_U[N]>{}, IC<PRO_I[N]>{});
      if constexpr (N == PRO_B) __builtin_amdgcn_s_barrier();
      if constexpr (N == PRO_X) {
        const unsigned vo = lane < 36 ? lane * 32 : OOB;
        gld8c(ep, F_EPOCH, c.rws, WS_FLAGS);
        gld16<0>(xr0, vo, rxx, (b * 8 + w) * (DM * 2));
        gld16<16>(xr1, vo, rxx, (b * 8 + w) * (DM * 2));
      }
    });
  }
  unsigned td2 = 0;
  auto xtouch = [&]() AI {
    const unsigned l5 = (lane * 52) >> 8, row = cb * 256 + (head & 3) * 64 + w * 8 + l5, ch = (rot + 3 + lane - 5 * l5) & 7;
    asm volatile("s_nop 4\n\tbuffer_load_dword %0, %1, %2, 0 offen" : "+v"(td2) : "v"(XT && lane < 40 ? row * 1024 + ch * 128 : OOB), "s"(rx) : "memory");
  };
  if constexpr (PRO_N == 18) xtouch();
  const unsigned kh = w >> 2, rg = kh ? 7 - w : w;
  const unsigned qmb = (kh ? cb : 15 - cb) * 4 + rg;
  const unsigned wm = w >> 1, wn = w & 1, g = lane >> 4, l15 = lane & 15, q = l15 >> 2, pp = lane & 3;
  unsigned xb[3][2], wb[3][4], wqb[3][4];
  {
    const unsigned sg = ((q >> 1) & 1) | ((g & 1) << 1);
#pragma unroll
    for (int i = 0; i < 3; ++i) {
      xb[i][0] = ldsb + i * SLOT + (wm * 64 + l15) * 128 + ((g ^ (l15 >> 1)) << 4); xb[i][1] = xb[i][0] ^ 64; PIN(xb[i][0]); PIN(xb[i][1]);
#pragma unroll
      for (int j = 0; j < 4; ++j) { wb[i][j] = ldsb + i * SLOT + wn * 4096 + (8 * g + q) * 128 + ((j ^ sg) << 5) + pp * 8; PIN(wb[i][j]); }
    }
  }
  if constexpr (LNE != 0) { wait_vm<vm_wait(0, -1)>(); PIN(xr0); PIN(xr1); ln1_row(c, xr0, xr1); }
  wait_vm<vm_wait(0, 0)>();
  asm volatile("" ::"v"(dummy));
  bar();
  STAMP(2);
  f32x4 acc[4][4];
  bf16x8 xf[2][4]; bf16x4 wl[2][4][2];
  u32x4 pk[8], pv[8], xq[XQ][2]; u32x2 xnf;
  auto rd = [&](auto T_, auto R_) AI {
    constexpr int T = decltype(T_)::v, R = decltype(R_)::v, B = T & 1, sl = slot_of(T >> 1);
    constexpr int K = R % 3, I = R / 3;
    if constexpr (K == 0) xf[B][I] = lds_b128(xb[sl][B] + I * 2048);
    else wl[B][I][K - 1] = lds_tr(wb[sl][I] + 32768 + B * 8192 + (K - 1) * 512);
  };
  bf16x4 wq8[8][2];
  auto rdq = [&](auto N_, auto H_) AI {
    constexpr int N = decltype(N_)::v, H = decltype(H_)::v, v = N >> 3, a = N & 7, sl = slot_of(16 + v / 6);
    wq8[N & 7][H] = lds_tr(wqb[sl][a & 3] + (v >= 12 ? 17 - v : v % 6) * 8192 + (a >> 2) * 4096 + H * 512);
  };
  const unsigned xvo = x_voff(c), xso = WS_XN + qmb * 36 * 1024;
  auto ldx = [&](auto V_) AI { constexpr int v = decltype(V_)::v; gld16c2<(v & 1) * 2048>(xq[v % XQ][0], xq[v % XQ][1], xvo, c.rws, xso + (v >> 1) * 4096); };
  const unsigned kst = WS_K + ((head * 64 + cb * 8 + wm * 2) * 8 + 4 * wn) * 1024, vst = WS_V + (((head * 32 + cb * 4 + wm) * 4 + 2 * wn) * 4) * 1024;
  static_for<12>([&](auto R) AI { rd(IC<0>{}, R); });
  SB();
  static_for<32>([&](auto T_) AI {
    constexpr int T = decltype(T_)::v, B = T & 1;
    if constexpr (B) { wait_vm<vm_wait(T, (T >> 1) + 1)>(); bar(); }
    if constexpr (T == 1) {
      PIN(ep);
      if constexpr (!LNE) { PIN(xr0); PIN(xr1); }
      u64 z = (((u64)rfl(ep[1]) << 32) | rfl(ep[0])) + 0x9E3779B97F4A7C15ull;
      z = (z ^ (z >> 30)) * 0xBF58476D1CE4E5B9ull; z = (z ^ (z >> 27)) * 0x94D049BB133111EBull; z ^= z >> 31;
      c.ep0 = (unsigned)z; c.ep1 = (unsigned)(z >> 32);
      if (w == 0) lds_set(ldsb + KW + (lane & 3) * 4, 0);
    }
    if constexpr (!LNE && (T == 1 || T == 2)) { if (kh == T - 1) ln1_row(c, xr0, xr1); }
    if constexpr (T == 3) { fput(u32x2{c.ep0, c.ep1}, c.rws, (w == 0 && lane < 5) ? WS_CNT + (b * 5 + lane) * 128 : OOB); xn_store(c); }
    if constexpr (T == TWV) asm volatile("s_nop 4\n\tbuffer_load_dword %0, %1, %2, 0 offen" : "+v"(td2) : "v"(lane < 16 ? (cb * 64 + w * 8 + (lane >> 1)) * 8192 + head * 256 + (lane & 1) * 128 : OOB), "s"(rwv) : "memory");
    if constexpr (T == TWQ) asm volatile("s_nop 4\n\tbuffer_load_dword %0, %1, %2, 0 offen" : "+v"(td2) : "v"(lane < 18 ? (cb * 72 + w * 9 + (lane >> 1)) * 8192 + head * 256 + (lane & 1) * 128 : OOB), "s"(rwq) : "memory");
    if constexpr (T == 31) asm volatile("" ::"v"(td2));
    if constexpr (T == 7) flag_store(c, F_XN + b * 8);
    if constexpr (T == XFL) flag_load(c, xnf, F_XN + qmb * 32, 4);
    if constexpr (T == XFL + 8) flag_check(c, xnf, F_XN + qmb * 32, 4);
    if constexpr (T == 27) { if constexpr (KSP == 1) flag_store(c, F_K + (head * 8 + cb) * 8); }
    if constexpr (T == 31) {
#pragma unroll
      for (int i = 0; i < 3; ++i)
#pragma unroll
        for (int j = 0; j < 4; ++j) { wqb[i][j] = wb[i][j] - wn * 4096; PIN(wqb[i][j]); }
    }
    if constexpr (T == 8 || T == 16 || T == 24) STAMP(100 + T / 2);
    SB();
    static_for<16>([&](auto M_) AI {
      constexpr int M = decltype(M_)::v, i = M >> 2, j = M & 3;
      const bf16x8 wf = cat(wl[B][j][0], wl[B][j][1]);
      const f32x4 cin = (T & 15) == 0 ? f32x4{} : acc[i][j];
      acc[i][j] = T < 16 ? MFMA16(wf, xf[B][i], cin) : MFMA16(xf[B][i], wf, cin);
      SB();
      if constexpr (M < 12) {
        if constexpr (T < 31) rd(IC<T + 1>{}, IC<M>{});
        else rdq(IC<(M >> 1)>{}, IC<(M & 1)>{});
      }
      if constexpr (M == 1 && T >= 16 && (T - 16) % KSP == 0 && (T - 16) / KSP < 8) {
        constexpr int e = (T - 16) / KSP;
        gst16c(pk[e], c.rws, xvo, kst + (e >> 2) * 8192 + (e & 1) * 2048 + ((e >> 1) & 1) * 256);
      }
      if constexpr (T >= 1 && (!WQL || T < 29) && (M == 4 || M == 8 || M == 12)) issue(IC<(T - 1) / 2 + 3>{}, IC<(B ? 0 : 3) + (M >> 2) - 1>{});
      if constexpr (T == 0 && (M & 1) && PRO_N + (M >> 1) < 18) pro(IC<PRO_U[PRO_N + (M >> 1)]>{}, IC<PRO_I[PRO_N + (M >> 1)]>{});
      if constexpr (T == 0 && M == 15 && PRO_N < 18) xtouch();
      if constexpr (XLS == 0 && T >= 29 && (M == 13 || M == 15)) ldx(IC<2 * (T - 29) + (M == 15)>{});
      if constexpr (XLS > 0 && T >= XL0 && (T - XL0) % XLS1 == 0 && M == 14) ldx(IC<(T - XL0) / XLS1>{});
      SB();
    });
    if constexpr (T == 15) {
#pragma unroll
      for (int e = 0; e < 8; ++e) { pk[e] = pack_pair(acc[e >> 1][2 * (e & 1)], acc[e >> 1][2 * (e & 1) + 1]); PIN(pk[e]); }
    }
    if constexpr (T == 31) {
#pragma unroll
      for (int e = 0; e < 8; ++e) { pv[e] = pack_pair(acc[2 * (e >> 2)][e & 3], acc[2 * (e >> 2) + 1][e & 3]); PIN(pv[e]); }
    }
  });
  STAMP(3);
  f32x4 qa[2][8];
  u32x2 kf; bool kdone = false;
  const unsigned kso = WS_K + head * (SEQ * HD * 2) + w * 4096;
  static_for<144>([&](auto N_) AI {
    constexpr int N = decltype(N_)::v, v = N >> 3, a = N & 7;
    if constexpr (a == 0) { wait_vm<vm_wait(100 + N, 200 + v)>(); PIN(xq[v % XQ][0]); PIN(xq[v % XQ][1]); }
    if constexpr (N == QB1) { wait_vm<vm_wait(100 + N, 17)>(); bar(); if constexpr (KSP == 2) flag_store(c, F_K + (head * 8 + cb) * 8); SB(); }
    if constexpr (N == QB2) {
      wait_vm<vm_wait(100 + N, 18)>();
      if (w == 0) wait_vm<vm_wait(100 + N, -9)>();
      PIN(kf);
      if (w == 0 && __all((kf[0] == c.ep0 && kf[1] == c.ep1) || lane >= 8)) lds_set(ldsb + KW + 8, 1);
      bar();
      kdone = rfl(lds_word(ldsb + KW + 8)) != 0;
      SB();
    }
    if constexpr (N == QB3) { wait_vm<vm_wait(100 + N, -10)>(); bar(); flag_store(c, F_V + (head * 8 + cb) * 8); SB(); }
    const bf16x8 wf = cat(wq8[N & 7][0], wq8[N & 7][1]);
    qa[0][a] = MFMA16(wf, asbf(xq[v % XQ][0]), v == 0 ? f32x4{} : qa[0][a]);
    SB();
    if constexpr (N + LAQ < 144) rdq(IC<N + LAQ>{}, IC<0>{});
    SB();
    qa[1][a] = MFMA16(wf, asbf(xq[v % XQ][1]), v == 0 ? f32x4{} : qa[1][a]);
    SB();
    if constexpr (N + LAQ < 144) rdq(IC<N + LAQ>{}, IC<1>{});
    if constexpr (!WQL && (N == 1 || N == 4 || N == 6)) issue(IC<18>{}, IC<(N == 1 ? 3 : N == 4 ? 4 : 5)>{});
    if constexpr (WQL && wq_piece(N) >= 0) issue(IC<17 + wq_piece(N) / 6>{}, IC<wq_piece(N) % 6>{});
    if constexpr (N == Q_KPOLL || N == Q_VPOLL) {
      kf = u32x2{0u, 0u};
      asm volatile("s_mov_b32 exec_lo, %3\n\ts_mov_b32 exec_hi, 0\n\ts_nop 4\n\tbuffer_atomic_add_x2 %0, %1, %2, 0 offen sc0 sc1\n\ts_mov_b64 exec, -1"
                   : "+v"(kf) : "v"((N == Q_KPOLL ? F_K : F_V) + (head * 8 + lane) * 8), "s"(c.rws), "s"(w == 0 ? 0xffu : 0u) : "memory");
    }
    if constexpr (N % Q_VST == 2 && N / Q_VST < 8) {
      constexpr int e = N / Q_VST;
      gst16c(pv[e], c.rws, xvo, vst + ((e >> 1) & 1) * 4096 + (e >> 2) * 2048 + (e & 1) * 256);
    }
    if constexpr (a == 7 && v + XQ < 18) ldx(IC<v + XQ>{});
    if constexpr (N > QB3 && N <= QB3 + 16 && (N - QB3) % 4 == 0) {
      constexpr int p = (N - QB3) / 4 - 1;
      if (kdone) dma16c(ldsb + kh * 16384 + w * 4096 + p * 1024, lane * 16, c.rws, kso + p * 1024);
    }
    SB();
  });
#pragma unroll
  for (int A = 0; A < 4; ++A) {
    u32x4 p0, p1;
#pragma unroll
    for (int e = 0; e < 4; ++e) {
      const int t = 2 * A + (e >> 1), k = 2 * (e & 1);
      const unsigned u0 = cvtpk(qa[0][t][k] * QSCALE, qa[0][t][k + 1] * QSCALE), u1 = cvtpk(qa[1][t][k] * QSCALE, qa[1][t][k + 1] * QSCALE);
      const auto s = __builtin_amdgcn_permlane16_swap(u0, u1, false, false);
      const auto r = __builtin_amdgcn_permlane32_swap(s[0], s[1], false, false);
      p0[e] = r[0]; p1[e] = r[1];
    }
    qo.q[2 * A] = p0; qo.q[2 * A + 1] = p1;
  }
  qo.kdone = kdone;
  STAMP(6);
  if (w == 0) wait_vm<0>();
  flag_check(c, kf, F_V + head * 64, w == 0 ? 8 : 0);
}

constexpr float THR = 32.f;

struct AttnState {
  bf16x8 qf[8];
  f32x16 o[4];
  f32x16 S[2][2];
  f32x16 negm;
  u32x4 pk[4];
  bf16x8 fr[8];
  float mhat, l0, l1, fct, mx0, mx1, t0, t1;
  unsigned pend, cold;
  unsigned kb[2], vb[2];
  unsigned voff, dk, n1k;
  unsigned u, o0, o1, o2;
  unsigned wide, drop, diag, first;
  u64 ta, tm; unsigned dummy;
};

constexpr int RD0[16] = {4, 5, 6, 7, 9, 10, 12, 13, 15, -1, -1, -1, -1, -1, -1, -1};
constexpr int RD1[16] = {-1, -1, -1, 8, -1, 11, -1, 14, -1, -1, -1, -1, -1, -1, -1, -1};

template <int CR, int R> DI void mask4(f32x16& s, int dq, float ninf) {
  float x0 = s[R], x1 = s[R + 1], x2 = s[R + 2], x3 = s[R + 3];
  u64 m0, m1, m2;
  asm volatile("v_cmp_le_i32_e64 %4, %9, %7\n\tv_cmp_le_i32_e64 %5, %10, %7\n\tv_cmp_le_i32_e64 %6, %11, %7\n\tv_cmp_le_i32_e32 vcc, %12, %7\n\t"
               "v_cndmask_b32_e64 %0, %8, %0, %4\n\tv_cndmask_b32_e64 %1, %8, %1, %5\n\tv_cndmask_b32_e64 %2, %8, %2, %6\n\tv_cndmask_b32_e32 %3, %8, %3, vcc"
               : "+v"(x0), "+v"(x1), "+v"(x2), "+v"(x3), "=&s"(m0), "=&s"(m1), "=&s"(m2) : "v"(dq), "v"(ninf), "i"(CR), "i"(CR + 1), "i"(CR + 2), "i"(CR + 3) : "vcc");
  s[R] = x0; s[R + 1] = x1; s[R + 2] = x2; s[R + 3] = x3;
}
template <int B, int... Q> DI void mask_rows(f32x16& s, int dq, float ninf, iseq<Q...>) { (mask4<B + 8 * Q, 4 * Q>(s, dq, ninf), ...); }
template <int P> DI void dma4(unsigned lds_addr, unsigned voff, rsrc_t r, unsigned soff) {
  if constexpr (P == 0) asm volatile(VP "s_mov_b32 m0, %0\n\ts_nop 0\n\tbuffer_load_dwordx4 %1, %2, %3 offen sc1 lds" ::"s"(lds_addr), "v"(voff), "s"(r), "s"(soff) : "memory", "m0");
  else asm volatile(VP "buffer_load_dwordx4 %0, %1, %2 offen offset:%3 sc1 lds" ::"v"(voff), "s"(r), "s"(soff), "i"(P * 1024) : "memory");
}
DI void mfma32_new(f32x16& d, const bf16x8& x, const bf16x8& y, const f32x16& cin) {
  asm volatile("v_mfma_f32_32x32x16_bf16 %0, %1, %2, %3" : "=&v"(d) : "v"(x), "v"(y), "v"(cin));
}
DI void add2(float& a0, float& a1, float b0, float b1) { asm volatile("v_add_f32_e32 %0, %0, %2\n\tv_add_f32_e32 %1, %1, %3" : "+v"(a0), "+v"(a1) : "v"(b0), "v"(b1)); }
DI void add2n(float& a0, float& a1, float b0, float b1, float c0, float c1) {
  asm volatile("v_add_f32_e32 %0, %2, %4\n\tv_add_f32_e32 %1, %3, %5" : "=&v"(a0), "=&v"(a1) : "v"(b0), "v"(b1), "v"(c0), "v"(c1));
}
#define SPIN(x) asm volatile("" : "+s"(x))
struct NoHook { template <class T> DI void operator()(T) const {} };
template <int X, bool PV, class Hook = NoHook>
DI void attn_iter(Ctx& c, AttnState& a, const unsigned s, const int dq, Hook&& hook = Hook{}) {
  constexpr int Y = X ^ 1;
  auto kaddr = [&](int slot, int n) AI { return a.kb[slot] + ((n & 1) * 8 + (n >> 1)) * 1024; };
  auto vaddr = [&](int slot, int n) AI { return a.vb[slot] + ((n & 3) * 4 + (n >> 2)) * 1024; };
  unsigned thr = 0;
  static_for<16>([&](auto M_) AI {
    constexpr int M = decltype(M_)::v, d = M >> 1, kb = M & 1;
    if constexpr (d == 0) mfma32_new(a.S[X][kb], a.fr[M & 7], a.qf[d], a.negm);
    else a.S[X][kb] = MFMA32(a.fr[M & 7], a.qf[d], a.S[X][kb]);
    SB();
    if constexpr (RD0[M] >= 0) a.fr[RD0[M] & 7] = lds_b128(kaddr(X, RD0[M]));
    if constexpr (RD1[M] >= 0) a.fr[RD1[M] & 7] = lds_b128(kaddr(X, RD1[M]));
    if constexpr (M == 0 || M == 2 || M == 6 || M == 9) dma4<M == 0 ? 0 : M == 2 ? 1 : M == 6 ? 2 : 3>(a.dk + X * 32768 + 65536, a.voff, c.rws, a.o0);
    if constexpr (PV) {
      if constexpr (M < 8) {
        a.S[Y][1][2 * M] = __builtin_amdgcn_exp2f(a.S[Y][1][2 * M]);
        a.S[Y][1][2 * M + 1] = __builtin_amdgcn_exp2f(a.S[Y][1][2 * M + 1]);
        a.pk[M >> 2][M & 3] = cvtpk(a.S[Y][0][2 * M], a.S[Y][0][2 * M + 1]);
        PIN(a.pk[M >> 2][M & 3]);
        if constexpr (M == 0) add2(a.l0, a.l1, a.S[Y][0][14], a.S[Y][0][15]);
      } else {
        constexpr int e = M - 8;
        a.pk[2 + (e >> 2)][e & 3] = cvtpk(a.S[Y][1][2 * e], a.S[Y][1][2 * e + 1]);
        PIN(a.pk[2 + (e >> 2)][e & 3]);
        add2(a.l0, a.l1, a.S[Y][1][2 * e], a.S[Y][1][2 * e + 1]);
      }
    }
    hook(M_);
    if constexpr (M == 10) { thr = ((a.first >> s) & 1) ? 0x7fc00000u : __float_as_uint(THR); SPIN(thr); }
    if constexpr (M == 11) { SB(); wait_vm<9>(); bar(); SB(); }
    if constexpr (PV && M >= 12) a.fr[(M - 12) & 7] = lds_b128(vaddr(Y, M - 12));
    SB();
  });
  static_for<16>([&](auto M_) AI {
    constexpr int M = decltype(M_)::v, ks = M >> 2, db = M & 3;
    if constexpr (PV) {
      a.o[db] = MFMA32(a.fr[M & 7], asbf(a.pk[ks]), a.o[db]);
      SB();
      if constexpr (RD0[M] >= 0) a.fr[RD0[M] & 7] = lds_b128(vaddr(Y, RD0[M]));
      if constexpr (RD1[M] >= 0) a.fr[RD1[M] & 7] = lds_b128(vaddr(Y, RD1[M]));
    }
    if constexpr (M == 0 || M == 1 || M == 6 || M == 9) dma4<M == 0 ? 0 : M == 1 ? 1 : M == 6 ? 2 : 3>(a.dk + X * 32768, a.voff, c.rws, a.o2);
    if constexpr (M == 0) { a.u += ((a.wide >> s) & 1) ? 32768u : 16384u; SPIN(a.u); }
    if constexpr (M == 1) { a.o0 = a.o1 + (WS_V - WS_K); SPIN(a.o0); }
    if constexpr (M == 2) {
      if constexpr (!PV) asm volatile("s_nop 7\n\ts_nop 3" : "+v"(a.S[X][0]), "+v"(a.S[X][1]));
      if (__builtin_expect((a.diag >> s) & 1, 0)) {
        float ninf;
        asm volatile("v_mov_b32 %0, 0xff800000" : "=v"(ninf));
        const int dq2 = dq + (s + 2 == a.n1k ? 64 : 0);
        mask_rows<0>(a.S[X][0], dq2, ninf, make_iseq<4>{}); mask_rows<32>(a.S[X][1], dq2, ninf, make_iseq<4>{});
      }
      asm volatile("v_max3_f32 %0, %2, %3, %6\n\tv_max3_f32 %1, %4, %5, %7\n\tv_max3_f32 %0, %0, %8, %9" : "=&v"(a.mx0), "=&v"(a.mx1)
                   : "v"(a.S[X][0][0]), "v"(a.S[X][0][1]), "v"(a.S[X][0][2]), "v"(a.S[X][0][3]), "v"(a.S[X][1][0]), "v"(a.S[X][1][1]), "v"(a.S[X][1][2]), "v"(a.S[X][1][3]));
    }
    if constexpr (M >= 3 && M < 6) {
      constexpr int r = 4 * (M - 2);
      asm volatile("v_max3_f32 %0, %0, %2, %3\n\tv_max3_f32 %1, %1, %4, %5\n\tv_max3_f32 %0, %0, %6, %7\n\tv_max3_f32 %1, %1, %8, %9" : "+v"(a.mx0), "+v"(a.mx1)
                   : "v"(a.S[X][0][r]), "v"(a.S[X][0][r + 1]), "v"(a.S[X][0][r + 2]), "v"(a.S[X][0][r + 3]), "v"(a.S[X][1][r]), "v"(a.S[X][1][r + 1]), "v"(a.S[X][1][r + 2]), "v"(a.S[X][1][r + 3]));
    }
    if constexpr (M == 6) asm volatile("v_max_f32_e32 %0, %0, %1\n\tv_mov_b32_e32 %1, %0\n\ts_nop 1\n\tv_permlane32_swap_b32_e32 %0, %1\n\tv_max_f32_e32 %0, %0, %1" : "+v"(a.mx0), "+v"(a.mx1));
    if constexpr (M == 7) {
      if (__builtin_expect(__builtin_amdgcn_ballot_w64(!(a.mx0 <= __uint_as_float(thr))) != 0, 0)) {
        const bool first = (a.first >> s) & 1;
        const float dl = first ? fmaxf(a.mx0, -1e30f) : fmaxf(a.mx0, 0.f);
        a.mhat = first ? dl : a.mhat + dl;
        a.fct = __builtin_amdgcn_exp2f(-dl);
#pragma unroll
        for (int r = 0; r < 16; ++r) { a.S[X][0][r] -= dl; a.S[X][1][r] -= dl; a.negm[r] = -a.mhat; }
        a.pend = first ? 0u : 1u;
        a.cold |= a.pend << s;
      }
    }
    if constexpr (M >= 8) {
      constexpr int e = M - 8;
      a.S[X][0][2 * e] = __builtin_amdgcn_exp2f(a.S[X][0][2 * e]);
      a.S[X][0][2 * e + 1] = __builtin_amdgcn_exp2f(a.S[X][0][2 * e + 1]);
      if constexpr (e == 7) { PIN(a.S[X][0][14]); PIN(a.S[X][0][15]); }
      if constexpr (e == 2) add2n(a.t0, a.t1, a.S[X][0][0], a.S[X][0][1], a.S[X][0][2], a.S[X][0][3]);
      if constexpr (e > 2) add2(a.t0, a.t1, a.S[X][0][2 * e - 2], a.S[X][0][2 * e - 1]);
    }
    if constexpr (M == 10) { a.o1 = a.o2; a.o2 = ((a.drop >> s) & 1) ? 0xF0000000u : a.u; SPIN(a.o1); SPIN(a.o2); }
    if constexpr (M == 11) { SB(); wait_vm<PV ? 9 : 8>(); bar(); }
    if constexpr (M == 13) { asm volatile("s_mov_b64 exec, %2\n\tglobal_load_dword %0, %1, off\n\ts_mov_b64 exec, -1" : "+v"(a.dummy) : "v"(a.ta), "s"(a.tm) : "memory"); a.tm <<= 4; }
    if constexpr (M >= 12) a.fr[(M - 12) & 7] = lds_b128(kaddr(Y, M - 12));
    SB();
  });
}

constexpr unsigned WP = 288;
constexpr unsigned LSYNC = 161280;
constexpr unsigned C_LAND = 0, C_FREE = 64, C_GB = 128, C_CTXD = 136, C_MRG = 140, C_TICK = 144, LSTAT = LSYNC + 256;
#define ARRV 2
constexpr int XM = 12;
constexpr int XDEP(int NK) { return NK == 16 ? XM : 8; }
DI unsigned land(unsigned ph, unsigned q) { return LSYNC + C_LAND + (ph * 5 + q) * 4; }
DI unsigned fre(unsigned ph, unsigned q) { return LSYNC + C_FREE + (ph * 5 + q) * 4; }
DI void cnt_add(Ctx& c, unsigned off) {
  asm volatile("s_mov_b64 exec, 1\n\tds_add_u32 %0, %1\n\ts_mov_b64 exec, -1" ::"v"(c.ldsb + off), "v"(1u) : "memory");
}
#define wlog(...)
DI unsigned cnt_add_rtn(Ctx& c, unsigned off) {
  unsigned r;
  asm volatile("s_mov_b64 exec, 1\n\tds_add_rtn_u32 %0, %1, %2\n\ts_mov_b64 exec, -1\n\ts_waitcnt lgkmcnt(0)" : "=&v"(r) : "v"(c.ldsb + off), "v"(1u) : "memory");
  return rfl(r);
}
DI unsigned cnt_read(Ctx& c, unsigned off) { return __hip_atomic_load((LAS unsigned*)(size_t)(c.ldsb + off), __ATOMIC_RELAXED, __HIP_MEMORY_SCOPE_WORKGROUP); }
DI void prio_lo() { __builtin_amdgcn_s_setprio(0); }
DI void prio_hi(Ctx& c) { __builtin_amdgcn_s_setprio(3); }
DI void prio_gemm(Ctx& c, bool behind) {
  if (c.w >= 4) __builtin_amdgcn_s_setprio(1); else if (behind) __builtin_amdgcn_s_setprio(0); else __builtin_amdgcn_s_setprio(2);
}
DI void cnt_wait(Ctx& c, unsigned off, unsigned target) {
  asm volatile("" ::: "memory");
  unsigned spins = 0;
#pragma unroll 1
  while (rfl(cnt_read(c, off)) < target) { __builtin_amdgcn_s_sleep(1); if (++spins > (1u << 20)) break; }
  asm volatile("" ::: "memory");
}
DI void flag_w(Ctx& c, unsigned flag_off) {
  wait_vm<0>();
  fadd(c.rws, c.lane == 0 ? flag_off : OOB);
}
DI bool cnt_full(Ctx& c, const u32x2& f, unsigned n) { return (((u64)rfl(f[1]) << 32) | rfl(f[0])) == (((u64)c.ep1 << 32) | c.ep0) + n; }
DI void dma16m(unsigned lds_addr, unsigned voff, rsrc_t r, unsigned soff, u64 mask) {
  asm volatile(VP "s_mov_b32 m0, %0\n\ts_mov_b64 exec, %4\n\ts_nop 0\n\tbuffer_load_dwordx4 %1, %2, %3 offen lds\n\ts_mov_b64 exec, -1" ::"s"(lds_addr), "v"(voff), "s"(r), "s"(soff), "s"(mask)
               : "memory", "m0");
}
struct WSrc { unsigned voff[9]; rsrc_t r; unsigned main, tail, pitch, colA, colB; unsigned idx, pend, ph; };
DI unsigned w_voff(const WSrc& s, unsigned lane, int p) {
  const unsigned ci = lane + 64 * p, rr = __umul24(ci, 3641) >> 16, cc = ci - __umul24(rr, 18);
  return __umul24(rr, s.pitch - WP) + ci * 16 + (cc < 16 ? s.colA : s.colB - 256);
}
DI unsigned opaque_lane() {
  unsigned lane;
  asm volatile("v_mbcnt_lo_u32_b32 %0, -1, 0\n\tv_mbcnt_hi_u32_b32 %0, -1, %0" : "=v"(lane));
  return lane;
}
template <int NV = 5> DI void wsrc_voff(Ctx& c, WSrc& s) {
  const unsigned lane = opaque_lane();
#pragma unroll
  for (int p = 0; p < NV; ++p) s.voff[p] = w_voff(s, lane, p);
  if constexpr (NV == 5) {
    s.voff[5] = lane >= 16 ? ~0u : 0u;
    const unsigned l7 = (lane & 7) < 5 ? lane & 7 : 0;
    s.voff[6] = c.ldsb + (lane < 8 ? LSYNC + C_TICK + (s.ph * 5 + l7) * 4 : fre(s.ph - 1, 4) - 4 * l7);
    s.voff[7] = lane == 0 ? 3 : 8;
  }
}
template <int NV = 0> DI void wsrc_setup(Ctx& c, WSrc& s, rsrc_t r, unsigned pitch, unsigned colA, unsigned colB, unsigned rowMain, unsigned rowTail, unsigned ph) {
  s.r = r; s.pitch = pitch; s.main = rowMain * pitch; s.tail = rowTail * pitch;
  s.colA = colA; s.colB = colB; s.ph = ph; s.idx = s.pend = 0;
  if constexpr (NV > 0) wsrc_voff<NV>(c, s);
}
DI void w_group(Ctx& c, const WSrc& s, unsigned q) {
  const unsigned r0 = 128 * q + 32 * (c.w & 3);
#pragma unroll
  for (int p = 0; p < 9; ++p) dma16(c.ldsb + r0 * WP + p * 1024, s.voff[p], s.r, s.main + r0 * s.pitch);
}
DI void poll_w(Ctx& c, unsigned off, unsigned n) {
  const unsigned voff = c.lane == 0 ? off : OOB;
  unsigned spins = 0;
#pragma unroll 1
  for (;;) {
    u32x2 f;
    fget(f, c.rws, voff);
    asm volatile("s_waitcnt vmcnt(0)" : "+v"(f)::"memory");
    if (cnt_full(c, f, n) || ++spins > (1u << 17)) break;
  }
}
constexpr unsigned nitems(unsigned q) { return q == 4 ? 3 : 8; }
DI bool w_try(Ctx& c, WSrc& s) {
  if (s.idx) return false;
  unsigned idx, t;
#pragma unroll 1
  for (;;) {
    const unsigned v = __hip_atomic_load((LAS unsigned*)(size_t)s.voff[6], __ATOMIC_RELAXED, __HIP_MEMORY_SCOPE_WORKGROUP);
    const unsigned m = (unsigned)__builtin_amdgcn_ballot_w64(v < s.voff[7]);
    if ((m & 31) == 0) s.idx = 1;
    const unsigned av = m & ~(m >> 8) & 31;
    if (av == 0) return false;
    idx = __builtin_ctz(av);
    t = cnt_add_rtn(c, LSYNC + C_TICK + (s.ph * 5 + idx) * 4);
    if (t < (idx ? 8u : 3u)) break;
  }
  s.pend += 1u << (4 * idx);
  wlog(c, s.ph, 1, idx, true); wlog(c, s.ph, 2, idx);
  const unsigned lrow = 128 * (4 - idx) + 16 * t, so = (idx ? s.main + lrow * s.pitch : s.tail + 16 * t * s.pitch), sk = lrow == 544 ? 8 * s.pitch : 0u;
  const unsigned la = c.ldsb + lrow * WP;
  dma16(la, s.voff[0], s.r, so);
  dma16(la + 1024, s.voff[1], s.r, so);
  dma16(la + 2048, s.voff[2] + (s.voff[5] & sk), s.r, so);
  dma16(la + 3072, s.voff[3], s.r, so + sk);
  dma16m(la + 4096, s.voff[4], s.r, so + sk, 0xffffffffull);
  return true;
}
DI void w_signal(Ctx& c, const WSrc& s, unsigned items) {
  if (items) {
    unsigned lane;
    asm volatile("v_mbcnt_lo_u32_b32 %0, -1, 0" : "=v"(lane));
    const unsigned n = (items >> (4 * lane)) & 15, ad = c.ldsb + land(s.ph, 4) - 4 * lane;
    asm volatile("s_mov_b64 exec, 31\n\tds_add_u32 %0, %1\n\ts_mov_b64 exec, -1" ::"v"(ad), "v"(n) : "memory");
  }
}
DI void w_flush(Ctx& c, WSrc& s) { w_signal(c, s, s.pend); s.pend = 0; }
#define GSRV 0
#define NMAX 2
#define PPRIO 3
DI void w_try_wait(Ctx& c, WSrc& s, unsigned mx) {
  unsigned k = 0;
#pragma unroll 1
  while (k < mx && w_try(c, s)) ++k;
  if (k == 0) wait_vm<0>(); else if (k == 1) wait_vm<5>(); else if (k == 2) wait_vm<10>(); else if (k == 3) wait_vm<15>(); else wait_vm<20>();
}
template <bool VOFF> DI void w_need(Ctx& c, WSrc& s, unsigned q, unsigned n) {
  unsigned spins = 0;
  if constexpr (VOFF) wsrc_voff(c, s);
#pragma unroll 1
  for (;;) {
    if (s.pend == 0) {
      unsigned k = 0;
#pragma unroll 1
      while (k < NMAX && w_try(c, s)) ++k;
    }
    if (s.pend) { wait_vm<0>(); w_flush(c, s); }
    if (rfl(cnt_read(c, land(s.ph, q))) >= n || ++spins > (1u << 20)) break;
    __builtin_amdgcn_s_sleep(1);
  }
  asm volatile("" ::: "memory");
}
DI void poll_srv(Ctx& c, WSrc& s, unsigned flag_off, unsigned n, unsigned mx, unsigned mx1) {
  const unsigned voff = c.lane == 0 ? flag_off : OOB;
  unsigned spins = 0;
  __builtin_amdgcn_s_setprio(PPRIO);
#pragma unroll 1
  for (;;) {
    u32x2 f;
    fget(f, c.rws, voff);
    if (s.pend) { wait_vm<1>(); w_flush(c, s); }
    w_try_wait(c, s, mx1);
    mx1 = mx;
    PIN(f);
    if (cnt_full(c, f, n) || ++spins > (1u << 17)) break;
  }
  prio_hi(c);
}
constexpr unsigned LB1 = LSTAT + 256, LB2 = LB1 + 320;
template <int P> DI void park_load(Ctx& c, const u16* w1, const u16* b1, const u16* w2, const u16* b2) {
  const unsigned xcd = c.b & 7, nt = (c.b >> 3) & 3, nt32 = xcd * 4 + nt, w = (c.w & 3) * 2 + (P & 1);
  const unsigned rr = c.lane >= 18 ? 1 : 0, cc = c.lane - 18 * rr, r0 = (w < 4 ? 40 : 48) + 2 * w;
  const unsigned c1 = cc < 16 ? nt32 * 256 + cc * 16 : 8192 + nt32 * 32 + (cc - 16) * 16, c2 = cc < 16 ? nt * 256 + cc * 16 : 1024 + nt * 32 + (cc - 16) * 16;
  if constexpr (P < 2) dma16m(c.ldsb + (512 + 2 * w) * WP, rr * (FF * 2) + c1, mk_rsrc(w1, DM * FF * 2), (512 + r0) * (FF * 2), 0xfffffffffull);
  else if constexpr (P < 4) dma16m(c.ldsb + (528 + 2 * w) * WP, rr * (DM * 2) + c2, mk_rsrc(w2, FF * DM * 2), (4096 + 64 * xcd + r0) * (DM * 2), 0xfffffffffull);
  else {
    const bool f = c.w == 4;
    dma16m(c.ldsb + (f ? LB1 : LB2 + (c.w - 5) * 384), f ? c1 : c.lane * 16, mk_rsrc(f ? b1 : b2, f ? FF * 2 : DM * 2), f ? 0 : (c.w - 5) * 384, f ? 0x3ffffull : 0xffffffull);
  }
}
DI void park_read(Ctx& c, u32x2 (&tl)[9], u32x2 (&tl2)[9]) {
  const unsigned ad = c.ldsb + (512 + (c.lane >> 2)) * WP + (c.lane & 3) * 8;
#pragma unroll
  for (int a = 0; a < 9; ++a) { tl[a] = __builtin_bit_cast(u32x2, lds_tr(ad + a * 32)); tl2[a] = __builtin_bit_cast(u32x2, lds_tr(ad + 16 * WP + a * 32)); }
  SB();
  cnt_add(c, fre(0, 4)); wlog(c, 1, 0, 0);
#pragma unroll
  for (int a = 0; a < 9; ++a) { PIN(tl[a]); PIN(tl2[a]); }
}
constexpr int KK(int NK, int v) { return NK == 18 ? (v < 2 ? 16 + v : 4 * (3 - (v - 2) / 4) + (v - 2) % 4) : 4 * (3 - v / 4) + v % 4; }
constexpr bool QSTART(int NK, int v) { return v < NK && v > 0 && (KK(NK, v) & 3) == 0; }
constexpr bool QEND(int NK, int v) { return (KK(NK, v) & 3) == 3 || KK(NK, v) == 17; }
template <int NK, int V> DI void x_ld(Ctx& c, u32x4 (&x)[2], unsigned voff, unsigned so, unsigned so2) {
  constexpr int k = KK(NK, V);
  gld16c2<(k & 1) * 2048>(x[0], x[1], voff, c.rws, k < 16 ? so + (k >> 1) * 4096 : so2);
}
template <int NK, int V0 = 0, int NV = 8> DI void x_pro(Ctx& c, u32x4 (&x)[XM][2], unsigned voff, unsigned so, unsigned so2) {
  static_for<NV>([&](auto V_) AI { x_ld<NK, V0 + decltype(V_)::v>(c, x[V0 + decltype(V_)::v], voff, so, so2); });
}
DI void x_ld4_masked(u32x4& a, u32x4& b, u32x4& d, u32x4& e, unsigned voff, rsrc_t r, unsigned so, unsigned mask) {
  asm volatile(VP "s_cmp_eq_u32 %7, 0\n\ts_cbranch_scc1 .Lxs%=\n\tbuffer_load_dwordx4 %0, %4, %5, %6 offen sc1\n\tbuffer_load_dwordx4 %1, %4, %5, %6 offen offset:256 sc1\n\t"
               "buffer_load_dwordx4 %2, %4, %5, %6 offen offset:2048 sc1\n\tbuffer_load_dwordx4 %3, %4, %5, %6 offen offset:2304 sc1\n.Lxs%=:"
               : "+v"(a), "+v"(b), "+v"(d), "+v"(e) : "v"(voff), "s"(r), "s"(so), "s"(mask) : "memory", "scc");
}
template <int IMM> DI void x_ld2_if(u32x4& a, u32x4& b, unsigned voff, rsrc_t r, unsigned so, unsigned skip) {
  asm volatile(VP "s_cmp_lg_u32 %5, 0\n\ts_cbranch_scc1 .Lxi%=\n\tbuffer_load_dwordx4 %0, %2, %3, %4 offen offset:%6 sc1\n\tbuffer_load_dwordx4 %1, %2, %3, %4 offen offset:%7 sc1\n.Lxi%=:"
               : "=&v"(a), "=&v"(b) : "v"(voff), "s"(r), "s"(so), "s"(skip), "i"(IMM), "i"(IMM + 256) : "memory", "scc");
}
template <int V> DI void x_ld16_if(Ctx& c, u32x4 (&x)[2], unsigned voff, unsigned so, unsigned skip) {
  constexpr int k = KK(16, V);
  x_ld2_if<(k & 1) * 2048>(x[0], x[1], voff, c.rws, so + (k >> 1) * 4096, skip);
}
DI void dma16_if(unsigned lds_addr, unsigned voff, rsrc_t r, unsigned soff, unsigned go) {
  asm volatile(VP "s_cmp_eq_u32 %4, 0\n\ts_cbranch_scc1 .Ldi%=\n\ts_mov_b32 m0, %0\n\ts_nop 0\n\tbuffer_load_dwordx4 %1, %2, %3 offen lds\n.Ldi%=:" ::"s"(lds_addr), "v"(voff), "s"(r), "s"(soff), "s"(go)
               : "memory", "m0", "scc");
}
DI void x_pro_masked16(Ctx& c, u32x4 (&x)[XM][2], unsigned so, unsigned mask) {
  const unsigned voff = x_voff(c);
#pragma unroll
  for (int p = 0; p < XM / 2; ++p) x_ld4_masked(x[2 * p][0], x[2 * p][1], x[2 * p + 1][0], x[2 * p + 1][1], voff, c.rws, so + (KK(16, 2 * p) >> 1) * 4096, mask);
}
template <int L> DI void wlane(unsigned& v, unsigned s) { asm volatile("v_writelane_b32 %0, %1, %2" : "+v"(v) : "s"(s), "i"(L)); }
struct Sched {
  int NK, XD;
  constexpr int T0() const { return 9 * (NK - 4); }
  constexpr int sv(int n) const { return n < T0() ? n / 9 : NK - 4 + (n - T0()) % 4; }
  constexpr int sa(int n) const { return n < T0() ? n % 9 : ((n - T0()) / 4 == 0 ? 8 : (n - T0()) / 4 - 1); }
  constexpr int g0(int G) const { return G == 3 ? T0() + 8 : 9 * (NK - 16 + 1 + 4 * G) + 1; }
  constexpr int gn(int G) const { return G == 0 ? 4 : 9; }
  constexpr int wg(int n) const { for (int G = 0; G < 4; ++G) if (n >= g0(G) && (n - g0(G)) % 3 == 0 && (n - g0(G)) / 3 < gn(G)) return G; return -1; }
  constexpr int wp(int n) const { return (n - g0(wg(n))) / 3; }
  constexpr int gsig(int n) const { for (int G = 0; G < 3; ++G) if (n == g0(G) + 3 * (gn(G) - 1) + 27) return G; return -1; }
  constexpr int st(int n) const {
    const int m = n - T0();
    if (m == 6) return 0;
    if (m >= 13 && (m - 13) % 8 == 0) return 1 + 2 * ((m - 13) / 8);
    if (m >= 15 && (m - 15) % 8 == 0) return 2 + 2 * ((m - 15) / 8);
    return -1;
  }
  constexpr bool sp(int n) const { return n < T0() ? (n % 18 == 0 && n >= 18) : ((n - T0()) % 18 == 0); }
  constexpr int between(int n0, int n1) const {
    int cnt = 0;
    for (int i = n0; i < n1; ++i) { if (i < T0() && sa(i) == 8 && sv(i) + XD < NK) cnt += 2; if (st(i) >= 0) cnt += 1; }
    return cnt;
  }
  constexpr int younger(int tag, int n, bool ldr) const {
    int cnt = 0, last = -1;
    for (int v = 0; v < XD; ++v) { if (100 + v == tag) last = cnt + 1; cnt += 2; }
    for (int i = 0; i < n; ++i) {
      if (i < T0() && sa(i) == 8 && sv(i) + XD < NK) { if (100 + sv(i) + XD == tag) last = cnt + 1; cnt += 2; }
      if (ldr && wg(i) >= 0) { if (200 + wg(i) == tag) last = cnt; cnt += 1; }
      if (st(i) >= 0) cnt += 1;
    }
    return cnt - 1 - last;
  }
};
template <int NK, int KORD, int PH, int GST, class St>
DI void gemm_g(Ctx& c, f32x4 (&acc)[2][9], u32x4 (&x)[XM][2], u32x2 (&tl)[9], u32x2 (&tl2)[9], WSrc& ws, WSrc& wn, unsigned so, unsigned so2, unsigned flag_off, St&& st) {
  constexpr unsigned RT = KORD == 0 ? 16 : 8;
  constexpr int LA = 6, NS = NK * 9;
  constexpr int XD = XDEP(NK);
  constexpr Sched S{NK, XD};
  constexpr bool LD = PH < 2;
  const unsigned g = c.lane >> 4, q = (c.lane & 15) >> 2, pp = c.lane & 3;
  const unsigned wrow1 = 16 * (g >> 1) + 4 * (g & 1) + q, wrow = KORD == 0 ? 4 * g + q : wrow1;
  unsigned wb[3], wt[2];
#pragma unroll
  for (int i = 0; i < 3; ++i) { wb[i] = c.ldsb + wrow * WP + pp * 8 + i * 7 * 32 * WP; PIN(wb[i]); }
  if constexpr (NK == 18) {
    wt[0] = c.ldsb + (512 + wrow1) * WP + pp * 8; wt[1] = c.ldsb + (544 + wrow1 - 8 * (g >> 1)) * WP + pp * 8;
    PIN(wt[0]); PIN(wt[1]);
  }
  unsigned voff = x_voff(c); PIN(voff);
  bf16x4 wl[8][2];
  auto rdw = [&](auto N_, auto H_) AI {
    constexpr int N = decltype(N_)::v, H = decltype(H_)::v, v = S.sv(N), a = S.sa(N), k = KK(NK, v);
    if constexpr (k < 16) wl[N & 7][H] = lds_tr(wb[k / 7] + ((k % 7) * 32 + H * RT) * WP + a * 32);
    else if constexpr (k == 16) wl[N & 7][H] = lds_tr(wt[0] + H * 8 * WP + a * 32);
    else if constexpr (H == 0) wl[N & 7][H] = lds_tr(wt[1] + a * 32);
    else {
      u32x2 t;
      asm volatile("s_nop 2\n\tv_mov_b64 %0, %1" : "=v"(t) : "v"(tl[a]));
      wl[N & 7][1] = __builtin_bit_cast(bf16x4, t);
    }
    if constexpr (LD && H == 1 && ((N < S.T0() && a == 8 && QEND(NK, v)) || N == NS - 1)) { SB(); cnt_add(c, fre(PH, k >> 2)); wlog(c, PH + 1, 0, 4 - (k >> 2)); }
  };
  bool behind = false;
  if constexpr (PH > 0) {
    if (ws.pend) { wait_vm<2 * XD>(); w_flush(c, ws); }
    const unsigned l4 = cnt_read(c, land(PH, 4)), f0 = cnt_read(c, fre(PH - 1, 0));
    behind = (c.w < 4) & (rfl(f0) < 8);
    if (__builtin_expect(rfl(l4) < 3, 0)) w_need<PH == 1>(c, ws, 4, 3);
  }
  prio_gemm(c, behind);
  static_for<LA>([&](auto N) AI { rdw(N, IC<0>{}); rdw(N, IC<1>{}); });
  unsigned lc = 0;
  bool srv = c.w < 4 && PH == 2 && ws.idx == 0;
  SB();
  static_for<NS>([&](auto N_) AI {
    constexpr int N = decltype(N_)::v, v = S.sv(N), a = S.sa(N);
    if constexpr (N < S.T0() ? a == 0 : N < S.T0() + 4) {
      wait_vm<S.younger(100 + v, N, false)>();
      PIN(x[v % XD][0]); PIN(x[v % XD][1]);
    }
    if constexpr (N < S.T0() && a == 0) {
      if constexpr (v == 0 || v == 8) STAMPG(GST + (v >> 3));
      if constexpr (PH == 0 && v == ARRV) { if (c.w < 4) { wait_vm<S.between(0, N)>(); cnt_add(c, land(0, 2)); } }
      constexpr bool chk2 = QSTART(NK, v + 2), chk1 = QSTART(NK, v + 1);
      if constexpr (chk1) {
        if (__builtin_expect(rfl(lc) < (PH > 0 ? 8 : 4), 0)) { if constexpr (PH > 0) { prio_hi(c); w_need<PH == 1>(c, ws, KK(NK, v + 1) >> 2, 8); prio_gemm(c, behind); } else cnt_wait(c, land(0, KK(NK, v + 1) >> 2), 4); }
      }
      if constexpr (PH == 0 && v == 7) park_read(c, tl, tl2);
      if constexpr (chk2) lc = cnt_read(c, land(PH, KK(NK, v + 2) >> 2));
    }
    if constexpr (PH > 0 && S.sp(N) && N < S.T0()) {
      if (__builtin_expect(behind, 0)) { behind = rfl(cnt_read(c, fre(PH - 1, 0))) < 8; prio_gemm(c, behind); }
    }
    if constexpr (GSRV > 0 && PH == 2 && S.sp(N) && N <= S.T0()) {
      if (__builtin_expect(srv, 0)) {
        if (ws.pend) { wait_vm<S.between(N - 18, N)>(); w_flush(c, ws); }
        if constexpr (N < S.T0()) {
          unsigned k = 0;
#pragma unroll 1
          while (k < GSRV && w_try(c, ws)) ++k;
          srv = (ws.idx == 0) | (ws.pend != 0);
        }
      }
    }
    SB();
    const bf16x8 wf = cat(wl[N & 7][0], wl[N & 7][1]);
    acc[0][a] = MFMA16(wf, asbf(x[v % XD][0]), acc[0][a]);
    SB();
    if constexpr (N + LA < NS) rdw(IC<N + LA>{}, IC<0>{});
    SB();
    acc[1][a] = MFMA16(wf, asbf(x[v % XD][1]), acc[1][a]);
    SB();
    if constexpr (N + LA < NS) rdw(IC<N + LA>{}, IC<1>{});
    if constexpr (N < S.T0() && a == 8 && v + XD < NK) x_ld<NK, v + XD>(c, x[v % XD], voff, so, so2);
    if constexpr (S.st(N) >= 0) st(IC<S.st(N)>{});
    SB();
  });
  prio_hi(c);
  st(IC<7>{}); st(IC<8>{});
  STAMPG(GST + 2);
  if constexpr (LD) wsrc_voff(c, wn);
  flag_w(c, flag_off);
}
DI f32x4 relu2(const f32x4& a) {
  const float inf = 3.4028234664e38f;
  const float r0 = __builtin_amdgcn_fmed3f(a[0], 0.f, inf), r1 = __builtin_amdgcn_fmed3f(a[1], 0.f, inf), r2 = __builtin_amdgcn_fmed3f(a[2], 0.f, inf), r3 = __builtin_amdgcn_fmed3f(a[3], 0.f, inf);
  return f32x4{r0 * r0, r1 * r1, r2 * r2, r3 * r3};
}
DI u32x4 pack_single(const f32x4& r0, const f32x4& r1) {
  const auto a = __builtin_amdgcn_permlane32_swap(cvtpk(r0[0], r0[1]), cvtpk(r1[0], r1[1]), false, false);
  const auto b = __builtin_amdgcn_permlane32_swap(cvtpk(r0[2], r0[3]), cvtpk(r1[2], r1[3]), false, false);
  return u32x4{a[0], b[0], a[1], b[1]};
}
template <int I> DI void part_store(Ctx& c, const f32x4 (&acc)[2][9], unsigned base, unsigned so) {
  const unsigned g = c.lane >> 4;
  if constexpr (I == 0) gst16c(pack_single(acc[0][8], acc[1][8]), c.rws, (g >> 1) * (4608 - 128) + base + 1024, so);
  else { constexpr int i = (I - 1) >> 1, u = (I - 1) & 1; gst16c(pack_pair(acc[u][2 * i], acc[u][2 * i + 1]), c.rws, base, so + u * 4608 + i * 256); }
}
DI unsigned part_base(Ctx& c) { const unsigned l15 = c.lane & 15; return (l15 >> 2) * 1152 + (l15 & 3) * 16 + (c.lane >> 4) * 64; }
struct Red { u32x4 p[8]; u32x4 s; u32x4 alo, ahi; };
DI void red_sel(Ctx& c, Red& d) {
  const unsigned i = c.lane & 15, one = 0x3F80u << ((i & 1) * 16);
#pragma unroll
  for (int e = 0; e < 4; ++e) { d.alo[e] = (i >> 1) == (unsigned)e ? one : 0u; d.ahi[e] = (i >> 1) == (unsigned)e + 4 ? one : 0u; }
  PIN(d.alo); PIN(d.ahi);
}
DI void red_load(Ctx& c, Red& d, unsigned so) {
  const unsigned vo = (c.lane >> 4) * 36864 + (c.lane & 15) * 16;
  gld16c<1024>(d.s, vo + ((c.lane >> 3) & 1) * (4 * 36864 - 128), c.rws, so);
  static_for<4>([&](auto P_) AI {
    constexpr int P = decltype(P_)::v;
    gld16c<P * 256>(d.p[2 * P], vo, c.rws, so);
    gld16c<P * 256>(d.p[2 * P + 1], vo, c.rws, so + 4 * 36864);
  });
}
#define RED_PIN(d) asm volatile("s_waitcnt vmcnt(0)" : "+v"(d.p[0]), "+v"(d.p[1]), "+v"(d.p[2]), "+v"(d.p[3]), "+v"(d.p[4]), "+v"(d.p[5]), "+v"(d.p[6]), "+v"(d.p[7]), "+v"(d.s)::"memory")
DI void gld8(u32x2& d, unsigned voff, rsrc_t r, unsigned soff) { asm volatile(VP "buffer_load_dwordx2 %0, %1, %2, %3 offen" : "=v"(d) : "v"(voff), "s"(r), "s"(soff) : "memory"); }
DI bool sing_lane(Ctx& c) { return (c.lane & 40) == 0; }
DI void job_offs(Ctx& c, unsigned row, unsigned& o0, unsigned& o2) {
  const unsigned pr = c.lane >> 4, g = (c.lane >> 2) & 3, nt = c.w & 3;
  o0 = row * (DM * 2) + (nt * 128 + pr * 32 + 4 * g) * 2;
  o2 = row * (DM * 2) + (512 + nt * 16 + 4 * (g & 1) + 8 * (pr & 1)) * 2;
}
DI void row_load(Ctx& c, u32x2 (&r)[3], rsrc_t rs, unsigned row) {
  unsigned o0, o2; job_offs(c, row, o0, o2);
  gld8(r[0], o0, rs, 0); gld8(r[1], o0, rs, 32); gld8(r[2], o2, rs, 0);
}
DI void red_sum(const Red& d, float (&a)[12]) {
  f32x4 s0 = MFMA16(asbf(d.alo), asbf(d.p[0]), f32x4{}), s1 = MFMA16(asbf(d.alo), asbf(d.p[2]), f32x4{}), s2 = MFMA16(asbf(d.alo), asbf(d.s), f32x4{});
  s0 = MFMA16(asbf(d.alo), asbf(d.p[1]), s0); s1 = MFMA16(asbf(d.alo), asbf(d.p[3]), s1);
  s0 = MFMA16(asbf(d.ahi), asbf(d.p[4]), s0); s1 = MFMA16(asbf(d.ahi), asbf(d.p[6]), s1);
  s0 = MFMA16(asbf(d.ahi), asbf(d.p[5]), s0); s1 = MFMA16(asbf(d.ahi), asbf(d.p[7]), s1);
#pragma unroll
  for (int e = 0; e < 4; ++e) {
    a[8 + e] = s2[e] + __uint_as_float(__builtin_amdgcn_update_dpp(0, __float_as_uint(s2[e]), 0x128, 0xf, 0xf, false));
    const auto r = __builtin_amdgcn_permlane16_swap(__float_as_uint(s0[e]), __float_as_uint(s1[e]), false, false);
    a[e] = __uint_as_float(r[0]); a[4 + e] = __uint_as_float(r[1]);
  }
}
DI float sum_tok4(float x) {
  x += __uint_as_float(__builtin_amdgcn_update_dpp(0, __float_as_uint(x), 0x124, 0xf, 0xf, false));
  x += __uint_as_float(__builtin_amdgcn_update_dpp(0, __float_as_uint(x), 0x128, 0xf, 0xf, false));
  { auto r = __builtin_amdgcn_permlane16_swap(__float_as_uint(x), __float_as_uint(x), false, false); x = __uint_as_float(r[0]) + __uint_as_float(r[1]); }
  return xhalf_sum(x);
}
DI void ln2_job(Ctx& c, const Red& d, const u32x2 (&xr)[3], unsigned (&hw)[6], unsigned row0) {
  const unsigned lane = c.lane, tok = lane & 3, g = (lane >> 2) & 3, pr = lane >> 4, nt = c.w & 3;
  const unsigned stat = c.ldsb + LSTAT + (c.w >> 2) * 128;
  const bool sing = sing_lane(c);
  float a[12], v[12];
  red_sum(d, a);
#pragma unroll
  for (int e = 0; e < 6; ++e) {
    const unsigned xx = xr[e >> 1][e & 1];
    hw[e] = cvtpk(bflo(xx) + a[2 * e], bfhi(xx) + a[2 * e + 1]); v[2 * e] = bflo(hw[e]); v[2 * e + 1] = bfhi(hw[e]);
  }
  const float s1 = ((v[0] + v[1]) + (v[2] + v[3])) + ((v[4] + v[5]) + (v[6] + v[7])), s0 = (v[8] + v[9]) + (v[10] + v[11]);
  const float mw = sum_tok4(sing ? s1 + s0 : s1) * (1.f / 144);
  float s2 = 0.f, s3 = 0.f;
#pragma unroll
  for (int e = 0; e < 8; ++e) { const float t = v[e] - mw; s2 += t * t; }
#pragma unroll
  for (int e = 0; e < 4; ++e) { const float t = v[8 + e] - mw; s3 += t * t; }
  s2 = sum_tok4(sing ? s2 + s3 : s2);
  if (lane < 4) *(LAS f32x2*)(size_t)(stat + (lane * 4 + nt) * 8) = f32x2{mw, s2};
  SB();
  cnt_add(c, LSYNC + C_GB + (c.w >> 2) * 4);
  const unsigned row = row0 + tok, mb = row >> 5, r32 = row & 31;
  unsigned o0 = ((mb * 36 + 2 * (nt * 4 + pr) + (g >> 1)) * 64 + (g & 1) * 32 + r32) * 16;
  unsigned o1 = lane < 8 ? ((mb * 36 + 32 + nt) * 64 + g * 32 + r32) * 16 : OOB;
  PIN(o0); PIN(o1);
  cnt_wait(c, LSYNC + C_GB + (c.w >> 2) * 4, 4);
  const f32x4 sa = *(LAS f32x4*)(size_t)(stat + tok * 32), sb = *(LAS f32x4*)(size_t)(stat + tok * 32 + 16);
  const float mean = ((sa[0] + sa[2]) + (sb[0] + sb[2])) * 0.25f;
  const float t0 = sa[0] - mean, t1 = sa[2] - mean, t2 = sb[0] - mean, t3 = sb[2] - mean;
  const float m2 = ((sa[1] + sa[3]) + (sb[1] + sb[3])) + 144.f * ((t0 * t0 + t1 * t1) + (t2 * t2 + t3 * t3));
  const float rs = rsqrtf(m2 * (1.f / DM) + 1e-5f);
#pragma unroll
  for (int e = 0; e < 12; ++e) v[e] = (v[e] - mean) * rs;
  gst16c(u32x4{cvtpk(v[0], v[1]), cvtpk(v[2], v[3]), cvtpk(v[4], v[5]), cvtpk(v[6], v[7])}, c.rws, o0, WS_HN);
  const unsigned u0 = cvtpk(v[8], v[9]), u1 = cvtpk(v[10], v[11]);
  const auto ra = __builtin_amdgcn_permlane16_swap(u0, u0, false, false), rb = __builtin_amdgcn_permlane16_swap(u1, u1, false, false);
  gst16c(u32x4{ra[0], rb[0], ra[1], rb[1]}, c.rws, o1, WS_HN);
}

#define XS0 4
#define XS1 12
DI u64 sel64(bool p, u64 x, u64 y) { return p ? x : y; }
DI unsigned phase_attn(Ctx& c, QOut& qo, WSrc& wso, u32x4 (&xr)[XM][2], const u16* wo, const u16* w1, const u16* b1, const u16* w2, const u16* b2) {
  AttnState a;
  const unsigned ldsb = c.ldsb, lane = c.lane, w = c.w, b = c.b;
  const unsigned xcd = b & 7, jj = b >> 3, head = xcd * 4 + (jj >> 3), pi = jj & 7;
  const unsigned l31 = lane & 31, h = lane >> 5;
  const unsigned kh = w >> 2, rg = kh ? 7 - w : w, partner = 7 - w;
  const unsigned n1 = 2 * pi + 2, n1k = kh ? n1 : 0, n1t = kh ? n1 : 99;
  const unsigned kbase = WS_K + head * (SEQ * HD * 2) + w * 4096;
  a.voff = lane * 16; a.dk = ldsb + w * 4096; a.n1k = n1k;
  a.wide = ~0u << (n1 - 2);
  a.drop = (((1u << n1k) - 1) >> 3) | (~0u << 14);
  a.u = kbase + 32768;
  a.o0 = kh ? 0xF0000000u : kbase + (WS_V - WS_K); a.o1 = kh ? 0xF0000000u : kbase + 16384; a.o2 = n1k > 2 ? 0xF0000000u : a.u;
  a.kb[0] = ldsb + lane * 16; a.kb[1] = a.kb[0] + 32768; a.vb[0] = a.kb[0] + 65536; a.vb[1] = a.kb[0] + 98304;
  PIN(a.kb[0]); PIN(a.kb[1]); PIN(a.vb[0]); PIN(a.vb[1]);
  if (kh == 0) {
#pragma unroll
    for (int d = 0; d < 8; ++d) *(LAS u32x4*)(size_t)(ldsb + LE + rg * 8192 + d * 1024 + lane * 16) = qo.q[d];
  }
  if (__builtin_expect(!qo.kdone, 0)) {
    wait_flags(c, F_K + head * 64, 8, 1);
#pragma unroll
    for (int p = 0; p < 4; ++p) dma16c(ldsb + kh * 16384 + w * 4096 + p * 1024, a.voff, c.rws, kbase + p * 1024);
  }
  STAMP(8);
#pragma unroll
  for (int d = 0; d < 8; ++d) a.qf[d] = asbf(qo.q[d]);
  a.negm = f32x16{}; a.mhat = 0.f; a.l0 = a.l1 = 0.f; a.fct = 1.f; a.pend = 0;
  if (kh == 0) wait_vm<0>();
  bar();
  STAMP(9);
#pragma unroll
  for (int n = 0; n < 4; ++n) a.fr[n] = lds_b128(a.kb[0] + ((n & 1) * 8 + (n >> 1)) * 1024);
  const int dq = (int)(rg * 32 + l31) - (int)(kh * 64) - 4 * (int)h;
  a.diag = (kh ? (rg < 2 ? 3u : 2u) << (n1 - 2) : 0u) | ((kh || rg < 2) ? 1u << 16 : 0u);
  a.first = 1u | (kh << n1);
  a.cold = (kh ? 7u << (n1 - 2) : 0u) | (w == 0 ? 2u << n1 : 0u) | (kh ? 0u : 1u << 15);
  const unsigned mtH = jj >> 2, fH = F_CTX + (mtH * 32 + xcd * 4) * 8;
  u32x2 fl = {0u, 0u};
  auto post = [&](auto X_, unsigned s) AI {
    constexpr int X = decltype(X_)::v;
    if (__builtin_expect((a.cold >> s) & 1, 0)) {
      if constexpr (X == 0) {
        if (s == n1t) {
          const float inv = __builtin_amdgcn_rcpf(xhalf_sum(a.l0 + a.l1));
          const unsigned so = WS_CTX + ((pi * 4 + rg) * 256 + head * 8) * 1024;
#pragma unroll
          for (int d = 0; d < 4; ++d) {
            gst16c(pack8<0>(a.o[d], inv), c.rws, a.voff, so + d * 2048);
            gst16c(pack8<1>(a.o[d], inv), c.rws, a.voff, so + d * 2048 + 1024);
            a.o[d] = f32x16{};
          }
          a.l0 = a.l1 = 0.f;
          a.vb[1] += 16384;
        }
        if (s + 2 == n1t) a.kb[0] += 16384;
      } else {
        if (s == n1 + 1) flag_store(c, F_CTX + (pi * 32 + head) * 8);
        if (s == 15) flag_load(c, fl, fH, kh ? 0 : 4);
        if (s + 1 == n1t) {
          unsigned qa = ldsb + LE + rg * 8192 + a.voff; PIN(qa);
#pragma unroll
          for (int d = 0; d < 8; ++d) a.qf[d] = lds_b128(qa + d * 1024);
          a.negm = f32x16{};
          a.kb[1] += 16384; a.vb[0] += 16384;
        }
      }
      if (a.pend) {
#pragma unroll
        for (int d = 0; d < 4; ++d)
#pragma unroll
          for (int r = 0; r < 16; ++r) a.o[d][r] *= a.fct;
        a.l0 *= a.fct; a.l1 *= a.fct; a.pend = 0;
      }
      asm volatile("s_nop 1" : "+v"(a.negm));
    }
    add2(a.l0, a.l1, a.t0, a.t1);
    SB();
  };
  asm volatile("s_nop 1" : "+v"(a.negm));
  a.dummy = 0; a.tm = 15;
  unsigned tg, tr, tc, to;
  attn_iter<0, false>(c, a, 0, dq, [&](auto M_) AI {
    constexpr int M = decltype(M_)::v;
    if constexpr (M >= 12) { a.o[M - 12] = f32x16{}; PIN(a.o[M - 12]); }
    if constexpr (M == 1) { tg = lane * 8 + w; const unsigned li = tg - (tg < 162 ? 0 : tg < 324 ? 162 : 324); tr = __umul24(li, 57) >> 9; tc = li - __umul24(tr, 9); PIN(tg); PIN(tr); PIN(tc); }
    if constexpr (M == 3) { to = __umul24(tr + (tr < 16 ? 512 * xcd + jj * 16 : 4096 + 64 * xcd + jj * 2 - 16), DM * 2) + tc * 128; PIN(to); }
    if constexpr (M == 4) { const unsigned o1 = __umul24(jj * 18 + tr, FF * 2) + (tc < 8 ? xcd * 1024 + tc * 128 : 8192 + xcd * 128); to = tg - 162 < 162 ? o1 : to + 0; PIN(to); }
    if constexpr (M == 5) {
      unsigned bytes;
      asm volatile("s_mov_b32 %0, .Lfb_end-.Lfb_begin" : "=s"(bytes));
      const unsigned oc = ((tg - 468) * 32 + jj) * 128;
      to = tg >= 468 ? (oc < bytes ? oc : 0) : to + 0; PIN(to);
    }
    if constexpr (M == 7) { a.ta = sel64(tg < 162, (u64)w2, sel64(tg < 324, (u64)w1, (u64)wo)); PIN(a.ta); }
    if constexpr (M == 8) { a.ta = sel64(tg >= 468, c.pc0, a.ta) + to; PIN(a.ta); }
  });
  post(IC<0>{}, 0);
  for (unsigned s = 1; s < 17; s += 2) {
    attn_iter<1, true>(c, a, s, dq); post(IC<1>{}, s);
    attn_iter<0, true>(c, a, s + 1, dq); post(IC<0>{}, s + 1);
  }
  if (w == 0) *(LAS unsigned*)(size_t)(ldsb + LSYNC + lane * 4) = 0;
  wsrc_setup<0>(c, wso, mk_rsrc(wo, NQ * DM * 2), DM * 2, (jj & 3) * 256, 1024 + (jj & 3) * 32, 512 * xcd, 0, 0);
  const unsigned ol = opaque_lane();
  wso.voff[0] = w_voff(wso, ol, 0); PIN(wso.voff[0]);
#pragma unroll
  for (int e = 0; e < 8; ++e) { a.pk[e >> 2][e & 3] = cvtpk(a.S[0][0][2 * e], a.S[0][0][2 * e + 1]); PIN(a.pk[e >> 2][e & 3]); }
  a.l0 = fadd_s(a.l0, a.S[0][0][14]); a.l1 = fadd_s(a.l1, a.S[0][0][15]);
  if (n1k == 16) wait_vm<8>(); else wait_vm<0>();
  bar();
  constexpr auto KSM = [](int m) constexpr { return m < 8 ? m >> 2 : 2 + (m & 1); };
  constexpr auto DBM = [](int m) constexpr { return m < 8 ? m & 3 : (m - 8) >> 1; };
  auto rdv = [&](auto M_) AI { constexpr int m = decltype(M_)::v; a.fr[m & 7] = lds_b128(a.vb[0] + (DBM(m) * 4 + KSM(m)) * 1024); };
  static_for<6>([&](auto M_) AI { rdv(M_); });
  PIN(fl);
  asm volatile("" ::"v"(a.dummy));
  STAMPG(33);
  const unsigned late = ((unsigned)__builtin_amdgcn_ballot_w64(fl[0] != c.ep0 || fl[1] != c.ep1) & 15u) | kh;
  const bool slow = late != 0 && kh == 0;
  const unsigned stat = ldsb + 544 * WP + rg * 512 + lane * 8, scr = ldsb + rg * 16384 + lane * 16;
  const unsigned xo = WS_CTX + ((mtH * 4 + (w & 3)) * 256 + xcd * 32) * 1024, xv = x_voff(c);
  const unsigned r3 = 384 + 32 * (w & 3);
  float lt = 0.f;
  auto put = [&](auto D_) AI {
    constexpr int d = decltype(D_)::v;
#pragma unroll
    for (int e = 0; e < 4; ++e) *(LAS f32x4*)(size_t)(scr + (d * 4 + e) * 1024) = f32x4{a.o[d][4 * e], a.o[d][4 * e + 1], a.o[d][4 * e + 2], a.o[d][4 * e + 3]};
  };
  if (__builtin_expect(slow, 0)) flag_load(c, fl, fH, 4);
  SB();
  static_for<16>([&](auto M_) AI {
    constexpr int M = decltype(M_)::v;
    a.o[DBM(M)] = MFMA32(a.fr[M & 7], asbf(a.pk[KSM(M)]), a.o[DBM(M)]);
    SB();
    if constexpr (M + 6 < 16) rdv(IC<M + 6>{});
    if constexpr (M < 9) dma16_if(ldsb + r3 * WP + M * 1024, wso.voff[M], wso.r, wso.main + r3 * wso.pitch, kh);
    if constexpr (M < 8) { wso.voff[M + 1] = w_voff(wso, ol, M + 1); PIN(wso.voff[M + 1]); }
    if constexpr (M < 4) {
#pragma unroll
      for (int e = 4 * M; e < 4 * M + 4; ++e) { a.S[0][1][e] = __builtin_amdgcn_exp2f(a.S[0][1][e]); PIN(a.S[0][1][e]); }
    } else if constexpr (M < 8) {
#pragma unroll
      for (int e = 2 * (M - 4); e < 2 * (M - 4) + 2; ++e) {
        a.pk[2 + (e >> 2)][e & 3] = cvtpk(a.S[0][1][2 * e], a.S[0][1][2 * e + 1]); PIN(a.pk[2 + (e >> 2)][e & 3]);
        a.l0 = fadd_s(a.l0, a.S[0][1][2 * e]); a.l1 = fadd_s(a.l1, a.S[0][1][2 * e + 1]);
      }
    }
    if constexpr (M == 8) { lt = xhalf_sum(a.l0 + a.l1); PIN(lt); }
    if constexpr (M >= XS0 && M - XS0 < XS1) x_ld16_if<M - XS0>(c, xr[M - XS0], xv, xo, late);
    if constexpr (M == 12) { if (kh == 0) { *(LAS f32x2*)(size_t)stat = f32x2{a.mhat, lt}; put(IC<0>{}); } }
    if constexpr (M == 14) { if (kh == 0) put(IC<1>{}); }
    SB();
  });
  STAMPG(34);
  if (kh == 0) { put(IC<2>{}); put(IC<3>{}); } else wait_vm<0>();
  STAMP(10);
  STAMPG(35);
  bar();
  STAMPG(36);
  if (kh == 0) {
    if (n1 == 16) flag_store(c, F_CTX + (7 * 32 + head) * 8);
    w_group(c, wso, 2);
    if (__builtin_expect(slow, 0)) { wait_vm<9>(); flag_check(c, fl, fH, 4); }
  }
  static_for<XM - XS1>([&](auto V_) AI { constexpr int v = XS1 + decltype(V_)::v; x_ld16_if<v>(c, xr[v], xv, xo, late); });
  if (kh) {
    f32x4 y[2][4];
    auto get = [&](int d) AI {
#pragma unroll
      for (int e = 0; e < 4; ++e) y[d & 1][e] = *(LAS f32x4*)(size_t)(scr + (d * 4 + e) * 1024);
    };
    const f32x2 ps = *(LAS f32x2*)(size_t)stat;
    get(0); get(1);
    SB();
    const float mm = fmaxf(a.mhat, ps[0]);
    const float fo = __builtin_amdgcn_exp2f(a.mhat - mm), fp = __builtin_amdgcn_exp2f(ps[0] - mm);
    const float inv = __builtin_amdgcn_rcpf(lt * fo + ps[1] * fp), f = fo * inv, f2 = fp * inv;
    const unsigned so = WS_CTX + (((15 - pi) * 4 + rg) * 256 + head * 8) * 1024;
#pragma unroll
    for (int d = 0; d < 4; ++d) {
      f32x16 x = a.o[d];
#pragma unroll
      for (int e = 0; e < 16; ++e) x[e] = x[e] * f + y[d & 1][e >> 2][e & 3] * f2;
      u32x4 p0 = pack8<0>(x), p1 = pack8<1>(x);
      PIN(p0); PIN(p1);
      SB();
      if (d < 2) get(d + 2);
      if (d == 1) { SB(); cnt_add(c, LSYNC + C_MRG); }
      gst16c(p0, c.rws, lane * 16, so + d * 2048);
      gst16c(p1, c.rws, lane * 16, so + d * 2048 + 1024);
      SB();
    }
    STAMPG(37);
    cnt_wait(c, LSYNC + C_MRG, 4);
    w_group(c, wso, 1);
    wait_vm<9>();
    STAMPG(38);
    fput(u32x2{c.ep0, c.ep1}, c.rws, lane == 0 ? F_CX2 + (((7 - pi) * 4 + rg) * 32 + head) * 8 : OOB);
    const unsigned fL = F_CX2 + ((mtH * 4 + (w & 3)) * 32 + xcd * 4) * 8;
    flag_load(c, fl, fL, 4);
    static_for<5>([&](auto P_) AI { park_load<decltype(P_)::v>(c, w1, b1, w2, b2); });
    w_group(c, wso, 0);
    wait_vm<9>(); cnt_add(c, land(0, 1));
    wait_vm<0>(); cnt_add(c, land(0, 0));
    STAMPG(39);
    prio_lo();
    flag_check(c, fl, fL, 4);
  }
  STAMP(11);
  return late;
}

struct TId { unsigned sp, nt, mt, nt32, mb, own, wq; bool isH; };
DI TId tids(Ctx& c) {
  fresh(c);
  TId t;
  const unsigned xcd = c.b & 7, jj = c.b >> 3;
  t.isH = c.w < 4; t.wq = c.w & 3; t.sp = xcd; t.nt = jj & 3; t.mt = (t.isH ? 0 : 8) + (jj >> 2);
  t.nt32 = xcd * 4 + t.nt; t.mb = t.mt * 4 + t.wq; t.own = t.nt * 8 + t.sp;
  return t;
}
DI void acc_zero(f32x4 (&acc)[2][9]) {
#pragma unroll
  for (int u = 0; u < 2; ++u)
#pragma unroll
    for (int a = 0; a < 9; ++a) acc[u][a] = f32x4{};
}
DI void phase_tail(Ctx& c, WSrc& wso, u32x4 (&xr)[XM][2], unsigned xlate, const u16* x, const u16* w1, const u16* b1, const u16* w2, const u16* b2, u16* out) {
  f32x4 acc[2][9]; u32x2 tl[9], tl2[9]; unsigned hw[6]; WSrc ws, wt;
  {
    const TId t = tids(c);
    const unsigned xo = WS_CTX + (t.mb * 256 + t.sp * 32) * 1024;
    prio_hi(c);
    STAMPG(12);
    x_pro_masked16(c, xr, xo, xlate);
    wsrc_setup(c, ws, mk_rsrc(w1, DM * FF * 2), FF * 2, t.nt32 * 256, 8192 + t.nt32 * 32, 0, 512, 1);
    acc_zero(acc);
    const unsigned pb = part_base(c), po = WS_P1 + (((t.mt * 4 + t.nt) * 8 + t.sp) * 8 + 2 * t.wq) * 4608;
    gemm_g<16, 1, 0, 13>(c, acc, xr, tl, tl2, wso, ws, xo, 0, cnt_at(t.mt, N_P1 + t.wq * 4 + t.nt), [&](auto I) AI { part_store<decltype(I)::v>(c, acc, pb, po); });
    STAMPG(16);
  }
  {
    const TId t = tids(c);
    u32x2 xres[3]; Red rd;
    row_load(c, xres, mk_rsrc(x, SEQ * DM * 2), t.mt * 128 + t.own * 4 + (c.lane & 3));
    unsigned dummy = 0;
    w_touch<4>(c, dummy, w2, w2, w2, c.w, false);
    poll_srv(c, ws, cnt_at(t.mt, N_P1 + t.nt * 4 + t.wq), 8, t.isH ? MXA : MXB, (SK1 & 1) ? 0 : 2);
    red_load(c, rd, WS_P1 + (t.mt * 256 + t.wq * 64 + (t.own >> 2)) * 4608 + (t.own & 3) * 1152);
    red_sel(c, rd);
    STAMPG(17);
    asm volatile("" ::"v"(dummy));
    PIN(xres[0]); PIN(xres[1]); PIN(xres[2]);
    RED_PIN(rd);
    w_flush(c, ws);
    STAMPG(18);
    ln2_job(c, rd, xres, hw, t.mt * 128 + t.own * 4);
#pragma unroll
    for (int e = 0; e < 6; ++e) PIN(hw[e]);
    STAMPG(19);
    flag_w(c, cnt_at(t.mt, N_HN + t.nt));
    STAMPG(20);
    poll_srv(c, ws, cnt_at(t.mt, N_HN + t.wq), 32, t.isH ? MXA : MXB, (SK1 & 2) ? 0 : 2);
    STAMPG(21);
    asm volatile("" ::"v"(dummy));
    const unsigned xh = WS_HN + t.mb * 36 * 1024;
    x_pro<18>(c, xr, x_voff(c), xh, xh + 32768);
    wsrc_setup(c, wt, mk_rsrc(w2, FF * DM * 2), DM * 2, t.nt * 256, 1024 + t.nt * 32, 512 * t.sp, 4096 + 64 * t.sp, 2);
#pragma unroll
    for (int a = 0; a < 9; ++a) {
      const u32x2 bias = *(LAS u32x2*)(size_t)(c.ldsb + LB1 + a * 32 + (c.lane >> 4) * 8);
      acc[0][a] = acc[1][a] = f32x4{bflo(bias[0]), bfhi(bias[0]), bflo(bias[1]), bfhi(bias[1])};
    }
    const unsigned vo = x_voff(c), ho = WS_H1 + (t.mb * 288 + 8 * t.nt32) * 1024, hs = WS_H1 + (t.mb * 288 + 256 + t.nt32) * 1024;
    gemm_g<18, 0, 1, 22>(c, acc, xr, tl, tl, ws, wt, xh, xh + 32768, cnt_at(t.mt, N_H1 + t.wq * 8 + t.sp), [&](auto I_) AI {
      constexpr int I = decltype(I_)::v;
      if constexpr (I == 0) {
        const unsigned g = c.lane >> 4;
        gst16c(pack_single(relu2(acc[0][8]), relu2(acc[1][8])), c.rws, ((g & 1) * 32 + (g >> 1) * 16 + (c.lane & 15)) * 16, hs);
      } else {
        constexpr int i = (I - 1) >> 1, u = (I - 1) & 1;
        gst16c(pack_pair(relu2(acc[u][2 * i]), relu2(acc[u][2 * i + 1])), c.rws, vo, ho + i * 2048 + u * 256);
      }
    });
    STAMPG(25);
  }
  {
    const TId t = tids(c);
    poll_srv(c, wt, cnt_at(t.mt, N_H1 + t.wq * 8 + t.sp), 4, t.isH ? MXC : MXD, ((SK1 & 4) && !t.isH) ? 0 : 2);
    STAMPG(26);
    const unsigned x1 = WS_H1 + (t.mb * 288 + t.sp * 32) * 1024, x1t = WS_H1 + (t.mb * 288 + 256 + t.sp * 4) * 1024;
    x_pro<18>(c, xr, x_voff(c), x1, x1t);
    acc_zero(acc);
    const unsigned pb = part_base(c), po = WS_P2 + (((t.mt * 4 + t.nt) * 8 + t.sp) * 8 + 2 * t.wq) * 4608;
    gemm_g<18, 0, 2, 27>(c, acc, xr, tl2, tl2, wt, wt, x1, x1t, cnt_at(t.mt, N_P2 + t.wq * 4 + t.nt), [&](auto I) AI { part_store<decltype(I)::v>(c, acc, pb, po); });
    STAMPG(30);
  }
  {
    const TId t = tids(c);
    float hb[12]; Red rd;
    {
      unsigned o0, o2; job_offs(c, 0, o0, o2);
      const u32x2 bb[3] = {*(LAS u32x2*)(size_t)(c.ldsb + LB2 + o0), *(LAS u32x2*)(size_t)(c.ldsb + LB2 + o0 + 32), *(LAS u32x2*)(size_t)(c.ldsb + LB2 + o2)};
#pragma unroll
      for (int e = 0; e < 6; ++e) { const unsigned bv = bb[e >> 1][e & 1]; hb[2 * e] = bflo(bv) + bflo(hw[e]); hb[2 * e + 1] = bfhi(bv) + bfhi(hw[e]); PIN(hb[2 * e]); PIN(hb[2 * e + 1]); }
      red_sel(c, rd);
    }
    poll_w(c, cnt_at(t.mt, N_P2 + t.nt * 4 + t.wq), 8);
    STAMPG(31);
    red_load(c, rd, WS_P2 + (t.mt * 256 + t.wq * 64 + (t.own >> 2)) * 4608 + (t.own & 3) * 1152);
    RED_PIN(rd);
    const rsrc_t ro = mk_rsrc(out, SEQ * DM * 2);
    float a[12]; unsigned ow[6];
    red_sum(rd, a);
#pragma unroll
    for (int e = 0; e < 6; ++e) ow[e] = cvtpk(a[2 * e] + hb[2 * e], a[2 * e + 1] + hb[2 * e + 1]);
    {
      const unsigned g = (c.lane >> 2) & 3, ro0 = (t.mt * 128 + t.own * 4 + (c.lane & 3)) * (DM * 2);
      u32x4 q, q2;
      q[0] = __builtin_amdgcn_update_dpp(ow[0], ow[2], 0x114, 0xf, 0xa, false); q[1] = __builtin_amdgcn_update_dpp(ow[1], ow[3], 0x114, 0xf, 0xa, false);
      q[2] = __builtin_amdgcn_update_dpp(ow[2], ow[0], 0x104, 0xf, 0x5, false); q[3] = __builtin_amdgcn_update_dpp(ow[3], ow[1], 0x104, 0xf, 0x5, false);
      q2[0] = ow[4]; q2[1] = ow[5];
      q2[2] = __builtin_amdgcn_update_dpp(0, ow[4], 0x104, 0xf, 0xf, false); q2[3] = __builtin_amdgcn_update_dpp(0, ow[5], 0x104, 0xf, 0xf, false);
      __builtin_amdgcn_raw_buffer_store_b128(q, ro, ro0 + t.wq * 256 + (c.lane >> 4) * 64 + (g & 1) * 32 + (g >> 1) * 16, 0, 0);
      __builtin_amdgcn_raw_buffer_store_b128(q2, ro, (c.lane & 44) == 0 ? ro0 + 1024 + t.wq * 32 + (c.lane >> 4) * 16 : OOB, 0, 0);
    }
    STAMPG(32);
    if (c.b == 0 && c.w == 0) gst8c(u32x2{c.ep0, c.ep1}, c.rws, c.lane == 0 ? F_EPOCH : OOB, WS_FLAGS);
  }
}

__global__ __launch_bounds__(512, 2) void fused_block(const u16* __restrict__ x, const u16* __restrict__ ckv, const u16* __restrict__ wq,
                                                      const u16* __restrict__ wuk, const u16* __restrict__ wuv, const u16* __restrict__ wo,
                                                      const u16* __restrict__ w1, const u16* __restrict__ b1, const u16* __restrict__ w2,
                                                      const u16* __restrict__ b2, u16* __restrict__ out, char* __restrict__ ws) {
  extern __shared__ __attribute__((aligned(16))) char lds[];
  Ctx c;
  asm volatile("s_getpc_b64 %0\n.Lfb_begin:" : "=s"(c.pc0));
  c.ldsb = (unsigned)(size_t)lds; c.lane = threadIdx.x & 63; c.w = rfl(threadIdx.x >> 6); c.b = blockIdx.x;
  c.rws = mk_rsrc(ws, WS_END);
  asm volatile("" : "+s"(wo), "+s"(w1), "+s"(w2), "+s"(b1), "+s"(b2), "+s"(out), "+s"(wq), "+s"(wuv));
  STAMP(0);
  QOut qo;
  WSrc wso; u32x4 xr[XM][2];
  phase_qkv(c, qo, x, ckv, wq, wuk, wuv);
  fresh(c);
  const unsigned xlate = phase_attn(c, qo, wso, xr, wo, w1, b1, w2, b2);
  fresh(c);
  phase_tail(c, wso, xr, xlate, x, w1, b1, w2, b2, out);
  asm volatile(".Lfb_end:");
}

extern "C" void kernel_launch(void* const* d_in, const int* in_sizes, int n_in, void* d_out, int out_size, void* d_ws, size_t ws_size,
                              hipStream_t stream) {
  hipFuncSetAttribute((const void*)fused_block, hipFuncAttributeMaxDynamicSharedMemorySize, 163840);
  fused_block<<<256, 512, 163840, stream>>>((const u16*)d_in[0], (const u16*)d_in[1], (const u16*)d_in[2], (const u16*)d_in[3], (const u16*)d_in[4],
                                            (const u16*)d_in[5], (const u16*)d_in[7], (const u16*)d_in[8], (const u16*)d_in[9], (const u16*)d_in[10],
                                            (u16*)d_out, (char*)d_ws);
}
```

```cpp
#include <hip/hip_runtime.h>
#include <stdint.h>
#include <utility>

typedef __bf16 bf16x8 __attribute__((ext_vector_type(8)));
typedef __bf16 bf16x4 __attribute__((ext_vector_type(4)));
typedef __bf16 bf16x2 __attribute__((ext_vector_type(2)));
typedef float f32x16 __attribute__((ext_vector_type(16)));
typedef float f32x4 __attribute__((ext_vector_type(4)));
typedef float f32x2 __attribute__((ext_vector_type(2)));
typedef unsigned u32x4 __attribute__((ext_vector_type(4)));
typedef unsigned u32x2 __attribute__((ext_vector_type(2)));
typedef unsigned short u16;
typedef unsigned long long u64;
#define LAS __attribute__((address_space(3)))
#define DI __device__ __forceinline__
#define AI __attribute__((always_inline))
typedef __amdgpu_buffer_rsrc_t rsrc_t;

constexpr int SEQ = 2048, DM = 576, NH = 32, HD = 128, DC = 512, FF = 4608, NQ = 4096;

constexpr unsigned WS_FLAGS = 0;
constexpr unsigned WS_XN = 131072;
constexpr unsigned WS_K = WS_XN + SEQ * DM * 2;
constexpr unsigned WS_V = WS_K + SEQ * NQ * 2;
constexpr unsigned WS_CTX = WS_V + SEQ * NQ * 2;
constexpr unsigned WS_P1 = WS_CTX + SEQ * NQ * 2;
constexpr unsigned WS_H = WS_P1 + 8 * SEQ * DM * 2;
constexpr unsigned WS_HN = WS_H + SEQ * DM * 2;
constexpr unsigned WS_H1 = WS_HN + SEQ * DM * 2;
constexpr unsigned WS_P2 = WS_H1 + SEQ * FF * 2;
constexpr unsigned WS_CNT = WS_P2 + 8 * SEQ * DM * 2;
constexpr unsigned WS_END = WS_CNT + 16 * 80 * 128;
constexpr unsigned F_EPOCH = 0, F_XN = 4096, F_KV = 8192, F_CTX = 12288, F_K = 81920, F_V = 90112, F_CX2 = 98304;

constexpr unsigned N_P1 = 0, N_P2 = 16, N_HN = 32, N_H1 = 36;
constexpr unsigned cnt_at(unsigned mt, unsigned i) { return WS_CNT + (mt * 80 + i) * 128; }

template <int... Is> using iseq = std::integer_sequence<int, Is...>;
template <int N> using make_iseq = std::make_integer_sequence<int, N>;
template <int I> struct IC { static constexpr int v = I; };
template <class F, int... Is> DI void static_for_(F&& f, iseq<Is...>) { (f(IC<Is>{}), ...); }
template <int N, class F> DI void static_for(F&& f) { static_for_(f, make_iseq<N>{}); }

#define SB() __builtin_amdgcn_sched_barrier(0)
#define PIN(x) asm volatile("" : "+v"(x))
DI unsigned rfl(unsigned x) { return __builtin_amdgcn_readfirstlane(x); }
DI rsrc_t mk_rsrc(const void* p, unsigned bytes) { return __builtin_amdgcn_make_buffer_rsrc((void*)p, (short)0, (int)bytes, 0x00020000); }
#define VPD
#define VP
constexpr unsigned OOB = 0xFFFFFF00u;
DI void dma16(unsigned lds_addr, unsigned voff, rsrc_t r, unsigned soff) {
  asm volatile(VP VPD "s_mov_b32 m0, %0\n\ts_nop 0\n\tbuffer_load_dwordx4 %1, %2, %3 offen lds" ::"s"(lds_addr), "v"(voff), "s"(r), "s"(soff) : "memory", "m0");
}
DI void dma16c(unsigned lds_addr, unsigned voff, rsrc_t r, unsigned soff) {
  asm volatile(VP "s_mov_b32 m0, %0\n\ts_nop 3\n\tbuffer_load_dwordx4 %1, %2, %3 offen sc1 lds" ::"s"(lds_addr), "v"(voff), "s"(r), "s"(soff) : "memory", "m0");
}
template <int IMM> DI void gld16c(u32x4& d, unsigned voff, rsrc_t r, unsigned soff) {
  asm volatile(VP "buffer_load_dwordx4 %0, %1, %2, %3 offen offset:%4 sc1" : "=v"(d) : "v"(voff), "s"(r), "s"(soff), "i"(IMM) : "memory");
}
template <int IMM> DI void gld16(u32x4& d, unsigned voff, rsrc_t r, unsigned soff) {
  asm volatile(VP "buffer_load_dwordx4 %0, %1, %2, %3 offen offset:%4" : "=v"(d) : "v"(voff), "s"(r), "s"(soff), "i"(IMM) : "memory");
}
DI void gld8c(u32x2& d, unsigned voff, rsrc_t r, unsigned soff) {
  asm volatile(VP "buffer_load_dwordx2 %0, %1, %2, %3 offen sc1" : "=v"(d) : "v"(voff), "s"(r), "s"(soff) : "memory");
}
DI void gst16c(u32x4 v, rsrc_t r, unsigned voff, unsigned soff) {
  asm volatile(VP "buffer_store_dwordx4 %0, %1, %2, %3 offen sc1\n\ts_nop 1" ::"v"(v), "v"(voff), "s"(r), "s"(soff) : "memory");
}
DI void gst8c(u32x2 v, rsrc_t r, unsigned voff, unsigned soff) { __builtin_amdgcn_raw_buffer_store_b64(v, r, voff, soff, 16); }
DI void fput(u32x2 v, rsrc_t r, unsigned voff) { asm volatile("s_nop 4\n\tbuffer_atomic_swap_x2 %0, %1, %2, 0 offen sc1\n\ts_nop 1" ::"v"(v), "v"(voff), "s"(r) : "memory"); }
DI void fadd(rsrc_t r, unsigned voff) { asm volatile("s_nop 4\n\tbuffer_atomic_add_x2 %0, %1, %2, 0 offen sc1\n\ts_nop 1" ::"v"(u32x2{1u, 0u}), "v"(voff), "s"(r) : "memory"); }
DI void fget(u32x2& d, rsrc_t r, unsigned voff) {
  d = u32x2{0u, 0u};
  asm volatile("s_nop 4\n\tbuffer_atomic_add_x2 %0, %1, %2, 0 offen sc0 sc1" : "+v"(d) : "v"(voff), "s"(r) : "memory");
}
template <int N> DI void wait_vm() { asm volatile("s_waitcnt vmcnt(%0)" ::"i"(N) : "memory"); }
DI void bar() { asm volatile("" ::: "memory"); __builtin_amdgcn_s_waitcnt(0xC07F); __builtin_amdgcn_s_barrier(); asm volatile("" ::: "memory"); }
DI unsigned cvtpk(float lo, float hi) { f32x2 v = {lo, hi}; bf16x2 b = __builtin_convertvector(v, bf16x2); return __builtin_bit_cast(unsigned, b); }
DI float bflo(unsigned x) { return __uint_as_float(x << 16); }
DI float bfhi(unsigned x) { return __uint_as_float(x & 0xffff0000u); }
DI bf16x8 lds_b128(unsigned a) { return *(LAS bf16x8*)(size_t)a; }
DI bf16x4 lds_tr(unsigned a) { return __builtin_amdgcn_ds_read_tr16_b64_v4bf16((LAS bf16x4*)(size_t)a); }
DI bf16x8 cat(bf16x4 a, bf16x4 b) { return __builtin_shufflevector(a, b, 0, 1, 2, 3, 4, 5, 6, 7); }
DI bf16x8 asbf(u32x4 x) { return __builtin_bit_cast(bf16x8, x); }
#define MFMA32(a, b, c) __builtin_amdgcn_mfma_f32_32x32x16_bf16((a), (b), (c), 0, 0, 0)
#define MFMA16(a, b, c) __builtin_amdgcn_mfma_f32_16x16x32_bf16((a), (b), (c), 0, 0, 0)
template <bool ZERO = false> DI void mfma16(f32x4& d, const bf16x8& x, const bf16x8& y) {
  if constexpr (ZERO) asm volatile("v_mfma_f32_16x16x32_bf16 %0, %1, %2, 0" : "=v"(d) : "v"(x), "v"(y));
  else asm volatile("v_mfma_f32_16x16x32_bf16 %0, %1, %2, %0" : "+v"(d) : "v"(x), "v"(y));
}
DI void mfma_pad() { SB(); asm volatile("s_nop 7"); SB(); }
template <int S> DI u32x4 pack8(const f32x16& x, float sc) {
  u32x4 w;
  w[0] = cvtpk(x[8 * S + 0] * sc, x[8 * S + 1] * sc); w[1] = cvtpk(x[8 * S + 2] * sc, x[8 * S + 3] * sc);
  w[2] = cvtpk(x[8 * S + 4] * sc, x[8 * S + 5] * sc); w[3] = cvtpk(x[8 * S + 6] * sc, x[8 * S + 7] * sc);
  return w;
}
template <int S> DI u32x4 pack8(const f32x16& x) {
  u32x4 w;
  w[0] = cvtpk(x[8 * S + 0], x[8 * S + 1]); w[1] = cvtpk(x[8 * S + 2], x[8 * S + 3]);
  w[2] = cvtpk(x[8 * S + 4], x[8 * S + 5]); w[3] = cvtpk(x[8 * S + 6], x[8 * S + 7]);
  return w;
}
DI float max3f(float a, float b, float c) { float r; asm("v_max3_f32 %0, %1, %2, %3" : "=v"(r) : "v"(a), "v"(b), "v"(c)); return r; }
DI float max2f(float a, float b) { float r; asm("v_max_f32_e32 %0, %1, %2" : "=v"(r) : "v"(a), "v"(b)); return r; }
DI float fadd_s(float a, float b) { float r; asm("v_add_f32_e32 %0, %1, %2" : "=v"(r) : "v"(a), "v"(b)); return r; }
DI float xhalf_sum(float x) {
  auto r = __builtin_amdgcn_permlane32_swap(__float_as_uint(x), __float_as_uint(x), false, false);
  return __uint_as_float(r[0]) + __uint_as_float(r[1]);
}
DI float wave_sum(float x) {
#pragma unroll
  for (int o = 1; o < 64; o <<= 1) x += __shfl_xor(x, o);
  return x;
}

#define STAMP(i)

struct Ctx {
  unsigned ldsb, w, lane, b;
  unsigned ep0, ep1;
  rsrc_t rws;
  u64 pc0;
};

DI void fresh(Ctx& c) {
  asm volatile("v_mbcnt_lo_u32_b32 %0, -1, 0\n\tv_mbcnt_hi_u32_b32 %0, -1, %0" : "=v"(c.lane));
  asm volatile("" : "+s"(c.w), "+s"(c.b), "+s"(c.ldsb));
}
DI void code_touch(Ctx& c, unsigned& dummy) {
  unsigned bytes;
  asm volatile("s_mov_b32 %0, .Lfb_end-.Lfb_begin" : "=s"(bytes));
  const rsrc_t r = mk_rsrc((const void*)c.pc0, bytes);
  asm volatile("s_nop 4\n\tbuffer_load_dword %0, %1, %2, 0 offen" : "+v"(dummy) : "v"(c.w == 0 ? (c.lane * 32 + (c.b >> 3)) * 128 : OOB), "s"(r) : "memory");
}
DI void publish(Ctx& c, unsigned flag_off) {
  wait_vm<0>();
  bar();
  fput(u32x2{c.ep0, c.ep1}, c.rws, (c.w == 0 && c.lane == 0) ? flag_off : OOB);
}
DI void poll_flags(Ctx& c, unsigned flag_off, unsigned n, unsigned stride) {
  const unsigned voff = c.lane < n ? flag_off + c.lane * 8 * stride : OOB;
  const u64 t0 = __builtin_amdgcn_s_memrealtime();
  for (;;) {
    u32x2 f;
    fget(f, c.rws, voff);
    asm volatile("s_waitcnt vmcnt(0)" : "+v"(f)::"memory");
    if (__all((f[0] == c.ep0 && f[1] == c.ep1) || c.lane >= n)) break;
    if (__builtin_amdgcn_s_memrealtime() - t0 > 5000000ull) break;
    __builtin_amdgcn_s_sleep(2);
  }
}
DI void wait_flags(Ctx& c, unsigned flag_off, unsigned n, unsigned stride) {
  if (c.w == 0) poll_flags(c, flag_off, n, stride);
  bar();
}

#define DBG_DRAIN()
#define DBG_NX(x)
#define STAMPG(i)
#define OAUX 0
#define OAUX2 OAUX
#define SK1 0
#define MXA 2
#define MXB 2
#define MXC 2
#define MXD 2
DI void flag_store(Ctx& c, unsigned flag_off) {
  fput(u32x2{c.ep0, c.ep1}, c.rws, (c.w == 0 && c.lane == 0) ? flag_off : OOB);
}
DI void flag_load(Ctx& c, u32x2& f, unsigned flag_off, unsigned n) { fget(f, c.rws, c.lane < n ? flag_off + c.lane * 8 : OOB); }
DI void flag_check(Ctx& c, u32x2& f, unsigned flag_off, unsigned n) {
  PIN(f);
  if (__builtin_expect(!__all((f[0] == c.ep0 && f[1] == c.ep1) || c.lane >= n), 0)) poll_flags(c, flag_off, n, 1);
}

constexpr float QSCALE = 0.08838834764831845f * 1.4426950408889634f;
constexpr unsigned LE = 131072;
constexpr unsigned LX = 147456;
constexpr unsigned KW = LX + 12288;
#define KVT 0
#define TA0 105
#define ROT 0
constexpr unsigned SLOT = 49152;
constexpr int slot_of(int S) { return (S + 2) % 3; }
#define XQN 8
#define QVS 14
#define QVO 2
constexpr int XQ = XQN;
constexpr int LAQ = 6;
constexpr int QB1 = 40, QB2 = 88, QB3 = 116;
#define QVP 143
constexpr int Q_KPOLL = 70, Q_VPOLL = QVP, Q_VST = QVS;
#define PRO 0
#define XLS 1
constexpr int XLS1 = XLS > 0 ? XLS : 1, XL0 = 31 - (XQ - 1) * XLS1, XFL = XLS == 0 ? 19 : ((XL0 - 8) | 1) - ((XL0 & 1) ? 0 : 2);
#define KSP 2
#define TWV 5
#define TWQ 13
#define XT 1
#define LNE 1
#define PRO_N 12
#define PRO_B 5
constexpr int PRO_U[18] = {0, 0, 0, 0, 0, 0, 1, 1, 1, 1, 1, 1, 2, 2, 2, 2, 2, 2}, PRO_I[18] = {0, 4, 1, 2, 5, 3, 0, 4, 1, 2, 5, 3, 0, 1, 2, 3, 4, 5}, PRO_X = LNE ? LNE - 1 : 5;
#define WQL 1
#define WQ2 41
#define WS1 4
#define WS2 4
constexpr int wq_piece(int q) { return q >= 1 && q < 1 + 6 * WS1 && (q - 1) % WS1 == 0 ? (q - 1) / WS1 : q >= WQ2 && q < WQ2 + 6 * WS2 && (q - WQ2) % WS2 == 0 ? 6 + (q - WQ2) / WS2 : -1; }
#define LEAP 7
constexpr int q_prio(int n) {
  const int b0 = n < QB1 ? -QB1 / 2 : n < QB2 ? QB1 : n < QB3 ? QB2 : QB3, b1 = n < QB1 ? QB1 : n < QB2 ? QB2 : n < QB3 ? QB3 : 144;
  for (int i = 0; i < 4; ++i) if (n == b0 + (b1 - b0) * i / 4) return 3 - i;
  return -1;
}
constexpr int vm_wait(int pos, int tag) {
  int seq[512] = {}; int n = 0;
  auto push = [&](int t, int cnt) { for (int i = 0; i < cnt; ++i) seq[n++] = t; };
  for (int i = 0; i < PRO_N; ++i) { push(PRO_U[i], 1); if (i == PRO_X) push(-1, 3); }
  if (PRO_N == 18) push(-12, 1);
  bool done = false;
  for (int T = 0; T < 32 && !done; ++T) {
    if (pos == T) { done = true; break; }
    if (T == 0 && PRO_N < 18) { for (int i = PRO_N; i < 18; ++i) push(PRO_U[i], 1); push(-12, 1); }
    if (T == 3) push(-2, 3);
    if (T == TWV) push(-6, 1);
    if (T == TWQ) push(-6, 1);
    if (T == 7) push(-3, 1);
    if (T == XFL) push(-5, 1);
    if (KSP == 1 && T == 27) push(-8, 1);
    if (T >= 16 && (T - 16) % KSP == 0 && (T - 16) / KSP < 8) push(-7, 1);
    if (T >= 1 && (!WQL || T < 29)) push((T - 1) / 2 + 3, 3);
    if (XLS == 0 && T >= 29) push(200 + 2 * (T - 29), 2), push(201 + 2 * (T - 29), 2);
    if (XLS > 0 && T >= XL0 && (T - XL0) % XLS1 == 0) push(200 + (T - XL0) / XLS1, 2);
  }
  for (int q = 0; q < 144 && !done; ++q) {
    if (pos == 100 + q) break;
    if (q == QB3) push(-11, 1);
    if (KSP == 2 && q == QB1) push(-8, 1);
    if (!WQL && (q == 1 || q == 4 || q == 6)) push(18, 1);
    if (WQL && wq_piece(q) >= 0) push(17 + wq_piece(q) / 6, 1);
    if (tag == -9 && q == Q_KPOLL) push(-9, 1);
    if (q >= QVO && (q - QVO) % Q_VST == 0 && (q - QVO) / Q_VST < 8) push(-10, 1);
    if ((q & 7) == 7 && (q >> 3) + XQ < 18) push(200 + (q >> 3) + XQ, 2);
  }
  int last = -1;
  for (int i = 0; i < n; ++i) if (seq[i] == tag) last = i;
  return n - 1 - last;
}
constexpr int q_v(int n) { return n < 128 ? n >> 3 : 16 + (n & 1); }
constexpr int q_a(int n) { return n < 128 ? n & 7 : (n - 128) >> 1; }
constexpr int imin(int a, int b) { return a < b ? a : b; }
DI unsigned lds_add_rtn(unsigned addr) {
  unsigned r;
  asm volatile("s_mov_b64 exec, 1\n\tds_add_rtn_u32 %0, %1, %2\n\ts_mov_b64 exec, -1\n\ts_waitcnt lgkmcnt(0)" : "=&v"(r) : "v"(addr), "v"(1u) : "memory");
  return rfl(r);
}
DI unsigned lds_word(unsigned addr) { return __hip_atomic_load((LAS unsigned*)(size_t)addr, __ATOMIC_RELAXED, __HIP_MEMORY_SCOPE_WORKGROUP); }
DI void lds_set(unsigned addr, unsigned v) { __hip_atomic_store((LAS unsigned*)(size_t)addr, v, __ATOMIC_RELAXED, __HIP_MEMORY_SCOPE_WORKGROUP); }
DI float wave_sum64(float x) {
  x += __uint_as_float(__builtin_amdgcn_update_dpp(0, __float_as_uint(x), 0xB1, 0xf, 0xf, false));
  x += __uint_as_float(__builtin_amdgcn_update_dpp(0, __float_as_uint(x), 0x4E, 0xf, 0xf, false));
  x += __uint_as_float(__builtin_amdgcn_update_dpp(0, __float_as_uint(x), 0x124, 0xf, 0xf, false));
  x += __uint_as_float(__builtin_amdgcn_update_dpp(0, __float_as_uint(x), 0x128, 0xf, 0xf, false));
  { auto r = __builtin_amdgcn_permlane16_swap(__float_as_uint(x), __float_as_uint(x), false, false); x = __uint_as_float(r[0]) + __uint_as_float(r[1]); }
  return xhalf_sum(x);
}
DI void ln1_row(Ctx& c, const u32x4& xr0, const u32x4& xr1) {
  const unsigned lane = c.lane;
  float v[16];
#pragma unroll
  for (int e = 0; e < 4; ++e) { v[2 * e] = bflo(xr0[e]); v[2 * e + 1] = bfhi(xr0[e]); v[8 + 2 * e] = bflo(xr1[e]); v[9 + 2 * e] = bfhi(xr1[e]); }
  float s = 0.f;
#pragma unroll
  for (int e = 0; e < 16; ++e) s += v[e];
  const float mean = wave_sum64(s) * (1.f / DM);
  float qq = 0.f;
#pragma unroll
  for (int e = 0; e < 16; ++e) { v[e] -= mean; qq += v[e] * v[e]; }
  qq = lane < 36 ? qq : 0.f;
  const float rs = rsqrtf(wave_sum64(qq) * (1.f / DM) + 1e-5f) * QSCALE;
  u32x4 o0, o1;
#pragma unroll
  for (int e = 0; e < 4; ++e) { o0[e] = cvtpk(v[2 * e] * rs, v[2 * e + 1] * rs); o1[e] = cvtpk(v[8 + 2 * e] * rs, v[9 + 2 * e] * rs); }
  const unsigned ad = c.ldsb + LX + (lane < 36 ? (2 * lane * 8 + c.w) * 16 : 9216 + (lane - 36) * 16);
  *(LAS u32x4*)(size_t)ad = o0;
  *(LAS u32x4*)(size_t)(ad + 128) = o1;
}
DI void xn_store(Ctx& c) {
  const unsigned w = c.w, lane = c.lane, mb = c.b >> 2, r0 = (c.b & 3) * 8;
  const unsigned c0 = 9 * w + (lane >> 3), c1 = 9 * w + 8;
  const u32x4 a0 = *(LAS u32x4*)(size_t)(c.ldsb + LX + (9 * w * 8 + lane) * 16);
  const u32x4 a1 = *(LAS u32x4*)(size_t)(c.ldsb + LX + (c1 * 8 + (lane & 7)) * 16);
  gst16c(a0, c.rws, ((mb * 36 + (c0 >> 1)) * 64 + (c0 & 1) * 32 + r0 + (lane & 7)) * 16, WS_XN);
  gst16c(a1, c.rws, lane < 8 ? ((mb * 36 + (c1 >> 1)) * 64 + (c1 & 1) * 32 + r0 + lane) * 16 : OOB, WS_XN);
}

template <int WH> DI void w_touch(Ctx& c, unsigned& dummy, const void* wo, const void* w1, const void* w2, unsigned wv, bool two) {
  unsigned lane;
  asm volatile("v_mbcnt_lo_u32_b32 %0, -1, 0\n\tv_mbcnt_hi_u32_b32 %0, -1, %0" : "=v"(lane));
  const unsigned xcd = c.b & 7, jj = (c.b >> 3) - ((lane >> 5) << 2), l = two ? lane & 31 : lane, i = 3 * wv + l - 18;
  const bool mn = l < 18, ex = l < 21 && i < 18;
  auto ld = [&](const void* p, unsigned bytes, unsigned off) AI {
    asm volatile("s_nop 4\n\tbuffer_load_dword %0, %1, %2, 0 offen" : "+v"(dummy) : "v"(off), "s"(mk_rsrc(p, bytes)) : "memory");
  };
  const unsigned o1 = xcd * 589824 + (jj * 144 + wv * 18 + l) * 128;
  if constexpr (WH & 1) ld(wo, NQ * DM * 2, mn ? o1 : OOB);
  if constexpr (WH & 2) ld(w1, FF * DM * 2, mn ? (jj * 18 + l) * (FF * 2) + xcd * 1024 + wv * 128 : ex ? (jj * 18 + i) * (FF * 2) + 8192 + xcd * 128 : OOB);
  if constexpr (WH & 4) ld(w2, FF * DM * 2, mn ? o1 : ex ? (4096 + 64 * xcd) * (DM * 2) + (jj * 18 + i) * 128 : OOB);
}
struct QOut { u32x4 q[8]; u32x2 vf; u64 ta; bool kdone; };
DI u64 sel64(bool p, u64 x, u64 y) { return p ? x : y; }
DI u32x4 pack_pair(const f32x4& r0, const f32x4& r1) { return u32x4{cvtpk(r0[0], r0[1]), cvtpk(r0[2], r0[3]), cvtpk(r1[0], r1[1]), cvtpk(r1[2], r1[3])}; }
template <int IMM> DI void gld16c2(u32x4& d0, u32x4& d1, unsigned voff, rsrc_t r, unsigned soff) {
  asm volatile(VP "buffer_load_dwordx4 %0, %2, %3, %4 offen offset:%5 sc1\n\tbuffer_load_dwordx4 %1, %2, %3, %4 offen offset:%6 sc1"
               : "=&v"(d0), "=&v"(d1) : "v"(voff), "s"(r), "s"(soff), "i"(IMM), "i"(IMM + 256) : "memory");
}
DI unsigned x_voff(Ctx& c) { const unsigned g = c.lane >> 4; return (g >> 1) * 1024 + ((g & 1) * 32 + (c.lane & 15)) * 16; }

DI void phase_qkv(Ctx& c, QOut& qo, const u16* x, const u16* ckv, const u16* wq, const u16* wuk, const u16* wuv, const u16* wo, const u16* w1, const u16* w2) {
  const unsigned w = c.w, lane = c.lane, ldsb = c.ldsb, b = c.b;
  const unsigned xcd = b & 7, jj = b >> 3, head = xcd * 4 + (jj >> 3), cb = jj & 7;
  const rsrc_t rxx = mk_rsrc(x, SEQ * DM * 2), rx = mk_rsrc(ckv, SEQ * DC * 2);
  const rsrc_t rwk = mk_rsrc(wuk, DC * NQ * 2), rwv = mk_rsrc(wuv, DC * NQ * 2), rwq = mk_rsrc(wq, DM * NQ * 2);
  unsigned dummy = 0;
  code_touch(c, dummy);
  const unsigned voffX0 = (lane >> 3) * 1024 + (((lane & 7) ^ (lane >> 4)) << 4), voffX1 = voffX0 ^ 64;
  const unsigned voffW = (lane >> 3) * 8192 + (((((lane >> 1) & 3) ^ (((lane >> 4) & 1) | ((w & 1) << 1))) << 5) | ((lane & 1) << 4));
  const unsigned sX = (cb * 256 + 32 * w) * 1024, sW = (8 * (w & 3)) * 8192 + (head * 128 + (w >> 2) * 64) * 2;
  const unsigned rot = ROT == 0 ? 2 * (cb & 3) : ROT == 1 ? 2 * ((cb + head) & 3) : ROT == 2 ? 2 * (cb & 3) + 4 * (head & 1) : (cb + 2 * head) & 7;
  const unsigned dX = ldsb + 4 * w * 1024, dW = ldsb + w * 1024;
  auto issue = [&](auto U_, auto P_) AI {
    constexpr int U = decltype(U_)::v, P = decltype(P_)::v;
    constexpr unsigned so = slot_of(U) * SLOT;
    if constexpr (U < 16) {
      const unsigned tr = (U + rot) & 7;
      constexpr int p = P < 2 ? P : P - 1;
      if constexpr (P % 3 != 2) dma16(dX + so + p * 1024, (p & 1) ? voffX1 : voffX0, rx, sX + 128 * tr + 8 * p * 1024);
      else dma16(dW + so + 32768 + (P / 3) * 8192, voffW, U < 8 ? rwk : rwv, sW + (64 * tr + 32 * (P / 3)) * 8192);
    } else {
      dma16(dW + so + (U == 18 ? 5 - P : P) * 8192, voffW, rwq, sW + 32 * (6 * (U - 16) + P) * 8192);
    }
  };
  u32x2 ep; u32x4 xr0, xr1;
    const unsigned hr = head & 3, vA = voffX0 ^ ((hr & 1) << 6), vB = vA ^ 64, wj = (cb >> 2) * 8192;
    auto pro = [&](auto U_, auto I_) AI {
      constexpr int U = decltype(U_)::v, I = decltype(I_)::v;
      constexpr unsigned so = slot_of(U) * SLOT;
      const unsigned tr = (U + rot) & 7;
      if constexpr (I < 4) { const unsigned po = ((I + hr) & 3) * 1024; dma16(dX + so + po, (I & 1) ? vB : vA, rx, sX + 128 * tr + 8 * po); }
      else { const unsigned j = wj ^ ((I - 4) * 8192); dma16(dW + so + 32768 + j, voffW, rwk, sW + 64 * tr * 8192 + 32 * j); }
    };
  {
    static_for<PRO_N>([&](auto N_) AI {
      constexpr int N = decltype(N_)::v;
      pro(IC<PRO_U[N]>{}, IC<PRO_I[N]>{});
      if constexpr (N == PRO_B) __builtin_amdgcn_s_barrier();
      if constexpr (N == PRO_X) {
        const unsigned vo = lane < 36 ? lane * 32 : OOB;
        gld8c(ep, F_EPOCH, c.rws, WS_FLAGS);
        gld16<0>(xr0, vo, rxx, (b * 8 + w) * (DM * 2));
        gld16<16>(xr1, vo, rxx, (b * 8 + w) * (DM * 2));
      }
    });
  }
  unsigned td2 = 0;
  auto xtouch = [&]() AI {
    const unsigned l5 = (lane * 52) >> 8, row = cb * 256 + (head & 3) * 64 + w * 8 + l5, ch = (rot + 3 + lane - 5 * l5) & 7;
    asm volatile("s_nop 4\n\tbuffer_load_dword %0, %1, %2, 0 offen" : "+v"(td2) : "v"(XT && lane < 40 ? row * 1024 + ch * 128 : OOB), "s"(rx) : "memory");
  };
  if constexpr (PRO_N == 18) xtouch();
  const unsigned kh = w >> 2, rg = kh ? 7 - w : w;
  const unsigned qmb = (kh ? cb : 15 - cb) * 4 + rg;
  const unsigned wm = w >> 1, wn = w & 1, g = lane >> 4, l15 = lane & 15, q = l15 >> 2, pp = lane & 3;
  unsigned xb[3][2], wb[3][4], wqb[3][4];
  {
    const unsigned sg = ((q >> 1) & 1) | ((g & 1) << 1);
#pragma unroll
    for (int i = 0; i < 3; ++i) {
      xb[i][0] = ldsb + i * SLOT + (wm * 64 + l15) * 128 + ((g ^ (l15 >> 1)) << 4); xb[i][1] = xb[i][0] ^ 64; PIN(xb[i][0]); PIN(xb[i][1]);
#pragma unroll
      for (int j = 0; j < 4; ++j) { wb[i][j] = ldsb + i * SLOT + wn * 4096 + (8 * g + q) * 128 + ((j ^ sg) << 5) + pp * 8; PIN(wb[i][j]); }
    }
  }
  if constexpr (LNE != 0) { wait_vm<vm_wait(0, -1)>(); PIN(xr0); PIN(xr1); ln1_row(c, xr0, xr1); }
  wait_vm<vm_wait(0, 0)>();
  asm volatile("" ::"v"(dummy));
  bar();
  STAMP(2);
  f32x4 acc[4][4];
  bf16x8 xf[2][4]; bf16x4 wl[2][4][2];
  u32x4 pk[8], pv[8], xq[XQ][2]; u32x2 xnf;
  auto rd = [&](auto T_, auto R_) AI {
    constexpr int T = decltype(T_)::v, R = decltype(R_)::v, B = T & 1, sl = slot_of(T >> 1);
    constexpr int K = R % 3, I = R / 3;
    if constexpr (K == 0) xf[B][I] = lds_b128(xb[sl][B] + I * 2048);
    else wl[B][I][K - 1] = lds_tr(wb[sl][I] + 32768 + B * 8192 + (K - 1) * 512);
  };
  bf16x4 wq8[8][2];
  auto rdq = [&](auto N_, auto H_) AI {
    constexpr int N = decltype(N_)::v, H = decltype(H_)::v, v = q_v(N), a = q_a(N), sl = slot_of(16 + v / 6);
    wq8[N & 7][H] = lds_tr(wqb[sl][a & 3] + (v >= 12 ? 17 - v : v % 6) * 8192 + (a >> 2) * 4096 + H * 512);
  };
  const unsigned xvo = x_voff(c), xso = WS_XN + qmb * 36 * 1024;
  auto ldx = [&](auto V_) AI { constexpr int v = decltype(V_)::v; gld16c2<(v & 1) * 2048>(xq[v % XQ][0], xq[v % XQ][1], xvo, c.rws, xso + (v >> 1) * 4096); };
  const unsigned kst = WS_K + ((head * 128 + cb * 16 + wm * 4) * 4 + 2 * wn) * 1024, vst = WS_V + ((head * 64 + cb * 8 + wm * 2) * 8 + 4 * wn) * 1024;
  const unsigned l16 = lane * 16;
  static_for<12>([&](auto R) AI { rd(IC<0>{}, R); });
  SB();
  static_for<32>([&](auto T_) AI {
    constexpr int T = decltype(T_)::v, B = T & 1;
    if constexpr (B) { wait_vm<vm_wait(T, (T >> 1) + 1)>(); bar(); }
    if constexpr (T == 1) {
      PIN(ep);
      if constexpr (!LNE) { PIN(xr0); PIN(xr1); }
      u64 z = (((u64)rfl(ep[1]) << 32) | rfl(ep[0])) + 0x9E3779B97F4A7C15ull;
      z = (z ^ (z >> 30)) * 0xBF58476D1CE4E5B9ull; z = (z ^ (z >> 27)) * 0x94D049BB133111EBull; z ^= z >> 31;
      c.ep0 = (unsigned)z; c.ep1 = (unsigned)(z >> 32);
      if (w == 0) lds_set(ldsb + KW + (lane & 3) * 4, 0);
    }
    if constexpr (!LNE && (T == 1 || T == 2)) { if (kh == T - 1) ln1_row(c, xr0, xr1); }
    if constexpr (T == 3) { fput(u32x2{c.ep0, c.ep1}, c.rws, (w == 0 && lane < 5) ? WS_CNT + (b * 5 + lane) * 128 : OOB); xn_store(c); }
    if constexpr (T == TWV) asm volatile("s_nop 4\n\tbuffer_load_dword %0, %1, %2, 0 offen" : "+v"(td2) : "v"(lane < 16 ? (cb * 64 + w * 8 + (lane >> 1)) * 8192 + head * 256 + (lane & 1) * 128 : OOB), "s"(rwv) : "memory");
    if constexpr (T == TWQ) asm volatile("s_nop 4\n\tbuffer_load_dword %0, %1, %2, 0 offen" : "+v"(td2) : "v"(lane < 18 ? (cb * 72 + w * 9 + (lane >> 1)) * 8192 + head * 256 + (lane & 1) * 128 : OOB), "s"(rwq) : "memory");
    if constexpr (T == 31) asm volatile("" ::"v"(td2));
    if constexpr (T == 7) flag_store(c, F_XN + b * 8);
    if constexpr (T == XFL) flag_load(c, xnf, F_XN + qmb * 32, 4);
    if constexpr (T == XFL + 8) flag_check(c, xnf, F_XN + qmb * 32, 4);
    if constexpr (T == 27) { if constexpr (KSP == 1) flag_store(c, F_K + (head * 8 + cb) * 8); }
    if constexpr (T == 31) {
#pragma unroll
      for (int i = 0; i < 3; ++i)
#pragma unroll
        for (int j = 0; j < 4; ++j) { wqb[i][j] = wb[i][j] - wn * 4096; PIN(wqb[i][j]); }
    }
    if constexpr (T == 8 || T == 16 || T == 24) STAMP(100 + T / 2);
    SB();
    static_for<16>([&](auto M_) AI {
      constexpr int M = decltype(M_)::v, i = M >> 2, j = M & 3;
      const bf16x8 wf = cat(wl[B][j][0], wl[B][j][1]);
      if constexpr (T < 16) mfma16<(T & 15) == 0>(acc[i][j], wf, xf[B][i]); else mfma16<(T & 15) == 0>(acc[i][j], xf[B][i], wf);
      SB();
      if constexpr ((LEAP & 1) && (M & 7) == 0) __builtin_amdgcn_s_setprio(3 - ((B ? 0 : 2) + (M >> 3)));
      if constexpr (M < 12) {
        if constexpr (T < 31) rd(IC<T + 1>{}, IC<M>{});
        else rdq(IC<(M >> 1)>{}, IC<(M & 1)>{});
      }
      if constexpr (M == 1 && T >= 16 && (T - 16) % KSP == 0 && (T - 16) / KSP < 8) {
        constexpr int e = (T - 16) / KSP;
        gst16c(pk[e], c.rws, l16, kst + (e >> 1) * 4096 + (e & 1) * 1024);
      }
      if constexpr (T >= 1 && (!WQL || T < 29) && (M == 4 || M == 8 || M == 12)) issue(IC<(T - 1) / 2 + 3>{}, IC<(B ? 0 : 3) + (M >> 2) - 1>{});
      if constexpr (T == 0 && (M & 1) && PRO_N + (M >> 1) < 18) pro(IC<PRO_U[PRO_N + (M >> 1)]>{}, IC<PRO_I[PRO_N + (M >> 1)]>{});
      if constexpr (T == 0 && M == 15 && PRO_N < 18) xtouch();
      if constexpr (XLS == 0 && T >= 29 && (M == 13 || M == 15)) ldx(IC<2 * (T - 29) + (M == 15)>{});
      if constexpr (XLS > 0 && T >= XL0 && (T - XL0) % XLS1 == 0 && M == 14) ldx(IC<(T - XL0) / XLS1>{});
      SB();
    });
    if constexpr (T == 15) {
#pragma unroll
      for (int e = 0; e < 8; ++e) { pk[e] = pack_pair(acc[e >> 1][2 * (e & 1)], acc[e >> 1][2 * (e & 1) + 1]); PIN(pk[e]); }
    }
    if constexpr (T == 31) {
#pragma unroll
      for (int e = 0; e < 8; ++e) { pv[e] = pack_pair(acc[2 * (e >> 2)][e & 3], acc[2 * (e >> 2) + 1][e & 3]); PIN(pv[e]); }
    }
  });
  STAMP(3);
  f32x4 qa[2][8];
  u32x2 kf; bool kdone = false;
  unsigned tg, tr, tc, to;
  const unsigned kso = WS_K + head * (SEQ * HD * 2) + w * 4096;
  auto qout = [&](auto A_) AI {
    constexpr int A = decltype(A_)::v;
    qo.q[A] = pack_pair(qa[A & 1][A & 6], qa[A & 1][A | 1]); PIN(qo.q[A]);
    if (kh == 0) *(LAS u32x4*)(size_t)(ldsb + LE + w * 8192 + l16 + A * 1024) = qo.q[A];
  };
  static_for<144>([&](auto N_) AI {
    constexpr int N = decltype(N_)::v, v = q_v(N), a = q_a(N);
    if constexpr (a == 0) { wait_vm<vm_wait(100 + N, 200 + v)>(); PIN(xq[v % XQ][0]); PIN(xq[v % XQ][1]); }
    if constexpr (N == QB1) { wait_vm<vm_wait(100 + N, 17)>(); bar(); if constexpr (KSP == 2) flag_store(c, F_K + (head * 8 + cb) * 8); SB(); }
    if constexpr (N == QB2) {
      wait_vm<vm_wait(100 + N, 18)>();
      if (w == 0) wait_vm<vm_wait(100 + N, -9)>();
      PIN(kf);
      if (w == 0 && __all((kf[0] == c.ep0 && kf[1] == c.ep1) || lane >= 8)) lds_set(ldsb + KW + 8, 1);
      bar();
      kdone = rfl(lds_word(ldsb + KW + 8)) != 0;
      SB();
    }
    if constexpr (N == QB3) { wait_vm<vm_wait(100 + N, -10)>(); bar(); flag_store(c, F_V + (head * 8 + cb) * 8); SB(); }
    const bf16x8 wf = cat(wq8[N & 7][0], wq8[N & 7][1]);
    mfma16<v == 0>(qa[0][a], wf, asbf(xq[v % XQ][0]));
    SB();
    if constexpr ((LEAP & 2) && q_prio(N) >= 0) __builtin_amdgcn_s_setprio(q_prio(N));
    if constexpr (N + LAQ < 144) rdq(IC<N + LAQ>{}, IC<0>{});
    SB();
    mfma16<v == 0>(qa[1][a], wf, asbf(xq[v % XQ][1]));
    SB();
    if constexpr (N + LAQ < 144) rdq(IC<N + LAQ>{}, IC<1>{});
    if constexpr (!WQL && (N == 1 || N == 4 || N == 6)) issue(IC<18>{}, IC<(N == 1 ? 3 : N == 4 ? 4 : 5)>{});
    if constexpr (WQL && wq_piece(N) >= 0) issue(IC<17 + wq_piece(N) / 6>{}, IC<wq_piece(N) % 6>{});
    if constexpr (N == Q_KPOLL || N == Q_VPOLL) {
      kf = u32x2{0u, 0u};
      asm volatile("s_mov_b32 exec_lo, %3\n\ts_mov_b32 exec_hi, 0\n\ts_nop 4\n\tbuffer_atomic_add_x2 %0, %1, %2, 0 offen sc0 sc1\n\ts_mov_b64 exec, -1"
                   : "+v"(kf) : "v"((N == Q_KPOLL ? F_K : F_V) + (head * 8 + lane) * 8), "s"(c.rws), "s"(w == (N == Q_KPOLL ? 0 : 7) ? 0xffu : 0u) : "memory");
    }
    if constexpr (N >= QVO && (N - QVO) % Q_VST == 0 && (N - QVO) / Q_VST < 8) {
      constexpr int e = (N - QVO) / Q_VST;
      gst16c(pv[e], c.rws, l16, vst + (e >> 2) * 8192 + (e & 3) * 1024);
    }
    if constexpr (a == 7 && v + XQ < 18) ldx(IC<v + XQ>{});
    if constexpr (N > QB3 && N <= QB3 + 16 && (N - QB3) % 2 == 0) {
      constexpr int p = (N - QB3) / 2 - 1;
      if (kdone && kh) dma16c(ldsb + w * 4096 + ((p >> 2) * 32768 + (p & 3) * 1024 - 16384), l16, c.rws, kso + ((p >> 2) * 16384 + (p & 3) * 1024 - 16384));
    }
    if constexpr (N == TA0) { tg = (l16 >> 1) + w; tc = tg < 162 ? 0 : tg < 324 ? 162 : 324; PIN(tg); PIN(tc); }
    if constexpr (N == TA0 + 2) { const unsigned li = tg - tc; tr = __umul24(li, 57) >> 9; tc = li - __umul24(tr, 9); PIN(tr); PIN(tc); }
    if constexpr (N == TA0 + 4) { unsigned r0 = 512 * xcd + jj * 16, r1 = 4096 + 64 * xcd + jj * 2 - 16; asm volatile("" : "+s"(r0), "+s"(r1)); to = tr + (tr < 16 ? r0 : r1); PIN(to); }
    if constexpr (N == TA0 + 6) { to = __umul24(to, DM * 2) + tc * 128; tr = __umul24(jj * 18 + tr, FF * 2); PIN(to); PIN(tr); }
    if constexpr (N == TA0 + 8) { tr += tc < 8 ? xcd * 1024 + tc * 128 : 8192 + xcd * 128; PIN(tr); }
    if constexpr (N == TA0 + 10) { to = tg - 162 < 162 ? tr : to + 0; tc = ((tg - 468) * 32 + jj) * 128; PIN(to); PIN(tc); }
    if constexpr (N == TA0 + 12) {
      unsigned bytes;
      asm volatile("s_mov_b32 %0, .Lfb_end-.Lfb_begin" : "=s"(bytes));
      to = tg >= 468 ? (tc < bytes ? tc : 0) : to + 0; PIN(to);
    }
    if constexpr (N == TA0 + 14) { qo.ta = sel64(tg < 324, (u64)w1, (u64)wo); PIN(qo.ta); }
    if constexpr (N == TA0 + 16) { qo.ta = sel64(tg < 162, (u64)w2, qo.ta); PIN(qo.ta); }
    if constexpr (N == TA0 + 18) { qo.ta = sel64(tg >= 468, c.pc0, qo.ta) + to; PIN(qo.ta); }
    if constexpr (N >= 133 && ((N - 133) & 3) < 2) qout(IC<((N - 133) >> 2) * 2 + ((N - 133) & 1)>{});
    SB();
  });
  mfma_pad();
  qout(IC<6>{}); qout(IC<7>{});
  qo.kdone = kdone;
  __builtin_amdgcn_s_setprio(0);
  STAMP(6);
  qo.vf = kf;
}

constexpr float THR = 32.f;

struct AttnState {
  bf16x8 qf[8];
  f32x4 o[8][2];
  f32x4 S[2][4][2];
  f32x4 negm[2], ls[2];
  u32x4 pk[2][2];
  u32x4 ones;
  bf16x8 fr[8];
  float dl[2], mx[2];
  unsigned pend, cold;
  unsigned kb[2], vb[2];
  unsigned voff, dk, d64, dsc;
  unsigned n1t, fs, p0, p1; u32x2 fl;
  unsigned u, o0, o1, o2;
  unsigned wide, drop, diag, first, slow;
  u64 ta, tm; unsigned dummy;
};

constexpr int RD0[16] = {4, 5, 6, 7, 9, 10, 12, 13, 15, -1, -1, -1, -1, -1, -1, -1};
constexpr int RD1[16] = {-1, -1, -1, 8, -1, 11, -1, 14, -1, -1, -1, -1, -1, -1, -1, -1};

#define SPIN(x) asm volatile("" : "+s"(x))
constexpr int R_O = 64, R_S = 128, R_L = 192, R_N = 200;
#define RLIST(F) F(64, 67) F(68, 71) F(72, 75) F(76, 79) F(80, 83) F(84, 87) F(88, 91) F(92, 95) F(96, 99) F(100, 103) F(104, 107) F(108, 111) F(112, 115) F(116, 119) F(120, 123) F(124, 127) F(128, 131) F(132, 135) F(136, 139) F(140, 143) F(144, 147) F(148, 151) F(152, 155) F(156, 159) F(160, 163) F(164, 167) F(168, 171) F(172, 175) F(176, 179) F(180, 183) F(184, 187) F(188, 191) F(192, 195) F(196, 199) F(200, 203) F(204, 207)
template <int R, bool NEW = false> DI void mfma_r(f32x4& d, const bf16x8& x, const bf16x8& y, const f32x4& cin = f32x4{}) {
#define F(lo, hi)                                                                                                               \
  if constexpr (R == lo && NEW) asm volatile("v_mfma_f32_16x16x32_bf16 %0, %1, %2, %3" : "=&{v[" #lo ":" #hi "]}"(d) : "v"(x), "v"(y), "v"(cin)); \
  if constexpr (R == lo && !NEW) asm volatile("v_mfma_f32_16x16x32_bf16 %0, %1, %2, %0" : "+{v[" #lo ":" #hi "]}"(d) : "v"(x), "v"(y));
  RLIST(F)
#undef F
}
template <int R> DI void pin_r(f32x4& d) {
#define F(lo, hi) if constexpr (R == lo) asm volatile("" : "+{v[" #lo ":" #hi "]}"(d));
  RLIST(F)
#undef F
}
template <int X> DI void slow_unit(AttnState& a, f32x4 (&S)[4][2], int dg, unsigned bit) {
  static_assert(THR == 32.f, "literals below");
  float t0, t1; u64 m0, m1, m2; unsigned st;
  if constexpr (X == 0) {
    asm volatile(
                 "\n\tv_mov_b32_e32 %8, %23\n\tv_mov_b32_e32 %9, %24\n\ts_and_b32 %17, %20, %19\n\ts_cbranch_scc0 .Lck%=\n\tv_cmp_le_i32_e64 %12, 0, %18\n\tv_cmp_le_i32_e64 %13, 1, %18"
                 "\n\tv_cmp_le_i32_e64 %14, 2, %18\n\tv_cmp_le_i32_e32 vcc, 3, %18\n\tv_mov_b32_e32 v152, 0xff800000\n\tv_mov_b32_e32 v153, 0xff800000\n\tv_mov_b32_e32 v154, 0xff800000"
                 "\n\tv_mov_b32_e32 v155, 0xff800000\n\ts_and_b32 %17, %22, %19\n\ts_cselect_b32 %17, 64, 0\n\ts_add_i32 %17, %17, %25\n\ts_cmp_eq_u32 %17, 32\n\ts_cbranch_scc0 .Lm0%="
                 "\n\tv_cndmask_b32_e64 v144, v152, v144, %12\n\tv_cndmask_b32_e64 v145, v152, v145, %13\n\tv_cndmask_b32_e64 v146, v152, v146, %14"
                 "\n\tv_cndmask_b32_e32 v147, v152, v147, vcc\n\tv_cndmask_b32_e64 v156, v152, v156, %12\n\tv_cndmask_b32_e64 v157, v152, v157, %13"
                 "\n\tv_cndmask_b32_e64 v158, v152, v158, %14\n\tv_cndmask_b32_e32 v159, v152, v159, vcc\n\ts_branch .Lmd%=\n.Lm0%=:\n\tv_cndmask_b32_e64 v128, v152, v128, %12"
                 "\n\tv_cndmask_b32_e64 v129, v152, v129, %13\n\tv_cndmask_b32_e64 v130, v152, v130, %14\n\tv_cndmask_b32_e32 v131, v152, v131, vcc"
                 "\n\tv_cndmask_b32_e64 v140, v152, v140, %12\n\tv_cndmask_b32_e64 v141, v152, v141, %13\n\tv_cndmask_b32_e64 v142, v152, v142, %14"
                 "\n\tv_cndmask_b32_e32 v143, v152, v143, vcc\n\tv_mov_b64_e32 v[136:137], v[152:153]\n\tv_mov_b64_e32 v[138:139], v[152:153]\n\tv_mov_b64_e32 v[144:145], v[152:153]"
                 "\n\tv_mov_b64_e32 v[146:147], v[152:153]\n\tv_mov_b64_e32 v[148:149], v[152:153]\n\tv_mov_b64_e32 v[150:151], v[152:153]\n\tv_mov_b64_e32 v[156:157], v[152:153]"
                 "\n\tv_mov_b64_e32 v[158:159], v[152:153]\n\ts_cmp_eq_u32 %17, 0\n\ts_cbranch_scc1 .Lmd%=\n\tv_mov_b64_e32 v[128:129], v[152:153]\n\tv_mov_b64_e32 v[130:131], v[152:153]"
                 "\n\tv_mov_b64_e32 v[132:133], v[152:153]\n\tv_mov_b64_e32 v[134:135], v[152:153]\n\tv_mov_b64_e32 v[140:141], v[152:153]\n\tv_mov_b64_e32 v[142:143], v[152:153]\n.Lmd%=:"
                 "\n\tv_max_f32_e32 %15, %8, %9\n\tv_cmp_nge_f32_e32 vcc, 0x42000000, %15\n\ts_and_b32 %17, %21, %19\n\ts_cbranch_scc1 .Lrc%=\n\ts_cbranch_vccz .Lend%=\n.Lrc%=:"
                 "\n\tv_max_f32_e32 %8, v128, v129\n\tv_max_f32_e32 %9, v132, v133\n\tv_max3_f32 %8, %8, v130, v131\n\tv_max3_f32 %9, %9, v134, v135\n\tv_max3_f32 %8, %8, v136, v137"
                 "\n\tv_max3_f32 %9, %9, v140, v141\n\tv_max3_f32 %8, %8, v138, v139\n\tv_max3_f32 %9, %9, v142, v143\n\tv_max3_f32 %8, %8, v144, v145\n\tv_max3_f32 %9, %9, v148, v149"
                 "\n\tv_max3_f32 %8, %8, v146, v147\n\tv_max3_f32 %9, %9, v150, v151\n\tv_max3_f32 %8, %8, v152, v153\n\tv_max3_f32 %9, %9, v156, v157\n\tv_max3_f32 %8, %8, v154, v155"
                 "\n\tv_max3_f32 %9, %9, v158, v159\n.Lck%=:\n\ts_and_b32 %17, %21, %19\n\ts_mov_b32 %17, 0xff800000\n\ts_cmov_b32 %17, 0xc2000000\n\tv_max_f32_e32 %15, %8, %9"
                 "\n\tv_min_f32_e32 %16, %8, %9\n\tv_cmp_nge_f32_e32 vcc, 0x42000000, %15\n\tv_cmp_nle_f32_e64 %12, %17, %16\n\ts_cselect_b32 %17, 0xf149f2ca, 0\n\ts_or_b64 vcc, vcc, %12"
                 "\n\ts_cbranch_vccz .Lend%=\n\tv_mov_b32_e32 %15, %8\n\tv_mov_b32_e32 %16, %9\n\ts_nop 1\n\tv_permlane16_swap_b32_e32 %8, %15\n\tv_permlane16_swap_b32_e32 %9, %16"
                 "\n\tv_max_f32_e32 %8, %8, %15\n\tv_max_f32_e32 %9, %9, %16\n\tv_mov_b32_e32 %15, %8\n\tv_mov_b32_e32 %16, %9\n\ts_nop 1\n\tv_permlane32_swap_b32_e32 %8, %15"
                 "\n\tv_permlane32_swap_b32_e32 %9, %16\n\tv_max3_f32 %8, %8, %15, %17\n\tv_max3_f32 %9, %9, %16, %17\n\tv_sub_f32_e32 v128, v128, %8\n\tv_sub_f32_e32 v129, v129, %8"
                 "\n\tv_sub_f32_e32 v130, v130, %8\n\tv_sub_f32_e32 v131, v131, %8\n\tv_sub_f32_e32 v132, v132, %9\n\tv_sub_f32_e32 v133, v133, %9\n\tv_sub_f32_e32 v134, v134, %9"
                 "\n\tv_sub_f32_e32 v135, v135, %9\n\tv_sub_f32_e32 v136, v136, %8\n\tv_sub_f32_e32 v137, v137, %8\n\tv_sub_f32_e32 v138, v138, %8\n\tv_sub_f32_e32 v139, v139, %8"
                 "\n\tv_sub_f32_e32 v140, v140, %9\n\tv_sub_f32_e32 v141, v141, %9\n\tv_sub_f32_e32 v142, v142, %9\n\tv_sub_f32_e32 v143, v143, %9\n\tv_sub_f32_e32 v144, v144, %8"
                 "\n\tv_sub_f32_e32 v145, v145, %8\n\tv_sub_f32_e32 v146, v146, %8\n\tv_sub_f32_e32 v147, v147, %8\n\tv_sub_f32_e32 v148, v148, %9\n\tv_sub_f32_e32 v149, v149, %9"
                 "\n\tv_sub_f32_e32 v150, v150, %9\n\tv_sub_f32_e32 v151, v151, %9\n\tv_sub_f32_e32 v152, v152, %8\n\tv_sub_f32_e32 v153, v153, %8\n\tv_sub_f32_e32 v154, v154, %8"
                 "\n\tv_sub_f32_e32 v155, v155, %8\n\tv_sub_f32_e32 v156, v156, %9\n\tv_sub_f32_e32 v157, v157, %9\n\tv_sub_f32_e32 v158, v158, %9\n\tv_sub_f32_e32 v159, v159, %9"
                 "\n\ts_or_b32 %10, %10, %19\n\ts_mov_b32 %11, 1\n.Lend%=:"
                 : "+{v[128:131]}"(S[0][0]), "+{v[132:135]}"(S[0][1]), "+{v[136:139]}"(S[1][0]), "+{v[140:143]}"(S[1][1]), "+{v[144:147]}"(S[2][0]), "+{v[148:151]}"(S[2][1]), "+{v[152:155]}"(S[3][0]), "+{v[156:159]}"(S[3][1]),
                   "+&v"(a.dl[0]), "+&v"(a.dl[1]), "+s"(a.cold), "+s"(a.pend), "=&s"(m0), "=&s"(m1), "=&s"(m2), "=&v"(t0), "=&v"(t1), "=&s"(st)
                 : "v"(dg), "s"(bit), "s"(a.diag), "s"(a.first), "s"(a.d64), "v"(a.mx[0]), "v"(a.mx[1]), "s"(a.dsc) : "vcc", "scc");
  } else {
    asm volatile(
                 "\n\tv_mov_b32_e32 %8, %23\n\tv_mov_b32_e32 %9, %24\n\ts_and_b32 %17, %20, %19\n\ts_cbranch_scc0 .Lck%=\n\tv_cmp_le_i32_e64 %12, 0, %18\n\tv_cmp_le_i32_e64 %13, 1, %18"
                 "\n\tv_cmp_le_i32_e64 %14, 2, %18\n\tv_cmp_le_i32_e32 vcc, 3, %18\n\tv_mov_b32_e32 v184, 0xff800000\n\tv_mov_b32_e32 v185, 0xff800000\n\tv_mov_b32_e32 v186, 0xff800000"
                 "\n\tv_mov_b32_e32 v187, 0xff800000\n\ts_and_b32 %17, %22, %19\n\ts_cselect_b32 %17, 64, 0\n\ts_add_i32 %17, %17, %25\n\ts_cmp_eq_u32 %17, 32\n\ts_cbranch_scc0 .Lm0%="
                 "\n\tv_cndmask_b32_e64 v176, v184, v176, %12\n\tv_cndmask_b32_e64 v177, v184, v177, %13\n\tv_cndmask_b32_e64 v178, v184, v178, %14"
                 "\n\tv_cndmask_b32_e32 v179, v184, v179, vcc\n\tv_cndmask_b32_e64 v188, v184, v188, %12\n\tv_cndmask_b32_e64 v189, v184, v189, %13"
                 "\n\tv_cndmask_b32_e64 v190, v184, v190, %14\n\tv_cndmask_b32_e32 v191, v184, v191, vcc\n\ts_branch .Lmd%=\n.Lm0%=:\n\tv_cndmask_b32_e64 v160, v184, v160, %12"
                 "\n\tv_cndmask_b32_e64 v161, v184, v161, %13\n\tv_cndmask_b32_e64 v162, v184, v162, %14\n\tv_cndmask_b32_e32 v163, v184, v163, vcc"
                 "\n\tv_cndmask_b32_e64 v172, v184, v172, %12\n\tv_cndmask_b32_e64 v173, v184, v173, %13\n\tv_cndmask_b32_e64 v174, v184, v174, %14"
                 "\n\tv_cndmask_b32_e32 v175, v184, v175, vcc\n\tv_mov_b64_e32 v[168:169], v[184:185]\n\tv_mov_b64_e32 v[170:171], v[184:185]\n\tv_mov_b64_e32 v[176:177], v[184:185]"
                 "\n\tv_mov_b64_e32 v[178:179], v[184:185]\n\tv_mov_b64_e32 v[180:181], v[184:185]\n\tv_mov_b64_e32 v[182:183], v[184:185]\n\tv_mov_b64_e32 v[188:189], v[184:185]"
                 "\n\tv_mov_b64_e32 v[190:191], v[184:185]\n\ts_cmp_eq_u32 %17, 0\n\ts_cbranch_scc1 .Lmd%=\n\tv_mov_b64_e32 v[160:161], v[184:185]\n\tv_mov_b64_e32 v[162:163], v[184:185]"
                 "\n\tv_mov_b64_e32 v[164:165], v[184:185]\n\tv_mov_b64_e32 v[166:167], v[184:185]\n\tv_mov_b64_e32 v[172:173], v[184:185]\n\tv_mov_b64_e32 v[174:175], v[184:185]\n.Lmd%=:"
                 "\n\tv_max_f32_e32 %15, %8, %9\n\tv_cmp_nge_f32_e32 vcc, 0x42000000, %15\n\ts_and_b32 %17, %21, %19\n\ts_cbranch_scc1 .Lrc%=\n\ts_cbranch_vccz .Lend%=\n.Lrc%=:"
                 "\n\tv_max_f32_e32 %8, v160, v161\n\tv_max_f32_e32 %9, v164, v165\n\tv_max3_f32 %8, %8, v162, v163\n\tv_max3_f32 %9, %9, v166, v167\n\tv_max3_f32 %8, %8, v168, v169"
                 "\n\tv_max3_f32 %9, %9, v172, v173\n\tv_max3_f32 %8, %8, v170, v171\n\tv_max3_f32 %9, %9, v174, v175\n\tv_max3_f32 %8, %8, v176, v177\n\tv_max3_f32 %9, %9, v180, v181"
                 "\n\tv_max3_f32 %8, %8, v178, v179\n\tv_max3_f32 %9, %9, v182, v183\n\tv_max3_f32 %8, %8, v184, v185\n\tv_max3_f32 %9, %9, v188, v189\n\tv_max3_f32 %8, %8, v186, v187"
                 "\n\tv_max3_f32 %9, %9, v190, v191\n.Lck%=:\n\ts_and_b32 %17, %21, %19\n\ts_mov_b32 %17, 0xff800000\n\ts_cmov_b32 %17, 0xc2000000\n\tv_max_f32_e32 %15, %8, %9"
                 "\n\tv_min_f32_e32 %16, %8, %9\n\tv_cmp_nge_f32_e32 vcc, 0x42000000, %15\n\tv_cmp_nle_f32_e64 %12, %17, %16\n\ts_cselect_b32 %17, 0xf149f2ca, 0\n\ts_or_b64 vcc, vcc, %12"
                 "\n\ts_cbranch_vccz .Lend%=\n\tv_mov_b32_e32 %15, %8\n\tv_mov_b32_e32 %16, %9\n\ts_nop 1\n\tv_permlane16_swap_b32_e32 %8, %15\n\tv_permlane16_swap_b32_e32 %9, %16"
                 "\n\tv_max_f32_e32 %8, %8, %15\n\tv_max_f32_e32 %9, %9, %16\n\tv_mov_b32_e32 %15, %8\n\tv_mov_b32_e32 %16, %9\n\ts_nop 1\n\tv_permlane32_swap_b32_e32 %8, %15"
                 "\n\tv_permlane32_swap_b32_e32 %9, %16\n\tv_max3_f32 %8, %8, %15, %17\n\tv_max3_f32 %9, %9, %16, %17\n\tv_sub_f32_e32 v160, v160, %8\n\tv_sub_f32_e32 v161, v161, %8"
                 "\n\tv_sub_f32_e32 v162, v162, %8\n\tv_sub_f32_e32 v163, v163, %8\n\tv_sub_f32_e32 v164, v164, %9\n\tv_sub_f32_e32 v165, v165, %9\n\tv_sub_f32_e32 v166, v166, %9"
                 "\n\tv_sub_f32_e32 v167, v167, %9\n\tv_sub_f32_e32 v168, v168, %8\n\tv_sub_f32_e32 v169, v169, %8\n\tv_sub_f32_e32 v170, v170, %8\n\tv_sub_f32_e32 v171, v171, %8"
                 "\n\tv_sub_f32_e32 v172, v172, %9\n\tv_sub_f32_e32 v173, v173, %9\n\tv_sub_f32_e32 v174, v174, %9\n\tv_sub_f32_e32 v175, v175, %9\n\tv_sub_f32_e32 v176, v176, %8"
                 "\n\tv_sub_f32_e32 v177, v177, %8\n\tv_sub_f32_e32 v178, v178, %8\n\tv_sub_f32_e32 v179, v179, %8\n\tv_sub_f32_e32 v180, v180, %9\n\tv_sub_f32_e32 v181, v181, %9"
                 "\n\tv_sub_f32_e32 v182, v182, %9\n\tv_sub_f32_e32 v183, v183, %9\n\tv_sub_f32_e32 v184, v184, %8\n\tv_sub_f32_e32 v185, v185, %8\n\tv_sub_f32_e32 v186, v186, %8"
                 "\n\tv_sub_f32_e32 v187, v187, %8\n\tv_sub_f32_e32 v188, v188, %9\n\tv_sub_f32_e32 v189, v189, %9\n\tv_sub_f32_e32 v190, v190, %9\n\tv_sub_f32_e32 v191, v191, %9"
                 "\n\ts_or_b32 %10, %10, %19\n\ts_mov_b32 %11, 1\n.Lend%=:"
                 : "+{v[160:163]}"(S[0][0]), "+{v[164:167]}"(S[0][1]), "+{v[168:171]}"(S[1][0]), "+{v[172:175]}"(S[1][1]), "+{v[176:179]}"(S[2][0]), "+{v[180:183]}"(S[2][1]), "+{v[184:187]}"(S[3][0]), "+{v[188:191]}"(S[3][1]),
                   "+&v"(a.dl[0]), "+&v"(a.dl[1]), "+s"(a.cold), "+s"(a.pend), "=&s"(m0), "=&s"(m1), "=&s"(m2), "=&v"(t0), "=&v"(t1), "=&s"(st)
                 : "v"(dg), "s"(bit), "s"(a.diag), "s"(a.first), "s"(a.d64), "v"(a.mx[0]), "v"(a.mx[1]), "s"(a.dsc) : "vcc", "scc");
  }
  SPIN(a.cold); SPIN(a.pend);
}
template <int X> DI void post_iter(Ctx& c, AttnState& a, unsigned s) {
  float t0, t1; unsigned st; u64 tp;
  const u64 ep = ((u64)c.ep1 << 32) | c.ep0;
  if constexpr (X == 0) {
    asm volatile(
                 "\n\ts_bitcmp1_b32 %27, %26\n\ts_cbranch_scc0 .Lend%=\n\ts_cmp_eq_u32 %20, 0\n\ts_cbranch_scc1 .Lnp%=\n\tv_sub_f32_e32 v200, v200, %29\n\tv_sub_f32_e32 v201, v201, %29"
                 "\n\tv_sub_f32_e32 v202, v202, %29\n\tv_sub_f32_e32 v203, v203, %29\n\tv_sub_f32_e32 v204, v204, %30\n\tv_sub_f32_e32 v205, v205, %30\n\tv_sub_f32_e32 v206, v206, %30"
                 "\n\tv_sub_f32_e32 v207, v207, %30\n\ts_mov_b32 %20, 0\n\ts_bitcmp1_b32 %28, %26\n\ts_cbranch_scc1 .Lnp%=\n\tv_exp_f32_e64 %23, -%29\n\tv_exp_f32_e64 %24, -%30\n\ts_nop 0"
                 "\n\tv_mul_f32_e32 v192, v192, %23\n\tv_mul_f32_e32 v193, v193, %23\n\tv_mul_f32_e32 v194, v194, %23\n\tv_mul_f32_e32 v195, v195, %23\n\tv_mul_f32_e32 v196, v196, %24"
                 "\n\tv_mul_f32_e32 v197, v197, %24\n\tv_mul_f32_e32 v198, v198, %24\n\tv_mul_f32_e32 v199, v199, %24\n\tv_mul_f32_e32 v64, v64, %23\n\tv_mul_f32_e32 v65, v65, %23"
                 "\n\tv_mul_f32_e32 v66, v66, %23\n\tv_mul_f32_e32 v67, v67, %23\n\tv_mul_f32_e32 v68, v68, %24\n\tv_mul_f32_e32 v69, v69, %24\n\tv_mul_f32_e32 v70, v70, %24"
                 "\n\tv_mul_f32_e32 v71, v71, %24\n\tv_mul_f32_e32 v72, v72, %23\n\tv_mul_f32_e32 v73, v73, %23\n\tv_mul_f32_e32 v74, v74, %23\n\tv_mul_f32_e32 v75, v75, %23"
                 "\n\tv_mul_f32_e32 v76, v76, %24\n\tv_mul_f32_e32 v77, v77, %24\n\tv_mul_f32_e32 v78, v78, %24\n\tv_mul_f32_e32 v79, v79, %24\n\tv_mul_f32_e32 v80, v80, %23"
                 "\n\tv_mul_f32_e32 v81, v81, %23\n\tv_mul_f32_e32 v82, v82, %23\n\tv_mul_f32_e32 v83, v83, %23\n\tv_mul_f32_e32 v84, v84, %24\n\tv_mul_f32_e32 v85, v85, %24"
                 "\n\tv_mul_f32_e32 v86, v86, %24\n\tv_mul_f32_e32 v87, v87, %24\n\tv_mul_f32_e32 v88, v88, %23\n\tv_mul_f32_e32 v89, v89, %23\n\tv_mul_f32_e32 v90, v90, %23"
                 "\n\tv_mul_f32_e32 v91, v91, %23\n\tv_mul_f32_e32 v92, v92, %24\n\tv_mul_f32_e32 v93, v93, %24\n\tv_mul_f32_e32 v94, v94, %24\n\tv_mul_f32_e32 v95, v95, %24"
                 "\n\tv_mul_f32_e32 v96, v96, %23\n\tv_mul_f32_e32 v97, v97, %23\n\tv_mul_f32_e32 v98, v98, %23\n\tv_mul_f32_e32 v99, v99, %23\n\tv_mul_f32_e32 v100, v100, %24"
                 "\n\tv_mul_f32_e32 v101, v101, %24\n\tv_mul_f32_e32 v102, v102, %24\n\tv_mul_f32_e32 v103, v103, %24\n\tv_mul_f32_e32 v104, v104, %23\n\tv_mul_f32_e32 v105, v105, %23"
                 "\n\tv_mul_f32_e32 v106, v106, %23\n\tv_mul_f32_e32 v107, v107, %23\n\tv_mul_f32_e32 v108, v108, %24\n\tv_mul_f32_e32 v109, v109, %24\n\tv_mul_f32_e32 v110, v110, %24"
                 "\n\tv_mul_f32_e32 v111, v111, %24\n\tv_mul_f32_e32 v112, v112, %23\n\tv_mul_f32_e32 v113, v113, %23\n\tv_mul_f32_e32 v114, v114, %23\n\tv_mul_f32_e32 v115, v115, %23"
                 "\n\tv_mul_f32_e32 v116, v116, %24\n\tv_mul_f32_e32 v117, v117, %24\n\tv_mul_f32_e32 v118, v118, %24\n\tv_mul_f32_e32 v119, v119, %24\n\tv_mul_f32_e32 v120, v120, %23"
                 "\n\tv_mul_f32_e32 v121, v121, %23\n\tv_mul_f32_e32 v122, v122, %23\n\tv_mul_f32_e32 v123, v123, %23\n\tv_mul_f32_e32 v124, v124, %24\n\tv_mul_f32_e32 v125, v125, %24"
                 "\n\tv_mul_f32_e32 v126, v126, %24\n\tv_mul_f32_e32 v127, v127, %24\n.Lnp%=:\n\ts_cmp_lg_u32 %26, %31\n\ts_cbranch_scc1 .Lne%=\n\tv_and_b32_e32 %23, 0x300, %34"
                 "\n\ts_add_i32 %25, %32, 0x1000\n\tv_add_u32_e32 %23, %23, %34\n\tv_add_u32_e32 %22, 0x4000, %22\n\tv_rcp_f32_e32 v192, v192\n\tv_rcp_f32_e32 v196, v196\n\ts_nop 0"
                 "\n\tv_mul_f32_e32 v64, v64, v192\n\tv_mul_f32_e32 v65, v65, v192\n\tv_mul_f32_e32 v66, v66, v192\n\tv_mul_f32_e32 v67, v67, v192\n\tv_mul_f32_e32 v72, v72, v192"
                 "\n\tv_mul_f32_e32 v73, v73, v192\n\tv_mul_f32_e32 v74, v74, v192\n\tv_mul_f32_e32 v75, v75, v192\n\tv_cvt_pk_bf16_f32 v64, v64, v65\n\tv_cvt_pk_bf16_f32 v65, v66, v67"
                 "\n\tv_cvt_pk_bf16_f32 v66, v72, v73\n\tv_cvt_pk_bf16_f32 v67, v74, v75\n\tbuffer_store_dwordx4 v[64:67], %23, %35, %32 offen offset:0 sc1\n\tv_mov_b64_e32 v[72:73], 0"
                 "\n\tv_mov_b64_e32 v[74:75], 0\n\tv_mov_b64_e32 v[64:65], 0\n\tv_mov_b64_e32 v[66:67], 0\n\tv_mul_f32_e32 v68, v68, v196\n\tv_mul_f32_e32 v69, v69, v196"
                 "\n\tv_mul_f32_e32 v70, v70, v196\n\tv_mul_f32_e32 v71, v71, v196\n\tv_mul_f32_e32 v76, v76, v196\n\tv_mul_f32_e32 v77, v77, v196\n\tv_mul_f32_e32 v78, v78, v196"
                 "\n\tv_mul_f32_e32 v79, v79, v196\n\tv_cvt_pk_bf16_f32 v68, v68, v69\n\tv_cvt_pk_bf16_f32 v69, v70, v71\n\tv_cvt_pk_bf16_f32 v70, v76, v77"
                 "\n\tv_cvt_pk_bf16_f32 v71, v78, v79\n\tbuffer_store_dwordx4 v[68:71], %23, %35, %32 offen offset:256 sc1\n\tv_mov_b64_e32 v[76:77], 0\n\tv_mov_b64_e32 v[78:79], 0"
                 "\n\tv_mov_b64_e32 v[68:69], 0\n\tv_mov_b64_e32 v[70:71], 0\n\tv_mul_f32_e32 v80, v80, v192\n\tv_mul_f32_e32 v81, v81, v192\n\tv_mul_f32_e32 v82, v82, v192"
                 "\n\tv_mul_f32_e32 v83, v83, v192\n\tv_mul_f32_e32 v88, v88, v192\n\tv_mul_f32_e32 v89, v89, v192\n\tv_mul_f32_e32 v90, v90, v192\n\tv_mul_f32_e32 v91, v91, v192"
                 "\n\tv_cvt_pk_bf16_f32 v80, v80, v81\n\tv_cvt_pk_bf16_f32 v81, v82, v83\n\tv_cvt_pk_bf16_f32 v82, v88, v89\n\tv_cvt_pk_bf16_f32 v83, v90, v91"
                 "\n\tbuffer_store_dwordx4 v[80:83], %23, %35, %32 offen offset:2048 sc1\n\tv_mov_b64_e32 v[88:89], 0\n\tv_mov_b64_e32 v[90:91], 0\n\tv_mov_b64_e32 v[80:81], 0"
                 "\n\tv_mov_b64_e32 v[82:83], 0\n\tv_mul_f32_e32 v84, v84, v196\n\tv_mul_f32_e32 v85, v85, v196\n\tv_mul_f32_e32 v86, v86, v196\n\tv_mul_f32_e32 v87, v87, v196"
                 "\n\tv_mul_f32_e32 v92, v92, v196\n\tv_mul_f32_e32 v93, v93, v196\n\tv_mul_f32_e32 v94, v94, v196\n\tv_mul_f32_e32 v95, v95, v196\n\tv_cvt_pk_bf16_f32 v84, v84, v85"
                 "\n\tv_cvt_pk_bf16_f32 v85, v86, v87\n\tv_cvt_pk_bf16_f32 v86, v92, v93\n\tv_cvt_pk_bf16_f32 v87, v94, v95"
                 "\n\tbuffer_store_dwordx4 v[84:87], %23, %35, %32 offen offset:2304 sc1\n\tv_mov_b64_e32 v[92:93], 0\n\tv_mov_b64_e32 v[94:95], 0\n\tv_mov_b64_e32 v[84:85], 0"
                 "\n\tv_mov_b64_e32 v[86:87], 0\n\tv_mul_f32_e32 v96, v96, v192\n\tv_mul_f32_e32 v97, v97, v192\n\tv_mul_f32_e32 v98, v98, v192\n\tv_mul_f32_e32 v99, v99, v192"
                 "\n\tv_mul_f32_e32 v104, v104, v192\n\tv_mul_f32_e32 v105, v105, v192\n\tv_mul_f32_e32 v106, v106, v192\n\tv_mul_f32_e32 v107, v107, v192"
                 "\n\tv_cvt_pk_bf16_f32 v96, v96, v97\n\tv_cvt_pk_bf16_f32 v97, v98, v99\n\tv_cvt_pk_bf16_f32 v98, v104, v105\n\tv_cvt_pk_bf16_f32 v99, v106, v107"
                 "\n\tbuffer_store_dwordx4 v[96:99], %23, %35, %25 offen offset:0 sc1\n\tv_mov_b64_e32 v[104:105], 0\n\tv_mov_b64_e32 v[106:107], 0\n\tv_mov_b64_e32 v[96:97], 0"
                 "\n\tv_mov_b64_e32 v[98:99], 0\n\tv_mul_f32_e32 v100, v100, v196\n\tv_mul_f32_e32 v101, v101, v196\n\tv_mul_f32_e32 v102, v102, v196\n\tv_mul_f32_e32 v103, v103, v196"
                 "\n\tv_mul_f32_e32 v108, v108, v196\n\tv_mul_f32_e32 v109, v109, v196\n\tv_mul_f32_e32 v110, v110, v196\n\tv_mul_f32_e32 v111, v111, v196"
                 "\n\tv_cvt_pk_bf16_f32 v100, v100, v101\n\tv_cvt_pk_bf16_f32 v101, v102, v103\n\tv_cvt_pk_bf16_f32 v102, v108, v109\n\tv_cvt_pk_bf16_f32 v103, v110, v111"
                 "\n\tbuffer_store_dwordx4 v[100:103], %23, %35, %25 offen offset:256 sc1\n\tv_mov_b64_e32 v[108:109], 0\n\tv_mov_b64_e32 v[110:111], 0\n\tv_mov_b64_e32 v[100:101], 0"
                 "\n\tv_mov_b64_e32 v[102:103], 0\n\tv_mul_f32_e32 v112, v112, v192\n\tv_mul_f32_e32 v113, v113, v192\n\tv_mul_f32_e32 v114, v114, v192\n\tv_mul_f32_e32 v115, v115, v192"
                 "\n\tv_mul_f32_e32 v120, v120, v192\n\tv_mul_f32_e32 v121, v121, v192\n\tv_mul_f32_e32 v122, v122, v192\n\tv_mul_f32_e32 v123, v123, v192"
                 "\n\tv_cvt_pk_bf16_f32 v112, v112, v113\n\tv_cvt_pk_bf16_f32 v113, v114, v115\n\tv_cvt_pk_bf16_f32 v114, v120, v121\n\tv_cvt_pk_bf16_f32 v115, v122, v123"
                 "\n\tbuffer_store_dwordx4 v[112:115], %23, %35, %25 offen offset:2048 sc1\n\tv_mov_b64_e32 v[120:121], 0\n\tv_mov_b64_e32 v[122:123], 0\n\tv_mov_b64_e32 v[112:113], 0"
                 "\n\tv_mov_b64_e32 v[114:115], 0\n\tv_mul_f32_e32 v116, v116, v196\n\tv_mul_f32_e32 v117, v117, v196\n\tv_mul_f32_e32 v118, v118, v196\n\tv_mul_f32_e32 v119, v119, v196"
                 "\n\tv_mul_f32_e32 v124, v124, v196\n\tv_mul_f32_e32 v125, v125, v196\n\tv_mul_f32_e32 v126, v126, v196\n\tv_mul_f32_e32 v127, v127, v196"
                 "\n\tv_cvt_pk_bf16_f32 v116, v116, v117\n\tv_cvt_pk_bf16_f32 v117, v118, v119\n\tv_cvt_pk_bf16_f32 v118, v124, v125\n\tv_cvt_pk_bf16_f32 v119, v126, v127"
                 "\n\tbuffer_store_dwordx4 v[116:119], %23, %35, %25 offen offset:2304 sc1\n\tv_mov_b64_e32 v[124:125], 0\n\tv_mov_b64_e32 v[126:127], 0\n\tv_mov_b64_e32 v[116:117], 0"
                 "\n\tv_mov_b64_e32 v[118:119], 0\n\tv_mov_b64_e32 v[192:193], 0\n\tv_mov_b64_e32 v[194:195], 0\n\tv_mov_b64_e32 v[196:197], 0\n\tv_mov_b64_e32 v[198:199], 0\n.Lne%=:"
                 "\n\ts_add_i32 %25, %26, 2\n\ts_cmp_eq_u32 %25, %31\n\ts_cselect_b32 %25, 0x4000, 0\n\tv_add_u32_e32 %21, %25, %21\n\ts_nop 1\n.Lend%=:"
                 : "+{v[64:67]}"(a.o[0][0]), "+{v[68:71]}"(a.o[0][1]), "+{v[72:75]}"(a.o[1][0]), "+{v[76:79]}"(a.o[1][1]), "+{v[80:83]}"(a.o[2][0]), "+{v[84:87]}"(a.o[2][1]), "+{v[88:91]}"(a.o[3][0]), "+{v[92:95]}"(a.o[3][1]), "+{v[96:99]}"(a.o[4][0]), "+{v[100:103]}"(a.o[4][1]),
                   "+{v[104:107]}"(a.o[5][0]), "+{v[108:111]}"(a.o[5][1]), "+{v[112:115]}"(a.o[6][0]), "+{v[116:119]}"(a.o[6][1]), "+{v[120:123]}"(a.o[7][0]), "+{v[124:127]}"(a.o[7][1]), "+{v[192:195]}"(a.ls[0]), "+{v[196:199]}"(a.ls[1]), "+{v[200:203]}"(a.negm[0]), "+{v[204:207]}"(a.negm[1]),
                   "+s"(a.pend), "+v"(a.kb[0]), "+v"(a.vb[1]), "=&v"(t0), "=&v"(t1), "=&s"(st)
                 : "s"(s), "s"(a.cold), "s"(a.first), "v"(a.dl[0]), "v"(a.dl[1]), "s"(a.n1t), "s"(a.p0), "s"(a.p1), "v"(a.voff), "s"(c.rws), "s"(a.fs), "s"(ep) : "scc", "memory");
  } else {
    asm volatile(
                 "\n\ts_bitcmp1_b32 %37, %36\n\ts_cbranch_scc0 .Lend%=\n\ts_add_i32 %25, %36, 1\n\ts_cmp_lg_u32 %25, %41\n\ts_cbranch_scc1 .Lnq%=\n\tv_add_u32_e32 %23, %43, %44"
                 "\n\tds_read_b128 %28, %23 offset:0\n\tds_read_b128 %29, %23 offset:1024\n\tds_read_b128 %30, %23 offset:2048\n\tds_read_b128 %31, %23 offset:3072"
                 "\n\tds_read_b128 %32, %23 offset:4096\n\tds_read_b128 %33, %23 offset:5120\n\tds_read_b128 %34, %23 offset:6144\n\tds_read_b128 %35, %23 offset:7168\n.Lnq%=:"
                 "\n\ts_cmp_eq_u32 %20, 0\n\ts_cbranch_scc1 .Lnp%=\n\tv_sub_f32_e32 v200, v200, %39\n\tv_sub_f32_e32 v201, v201, %39\n\tv_sub_f32_e32 v202, v202, %39"
                 "\n\tv_sub_f32_e32 v203, v203, %39\n\tv_sub_f32_e32 v204, v204, %40\n\tv_sub_f32_e32 v205, v205, %40\n\tv_sub_f32_e32 v206, v206, %40\n\tv_sub_f32_e32 v207, v207, %40"
                 "\n\ts_mov_b32 %20, 0\n\ts_bitcmp1_b32 %38, %36\n\ts_cbranch_scc1 .Lnp%=\n\tv_exp_f32_e64 %23, -%39\n\tv_exp_f32_e64 %24, -%40\n\ts_nop 0\n\tv_mul_f32_e32 v192, v192, %23"
                 "\n\tv_mul_f32_e32 v193, v193, %23\n\tv_mul_f32_e32 v194, v194, %23\n\tv_mul_f32_e32 v195, v195, %23\n\tv_mul_f32_e32 v196, v196, %24\n\tv_mul_f32_e32 v197, v197, %24"
                 "\n\tv_mul_f32_e32 v198, v198, %24\n\tv_mul_f32_e32 v199, v199, %24\n\tv_mul_f32_e32 v64, v64, %23\n\tv_mul_f32_e32 v65, v65, %23\n\tv_mul_f32_e32 v66, v66, %23"
                 "\n\tv_mul_f32_e32 v67, v67, %23\n\tv_mul_f32_e32 v68, v68, %24\n\tv_mul_f32_e32 v69, v69, %24\n\tv_mul_f32_e32 v70, v70, %24\n\tv_mul_f32_e32 v71, v71, %24"
                 "\n\tv_mul_f32_e32 v72, v72, %23\n\tv_mul_f32_e32 v73, v73, %23\n\tv_mul_f32_e32 v74, v74, %23\n\tv_mul_f32_e32 v75, v75, %23\n\tv_mul_f32_e32 v76, v76, %24"
                 "\n\tv_mul_f32_e32 v77, v77, %24\n\tv_mul_f32_e32 v78, v78, %24\n\tv_mul_f32_e32 v79, v79, %24\n\tv_mul_f32_e32 v80, v80, %23\n\tv_mul_f32_e32 v81, v81, %23"
                 "\n\tv_mul_f32_e32 v82, v82, %23\n\tv_mul_f32_e32 v83, v83, %23\n\tv_mul_f32_e32 v84, v84, %24\n\tv_mul_f32_e32 v85, v85, %24\n\tv_mul_f32_e32 v86, v86, %24"
                 "\n\tv_mul_f32_e32 v87, v87, %24\n\tv_mul_f32_e32 v88, v88, %23\n\tv_mul_f32_e32 v89, v89, %23\n\tv_mul_f32_e32 v90, v90, %23\n\tv_mul_f32_e32 v91, v91, %23"
                 "\n\tv_mul_f32_e32 v92, v92, %24\n\tv_mul_f32_e32 v93, v93, %24\n\tv_mul_f32_e32 v94, v94, %24\n\tv_mul_f32_e32 v95, v95, %24\n\tv_mul_f32_e32 v96, v96, %23"
                 "\n\tv_mul_f32_e32 v97, v97, %23\n\tv_mul_f32_e32 v98, v98, %23\n\tv_mul_f32_e32 v99, v99, %23\n\tv_mul_f32_e32 v100, v100, %24\n\tv_mul_f32_e32 v101, v101, %24"
                 "\n\tv_mul_f32_e32 v102, v102, %24\n\tv_mul_f32_e32 v103, v103, %24\n\tv_mul_f32_e32 v104, v104, %23\n\tv_mul_f32_e32 v105, v105, %23\n\tv_mul_f32_e32 v106, v106, %23"
                 "\n\tv_mul_f32_e32 v107, v107, %23\n\tv_mul_f32_e32 v108, v108, %24\n\tv_mul_f32_e32 v109, v109, %24\n\tv_mul_f32_e32 v110, v110, %24\n\tv_mul_f32_e32 v111, v111, %24"
                 "\n\tv_mul_f32_e32 v112, v112, %23\n\tv_mul_f32_e32 v113, v113, %23\n\tv_mul_f32_e32 v114, v114, %23\n\tv_mul_f32_e32 v115, v115, %23\n\tv_mul_f32_e32 v116, v116, %24"
                 "\n\tv_mul_f32_e32 v117, v117, %24\n\tv_mul_f32_e32 v118, v118, %24\n\tv_mul_f32_e32 v119, v119, %24\n\tv_mul_f32_e32 v120, v120, %23\n\tv_mul_f32_e32 v121, v121, %23"
                 "\n\tv_mul_f32_e32 v122, v122, %23\n\tv_mul_f32_e32 v123, v123, %23\n\tv_mul_f32_e32 v124, v124, %24\n\tv_mul_f32_e32 v125, v125, %24\n\tv_mul_f32_e32 v126, v126, %24"
                 "\n\tv_mul_f32_e32 v127, v127, %24\n.Lnp%=:\n\ts_cmp_lg_u32 %25, %41\n\ts_cbranch_scc1 .Lnz%=\n\tv_mov_b64_e32 v[200:201], 0\n\tv_mov_b64_e32 v[202:203], 0"
                 "\n\tv_mov_b64_e32 v[204:205], 0\n\tv_mov_b64_e32 v[206:207], 0\n\tv_add_u32_e32 %21, 0x4000, %21\n\tv_add_u32_e32 %22, 0x4000, %22\n\ts_waitcnt lgkmcnt(0)\n.Lnz%=:"
                 "\n\ts_cmp_lg_u32 %36, %46\n\ts_cbranch_scc1 .Lnf%=\n\tv_mov_b64_e32 %26, %47\n\tv_mov_b32_e32 %23, %42\n\ts_mov_b64 exec, 1"
                 "\n\tbuffer_atomic_swap_x2 %26, %23, %45, 0 offen sc1\n\ts_mov_b64 exec, -1\n.Lnf%=:\n\ts_add_i32 %25, %36, 84\n\ts_cmp_lg_u32 %25, %41\n\ts_cbranch_scc1 .Lnl%="
                 "\n\tv_lshrrev_b32_e32 %24, 1, %44\n\ts_mov_b64 exec, 15\n\tbuffer_atomic_add_x2 %27, %24, %45, %43 offen sc0 sc1\n\ts_mov_b64 exec, -1\n.Lnl%=:\n\ts_nop 1\n.Lend%=:"
                 : "+{v[64:67]}"(a.o[0][0]), "+{v[68:71]}"(a.o[0][1]), "+{v[72:75]}"(a.o[1][0]), "+{v[76:79]}"(a.o[1][1]), "+{v[80:83]}"(a.o[2][0]), "+{v[84:87]}"(a.o[2][1]), "+{v[88:91]}"(a.o[3][0]), "+{v[92:95]}"(a.o[3][1]), "+{v[96:99]}"(a.o[4][0]), "+{v[100:103]}"(a.o[4][1]),
                   "+{v[104:107]}"(a.o[5][0]), "+{v[108:111]}"(a.o[5][1]), "+{v[112:115]}"(a.o[6][0]), "+{v[116:119]}"(a.o[6][1]), "+{v[120:123]}"(a.o[7][0]), "+{v[124:127]}"(a.o[7][1]), "+{v[192:195]}"(a.ls[0]), "+{v[196:199]}"(a.ls[1]), "+{v[200:203]}"(a.negm[0]), "+{v[204:207]}"(a.negm[1]),
                   "+s"(a.pend), "+v"(a.kb[1]), "+v"(a.vb[0]), "=&v"(t0), "=&v"(t1), "=&s"(st), "=&v"(tp), "+v"(a.fl), "+v"(a.qf[0]), "+v"(a.qf[1]), "+v"(a.qf[2]), "+v"(a.qf[3]), "+v"(a.qf[4]), "+v"(a.qf[5]), "+v"(a.qf[6]), "+v"(a.qf[7])
                 : "s"(s), "s"(a.cold), "s"(a.first), "v"(a.dl[0]), "v"(a.dl[1]), "s"(a.n1t), "s"(a.p0), "s"(a.p1), "v"(a.voff), "s"(c.rws), "s"(a.fs), "s"(ep) : "scc", "memory");
  }
  SPIN(a.pend);
}
template <int P> DI void dma4(unsigned lds_addr, unsigned voff, rsrc_t r, unsigned soff) {
  if constexpr (P == 0) asm volatile(VP "s_mov_b32 m0, %0\n\ts_nop 0\n\tbuffer_load_dwordx4 %1, %2, %3 offen sc1 lds" ::"s"(lds_addr), "v"(voff), "s"(r), "s"(soff) : "memory", "m0");
  else asm volatile(VP "buffer_load_dwordx4 %0, %1, %2 offen offset:%3 sc1 lds" ::"v"(voff), "s"(r), "s"(soff), "i"(P * 1024) : "memory");
}
DI void mx2(float& m, float x, float y) { asm volatile("v_max3_f32 %0, %0, %1, %2" : "+v"(m) : "v"(x), "v"(y)); }
#define EB 22
#define CHK 4
template <int E> DI void exp_el(f32x4 (&S)[4][2]) {
  constexpr int t = 2 * (E >> 4) + ((E >> 2) & 1), u = (E >> 3) & 1, j = E & 3;
  float x = __builtin_amdgcn_exp2f(S[t][u][j]);
  PIN(x);
  S[t][u][j] = x;
}
template <int C> DI void cvt_el(u32x4 (&pk)[2][2], const f32x4 (&S)[4][2]) {
  constexpr int t = 2 * (C >> 3) + ((C >> 1) & 1), u = (C >> 2) & 1, j = 2 * (C & 1);
  unsigned x = cvtpk(S[t][u][j], S[t][u][j + 1]);
  PIN(x);
  pk[C >> 3][u][C & 3] = x;
}
constexpr int SUMP[4] = {8, 13, 26, 31};
constexpr int b_sum(int G) { for (int i = 0; i < 4; ++i) if (SUMP[i] == G) return i; return -1; }
constexpr int b_pv(int G) { int n = G; for (int i = 0; i < 4; ++i) if (SUMP[i] < G) --n; return n; }
constexpr int b_exp(int G) {
  if (G <= CHK) return -1;
  const int n = 35 - CHK, i = G - CHK - 1, e0 = i * EB / n, e1 = (i + 1) * EB / n;
  return e1 > e0 ? e0 : -1;
}
#define IST(k)
struct NoHook { template <class T> DI void operator()(T) const {} };
template <int X, bool PV, class Hook = NoHook>
DI void attn_iter(Ctx& c, AttnState& a, const unsigned s, const int& dq, Hook&& hook = Hook{}) {
  constexpr int Y = X ^ 1, NA = 32 - EB;
  unsigned thr = 0;
  static_for<32>([&](auto G_) AI {
    constexpr int G = decltype(G_)::v, M = G >> 1, u = G & 1, t = M >> 2, f = M & 3;
    mfma_r<R_S + 32 * X + 8 * t + 4 * u, f == 0>(a.S[X][t][u], a.fr[M & 7], a.qf[2 * f + u], a.negm[u]);
    SB();
    if constexpr ((LEAP & 4) && (G & 7) == 0) __builtin_amdgcn_s_setprio(3 - (((M >> 2) + 1) & 3));
    if constexpr (u == 0 && RD0[M] >= 0) a.fr[RD0[M] & 7] = lds_b128(a.kb[X] + RD0[M] * 1024);
    if constexpr (u == 1 && RD1[M] >= 0) a.fr[RD1[M] & 7] = lds_b128(a.kb[X] + RD1[M] * 1024);
    if constexpr (PV && u == 1 && (M == 0 || M == 2 || M == 6 || M == 9)) dma4<M == 0 ? 0 : M == 2 ? 1 : M == 6 ? 2 : 3>(a.dk + X * 32768 + 65536, a.voff, c.rws, a.o0);
    if constexpr (!PV && u == 1 && M >= 12) dma4<M - 12>(a.dk + X * 32768 + 65536, a.voff, c.rws, a.o0);
    if constexpr (PV) {
      if constexpr (G < NA) exp_el<EB + G>(a.S[Y]);
      else if constexpr (G < NA + 16) cvt_el<G - NA>(a.pk, a.S[Y]);
    }
    if constexpr (G >= 12 && (G & 7) >= 4) {
      constexpr int tt = (G - 12) >> 3, j = G & 2;
      if constexpr (G < 14) asm volatile("v_max_f32_e32 %0, %1, %2" : "=v"(a.mx[u]) : "v"(a.S[X][0][u][0]), "v"(a.S[X][0][u][1]));
      else mx2(a.mx[u], a.S[X][tt][u][j], a.S[X][tt][u][j + 1]);
    }
    if constexpr (G == 0) IST(0);
    if constexpr (G == 12) IST(1);
    if constexpr (G == 23) IST(2);
    hook(G_);
    if constexpr (G == 23) IST(3);
    if constexpr (G == 24) IST(4);
    if constexpr (G == 31) IST(5);
    if constexpr (PV && G == 20) { thr = ((a.slow >> s) & 1) ? 0x7fc00000u : __float_as_uint(THR); SPIN(thr); }
    if constexpr (G == 23) { SB(); if constexpr (PV) wait_vm<9>(); bar(); SB(); }
    if constexpr (PV && u == 0 && M >= 12) a.fr[(M - 12) & 7] = lds_b128(a.vb[Y] + (M - 12) * 1024);
    SB();
  });
  static_for<36>([&](auto G_) AI {
    constexpr int G = decltype(G_)::v, sm = b_sum(G), P = b_pv(G), M = P >> 1, u = P & 1;
    if constexpr (PV) {
      if constexpr (sm >= 0) mfma_r<R_L + 4 * (sm & 1)>(a.ls[sm & 1], asbf(a.ones), asbf(a.pk[sm >> 1][sm & 1]));
      else mfma_r<R_O + 8 * (M & 7) + 4 * u>(a.o[M & 7][u], a.fr[M & 7], asbf(a.pk[M >> 3][u]));
      SB();
      if constexpr ((LEAP & 4) && sm < 0 && (P & 7) == 0) __builtin_amdgcn_s_setprio(3 - (((M >> 2) + 1) & 3));
      if constexpr (sm < 0 && u == 0 && RD0[M] >= 0) a.fr[RD0[M] & 7] = lds_b128(a.vb[Y] + RD0[M] * 1024);
      if constexpr (sm < 0 && u == 1 && RD1[M] >= 0) a.fr[RD1[M] & 7] = lds_b128(a.vb[Y] + RD1[M] * 1024);
    }
    if constexpr (sm < 0 && u == 1 && (M == 0 || M == 2 || M == 6 || M == 9)) dma4<M == 0 ? 0 : M == 2 ? 1 : M == 6 ? 2 : 3>(a.dk + X * 32768, a.voff, c.rws, a.o2);
    if constexpr (G == 0) { a.u += ((a.wide >> s) & 1) ? 32768u : 16384u; SPIN(a.u); }
    if constexpr (G == 1) { a.o0 = a.o1 + (WS_V - WS_K); SPIN(a.o0); }
    if constexpr (G == CHK - 2 || G == CHK - 1) {
      constexpr int uu = G - (CHK - 2);
      if constexpr (!PV && uu == 0) asm volatile("s_nop 0");
      asm volatile("v_max3_f32 %0, %0, %1, %2\n\tv_max3_f32 %0, %0, %3, %4" : "+v"(a.mx[uu]) : "v"(a.S[X][3][uu][0]), "v"(a.S[X][3][uu][1]), "v"(a.S[X][3][uu][2]), "v"(a.S[X][3][uu][3]));
    }
    if constexpr (G == CHK) {
      if constexpr (!PV) slow_unit<X>(a, a.S[X], dq, 1u);
      else if (__builtin_expect((__builtin_amdgcn_ballot_w64(!(a.mx[0] <= __uint_as_float(thr))) | __builtin_amdgcn_ballot_w64(!(a.mx[1] <= __uint_as_float(thr)))) != 0, 0)) slow_unit<X>(a, a.S[X], dq, 1u << s);
    }
    if constexpr (G == CHK) IST(6);
    hook(IC<32 + G>{});
    if constexpr (b_exp(G) >= 0) exp_el<b_exp(G)>(a.S[X]);
    if constexpr (sm < 0 && P == 21) { a.o1 = a.o2; a.o2 = ((a.drop >> s) & 1) ? 0xF0000000u : a.u; SPIN(a.o1); SPIN(a.o2); }
    if constexpr (sm < 0 && P == 23) { IST(7); SB(); wait_vm<PV ? 9 : 8>(); IST(8); bar(); IST(9); }
    if constexpr (sm < 0 && P == 27) { asm volatile("s_mov_b64 exec, %2\n\tglobal_load_dword %0, %1, off\n\ts_mov_b64 exec, -1" : "+v"(a.dummy) : "v"(a.ta), "s"(a.tm) : "memory"); a.tm <<= 4; }
    if constexpr (sm < 0 && u == 0 && M >= 12) a.fr[(M - 12) & 7] = lds_b128(a.kb[Y] + (M - 12) * 1024);
    SB();
  });
}

constexpr unsigned WP = 288;
constexpr unsigned LSYNC = 161280;
constexpr unsigned C_LAND = 0, C_FREE = 64, C_GB = 128, C_CTXD = 136, C_MRG = 140, C_TICK = 144, LSTAT = LSYNC + 256;
#define ARRV 2
constexpr int XM = 12;
constexpr int XDEP(int NK) { return NK == 16 ? XM : 8; }
DI unsigned land(unsigned ph, unsigned q) { return LSYNC + C_LAND + (ph * 5 + q) * 4; }
DI unsigned fre(unsigned ph, unsigned q) { return LSYNC + C_FREE + (ph * 5 + q) * 4; }
DI void cnt_add(Ctx& c, unsigned off) {
  asm volatile("s_mov_b64 exec, 1\n\tds_add_u32 %0, %1\n\ts_mov_b64 exec, -1" ::"v"(c.ldsb + off), "v"(1u) : "memory");
}
#define wlog(...)
DI unsigned cnt_add_rtn(Ctx& c, unsigned off) {
  unsigned r;
  asm volatile("s_mov_b64 exec, 1\n\tds_add_rtn_u32 %0, %1, %2\n\ts_mov_b64 exec, -1\n\ts_waitcnt lgkmcnt(0)" : "=&v"(r) : "v"(c.ldsb + off), "v"(1u) : "memory");
  return rfl(r);
}
DI unsigned cnt_read(Ctx& c, unsigned off) { return __hip_atomic_load((LAS unsigned*)(size_t)(c.ldsb + off), __ATOMIC_RELAXED, __HIP_MEMORY_SCOPE_WORKGROUP); }
DI void prio_lo() { __builtin_amdgcn_s_setprio(0); }
DI void prio_hi(Ctx& c) { __builtin_amdgcn_s_setprio(3); }
DI void prio_gemm(Ctx& c, bool behind) {
  if (c.w >= 4) __builtin_amdgcn_s_setprio(1); else if (behind) __builtin_amdgcn_s_setprio(0); else __builtin_amdgcn_s_setprio(2);
}
DI void cnt_wait(Ctx& c, unsigned off, unsigned target) {
  asm volatile("" ::: "memory");
  unsigned spins = 0;
#pragma unroll 1
  while (rfl(cnt_read(c, off)) < target) { __builtin_amdgcn_s_sleep(1); if (++spins > (1u << 20)) break; }
  asm volatile("" ::: "memory");
}
DI void flag_w(Ctx& c, unsigned flag_off) {
  wait_vm<0>();
  fadd(c.rws, c.lane == 0 ? flag_off : OOB);
}
DI bool cnt_full(Ctx& c, const u32x2& f, unsigned n) { return (((u64)rfl(f[1]) << 32) | rfl(f[0])) == (((u64)c.ep1 << 32) | c.ep0) + n; }
DI void dma16m(unsigned lds_addr, unsigned voff, rsrc_t r, unsigned soff, u64 mask) {
  asm volatile(VP "s_mov_b32 m0, %0\n\ts_mov_b64 exec, %4\n\ts_nop 0\n\tbuffer_load_dwordx4 %1, %2, %3 offen lds\n\ts_mov_b64 exec, -1" ::"s"(lds_addr), "v"(voff), "s"(r), "s"(soff), "s"(mask)
               : "memory", "m0");
}
struct WSrc { unsigned voff[9]; rsrc_t r; unsigned main, tail, pitch, colA, colB; unsigned idx, pend, ph; };
DI unsigned w_voff(const WSrc& s, unsigned lane, int p) {
  const unsigned ci = lane + 64 * p, rr = __umul24(ci, 3641) >> 16, cc = ci - __umul24(rr, 18);
  return __umul24(rr, s.pitch - WP) + ci * 16 + (cc < 16 ? s.colA : s.colB - 256);
}
DI unsigned opaque_lane() {
  unsigned lane;
  asm volatile("v_mbcnt_lo_u32_b32 %0, -1, 0\n\tv_mbcnt_hi_u32_b32 %0, -1, %0" : "=v"(lane));
  return lane;
}
template <int NV = 5> DI void wsrc_voff(Ctx& c, WSrc& s) {
  const unsigned lane = opaque_lane();
#pragma unroll
  for (int p = 0; p < NV; ++p) s.voff[p] = w_voff(s, lane, p);
  if constexpr (NV == 5) {
    s.voff[5] = lane >= 16 ? ~0u : 0u;
    const unsigned l7 = (lane & 7) < 5 ? lane & 7 : 0;
    s.voff[6] = c.ldsb + (lane < 8 ? LSYNC + C_TICK + (s.ph * 5 + l7) * 4 : fre(s.ph - 1, 4) - 4 * l7);
    s.voff[7] = lane == 0 ? 3 : 8;
  }
}
template <int NV = 0> DI void wsrc_setup(Ctx& c, WSrc& s, rsrc_t r, unsigned pitch, unsigned colA, unsigned colB, unsigned rowMain, unsigned rowTail, unsigned ph) {
  s.r = r; s.pitch = pitch; s.main = rowMain * pitch; s.tail = rowTail * pitch;
  s.colA = colA; s.colB = colB; s.ph = ph; s.idx = s.pend = 0;
  if constexpr (NV > 0) wsrc_voff<NV>(c, s);
}
DI void w_group(Ctx& c, const WSrc& s, unsigned q) {
  const unsigned r0 = 128 * q + 32 * (c.w & 3);
#pragma unroll
  for (int p = 0; p < 9; ++p) dma16(c.ldsb + r0 * WP + p * 1024, s.voff[p], s.r, s.main + r0 * s.pitch);
}
DI void poll_w(Ctx& c, unsigned off, unsigned n) {
  const unsigned voff = c.lane == 0 ? off : OOB;
  unsigned spins = 0;
#pragma unroll 1
  for (;;) {
    u32x2 f;
    fget(f, c.rws, voff);
    asm volatile("s_waitcnt vmcnt(0)" : "+v"(f)::"memory");
    if (cnt_full(c, f, n) || ++spins > (1u << 17)) break;
  }
}
constexpr unsigned nitems(unsigned q) { return q == 4 ? 3 : 8; }
DI bool w_try(Ctx& c, WSrc& s) {
  if (s.idx) return false;
  unsigned idx, t;
#pragma unroll 1
  for (;;) {
    const unsigned v = __hip_atomic_load((LAS unsigned*)(size_t)s.voff[6], __ATOMIC_RELAXED, __HIP_MEMORY_SCOPE_WORKGROUP);
    const unsigned m = (unsigned)__builtin_amdgcn_ballot_w64(v < s.voff[7]);
    if ((m & 31) == 0) s.idx = 1;
    const unsigned av = m & ~(m >> 8) & 31;
    if (av == 0) return false;
    idx = __builtin_ctz(av);
    t = cnt_add_rtn(c, LSYNC + C_TICK + (s.ph * 5 + idx) * 4);
    if (t < (idx ? 8u : 3u)) break;
  }
  s.pend += 1u << (4 * idx);
  wlog(c, s.ph, 1, idx, true); wlog(c, s.ph, 2, idx);
  const unsigned lrow = 128 * (4 - idx) + 16 * t, so = (idx ? s.main + lrow * s.pitch : s.tail + 16 * t * s.pitch), sk = lrow == 544 ? 8 * s.pitch : 0u;
  const unsigned la = c.ldsb + lrow * WP;
  dma16(la, s.voff[0], s.r, so);
  dma16(la + 1024, s.voff[1], s.r, so);
  dma16(la + 2048, s.voff[2] + (s.voff[5] & sk), s.r, so);
  dma16(la + 3072, s.voff[3], s.r, so + sk);
  dma16m(la + 4096, s.voff[4], s.r, so + sk, 0xffffffffull);
  return true;
}
DI void w_signal(Ctx& c, const WSrc& s, unsigned items) {
  if (items) {
    unsigned lane;
    asm volatile("v_mbcnt_lo_u32_b32 %0, -1, 0" : "=v"(lane));
    const unsigned n = (items >> (4 * lane)) & 15, ad = c.ldsb + land(s.ph, 4) - 4 * lane;
    asm volatile("s_mov_b64 exec, 31\n\tds_add_u32 %0, %1\n\ts_mov_b64 exec, -1" ::"v"(ad), "v"(n) : "memory");
  }
}
DI void w_flush(Ctx& c, WSrc& s) { w_signal(c, s, s.pend); s.pend = 0; }
#define GSRV 0
#define NMAX 2
#define PPRIO 3
DI void w_try_wait(Ctx& c, WSrc& s, unsigned mx) {
  unsigned k = 0;
#pragma unroll 1
  while (k < mx && w_try(c, s)) ++k;
  if (k == 0) wait_vm<0>(); else if (k == 1) wait_vm<5>(); else if (k == 2) wait_vm<10>(); else if (k == 3) wait_vm<15>(); else wait_vm<20>();
}
template <bool VOFF> DI void w_need(Ctx& c, WSrc& s, unsigned q, unsigned n) {
  unsigned spins = 0;
  if constexpr (VOFF) wsrc_voff(c, s);
#pragma unroll 1
  for (;;) {
    if (s.pend == 0) {
      unsigned k = 0;
#pragma unroll 1
      while (k < NMAX && w_try(c, s)) ++k;
    }
    if (s.pend) { wait_vm<0>(); w_flush(c, s); }
    if (rfl(cnt_read(c, land(s.ph, q))) >= n || ++spins > (1u << 20)) break;
    __builtin_amdgcn_s_sleep(1);
  }
  asm volatile("" ::: "memory");
}
DI void poll_srv(Ctx& c, WSrc& s, unsigned flag_off, unsigned n, unsigned mx, unsigned mx1) {
  const unsigned voff = c.lane == 0 ? flag_off : OOB;
  unsigned spins = 0;
  __builtin_amdgcn_s_setprio(PPRIO);
#pragma unroll 1
  for (;;) {
    u32x2 f;
    fget(f, c.rws, voff);
    if (s.pend) { wait_vm<1>(); w_flush(c, s); }
    w_try_wait(c, s, mx1);
    mx1 = mx;
    PIN(f);
    if (cnt_full(c, f, n) || ++spins > (1u << 17)) break;
  }
  prio_hi(c);
}
constexpr unsigned LB1 = LSTAT + 256, LB2 = LB1 + 320;
template <int P> DI void park_load(Ctx& c, const u16* w1, const u16* b1, const u16* w2, const u16* b2) {
  const unsigned xcd = c.b & 7, nt = (c.b >> 3) & 3, nt32 = xcd * 4 + nt, w = (c.w & 3) * 2 + (P & 1);
  const unsigned rr = c.lane >= 18 ? 1 : 0, cc = c.lane - 18 * rr, r0 = (w < 4 ? 40 : 48) + 2 * w;
  const unsigned c1 = cc < 16 ? nt32 * 256 + cc * 16 : 8192 + nt32 * 32 + (cc - 16) * 16, c2 = cc < 16 ? nt * 256 + cc * 16 : 1024 + nt * 32 + (cc - 16) * 16;
  if constexpr (P < 2) dma16m(c.ldsb + (512 + 2 * w) * WP, rr * (FF * 2) + c1, mk_rsrc(w1, DM * FF * 2), (512 + r0) * (FF * 2), 0xfffffffffull);
  else if constexpr (P < 4) dma16m(c.ldsb + (528 + 2 * w) * WP, rr * (DM * 2) + c2, mk_rsrc(w2, FF * DM * 2), (4096 + 64 * xcd + r0) * (DM * 2), 0xfffffffffull);
  else {
    const bool f = c.w == 4;
    dma16m(c.ldsb + (f ? LB1 : LB2 + (c.w - 5) * 384), f ? c1 : c.lane * 16, mk_rsrc(f ? b1 : b2, f ? FF * 2 : DM * 2), f ? 0 : (c.w - 5) * 384, f ? 0x3ffffull : 0xffffffull);
  }
}
DI void park_read(Ctx& c, u32x2 (&tl)[9], u32x2 (&tl2)[9]) {
  const unsigned ad = c.ldsb + (512 + (c.lane >> 2)) * WP + (c.lane & 3) * 8;
#pragma unroll
  for (int a = 0; a < 9; ++a) { tl[a] = __builtin_bit_cast(u32x2, lds_tr(ad + a * 32)); tl2[a] = __builtin_bit_cast(u32x2, lds_tr(ad + 16 * WP + a * 32)); }
  SB();
  cnt_add(c, fre(0, 4)); wlog(c, 1, 0, 0);
#pragma unroll
  for (int a = 0; a < 9; ++a) { PIN(tl[a]); PIN(tl2[a]); }
}
constexpr int KK(int NK, int v) { return NK == 18 ? (v < 2 ? 16 + v : 4 * (3 - (v - 2) / 4) + (v - 2) % 4) : 4 * (3 - v / 4) + v % 4; }
constexpr bool QSTART(int NK, int v) { return v < NK && v > 0 && (KK(NK, v) & 3) == 0; }
constexpr bool QEND(int NK, int v) { return (KK(NK, v) & 3) == 3 || KK(NK, v) == 17; }
template <int NK, int V> DI void x_ld(Ctx& c, u32x4 (&x)[2], unsigned voff, unsigned so, unsigned so2) {
  constexpr int k = KK(NK, V);
  gld16c2<(k & 1) * 2048>(x[0], x[1], voff, c.rws, k < 16 ? so + (k >> 1) * 4096 : so2);
}
template <int NK, int V0 = 0, int NV = 8> DI void x_pro(Ctx& c, u32x4 (&x)[XM][2], unsigned voff, unsigned so, unsigned so2) {
  static_for<NV>([&](auto V_) AI { x_ld<NK, V0 + decltype(V_)::v>(c, x[V0 + decltype(V_)::v], voff, so, so2); });
}
DI void x_ld4_masked(u32x4& a, u32x4& b, u32x4& d, u32x4& e, unsigned voff, rsrc_t r, unsigned so, unsigned mask) {
  asm volatile(VP "s_cmp_eq_u32 %7, 0\n\ts_cbranch_scc1 .Lxs%=\n\tbuffer_load_dwordx4 %0, %4, %5, %6 offen sc1\n\tbuffer_load_dwordx4 %1, %4, %5, %6 offen offset:256 sc1\n\t"
               "buffer_load_dwordx4 %2, %4, %5, %6 offen offset:2048 sc1\n\tbuffer_load_dwordx4 %3, %4, %5, %6 offen offset:2304 sc1\n.Lxs%=:"
               : "+v"(a), "+v"(b), "+v"(d), "+v"(e) : "v"(voff), "s"(r), "s"(so), "s"(mask) : "memory", "scc");
}
template <int IMM> DI void x_ld2_if(u32x4& a, u32x4& b, unsigned voff, rsrc_t r, unsigned so, unsigned skip) {
  asm volatile(VP "s_cmp_lg_u32 %5, 0\n\ts_cbranch_scc1 .Lxi%=\n\tbuffer_load_dwordx4 %0, %2, %3, %4 offen offset:%6 sc1\n\tbuffer_load_dwordx4 %1, %2, %3, %4 offen offset:%7 sc1\n.Lxi%=:"
               : "=&v"(a), "=&v"(b) : "v"(voff), "s"(r), "s"(so), "s"(skip), "i"(IMM), "i"(IMM + 256) : "memory", "scc");
}
template <int V> DI void x_ld16_if(Ctx& c, u32x4 (&x)[2], unsigned voff, unsigned so, unsigned skip) {
  constexpr int k = KK(16, V);
  x_ld2_if<(k & 1) * 2048>(x[0], x[1], voff, c.rws, so + (k >> 1) * 4096, skip);
}
DI void dma16_if(unsigned lds_addr, unsigned voff, rsrc_t r, unsigned soff, unsigned go) {
  asm volatile(VP "s_cmp_eq_u32 %4, 0\n\ts_cbranch_scc1 .Ldi%=\n\ts_mov_b32 m0, %0\n\ts_nop 0\n\tbuffer_load_dwordx4 %1, %2, %3 offen lds\n.Ldi%=:" ::"s"(lds_addr), "v"(voff), "s"(r), "s"(soff), "s"(go)
               : "memory", "m0", "scc");
}
DI void x_pro_masked16(Ctx& c, u32x4 (&x)[XM][2], unsigned so, unsigned mask) {
  const unsigned voff = x_voff(c);
#pragma unroll
  for (int p = 0; p < XM / 2; ++p) x_ld4_masked(x[2 * p][0], x[2 * p][1], x[2 * p + 1][0], x[2 * p + 1][1], voff, c.rws, so + (KK(16, 2 * p) >> 1) * 4096, mask);
}
template <int L> DI void wlane(unsigned& v, unsigned s) { asm volatile("v_writelane_b32 %0, %1, %2" : "+v"(v) : "s"(s), "i"(L)); }
struct Sched {
  int NK, XD;
  constexpr int T0() const { return 9 * (NK - 4); }
  constexpr int sv(int n) const { return n < T0() ? n / 9 : NK - 4 + (n - T0()) % 4; }
  constexpr int sa(int n) const { return n < T0() ? n % 9 : ((n - T0()) / 4 == 0 ? 8 : (n - T0()) / 4 - 1); }
  constexpr int g0(int G) const { return G == 3 ? T0() + 8 : 9 * (NK - 16 + 1 + 4 * G) + 1; }
  constexpr int gn(int G) const { return G == 0 ? 4 : 9; }
  constexpr int wg(int n) const { for (int G = 0; G < 4; ++G) if (n >= g0(G) && (n - g0(G)) % 3 == 0 && (n - g0(G)) / 3 < gn(G)) return G; return -1; }
  constexpr int wp(int n) const { return (n - g0(wg(n))) / 3; }
  constexpr int gsig(int n) const { for (int G = 0; G < 3; ++G) if (n == g0(G) + 3 * (gn(G) - 1) + 27) return G; return -1; }
  constexpr int st(int n) const {
    const int m = n - T0();
    if (m == 6) return 0;
    if (m >= 13 && (m - 13) % 8 == 0) return 1 + 2 * ((m - 13) / 8);
    if (m >= 15 && (m - 15) % 8 == 0) return 2 + 2 * ((m - 15) / 8);
    return -1;
  }
  constexpr bool sp(int n) const { return n < T0() ? (n % 18 == 0 && n >= 18) : ((n - T0()) % 18 == 0); }
  constexpr int between(int n0, int n1) const {
    int cnt = 0;
    for (int i = n0; i < n1; ++i) { if (i < T0() && sa(i) == 8 && sv(i) + XD < NK) cnt += 2; if (st(i) >= 0) cnt += 1; }
    return cnt;
  }
  constexpr int younger(int tag, int n, bool ldr) const {
    int cnt = 0, last = -1;
    for (int v = 0; v < XD; ++v) { if (100 + v == tag) last = cnt + 1; cnt += 2; }
    for (int i = 0; i < n; ++i) {
      if (i < T0() && sa(i) == 8 && sv(i) + XD < NK) { if (100 + sv(i) + XD == tag) last = cnt + 1; cnt += 2; }
      if (ldr && wg(i) >= 0) { if (200 + wg(i) == tag) last = cnt; cnt += 1; }
      if (st(i) >= 0) cnt += 1;
    }
    return cnt - 1 - last;
  }
};
template <int NK, int KORD, int PH, int GST, class St>
DI void gemm_g(Ctx& c, f32x4 (&acc)[2][9], u32x4 (&x)[XM][2], u32x2 (&tl)[9], u32x2 (&tl2)[9], WSrc& ws, WSrc& wn, unsigned so, unsigned so2, unsigned flag_off, St&& st) {
  constexpr unsigned RT = KORD == 0 ? 16 : 8;
  constexpr int LA = 6, NS = NK * 9;
  constexpr int XD = XDEP(NK);
  constexpr Sched S{NK, XD};
  constexpr bool LD = PH < 2;
  const unsigned g = c.lane >> 4, q = (c.lane & 15) >> 2, pp = c.lane & 3;
  const unsigned wrow1 = 16 * (g >> 1) + 4 * (g & 1) + q, wrow = KORD == 0 ? 4 * g + q : wrow1;
  unsigned wb[3], wt[2];
#pragma unroll
  for (int i = 0; i < 3; ++i) { wb[i] = c.ldsb + wrow * WP + pp * 8 + i * 7 * 32 * WP; PIN(wb[i]); }
  if constexpr (NK == 18) {
    wt[0] = c.ldsb + (512 + wrow1) * WP + pp * 8; wt[1] = c.ldsb + (544 + wrow1 - 8 * (g >> 1)) * WP + pp * 8;
    PIN(wt[0]); PIN(wt[1]);
  }
  unsigned voff = x_voff(c); PIN(voff);
  bf16x4 wl[8][2];
  auto rdw = [&](auto N_, auto H_) AI {
    constexpr int N = decltype(N_)::v, H = decltype(H_)::v, v = S.sv(N), a = S.sa(N), k = KK(NK, v);
    if constexpr (k < 16) wl[N & 7][H] = lds_tr(wb[k / 7] + ((k % 7) * 32 + H * RT) * WP + a * 32);
    else if constexpr (k == 16) wl[N & 7][H] = lds_tr(wt[0] + H * 8 * WP + a * 32);
    else if constexpr (H == 0) wl[N & 7][H] = lds_tr(wt[1] + a * 32);
    else {
      u32x2 t;
      asm volatile("v_mov_b64 %0, %1" : "=v"(t) : "v"(tl[a]));
      wl[N & 7][1] = __builtin_bit_cast(bf16x4, t);
    }
    if constexpr (LD && H == 1 && ((N < S.T0() && a == 8 && QEND(NK, v)) || N == NS - 1)) { SB(); cnt_add(c, fre(PH, k >> 2)); wlog(c, PH + 1, 0, 4 - (k >> 2)); }
  };
  bool behind = false;
  if constexpr (PH > 0) {
    if (ws.pend) { wait_vm<2 * XD>(); w_flush(c, ws); }
    const unsigned l4 = cnt_read(c, land(PH, 4)), f0 = cnt_read(c, fre(PH - 1, 0));
    behind = (c.w < 4) & (rfl(f0) < 8);
    if (__builtin_expect(rfl(l4) < 3, 0)) w_need<PH == 1>(c, ws, 4, 3);
  }
  prio_gemm(c, behind);
  static_for<LA>([&](auto N) AI { rdw(N, IC<0>{}); rdw(N, IC<1>{}); });
  unsigned lc = 0;
  bool srv = c.w < 4 && PH == 2 && ws.idx == 0;
  SB();
  static_for<NS>([&](auto N_) AI {
    constexpr int N = decltype(N_)::v, v = S.sv(N), a = S.sa(N);
    if constexpr (N < S.T0() ? a == 0 : N < S.T0() + 4) {
      wait_vm<S.younger(100 + v, N, false)>();
      PIN(x[v % XD][0]); PIN(x[v % XD][1]);
    }
    if constexpr (N < S.T0() && a == 0) {
      if constexpr (v == 0 || v == 8) STAMPG(GST + (v >> 3));
      if constexpr (PH == 0 && v == ARRV) { if (c.w < 4) { wait_vm<S.between(0, N)>(); cnt_add(c, land(0, 2)); } }
      constexpr bool chk2 = QSTART(NK, v + 2), chk1 = QSTART(NK, v + 1);
      if constexpr (chk1) {
        if (__builtin_expect(rfl(lc) < (PH > 0 ? 8 : 4), 0)) { if constexpr (PH > 0) { prio_hi(c); w_need<PH == 1>(c, ws, KK(NK, v + 1) >> 2, 8); prio_gemm(c, behind); } else cnt_wait(c, land(0, KK(NK, v + 1) >> 2), 4); }
      }
      if constexpr (PH == 0 && v == 7) park_read(c, tl, tl2);
      if constexpr (chk2) lc = cnt_read(c, land(PH, KK(NK, v + 2) >> 2));
    }
    if constexpr (PH > 0 && S.sp(N) && N < S.T0()) {
      if (__builtin_expect(behind, 0)) { behind = rfl(cnt_read(c, fre(PH - 1, 0))) < 8; prio_gemm(c, behind); }
    }
    if constexpr (GSRV > 0 && PH == 2 && S.sp(N) && N <= S.T0()) {
      if (__builtin_expect(srv, 0)) {
        if (ws.pend) { wait_vm<S.between(N - 18, N)>(); w_flush(c, ws); }
        if constexpr (N < S.T0()) {
          unsigned k = 0;
#pragma unroll 1
          while (k < GSRV && w_try(c, ws)) ++k;
          srv = (ws.idx == 0) | (ws.pend != 0);
        }
      }
    }
    SB();
    const bf16x8 wf = cat(wl[N & 7][0], wl[N & 7][1]);
    mfma16<PH != 1 && v == 0>(acc[0][a], wf, asbf(x[v % XD][0]));
    SB();
    if constexpr (N + LA < NS) rdw(IC<N + LA>{}, IC<0>{});
    SB();
    mfma16<PH != 1 && v == 0>(acc[1][a], wf, asbf(x[v % XD][1]));
    SB();
    if constexpr (N + LA < NS) rdw(IC<N + LA>{}, IC<1>{});
    if constexpr (N < S.T0() && a == 8 && v + XD < NK) x_ld<NK, v + XD>(c, x[v % XD], voff, so, so2);
    if constexpr (S.st(N) >= 0) st(IC<S.st(N)>{});
    SB();
  });
  prio_hi(c);
  mfma_pad();
  st(IC<7>{}); st(IC<8>{});
  STAMPG(GST + 2);
  if constexpr (LD) wsrc_voff(c, wn);
  flag_w(c, flag_off);
}
DI f32x4 relu2(const f32x4& a) {
  const float inf = 3.4028234664e38f;
  const float r0 = __builtin_amdgcn_fmed3f(a[0], 0.f, inf), r1 = __builtin_amdgcn_fmed3f(a[1], 0.f, inf), r2 = __builtin_amdgcn_fmed3f(a[2], 0.f, inf), r3 = __builtin_amdgcn_fmed3f(a[3], 0.f, inf);
  return f32x4{r0 * r0, r1 * r1, r2 * r2, r3 * r3};
}
DI u32x4 pack_single(const f32x4& r0, const f32x4& r1) {
  const auto a = __builtin_amdgcn_permlane32_swap(cvtpk(r0[0], r0[1]), cvtpk(r1[0], r1[1]), false, false);
  const auto b = __builtin_amdgcn_permlane32_swap(cvtpk(r0[2], r0[3]), cvtpk(r1[2], r1[3]), false, false);
  return u32x4{a[0], b[0], a[1], b[1]};
}
template <int I> DI void part_store(Ctx& c, const f32x4 (&acc)[2][9], unsigned base, unsigned so) {
  const unsigned g = c.lane >> 4;
  if constexpr (I == 0) gst16c(pack_single(acc[0][8], acc[1][8]), c.rws, (g >> 1) * (4608 - 128) + base + 1024, so);
  else { constexpr int i = (I - 1) >> 1, u = (I - 1) & 1; gst16c(pack_pair(acc[u][2 * i], acc[u][2 * i + 1]), c.rws, base, so + u * 4608 + i * 256); }
}
DI unsigned part_base(Ctx& c) { const unsigned l15 = c.lane & 15; return (l15 >> 2) * 1152 + (l15 & 3) * 16 + (c.lane >> 4) * 64; }
struct Red { u32x4 p[8]; u32x4 s; u32x4 alo, ahi; };
DI void red_sel(Ctx& c, Red& d) {
  const unsigned i = c.lane & 15, one = 0x3F80u << ((i & 1) * 16);
#pragma unroll
  for (int e = 0; e < 4; ++e) { d.alo[e] = (i >> 1) == (unsigned)e ? one : 0u; d.ahi[e] = (i >> 1) == (unsigned)e + 4 ? one : 0u; }
  PIN(d.alo); PIN(d.ahi);
}
DI void red_load(Ctx& c, Red& d, unsigned so) {
  const unsigned vo = (c.lane >> 4) * 36864 + (c.lane & 15) * 16;
  gld16c<1024>(d.s, vo + ((c.lane >> 3) & 1) * (4 * 36864 - 128), c.rws, so);
  static_for<4>([&](auto P_) AI {
    constexpr int P = decltype(P_)::v;
    gld16c<P * 256>(d.p[2 * P], vo, c.rws, so);
    gld16c<P * 256>(d.p[2 * P + 1], vo, c.rws, so + 4 * 36864);
  });
}
#define RED_PIN(d) asm volatile("s_waitcnt vmcnt(0)" : "+v"(d.p[0]), "+v"(d.p[1]), "+v"(d.p[2]), "+v"(d.p[3]), "+v"(d.p[4]), "+v"(d.p[5]), "+v"(d.p[6]), "+v"(d.p[7]), "+v"(d.s)::"memory")
DI void gld8(u32x2& d, unsigned voff, rsrc_t r, unsigned soff) { asm volatile(VP "buffer_load_dwordx2 %0, %1, %2, %3 offen" : "=v"(d) : "v"(voff), "s"(r), "s"(soff) : "memory"); }
DI bool sing_lane(Ctx& c) { return (c.lane & 40) == 0; }
DI void job_offs(Ctx& c, unsigned row, unsigned& o0, unsigned& o2) {
  const unsigned pr = c.lane >> 4, g = (c.lane >> 2) & 3, nt = c.w & 3;
  o0 = row * (DM * 2) + (nt * 128 + pr * 32 + 4 * g) * 2;
  o2 = row * (DM * 2) + (512 + nt * 16 + 4 * (g & 1) + 8 * (pr & 1)) * 2;
}
DI void row_load(Ctx& c, u32x2 (&r)[3], rsrc_t rs, unsigned row) {
  unsigned o0, o2; job_offs(c, row, o0, o2);
  gld8(r[0], o0, rs, 0); gld8(r[1], o0, rs, 32); gld8(r[2], o2, rs, 0);
}
DI void red_sum(const Red& d, float (&a)[12]) {
  f32x4 s0 = MFMA16(asbf(d.alo), asbf(d.p[0]), f32x4{}), s1 = MFMA16(asbf(d.alo), asbf(d.p[2]), f32x4{}), s2 = MFMA16(asbf(d.alo), asbf(d.s), f32x4{});
  s0 = MFMA16(asbf(d.alo), asbf(d.p[1]), s0); s1 = MFMA16(asbf(d.alo), asbf(d.p[3]), s1);
  s0 = MFMA16(asbf(d.ahi), asbf(d.p[4]), s0); s1 = MFMA16(asbf(d.ahi), asbf(d.p[6]), s1);
  s0 = MFMA16(asbf(d.ahi), asbf(d.p[5]), s0); s1 = MFMA16(asbf(d.ahi), asbf(d.p[7]), s1);
#pragma unroll
  for (int e = 0; e < 4; ++e) {
    a[8 + e] = s2[e] + __uint_as_float(__builtin_amdgcn_update_dpp(0, __float_as_uint(s2[e]), 0x128, 0xf, 0xf, false));
    const auto r = __builtin_amdgcn_permlane16_swap(__float_as_uint(s0[e]), __float_as_uint(s1[e]), false, false);
    a[e] = __uint_as_float(r[0]); a[4 + e] = __uint_as_float(r[1]);
  }
}
DI float sum_tok4(float x) {
  x += __uint_as_float(__builtin_amdgcn_update_dpp(0, __float_as_uint(x), 0x124, 0xf, 0xf, false));
  x += __uint_as_float(__builtin_amdgcn_update_dpp(0, __float_as_uint(x), 0x128, 0xf, 0xf, false));
  { auto r = __builtin_amdgcn_permlane16_swap(__float_as_uint(x), __float_as_uint(x), false, false); x = __uint_as_float(r[0]) + __uint_as_float(r[1]); }
  return xhalf_sum(x);
}
DI void ln2_job(Ctx& c, const Red& d, const u32x2 (&xr)[3], unsigned (&hw)[6], unsigned row0) {
  const unsigned lane = c.lane, tok = lane & 3, g = (lane >> 2) & 3, pr = lane >> 4, nt = c.w & 3;
  const unsigned stat = c.ldsb + LSTAT + (c.w >> 2) * 128;
  const bool sing = sing_lane(c);
  float a[12], v[12];
  red_sum(d, a);
#pragma unroll
  for (int e = 0; e < 6; ++e) {
    const unsigned xx = xr[e >> 1][e & 1];
    hw[e] = cvtpk(bflo(xx) + a[2 * e], bfhi(xx) + a[2 * e + 1]); v[2 * e] = bflo(hw[e]); v[2 * e + 1] = bfhi(hw[e]);
  }
  const float s1 = ((v[0] + v[1]) + (v[2] + v[3])) + ((v[4] + v[5]) + (v[6] + v[7])), s0 = (v[8] + v[9]) + (v[10] + v[11]);
  const float mw = sum_tok4(sing ? s1 + s0 : s1) * (1.f / 144);
  float s2 = 0.f, s3 = 0.f;
#pragma unroll
  for (int e = 0; e < 8; ++e) { const float t = v[e] - mw; s2 += t * t; }
#pragma unroll
  for (int e = 0; e < 4; ++e) { const float t = v[8 + e] - mw; s3 += t * t; }
  s2 = sum_tok4(sing ? s2 + s3 : s2);
  if (lane < 4) *(LAS f32x2*)(size_t)(stat + (lane * 4 + nt) * 8) = f32x2{mw, s2};
  SB();
  cnt_add(c, LSYNC + C_GB + (c.w >> 2) * 4);
  const unsigned row = row0 + tok, mb = row >> 5, r32 = row & 31;
  unsigned o0 = ((mb * 36 + 2 * (nt * 4 + pr) + (g >> 1)) * 64 + (g & 1) * 32 + r32) * 16;
  unsigned o1 = lane < 8 ? ((mb * 36 + 32 + nt) * 64 + g * 32 + r32) * 16 : OOB;
  PIN(o0); PIN(o1);
  cnt_wait(c, LSYNC + C_GB + (c.w >> 2) * 4, 4);
  const f32x4 sa = *(LAS f32x4*)(size_t)(stat + tok * 32), sb = *(LAS f32x4*)(size_t)(stat + tok * 32 + 16);
  const float mean = ((sa[0] + sa[2]) + (sb[0] + sb[2])) * 0.25f;
  const float t0 = sa[0] - mean, t1 = sa[2] - mean, t2 = sb[0] - mean, t3 = sb[2] - mean;
  const float m2 = ((sa[1] + sa[3]) + (sb[1] + sb[3])) + 144.f * ((t0 * t0 + t1 * t1) + (t2 * t2 + t3 * t3));
  const float rs = rsqrtf(m2 * (1.f / DM) + 1e-5f);
#pragma unroll
  for (int e = 0; e < 12; ++e) v[e] = (v[e] - mean) * rs;
  gst16c(u32x4{cvtpk(v[0], v[1]), cvtpk(v[2], v[3]), cvtpk(v[4], v[5]), cvtpk(v[6], v[7])}, c.rws, o0, WS_HN);
  const unsigned u0 = cvtpk(v[8], v[9]), u1 = cvtpk(v[10], v[11]);
  const auto ra = __builtin_amdgcn_permlane16_swap(u0, u0, false, false), rb = __builtin_amdgcn_permlane16_swap(u1, u1, false, false);
  gst16c(u32x4{ra[0], rb[0], ra[1], rb[1]}, c.rws, o1, WS_HN);
}

#define XS0 4
#define XS1 12
DI unsigned phase_attn(Ctx& c, QOut& qo, WSrc& wso, u32x4 (&xr)[XM][2], const u16* wo, const u16* w1, const u16* b1, const u16* w2, const u16* b2) {
  AttnState a;
  const unsigned ldsb = c.ldsb, lane = c.lane, w = c.w, b = c.b;
  const unsigned xcd = b & 7, jj = b >> 3, head = xcd * 4 + (jj >> 3), pi = jj & 7;
  const unsigned kh = w >> 2;
  a.voff = lane * 16;
  a.kb[0] = ldsb + a.voff; PIN(a.kb[0]);
  if (__builtin_expect(!qo.kdone, 0)) {
    wait_flags(c, F_K + head * 64, 8, 1);
#pragma unroll
    for (int p = 0; p < 4; ++p) dma16c(ldsb + kh * 16384 + w * 4096 + p * 1024, a.voff, c.rws, WS_K + head * (SEQ * HD * 2) + w * 4096 + p * 1024);
    wait_vm<0>();
  }
  STAMP(8);
#pragma unroll
  for (int d = 0; d < 8; ++d) a.qf[d] = asbf(qo.q[d]);
  a.negm[0] = a.negm[1] = f32x4{}; pin_r<R_N>(a.negm[0]); pin_r<R_N + 4>(a.negm[1]);
  wait_vm<4>();
  bar();
  STAMP(9);
#pragma unroll
  for (int n = 0; n < 4; ++n) a.fr[n] = lds_b128(a.kb[0] + n * 1024);
  unsigned rg, n1, m2, n1k, kbase, hm; int dg;
  const unsigned mtH = jj >> 2;
  const unsigned fH = F_CTX + (mtH * 32 + xcd * 4) * 8;
  u32x2& fl = a.fl;
  auto post = [&](auto X_, unsigned s) AI {
    post_iter<decltype(X_)::v>(c, a, s);
    SB();
  };
  a.ta = qo.ta;
  SB();
  attn_iter<0, false>(c, a, 0, dg, [&](auto G_) AI {
    constexpr int G = decltype(G_)::v;
    if constexpr (G == 0) { rg = kh ? 7 - w : w; SPIN(rg); }
    if constexpr (G == 1) { n1 = 2 * pi + 2; m2 = 2 * pi; SPIN(n1); SPIN(m2); }
    if constexpr (G == 2) { n1k = kh ? n1 : 0; a.n1t = kh ? n1 : 99; SPIN(n1k); SPIN(a.n1t); }
    if constexpr (G == 3) { kbase = WS_K + head * (SEQ * HD * 2) + w * 4096; SPIN(kbase); }
    if constexpr (G == 4) { a.dk = ldsb + w * 4096; a.d64 = kh << m2; SPIN(a.dk); SPIN(a.d64); }
    if constexpr (G == 5) { a.wide = ~0u << m2; a.u = kbase + 32768; SPIN(a.wide); SPIN(a.u); }
    if constexpr (G == 6) { a.drop = ((a.d64 - kh) >> 1) | (~0u << 14); SPIN(a.drop); }
    if constexpr (G == 7) { a.o0 = kh ? 0xF0000000u : kbase + (WS_V - WS_K); SPIN(a.o0); }
    if constexpr (G == 8) { a.o1 = kh ? 0xF0000000u : kbase + 16384; SPIN(a.o1); }
    if constexpr (G == 9) { a.o2 = n1k > 2 ? 0xF0000000u : a.u; SPIN(a.o2); }
    if constexpr (G == 10) { a.diag = a.d64 * (rg < 2 ? 3u : 2u); SPIN(a.diag); }
    if constexpr (G == 11) { a.diag |= (w & 6) != 2 ? 1u << 16 : 0u; SPIN(a.diag); }
    if constexpr (G == 12) { a.first = 1u | (a.d64 << 2); a.slow = a.first | a.diag; SPIN(a.first); SPIN(a.slow); }
    if constexpr (G == 13) { a.cold = kh ? 7u * a.d64 : 1u << 15; SPIN(a.cold); }
    if constexpr (G == 14) { a.fs = w == 0 ? n1 + 1 : 31; SPIN(a.fs); }
    if constexpr (G == 15) { a.cold |= 1u << a.fs; a.pend = 0; a.tm = 15; SPIN(a.cold); SPIN(a.pend); SPIN(a.tm); }
    if constexpr (G == 16) { a.dsc = rg * 32 - kh * 64; SPIN(a.dsc); }
    if constexpr (G == 17) { a.p1 = ldsb + LE + rg * 8192; SPIN(a.p1); }
    if constexpr (G == 18) { a.p1 = kh ? a.p1 : fH; SPIN(a.p1); }
    if constexpr (G == 19) { a.p0 = WS_CTX + ((pi * 4 + rg) * 256 + head * 8) * 1024; SPIN(a.p0); }
    if constexpr (G == 20) { hm = F_CTX + (pi * 32 + head) * 8; SPIN(hm); }
    if constexpr (G == 21) { a.p0 = kh ? a.p0 : hm; SPIN(a.p0); }
    if constexpr (G < 12) { a.o[G >> 1][G & 1] = f32x4{}; pin_r<R_O + 4 * (G & 15)>(a.o[G >> 1][G & 1]); }
    if constexpr (G >= 16 && G < 20) { a.o[(G - 4) >> 1][G & 1] = f32x4{}; pin_r<R_O + 4 * ((G - 4) & 15)>(a.o[(G - 4) >> 1][G & 1]); }
    if constexpr (G == 12) { a.kb[1] = a.kb[0] + 32768; PIN(a.kb[1]); }
    if constexpr (G == 13) { a.vb[0] = a.kb[0] + 65536; PIN(a.vb[0]); }
    if constexpr (G == 14) { a.vb[1] = a.kb[0] + 98304; PIN(a.vb[1]); }
    if constexpr (G == 15) { a.dummy = 0; PIN(a.dummy); }
    if constexpr (G == 20) { fl = u32x2{0u, 0u}; PIN(fl); }
    if constexpr (G == 24 || G == 25) { a.ls[G & 1] = f32x4{}; pin_r<R_L + 4 * (G & 1)>(a.ls[G & 1]); }
    if constexpr (G == 26) { dg = (int)(lane & 15) - (int)((lane >> 2) & 12); PIN(dg); }
    if constexpr (G == 27) { a.dl[0] = a.dl[1] = 0.f; PIN(a.dl[0]); PIN(a.dl[1]); }
    if constexpr (G == 32) { a.ones = u32x4{0x3F803F80u, 0x3F803F80u, 0x3F803F80u, 0x3F803F80u}; PIN(a.ones); }
    if constexpr (G == 23) { if (w == 7) { wait_vm<0>(); flag_check(c, qo.vf, F_V + head * 64, 8); } }
  });
  post(IC<0>{}, 0);
  for (unsigned s = 1; s < 17; s += 2) {
    attn_iter<1, true>(c, a, s, dg); post(IC<1>{}, s);
    attn_iter<0, true>(c, a, s + 1, dg); post(IC<0>{}, s + 1);
  }
  if (w == 0) *(LAS unsigned*)(size_t)(ldsb + LSYNC + lane * 4) = 0;
  wsrc_setup<0>(c, wso, mk_rsrc(wo, NQ * DM * 2), DM * 2, (jj & 3) * 256, 1024 + (jj & 3) * 32, 512 * xcd, 0, 0);
  const unsigned ol = opaque_lane();
  wso.voff[0] = w_voff(wso, ol, 0); PIN(wso.voff[0]);
  static_for<8>([&](auto C_) AI { cvt_el<decltype(C_)::v>(a.pk, a.S[0]); });
  if (n1k == 16) wait_vm<8>(); else wait_vm<0>();
  bar();
  auto rdv = [&](auto M_) AI { constexpr int m = decltype(M_)::v; a.fr[m & 7] = lds_b128(a.vb[0] + m * 1024); };
  static_for<6>([&](auto M_) AI { rdv(M_); });
  PIN(fl);
  asm volatile("" ::"v"(a.dummy));
  STAMPG(33);
  const unsigned late = ((unsigned)__builtin_amdgcn_ballot_w64(fl[0] != c.ep0 || fl[1] != c.ep1) & 15u) | kh;
  const bool slow = late != 0 && kh == 0;
  const unsigned stat = ldsb + 544 * WP + rg * 1024 + lane * 16, scr = ldsb + rg * 16384 + lane * 16;
  const unsigned xo = WS_CTX + ((mtH * 4 + (w & 3)) * 256 + xcd * 32) * 1024, xv = x_voff(c);
  const unsigned r3 = 384 + 32 * (w & 3);
  auto put = [&](auto D_) AI {
    constexpr int d = decltype(D_)::v;
    *(LAS f32x4*)(size_t)(scr + d * 2048) = a.o[d][0]; *(LAS f32x4*)(size_t)(scr + d * 2048 + 1024) = a.o[d][1];
  };
  if (__builtin_expect(slow, 0)) flag_load(c, fl, fH, 4);
  SB();
  mfma_r<R_L>(a.ls[0], asbf(a.ones), asbf(a.pk[0][0])); mfma_r<R_L + 4>(a.ls[1], asbf(a.ones), asbf(a.pk[0][1]));
  SB();
  static_for<32>([&](auto G_) AI {
    constexpr int G = decltype(G_)::v, M = G >> 1, u = G & 1;
    if constexpr (G == 16) { mfma_r<R_L>(a.ls[0], asbf(a.ones), asbf(a.pk[1][0])); mfma_r<R_L + 4>(a.ls[1], asbf(a.ones), asbf(a.pk[1][1])); SB(); }
    mfma_r<R_O + 8 * (M & 7) + 4 * u>(a.o[M & 7][u], a.fr[M & 7], asbf(a.pk[M >> 3][u]));
    SB();
    if constexpr (u == 1 && M + 6 < 16) rdv(IC<M + 6>{});
    if constexpr (u == 0 && M < 9) dma16_if(ldsb + r3 * WP + M * 1024, wso.voff[M], wso.r, wso.main + r3 * wso.pitch, kh);
    if constexpr (u == 1 && M < 8) { wso.voff[M + 1] = w_voff(wso, ol, M + 1); PIN(wso.voff[M + 1]); }
    if constexpr (EB + G < 32) exp_el<EB + G>(a.S[0]);
    if constexpr (G >= 6 && G < 14) cvt_el<G + 2>(a.pk, a.S[0]);
    if constexpr (u == 0 && M >= XS0 && M - XS0 < XS1) x_ld16_if<M - XS0>(c, xr[M - XS0], xv, xo, late);
    if constexpr (G == 23) { if (kh == 0) { *(LAS f32x4*)(size_t)stat = f32x4{a.negm[0][0], a.negm[1][0], a.ls[0][0], a.ls[1][0]}; put(IC<0>{}); } }
    if constexpr (G == 27) { if (kh == 0) { put(IC<1>{}); put(IC<2>{}); } }
    if constexpr (G == 31) { if (kh == 0) { put(IC<3>{}); put(IC<4>{}); } }
    SB();
  });
  mfma_pad();
  STAMPG(34);
  if (kh == 0) { put(IC<5>{}); put(IC<6>{}); put(IC<7>{}); } else wait_vm<0>();
  STAMP(10);
  STAMPG(35);
  bar();
  STAMPG(36);
  if (kh == 0) {
    if (n1 == 16) flag_store(c, F_CTX + (7 * 32 + head) * 8);
    w_group(c, wso, 2);
    if (__builtin_expect(slow, 0)) { wait_vm<9>(); flag_check(c, fl, fH, 4); }
  }
  static_for<XM - XS1>([&](auto V_) AI { constexpr int v = XS1 + decltype(V_)::v; x_ld16_if<v>(c, xr[v], xv, xo, late); });
  if (kh) {
    f32x4 y[2][4];
    auto get = [&](int d) AI {
#pragma unroll
      for (int e = 0; e < 4; ++e) y[d & 1][e] = *(LAS f32x4*)(size_t)(scr + (d * 4 + e) * 1024);
    };
    const f32x4 ps = *(LAS f32x4*)(size_t)stat;
    get(0); get(1);
    SB();
    float f[2], f2[2];
#pragma unroll
    for (int u = 0; u < 2; ++u) {
      const float mm = fminf(a.negm[u][0], ps[u]);
      const float fo = __builtin_amdgcn_exp2f(mm - a.negm[u][0]), fp = __builtin_amdgcn_exp2f(mm - ps[u]);
      const float inv = __builtin_amdgcn_rcpf(a.ls[u][0] * fo + ps[2 + u] * fp);
      f[u] = fo * inv; f2[u] = fp * inv;
    }
    const unsigned so = WS_CTX + (((15 - pi) * 4 + rg) * 256 + head * 8) * 1024;
#pragma unroll
    for (int d = 0; d < 4; ++d) {
      u32x4 p0 = pack_pair(a.o[2 * d][0] * f[0] + y[d & 1][0] * f2[0], a.o[2 * d + 1][0] * f[0] + y[d & 1][2] * f2[0]);
      u32x4 p1 = pack_pair(a.o[2 * d][1] * f[1] + y[d & 1][1] * f2[1], a.o[2 * d + 1][1] * f[1] + y[d & 1][3] * f2[1]);
      PIN(p0); PIN(p1);
      SB();
      if (d < 2) get(d + 2);
      if (d == 1) { SB(); cnt_add(c, LSYNC + C_MRG); }
      gst16c(p0, c.rws, xv, so + d * 2048);
      gst16c(p1, c.rws, xv, so + d * 2048 + 256);
      SB();
    }
    STAMPG(37);
    cnt_wait(c, LSYNC + C_MRG, 4);
    w_group(c, wso, 1);
    wait_vm<9>();
    STAMPG(38);
    fput(u32x2{c.ep0, c.ep1}, c.rws, lane == 0 ? F_CX2 + (((7 - pi) * 4 + rg) * 32 + head) * 8 : OOB);
    const unsigned fL = F_CX2 + ((mtH * 4 + (w & 3)) * 32 + xcd * 4) * 8;
    flag_load(c, fl, fL, 4);
    static_for<5>([&](auto P_) AI { park_load<decltype(P_)::v>(c, w1, b1, w2, b2); });
    w_group(c, wso, 0);
    wait_vm<9>(); cnt_add(c, land(0, 1));
    wait_vm<0>(); cnt_add(c, land(0, 0));
    STAMPG(39);
    prio_lo();
    flag_check(c, fl, fL, 4);
  }
  STAMP(11);
  return late;
}

struct TId { unsigned sp, nt, mt, nt32, mb, own, wq; bool isH; };
DI TId tids(Ctx& c) {
  fresh(c);
  TId t;
  const unsigned xcd = c.b & 7, jj = c.b >> 3;
  t.isH = c.w < 4; t.wq = c.w & 3; t.sp = xcd; t.nt = jj & 3; t.mt = (t.isH ? 0 : 8) + (jj >> 2);
  t.nt32 = xcd * 4 + t.nt; t.mb = t.mt * 4 + t.wq; t.own = t.nt * 8 + t.sp;
  return t;
}
DI void acc_zero(f32x4 (&acc)[2][9]) {
#pragma unroll
  for (int u = 0; u < 2; ++u)
#pragma unroll
    for (int a = 0; a < 9; ++a) acc[u][a] = f32x4{};
}
DI void phase_tail(Ctx& c, WSrc& wso, u32x4 (&xr)[XM][2], unsigned xlate, const u16* x, const u16* w1, const u16* b1, const u16* w2, const u16* b2, u16* out) {
  f32x4 acc[2][9]; u32x2 tl[9], tl2[9]; unsigned hw[6]; WSrc ws, wt;
  {
    const TId t = tids(c);
    const unsigned xo = WS_CTX + (t.mb * 256 + t.sp * 32) * 1024;
    prio_hi(c);
    STAMPG(12);
    x_pro_masked16(c, xr, xo, xlate);
    wsrc_setup(c, ws, mk_rsrc(w1, DM * FF * 2), FF * 2, t.nt32 * 256, 8192 + t.nt32 * 32, 0, 512, 1);
    const unsigned pb = part_base(c), po = WS_P1 + (((t.mt * 4 + t.nt) * 8 + t.sp) * 8 + 2 * t.wq) * 4608;
    gemm_g<16, 0, 0, 13>(c, acc, xr, tl, tl2, wso, ws, xo, 0, cnt_at(t.mt, N_P1 + t.wq * 4 + t.nt), [&](auto I) AI { part_store<decltype(I)::v>(c, acc, pb, po); });
    STAMPG(16);
  }
  {
    const TId t = tids(c);
    u32x2 xres[3]; Red rd;
    row_load(c, xres, mk_rsrc(x, SEQ * DM * 2), t.mt * 128 + t.own * 4 + (c.lane & 3));
    unsigned dummy = 0;
    w_touch<4>(c, dummy, w2, w2, w2, c.w, false);
    poll_srv(c, ws, cnt_at(t.mt, N_P1 + t.nt * 4 + t.wq), 8, t.isH ? MXA : MXB, (SK1 & 1) ? 0 : 2);
    red_load(c, rd, WS_P1 + (t.mt * 256 + t.wq * 64 + (t.own >> 2)) * 4608 + (t.own & 3) * 1152);
    red_sel(c, rd);
    STAMPG(17);
    asm volatile("" ::"v"(dummy));
    PIN(xres[0]); PIN(xres[1]); PIN(xres[2]);
    RED_PIN(rd);
    w_flush(c, ws);
    STAMPG(18);
    ln2_job(c, rd, xres, hw, t.mt * 128 + t.own * 4);
#pragma unroll
    for (int e = 0; e < 6; ++e) PIN(hw[e]);
    STAMPG(19);
    flag_w(c, cnt_at(t.mt, N_HN + t.nt));
    STAMPG(20);
    poll_srv(c, ws, cnt_at(t.mt, N_HN + t.wq), 32, t.isH ? MXA : MXB, (SK1 & 2) ? 0 : 2);
    STAMPG(21);
    asm volatile("" ::"v"(dummy));
    const unsigned xh = WS_HN + t.mb * 36 * 1024;
    x_pro<18>(c, xr, x_voff(c), xh, xh + 32768);
    wsrc_setup(c, wt, mk_rsrc(w2, FF * DM * 2), DM * 2, t.nt * 256, 1024 + t.nt * 32, 512 * t.sp, 4096 + 64 * t.sp, 2);
#pragma unroll
    for (int a = 0; a < 9; ++a) {
      const u32x2 bias = *(LAS u32x2*)(size_t)(c.ldsb + LB1 + a * 32 + (c.lane >> 4) * 8);
      acc[0][a] = acc[1][a] = f32x4{bflo(bias[0]), bfhi(bias[0]), bflo(bias[1]), bfhi(bias[1])};
      PIN(acc[0][a]); PIN(acc[1][a]);
    }
    const unsigned vo = x_voff(c), ho = WS_H1 + (t.mb * 288 + 8 * t.nt32) * 1024, hs = WS_H1 + (t.mb * 288 + 256 + t.nt32) * 1024;
    gemm_g<18, 0, 1, 22>(c, acc, xr, tl, tl, ws, wt, xh, xh + 32768, cnt_at(t.mt, N_H1 + t.wq * 8 + t.sp), [&](auto I_) AI {
      constexpr int I = decltype(I_)::v;
      if constexpr (I == 0) {
        const unsigned g = c.lane >> 4;
        gst16c(pack_single(relu2(acc[0][8]), relu2(acc[1][8])), c.rws, ((g & 1) * 32 + (g >> 1) * 16 + (c.lane & 15)) * 16, hs);
      } else {
        constexpr int i = (I - 1) >> 1, u = (I - 1) & 1;
        gst16c(pack_pair(relu2(acc[u][2 * i]), relu2(acc[u][2 * i + 1])), c.rws, vo, ho + i * 2048 + u * 256);
      }
    });
    STAMPG(25);
  }
  {
    const TId t = tids(c);
    poll_srv(c, wt, cnt_at(t.mt, N_H1 + t.wq * 8 + t.sp), 4, t.isH ? MXC : MXD, ((SK1 & 4) && !t.isH) ? 0 : 2);
    STAMPG(26);
    const unsigned x1 = WS_H1 + (t.mb * 288 + t.sp * 32) * 1024, x1t = WS_H1 + (t.mb * 288 + 256 + t.sp * 4) * 1024;
    x_pro<18>(c, xr, x_voff(c), x1, x1t);
    const unsigned pb = part_base(c), po = WS_P2 + (((t.mt * 4 + t.nt) * 8 + t.sp) * 8 + 2 * t.wq) * 4608;
    gemm_g<18, 0, 2, 27>(c, acc, xr, tl2, tl2, wt, wt, x1, x1t, cnt_at(t.mt, N_P2 + t.wq * 4 + t.nt), [&](auto I) AI { part_store<decltype(I)::v>(c, acc, pb, po); });
    STAMPG(30);
  }
  {
    const TId t = tids(c);
    float hb[12]; Red rd;
    {
      unsigned o0, o2; job_offs(c, 0, o0, o2);
      const u32x2 bb[3] = {*(LAS u32x2*)(size_t)(c.ldsb + LB2 + o0), *(LAS u32x2*)(size_t)(c.ldsb + LB2 + o0 + 32), *(LAS u32x2*)(size_t)(c.ldsb + LB2 + o2)};
#pragma unroll
      for (int e = 0; e < 6; ++e) { const unsigned bv = bb[e >> 1][e & 1]; hb[2 * e] = bflo(bv) + bflo(hw[e]); hb[2 * e + 1] = bfhi(bv) + bfhi(hw[e]); PIN(hb[2 * e]); PIN(hb[2 * e + 1]); }
      red_sel(c, rd);
    }
    poll_w(c, cnt_at(t.mt, N_P2 + t.nt * 4 + t.wq), 8);
    STAMPG(31);
    red_load(c, rd, WS_P2 + (t.mt * 256 + t.wq * 64 + (t.own >> 2)) * 4608 + (t.own & 3) * 1152);
    RED_PIN(rd);
    const rsrc_t ro = mk_rsrc(out, SEQ * DM * 2);
    float a[12]; unsigned ow[6];
    red_sum(rd, a);
#pragma unroll
    for (int e = 0; e < 6; ++e) ow[e] = cvtpk(a[2 * e] + hb[2 * e], a[2 * e + 1] + hb[2 * e + 1]);
    {
      const unsigned g = (c.lane >> 2) & 3, ro0 = (t.mt * 128 + t.own * 4 + (c.lane & 3)) * (DM * 2);
      u32x4 q, q2;
      q[0] = __builtin_amdgcn_update_dpp(ow[0], ow[2], 0x114, 0xf, 0xa, false); q[1] = __builtin_amdgcn_update_dpp(ow[1], ow[3], 0x114, 0xf, 0xa, false);
      q[2] = __builtin_amdgcn_update_dpp(ow[2], ow[0], 0x104, 0xf, 0x5, false); q[3] = __builtin_amdgcn_update_dpp(ow[3], ow[1], 0x104, 0xf, 0x5, false);
      q2[0] = ow[4]; q2[1] = ow[5];
      q2[2] = __builtin_amdgcn_update_dpp(0, ow[4], 0x104, 0xf, 0xf, false); q2[3] = __builtin_amdgcn_update_dpp(0, ow[5], 0x104, 0xf, 0xf, false);
      __builtin_amdgcn_raw_buffer_store_b128(q, ro, ro0 + t.wq * 256 + (c.lane >> 4) * 64 + (g & 1) * 32 + (g >> 1) * 16, 0, 0);
      __builtin_amdgcn_raw_buffer_store_b128(q2, ro, (c.lane & 44) == 0 ? ro0 + 1024 + t.wq * 32 + (c.lane >> 4) * 16 : OOB, 0, 0);
    }
    STAMPG(32);
    if (c.b == 0 && c.w == 0) gst8c(u32x2{c.ep0, c.ep1}, c.rws, c.lane == 0 ? F_EPOCH : OOB, WS_FLAGS);
  }
}

__global__ __launch_bounds__(512, 2) void fused_block(const u16* __restrict__ x, const u16* __restrict__ ckv, const u16* __restrict__ wq,
                                                      const u16* __restrict__ wuk, const u16* __restrict__ wuv, const u16* __restrict__ wo,
                                                      const u16* __restrict__ w1, const u16* __restrict__ b1, const u16* __restrict__ w2,
                                                      const u16* __restrict__ b2, u16* __restrict__ out, char* __restrict__ ws) {
  extern __shared__ __attribute__((aligned(16))) char lds[];
  Ctx c;
  asm volatile("s_getpc_b64 %0\n.Lfb_begin:" : "=s"(c.pc0));
  c.ldsb = (unsigned)(size_t)lds; c.lane = threadIdx.x & 63; c.w = rfl(threadIdx.x >> 6); c.b = blockIdx.x;
  c.rws = mk_rsrc(ws, WS_END);
  asm volatile("" : "+s"(wo), "+s"(w1), "+s"(w2), "+s"(b1), "+s"(b2), "+s"(out), "+s"(wq), "+s"(wuv));
  STAMP(0);
  QOut qo;
  WSrc wso; u32x4 xr[XM][2];
  phase_qkv(c, qo, x, ckv, wq, wuk, wuv, wo, w1, w2);
  fresh(c);
  const unsigned xlate = phase_attn(c, qo, wso, xr, wo, w1, b1, w2, b2);
  fresh(c);
  phase_tail(c, wso, xr, xlate, x, w1, b1, w2, b2, out);
  asm volatile(".Lfb_end:");
}

extern "C" void kernel_launch(void* const* d_in, const int* in_sizes, int n_in, void* d_out, int out_size, void* d_ws, size_t ws_size,
                              hipStream_t stream) {
  hipFuncSetAttribute((const void*)fused_block, hipFuncAttributeMaxDynamicSharedMemorySize, 163840);
  fused_block<<<256, 512, 163840, stream>>>((const u16*)d_in[0], (const u16*)d_in[1], (const u16*)d_in[2], (const u16*)d_in[3], (const u16*)d_in[4],
                                            (const u16*)d_in[5], (const u16*)d_in[7], (const u16*)d_in[8], (const u16*)d_in[9], (const u16*)d_in[10],
                                            (u16*)d_out, (char*)d_ws);
}
```
